```python
import torch

def _softmax(x, axis=-1):
    return torch.softmax(x, dim=axis)
def _gelu(x):
    return torch.nn.functional.gelu(x, approximate="tanh")
def _silu(x):
    return torch.nn.functional.silu(x)
def _sigmoid(x):
    return torch.sigmoid(x)
def _layernorm(x, axis=-1, eps=1e-5):
    m = x.mean(dim=axis, keepdim=True)
    v = x.var(dim=axis, keepdim=True, unbiased=False)
    return (x - m) / torch.sqrt(v + eps)
def _scatter_add(tgt, idx, src, axis=0):
    return tgt.clone().index_add_(axis, idx, src)
def _segment_sum(src, seg, n):
    out = torch.zeros((n,) + src.shape[1:], dtype=src.dtype)
    return out.index_add_(0, seg, src)
def _pad(x, pw):
    flat = tuple(v for pair in reversed(pw) for v in pair)
    return torch.nn.functional.pad(x, flat)

kernel_name = 'standalone__mha_block_cached__near_ridge'

def setup_inputs():

    _w = (lambda a: (lambda t: t.to(torch.bfloat16) if t.is_floating_point() else t)(torch.as_tensor(a)))
    B, Sq, D = 16, 144, 576
    H, Sk, Dh = 32, 1024, 128
    return {
        "x": _w((lambda *s: torch.randn(*s, dtype=torch.bfloat16) * 0.8)(B, Sq, D)),
        "k_cache": _w((lambda *s: torch.randn(*s, dtype=torch.bfloat16) * 0.8)(B, H, Sk, Dh)),
        "v_cache": _w((lambda *s: torch.randn(*s, dtype=torch.bfloat16) * 0.05)(B, H, Sk, Dh)),
        "wq": _w((lambda *s: torch.randn(*s, dtype=torch.bfloat16) * 0.8)(D, H * Dh) * (D ** -0.5)),
        "wo": _w((lambda *s: torch.randn(*s, dtype=torch.bfloat16) * 0.05)(H * Dh, D) * ((H * Dh) ** -0.5)),
    }

def reference(x, k_cache, v_cache, wq, wo):
    B, Sq, D = x.shape
    H, Dh = 32, 128
    q = (lambda x, *d: x.permute(*d))((x @ wq).reshape(B, Sq, H, Dh), 0, 2, 1, 3)
    scores = torch.einsum("bhqd,bhkd->bhqk", q, k_cache) * (Dh ** -0.5)
    p = _softmax(scores, axis=-1)
    o = torch.einsum("bhqk,bhkd->bhqd", p, v_cache)
    return (lambda x, *d: x.permute(*d))(o, 0, 2, 1, 3).reshape(B, Sq, H * Dh) @ wo
```

```cpp
#include <hip/hip_runtime.h>
#include <hip/hip_bf16.h>
typedef unsigned short u16;
__device__ __forceinline__ float bf2f(u16 x){ return __uint_as_float(((unsigned)x)<<16); }
__device__ __forceinline__ u16 f2bf(float f){ unsigned u=__float_as_uint(f); u += 0x7fffu + ((u>>16)&1u); return (u16)(u>>16); }
constexpr int B=16,SQ=144,D=576,H=32,SK=1024,DH=128;
__global__ void gemm(const u16* A,const u16* W,u16* C,int M,int N,int K){
  int n=blockIdx.x*64+threadIdx.x; int m0=blockIdx.y*8;
  float acc[8]={0,0,0,0,0,0,0,0};
  for(int k=0;k<K;k++){ float w=bf2f(W[(size_t)k*N+n]);
    #pragma unroll
    for(int i=0;i<8;i++) acc[i]+=bf2f(A[(size_t)(m0+i)*K+k])*w; }
  #pragma unroll
  for(int i=0;i<8;i++) C[(size_t)(m0+i)*N+n]=f2bf(acc[i]);
}
__global__ void attn(const u16* Q,const u16* Kc,const u16* Vc,u16* O){
  __shared__ float s[SK]; __shared__ float q[DH]; __shared__ float red[256];
  int r=blockIdx.x, h=blockIdx.y, b=blockIdx.z, t=threadIdx.x;
  if(t<DH) q[t]=bf2f(Q[(size_t)(b*SQ+r)*(H*DH)+h*DH+t]);
  __syncthreads();
  const u16* K=Kc+(size_t)(b*H+h)*SK*DH; const u16* V=Vc+(size_t)(b*H+h)*SK*DH;
  float mx=-1e30f;
  for(int k=t;k<SK;k+=256){ float a=0; for(int d=0;d<DH;d++) a+=q[d]*bf2f(K[(size_t)k*DH+d]); a*=0.08838834764831845f; s[k]=a; mx=fmaxf(mx,a);}
  red[t]=mx; __syncthreads();
  for(int o=128;o>0;o>>=1){ if(t<o) red[t]=fmaxf(red[t],red[t+o]); __syncthreads(); }
  mx=red[0]; __syncthreads();
  float sum=0; for(int k=t;k<SK;k+=256){ float e=__expf(s[k]-mx); s[k]=e; sum+=e; }
  red[t]=sum; __syncthreads();
  for(int o=128;o>0;o>>=1){ if(t<o) red[t]+=red[t+o]; __syncthreads(); }
  sum=red[0];
  if(t<DH){ float a=0; for(int k=0;k<SK;k++) a+=s[k]*bf2f(V[(size_t)k*DH+t]); O[(size_t)(b*SQ+r)*(H*DH)+h*DH+t]=f2bf(a/sum); }
}
extern "C" void kernel_launch(void* const* d_in, const int* in_sizes, int n_in, void* d_out, int out_size, void* d_ws, size_t ws_size, hipStream_t stream) {
  const u16* x=(const u16*)d_in[0]; const u16* k=(const u16*)d_in[1]; const u16* v=(const u16*)d_in[2]; const u16* wq=(const u16*)d_in[3]; const u16* wo=(const u16*)d_in[4];
  u16* q=(u16*)d_ws; u16* o=q+(size_t)B*SQ*H*DH;
  gemm<<<dim3(H*DH/64,B*SQ/8),64,0,stream>>>(x,wq,q,B*SQ,H*DH,D);
  attn<<<dim3(SQ,H,B),256,0,stream>>>(q,k,v,o);
  gemm<<<dim3(D/64,B*SQ/8),64,0,stream>>>(o,wo,(u16*)d_out,B*SQ,D,H*DH);
}
```

```cpp
#include <hip/hip_runtime.h>
#include <hip/hip_bf16.h>
typedef unsigned short u16;
__device__ __forceinline__ float bf2f(u16 x){ return __uint_as_float(((unsigned)x)<<16); }
__device__ __forceinline__ u16 f2bf(float f){ unsigned u=__float_as_uint(f); u += 0x7fffu + ((u>>16)&1u); return (u16)(u>>16); }
constexpr int B=16,SQ=144,D=576,H=32,SK=1024,DH=128;
__global__ void gemm(const u16* A,const u16* W,u16* C,int M,int N,int K){
  int n=blockIdx.x*64+threadIdx.x; int m0=blockIdx.y*8;
  float acc[8]={0,0,0,0,0,0,0,0};
  for(int k=0;k<K;k++){ float w=bf2f(W[(size_t)k*N+n]);
    #pragma unroll
    for(int i=0;i<8;i++) acc[i]+=bf2f(A[(size_t)(m0+i)*K+k])*w; }
  #pragma unroll
  for(int i=0;i<8;i++) C[(size_t)(m0+i)*N+n]=f2bf(acc[i]);
}
__global__ void attn(const u16* Q,const u16* Kc,const u16* Vc,u16* O){
  __shared__ float s[SK]; __shared__ float q[DH]; __shared__ float red[256];
  int r=blockIdx.x, h=blockIdx.y, b=blockIdx.z, t=threadIdx.x;
  if(t<DH) q[t]=bf2f(Q[(size_t)(b*SQ+r)*(H*DH)+h*DH+t]);
  __syncthreads();
  const u16* K=Kc+(size_t)(b*H+h)*SK*DH; const u16* V=Vc+(size_t)(b*H+h)*SK*DH;
  float mx=-1e30f;
  for(int k=t;k<SK;k+=256){ float a=0; for(int d=0;d<DH;d++) a+=q[d]*bf2f(K[(size_t)k*DH+d]); a*=0.08838834764831845f; s[k]=a; mx=fmaxf(mx,a);}
  red[t]=mx; __syncthreads();
  for(int o=128;o>0;o>>=1){ if(t<o) red[t]=fmaxf(red[t],red[t+o]); __syncthreads(); }
  mx=red[0]; __syncthreads();
  float sum=0; for(int k=t;k<SK;k+=256){ float e=__expf(s[k]-mx); s[k]=e; sum+=e; }
  red[t]=sum; __syncthreads();
  for(int o=128;o>0;o>>=1){ if(t<o) red[t]+=red[t+o]; __syncthreads(); }
  sum=red[0];
  if(t<DH){ float a=0; for(int k=0;k<SK;k++) a+=s[k]*bf2f(V[(size_t)k*DH+t]); O[(size_t)(b*SQ+r)*(H*DH)+h*DH+t]=f2bf(a/sum); }
}
template<int NW,int DEPTH,int AUX>
__global__ __launch_bounds__(NW*64) void rd_dma(const char* __restrict__ K, const char* __restrict__ V, unsigned* out){
  extern __shared__ __attribute__((aligned(16))) char lds[];
  const int w=__builtin_amdgcn_readfirstlane(threadIdx.x>>6), lane=threadIdx.x&63;
  constexpr int PER=32/NW;
  __amdgpu_buffer_rsrc_t rk=__builtin_amdgcn_make_buffer_rsrc((void*)K,(short)0,0x8000000,0x00020000);
  __amdgpu_buffer_rsrc_t rv=__builtin_amdgcn_make_buffer_rsrc((void*)V,(short)0,0x8000000,0x00020000);
  unsigned ldsb=(unsigned)(size_t)lds;
  unsigned voff=lane*16;
  unsigned acc=0;
  const int NTILE=32;
  auto issue=[&](int t){
    int head=blockIdx.x+256*(t>>4); unsigned so=head*262144u+(t&15)*16384u+w*(PER/2)*1024u;
    unsigned slot=ldsb+(t%(DEPTH+1))*32768u+w*(PER/2)*1024u;
    #pragma unroll
    for(int p=0;p<PER/2;p++){
      if(AUX==2){
      asm volatile("s_mov_b32 m0, %0\n\ts_nop 0\n\tbuffer_load_dwordx4 %1, %2, %3 offen nt lds"::"s"(slot+p*1024u),"v"(voff),"s"(rk),"s"(so+p*1024u):"memory");
      asm volatile("s_mov_b32 m0, %0\n\ts_nop 0\n\tbuffer_load_dwordx4 %1, %2, %3 offen nt lds"::"s"(slot+16384u+p*1024u),"v"(voff),"s"(rv),"s"(so+p*1024u):"memory");
      } else {
      asm volatile("s_mov_b32 m0, %0\n\ts_nop 0\n\tbuffer_load_dwordx4 %1, %2, %3 offen lds"::"s"(slot+p*1024u),"v"(voff),"s"(rk),"s"(so+p*1024u):"memory");
      asm volatile("s_mov_b32 m0, %0\n\ts_nop 0\n\tbuffer_load_dwordx4 %1, %2, %3 offen lds"::"s"(slot+16384u+p*1024u),"v"(voff),"s"(rv),"s"(so+p*1024u):"memory");
      }
    }
  };
  for(int t=0;t<DEPTH;t++) issue(t);
  for(int t=0;t<NTILE;t++){
    if(t+DEPTH<=NTILE) { asm volatile("s_waitcnt vmcnt(%0)"::"i"((DEPTH-1)*PER):"memory"); }
    else asm volatile("s_waitcnt vmcnt(0)":::"memory");
    __builtin_amdgcn_s_barrier();
    if(t+DEPTH<NTILE) issue(t+DEPTH);
    acc^=*(volatile unsigned*)(lds+(t%(DEPTH+1))*32768+threadIdx.x*4);
  }
  if(acc==0x12345678) out[0]=1;
}
extern "C" void kernel_launch(void* const* d_in, const int* in_sizes, int n_in, void* d_out, int out_size, void* d_ws, size_t ws_size, hipStream_t stream) {
  const u16* x=(const u16*)d_in[0]; const u16* k=(const u16*)d_in[1]; const u16* v=(const u16*)d_in[2]; const u16* wq=(const u16*)d_in[3]; const u16* wo=(const u16*)d_in[4];
  u16* q=(u16*)d_ws; u16* o=q+(size_t)B*SQ*H*DH;
  gemm<<<dim3(H*DH/64,B*SQ/8),64,0,stream>>>(x,wq,q,B*SQ,H*DH,D);
  attn<<<dim3(SQ,H,B),256,0,stream>>>(q,k,v,o);
  gemm<<<dim3(D/64,B*SQ/8),64,0,stream>>>(o,wo,(u16*)d_out,B*SQ,D,H*DH);
}
```

```cpp
#include <hip/hip_runtime.h>
__global__ __launch_bounds__(512,2) void mha_k1(const void* x,const void* k,const void* v,const void* wq,void* ws,const void* wo,void* out){
  auto ka=__builtin_amdgcn_kernarg_segment_ptr();
  unsigned w=__builtin_amdgcn_readfirstlane(threadIdx.x>>6);
  asm volatile(
"s_mov_b64 s[4:5], %0\n"
"s_mov_b32 s6, %1\n"
"s_mov_b32 s7, %2\n"
"s_load_dwordx8 s[8:15], s[4:5], 0x0\n"
"s_load_dwordx4 s[16:19], s[4:5], 0x20\n"
"s_load_dwordx2 s[96:97], s[4:5], 0x30\n"
"s_memrealtime s[72:73]\n"
"v_mbcnt_lo_u32_b32 v238, -1, 0\n"
"v_mbcnt_hi_u32_b32 v238, -1, v238\n"
"s_and_b32 s44, s7, 3\n"
"s_lshr_b32 s45, s7, 2\n"
"s_lshl_b32 s48, s44, 12\n"
"s_lshl_b32 s59, s44, 11\n"
"s_and_b32 s60, s6, 4\n"
"s_lshl_b32 s46, s60, 1\n"
"s_lshr_b32 s60, s6, 5\n"
"s_add_u32 s46, s46, s60\n"
"s_and_b32 s60, s6, 3\n"
"s_lshl_b32 s47, s60, 2\n"
"s_lshr_b32 s60, s6, 3\n"
"s_and_b32 s60, s60, 3\n"
"s_add_u32 s47, s47, s60\n"
"s_lshl_b32 s60, s45, 4\n"
"s_add_u32 s66, s47, s60\n"
"s_mov_b32 s65, 0\n"
"s_mov_b32 s70, 0x4000\n"
"s_mov_b32 s63, 0x3e0293ee\n"
"s_mov_b32 s62, 0x3e0293ee\n"
"s_mov_b32 s64, 0x46800000\n"
"v_bfe_u32 v247, v238, 2, 3\n"
"v_bfe_u32 v236, v238, 4, 1\n"
"v_lshrrev_b32 v237, 5, v238\n"
"v_lshlrev_b32 v237, 6, v237\n"
"v_and_b32 v239, 3, v238\n"
"v_mul_u32_u24 v200, 3, v236\n"
"v_xor_b32 v200, v200, v239\n"
"v_lshl_add_u32 v200, v200, 4, v237\n"
"v_lshl_add_u32 v206, v247, 8, v200\n"
"v_xor_b32 v207, 32, v206\n"
"v_mul_u32_u24 v210, 0x480, v247\n"
"v_add_u32 v210, v210, v200\n"
"v_xor_b32 v211, 32, v210\n"
"v_add_u32 v211, 0x2400, v211\n"
"v_lshlrev_b32 v200, 1, v236\n"
"v_xor_b32 v200, v200, v239\n"
"v_lshl_add_u32 v200, v200, 4, v237\n"
"v_lshl_add_u32 v208, v247, 8, v200\n"
"v_lshlrev_b32 v209, 3, v236\n"
"v_and_b32 v201, 3, v247\n"
"v_add_u32 v209, v209, v201\n"
"v_lshl_add_u32 v209, v209, 13, v200\n"
"s_mul_i32 s57, s44, 0x9000\n"
"s_and_b32 s60, s44, 1\n"
"s_lshl_b32 s54, s60, 12\n"
"s_lshl_b32 s55, s60, 17\n"
"s_lshr_b32 s60, s44, 1\n"
"s_lshl_b32 s60, s60, 15\n"
"s_add_u32 s55, s55, s60\n"
"s_mul_i32 s33, s6, 0x5000\n"
"s_add_u32 s56, s48, s33\n"
"s_add_u32 s54, s54, s33\n"
"s_add_u32 s49, s56, 0x3e000\n"
"s_waitcnt lgkmcnt(0)\n"
"s_mov_b32 s75, s72\n"
"s_mul_i32 s60, s46, 0x28800\n"
"s_add_u32 s28, s8, s60\n"
"s_addc_u32 s29, s9, 0\n"
"s_and_b32 s29, s29, 0xffff\n"
"s_mov_b32 s28, s28\n"
"s_mov_b32 s30, 0x28800\n"
"s_mov_b32 s31, 0x20000\n"
"s_lshl_b32 s60, s47, 8\n"
"s_add_u32 s36, s14, s60\n"
"s_addc_u32 s37, s15, 0\n"
"s_and_b32 s37, s37, 0xffff\n"
"s_mov_b32 s36, s36\n"
"s_mov_b32 s38, 0\n"
"s_mov_b32 s39, 0x20000\n"
"s_sub_u32 s38, 0x480000, s60\n"
"s_cmp_eq_u32 s45, 0\n"
"s_cbranch_scc0 .Lk1_w1\n"
"s_and_b32 s89, s15, 0xffff\n"
"s_mov_b32 s88, s14\n"
"s_mov_b32 s90, 0x480000\n"
"s_mov_b32 s91, 0x20000\n"
"s_and_b32 s93, s9, 0xffff\n"
"s_mov_b32 s92, s8\n"
"s_mov_b32 s94, 0x288000\n"
"s_mov_b32 s95, 0x20000\n"
"s_lshl_b32 s67, s6, 2\n"
"s_add_u32 s67, s67, s44\n"
"v_lshlrev_b32 v247, 4, v238\n"
"s_lshr_b32 s60, s67, 2\n"
"s_lshl_b32 s60, s60, 13\n"
"s_and_b32 s61, s67, 3\n"
"s_lshl_b32 s61, s61, 10\n"
"s_add_u32 s68, s60, s61\n"
"s_lshl_b32 s69, s67, 10\n"
"s_add_u32 s60, s68, 0\n"
"buffer_load_dwordx4 v[240:243], v247, s[88:91], s60 offen\n"
"s_add_u32 s60, s69, 0\n"
"buffer_load_dwordx4 v[240:243], v247, s[92:95], s60 offen\n"
"s_add_u32 s60, s68, 0x200000\n"
"buffer_load_dwordx4 v[240:243], v247, s[88:91], s60 offen\n"
"s_add_u32 s60, s69, 0x100000\n"
"buffer_load_dwordx4 v[240:243], v247, s[92:95], s60 offen\n"
"s_cmp_lt_u32 s67, 0x100\n"
"s_cbranch_scc0 .Lk1_mw1\n"
"s_add_u32 s60, s68, 0x400000\n"
"buffer_load_dwordx4 v[240:243], v247, s[88:91], s60 offen\n"
".Lk1_mw1:\n"
"s_cmp_lt_u32 s67, 0x220\n"
"s_cbranch_scc0 .Lk1_mw2\n"
"s_add_u32 s60, s69, 0x200000\n"
"buffer_load_dwordx4 v[240:243], v247, s[92:95], s60 offen\n"
".Lk1_mw2:\n"
"s_getpc_b64 s[88:89]\n"
".Lk1_pc:\n"
"s_and_b32 s89, s89, 0xffff\n"
"s_mov_b32 s90, .Lk1_fin-.Lk1_pc\n"
"s_mov_b32 s91, 0x20000\n"
"s_lshr_b32 s60, s6, 3\n"
"s_lshl_b32 s60, s60, 2\n"
"s_add_u32 s60, s60, s7\n"
"s_lshl_b32 s60, s60, 10\n"
"v_lshlrev_b32 v247, 4, v238\n"
"buffer_load_dwordx4 v[240:243], v247, s[88:91], s60 offen\n"
"s_lshl_b32 s60, s46, 5\n"
"s_add_u32 s60, s60, s47\n"
"s_lshl_b32 s60, s60, 18\n"
"s_add_u32 s20, s10, s60\n"
"s_addc_u32 s21, s11, 0\n"
"s_and_b32 s21, s21, 0xffff\n"
"s_mov_b32 s20, s20\n"
"s_mov_b32 s22, 0x440000\n"
"s_mov_b32 s23, 0x20000\n"
"s_add_u32 s24, s12, s60\n"
"s_addc_u32 s25, s13, 0\n"
"s_and_b32 s25, s25, 0xffff\n"
"s_mov_b32 s24, s24\n"
"s_mov_b32 s26, 0x440000\n"
"s_mov_b32 s27, 0x20000\n"
"s_mul_i32 s60, s46, 0x120000\n"
"s_add_u32 s40, s16, s60\n"
"s_addc_u32 s41, s17, 0\n"
"s_and_b32 s41, s41, 0xffff\n"
"s_mov_b32 s40, s40\n"
"s_mov_b32 s42, 0x120000\n"
"s_mov_b32 s43, 0x20000\n"
"s_add_u32 s84, s16, 0x1f00000\n"
"s_addc_u32 s85, s17, 0\n"
"s_load_dwordx2 s[98:99], s[84:85], 0x0\n"
"s_and_b32 s85, s85, 0xffff\n"
"s_mov_b32 s84, s84\n"
"s_mov_b32 s86, 0x19000\n"
"s_mov_b32 s87, 0x20000\n"
"v_bfe_u32 v247, v238, 2, 3\n"
"v_bfe_u32 v236, v238, 4, 1\n"
"v_lshrrev_b32 v237, 5, v238\n"
"v_lshlrev_b32 v237, 6, v237\n"
"v_and_b32 v239, 3, v238\n"
"v_lshlrev_b32 v237, 5, v237\n"
"v_lshl_add_u32 v237, v247, 6, v237\n"
"v_lshl_add_u32 v237, v236, 5, v237\n"
"v_lshl_add_u32 v237, v239, 3, v237\n"
"s_mov_b32 s71, s45\n"
"s_lshl_b32 s61, s71, 14\n"
"s_add_u32 s60, s61, 0xc000\n"
"v_add_u32 v249, s60, v237\n"
"v_xor_b32 v250, 32, v249\n"
"v_add_u32 v232, 0x8000, v237\n"
"v_xor_b32 v233, 32, v232\n"
"v_add_u32 v234, 0x18000, v237\n"
"v_xor_b32 v235, 32, v234\n"
"v_bfe_u32 v247, v238, 2, 2\n"
"v_sub_u32 v247, 4, v247\n"
"v_lshrrev_b32 v236, 4, v238\n"
"v_xor_b32 v247, v247, v236\n"
"v_and_b32 v247, 3, v247\n"
"v_lshlrev_b32 v247, 4, v247\n"
"v_and_b32 v236, 7, v238\n"
"v_lshl_add_u32 v247, v236, 6, v247\n"
"v_bfe_u32 v236, v238, 3, 1\n"
"v_lshl_add_u32 v237, v236, 10, v247\n"
"v_add_u32 v253, 0x8000, v237\n"
"v_add_u32 v254, 0x18000, v237\n"
"v_lshl_add_u32 v248, v236, 11, v247\n"
"v_add_u32 v248, s61, v248\n"
"s_waitcnt lgkmcnt(0)\n"
"s_mul_i32 s98, s98, 0x19660d\n"
"s_add_u32 s98, s98, 0x3c6ef35f\n"
"s_mul_i32 s99, s99, 0x15a4e35\n"
"s_add_u32 s99, s99, s98\n"
"s_add_u32 s99, s99, 1\n"
"s_cmp_eq_u32 s44, 3\n"
"s_cbranch_scc1 .Lk1_aux0\n"
"v_mov_b32 v0, 0\n"
"v_mov_b32 v1, 0\n"
"v_mov_b32 v2, 0\n"
"v_mov_b32 v3, 0\n"
"v_mov_b32 v4, 0\n"
"v_mov_b32 v5, 0\n"
"v_mov_b32 v6, 0\n"
"v_mov_b32 v7, 0\n"
"v_mov_b32 v8, 0\n"
"v_mov_b32 v9, 0\n"
"v_mov_b32 v10, 0\n"
"v_mov_b32 v11, 0\n"
"v_mov_b32 v12, 0\n"
"v_mov_b32 v13, 0\n"
"v_mov_b32 v14, 0\n"
"v_mov_b32 v15, 0\n"
"v_mov_b32 v16, 0\n"
"v_mov_b32 v17, 0\n"
"v_mov_b32 v18, 0\n"
"v_mov_b32 v19, 0\n"
"v_mov_b32 v20, 0\n"
"v_mov_b32 v21, 0\n"
"v_mov_b32 v22, 0\n"
"v_mov_b32 v23, 0\n"
"v_mov_b32 v24, 0\n"
"v_mov_b32 v25, 0\n"
"v_mov_b32 v26, 0\n"
"v_mov_b32 v27, 0\n"
"v_mov_b32 v28, 0\n"
"v_mov_b32 v29, 0\n"
"v_mov_b32 v30, 0\n"
"v_mov_b32 v31, 0\n"
"v_mov_b32 v32, 0\n"
"v_mov_b32 v33, 0\n"
"v_mov_b32 v34, 0\n"
"v_mov_b32 v35, 0\n"
"v_mov_b32 v36, 0\n"
"v_mov_b32 v37, 0\n"
"v_mov_b32 v38, 0\n"
"v_mov_b32 v39, 0\n"
"v_mov_b32 v40, 0\n"
"v_mov_b32 v41, 0\n"
"v_mov_b32 v42, 0\n"
"v_mov_b32 v43, 0\n"
"v_mov_b32 v44, 0\n"
"v_mov_b32 v45, 0\n"
"v_mov_b32 v46, 0\n"
"v_mov_b32 v47, 0\n"
"v_mov_b32 v48, 0\n"
"v_mov_b32 v49, 0\n"
"v_mov_b32 v50, 0\n"
"v_mov_b32 v51, 0\n"
"v_mov_b32 v52, 0\n"
"v_mov_b32 v53, 0\n"
"v_mov_b32 v54, 0\n"
"v_mov_b32 v55, 0\n"
"v_mov_b32 v56, 0\n"
"v_mov_b32 v57, 0\n"
"v_mov_b32 v58, 0\n"
"v_mov_b32 v59, 0\n"
"v_mov_b32 v60, 0\n"
"v_mov_b32 v61, 0\n"
"v_mov_b32 v62, 0\n"
"v_mov_b32 v63, 0\n"
"v_mov_b32 v64, 0\n"
"v_mov_b32 v65, 0\n"
"v_mov_b32 v66, 0\n"
"v_mov_b32 v67, 0\n"
"v_mov_b32 v68, 0\n"
"v_mov_b32 v69, 0\n"
"v_mov_b32 v70, 0\n"
"v_mov_b32 v71, 0\n"
"v_mov_b32 v72, 0\n"
"v_mov_b32 v73, 0\n"
"v_mov_b32 v74, 0\n"
"v_mov_b32 v75, 0\n"
"v_mov_b32 v76, 0\n"
"v_mov_b32 v77, 0\n"
"v_mov_b32 v78, 0\n"
"v_mov_b32 v79, 0\n"
"v_mov_b32 v80, 0\n"
"v_mov_b32 v81, 0\n"
"v_mov_b32 v82, 0\n"
"v_mov_b32 v83, 0\n"
"v_mov_b32 v84, 0\n"
"v_mov_b32 v85, 0\n"
"v_mov_b32 v86, 0\n"
"v_mov_b32 v87, 0\n"
"v_mov_b32 v88, 0\n"
"v_mov_b32 v89, 0\n"
"v_mov_b32 v90, 0\n"
"v_mov_b32 v91, 0\n"
"v_mov_b32 v92, 0\n"
"v_mov_b32 v93, 0\n"
"v_mov_b32 v94, 0\n"
"v_mov_b32 v95, 0\n"
"v_mov_b32 v244, 0\n"
"v_mov_b32 v245, 0\n"
"v_mov_b32 v246, 0\n"
"s_mul_i32 s60, s44, 0x1800\n"
"v_add_u32 v253, s60, v253\n"
"v_add_u32 v254, s60, v254\n"
"s_barrier\n"
"ds_read_b128 v[108:111], v254 offset:28672\n"
"ds_read_b128 v[112:115], v254 offset:30720\n"
"ds_read_b128 v[116:119], v254 offset:32768\n"
"ds_read_b64_tr_b16 v[96:97], v234 offset:4096\n"
"ds_read_b64_tr_b16 v[98:99], v234 offset:8192\n"
"ds_read_b64_tr_b16 v[100:101], v235 offset:4096\n"
"ds_read_b64_tr_b16 v[102:103], v235 offset:8192\n"
"s_waitcnt lgkmcnt(2)\n"
"v_mfma_f32_16x16x32_bf16 v[132:135], v[96:99], v[108:111], 0\n"
"v_mfma_f32_16x16x32_bf16 v[164:167], v[96:99], v[112:115], 0\n"
"v_mfma_f32_16x16x32_bf16 v[196:199], v[96:99], v[116:119], 0\n"
"ds_read_b64_tr_b16 v[96:97], v234 offset:5120\n"
"ds_read_b64_tr_b16 v[98:99], v234 offset:9216\n"
"ds_read_b64_tr_b16 v[104:105], v234 offset:4608\n"
"ds_read_b64_tr_b16 v[106:107], v234 offset:8704\n"
"s_waitcnt lgkmcnt(4)\n"
"v_mfma_f32_16x16x32_bf16 v[136:139], v[100:103], v[108:111], 0\n"
"v_mfma_f32_16x16x32_bf16 v[168:171], v[100:103], v[112:115], 0\n"
"v_mfma_f32_16x16x32_bf16 v[200:203], v[100:103], v[116:119], 0\n"
"ds_read_b64_tr_b16 v[100:101], v235 offset:5120\n"
"ds_read_b64_tr_b16 v[102:103], v235 offset:9216\n"
"ds_read_b64_tr_b16 v[228:229], v235 offset:4608\n"
"ds_read_b64_tr_b16 v[230:231], v235 offset:8704\n"
"s_waitcnt lgkmcnt(4)\n"
"v_mfma_f32_16x16x32_bf16 v[140:143], v[104:107], v[108:111], 0\n"
"v_mfma_f32_16x16x32_bf16 v[172:175], v[104:107], v[112:115], 0\n"
"v_mfma_f32_16x16x32_bf16 v[204:207], v[104:107], v[116:119], 0\n"
"s_waitcnt lgkmcnt(0)\n"
"ds_read_b64_tr_b16 v[104:105], v234 offset:5632\n"
"ds_read_b64_tr_b16 v[106:107], v234 offset:9728\n"
"v_mfma_f32_16x16x32_bf16 v[144:147], v[228:231], v[108:111], 0\n"
"v_mfma_f32_16x16x32_bf16 v[176:179], v[228:231], v[112:115], 0\n"
"v_mfma_f32_16x16x32_bf16 v[208:211], v[228:231], v[116:119], 0\n"
"ds_read_b64_tr_b16 v[228:229], v235 offset:5632\n"
"ds_read_b64_tr_b16 v[230:231], v235 offset:9728\n"
"v_mfma_f32_16x16x32_bf16 v[148:151], v[96:99], v[108:111], 0\n"
"v_mfma_f32_16x16x32_bf16 v[180:183], v[96:99], v[112:115], 0\n"
"v_mfma_f32_16x16x32_bf16 v[212:215], v[96:99], v[116:119], 0\n"
"v_mfma_f32_16x16x32_bf16 v[152:155], v[100:103], v[108:111], 0\n"
"v_mfma_f32_16x16x32_bf16 v[184:187], v[100:103], v[112:115], 0\n"
"v_mfma_f32_16x16x32_bf16 v[216:219], v[100:103], v[116:119], 0\n"
"s_waitcnt lgkmcnt(2)\n"
"v_mfma_f32_16x16x32_bf16 v[156:159], v[104:107], v[108:111], 0\n"
"v_mfma_f32_16x16x32_bf16 v[188:191], v[104:107], v[112:115], 0\n"
"v_mfma_f32_16x16x32_bf16 v[220:223], v[104:107], v[116:119], 0\n"
"s_waitcnt lgkmcnt(0)\n"
"v_mfma_f32_16x16x32_bf16 v[160:163], v[228:231], v[108:111], 0\n"
"v_mfma_f32_16x16x32_bf16 v[192:195], v[228:231], v[112:115], 0\n"
"v_mfma_f32_16x16x32_bf16 v[224:227], v[228:231], v[116:119], 0\n"
"s_barrier\n"
"ds_read_b128 v[120:123], v254 offset:29184\n"
"ds_read_b128 v[124:127], v254 offset:31232\n"
"ds_read_b128 v[128:131], v254 offset:33280\n"
"ds_read_b64_tr_b16 v[96:97], v234 offset:12288\n"
"ds_read_b64_tr_b16 v[98:99], v234 offset:16384\n"
"ds_read_b64_tr_b16 v[100:101], v235 offset:12288\n"
"ds_read_b64_tr_b16 v[102:103], v235 offset:16384\n"
"s_waitcnt lgkmcnt(2)\n"
"v_mfma_f32_16x16x32_bf16 v[132:135], v[96:99], v[120:123], v[132:135]\n"
"v_mfma_f32_16x16x32_bf16 v[164:167], v[96:99], v[124:127], v[164:167]\n"
"v_mfma_f32_16x16x32_bf16 v[196:199], v[96:99], v[128:131], v[196:199]\n"
"ds_read_b64_tr_b16 v[96:97], v234 offset:13312\n"
"ds_read_b64_tr_b16 v[98:99], v234 offset:17408\n"
"ds_read_b64_tr_b16 v[104:105], v234 offset:12800\n"
"ds_read_b64_tr_b16 v[106:107], v234 offset:16896\n"
"s_waitcnt lgkmcnt(4)\n"
"v_mfma_f32_16x16x32_bf16 v[136:139], v[100:103], v[120:123], v[136:139]\n"
"v_mfma_f32_16x16x32_bf16 v[168:171], v[100:103], v[124:127], v[168:171]\n"
"v_mfma_f32_16x16x32_bf16 v[200:203], v[100:103], v[128:131], v[200:203]\n"
"ds_read_b64_tr_b16 v[100:101], v235 offset:13312\n"
"ds_read_b64_tr_b16 v[102:103], v235 offset:17408\n"
"ds_read_b64_tr_b16 v[228:229], v235 offset:12800\n"
"ds_read_b64_tr_b16 v[230:231], v235 offset:16896\n"
"s_waitcnt lgkmcnt(4)\n"
"v_mfma_f32_16x16x32_bf16 v[140:143], v[104:107], v[120:123], v[140:143]\n"
"v_mfma_f32_16x16x32_bf16 v[172:175], v[104:107], v[124:127], v[172:175]\n"
"v_mfma_f32_16x16x32_bf16 v[204:207], v[104:107], v[128:131], v[204:207]\n"
"s_waitcnt lgkmcnt(0)\n"
"ds_read_b64_tr_b16 v[104:105], v234 offset:13824\n"
"ds_read_b64_tr_b16 v[106:107], v234 offset:17920\n"
"v_mfma_f32_16x16x32_bf16 v[144:147], v[228:231], v[120:123], v[144:147]\n"
"v_mfma_f32_16x16x32_bf16 v[176:179], v[228:231], v[124:127], v[176:179]\n"
"v_mfma_f32_16x16x32_bf16 v[208:211], v[228:231], v[128:131], v[208:211]\n"
"ds_read_b64_tr_b16 v[228:229], v235 offset:13824\n"
"ds_read_b64_tr_b16 v[230:231], v235 offset:17920\n"
"v_mfma_f32_16x16x32_bf16 v[148:151], v[96:99], v[120:123], v[148:151]\n"
"v_mfma_f32_16x16x32_bf16 v[180:183], v[96:99], v[124:127], v[180:183]\n"
"v_mfma_f32_16x16x32_bf16 v[212:215], v[96:99], v[128:131], v[212:215]\n"
"v_mfma_f32_16x16x32_bf16 v[152:155], v[100:103], v[120:123], v[152:155]\n"
"v_mfma_f32_16x16x32_bf16 v[184:187], v[100:103], v[124:127], v[184:187]\n"
"v_mfma_f32_16x16x32_bf16 v[216:219], v[100:103], v[128:131], v[216:219]\n"
"s_waitcnt lgkmcnt(2)\n"
"v_mfma_f32_16x16x32_bf16 v[156:159], v[104:107], v[120:123], v[156:159]\n"
"v_mfma_f32_16x16x32_bf16 v[188:191], v[104:107], v[124:127], v[188:191]\n"
"v_mfma_f32_16x16x32_bf16 v[220:223], v[104:107], v[128:131], v[220:223]\n"
"s_waitcnt lgkmcnt(0)\n"
"v_mfma_f32_16x16x32_bf16 v[160:163], v[228:231], v[120:123], v[160:163]\n"
"v_mfma_f32_16x16x32_bf16 v[192:195], v[228:231], v[124:127], v[192:195]\n"
"v_mfma_f32_16x16x32_bf16 v[224:227], v[228:231], v[128:131], v[224:227]\n"
"s_barrier\n"
"ds_read_b128 v[108:111], v254 offset:47104\n"
"ds_read_b128 v[112:115], v254 offset:49152\n"
"ds_read_b128 v[116:119], v254 offset:51200\n"
"ds_read_b64_tr_b16 v[96:97], v234 offset:20480\n"
"ds_read_b64_tr_b16 v[98:99], v234 offset:24576\n"
"ds_read_b64_tr_b16 v[100:101], v235 offset:20480\n"
"ds_read_b64_tr_b16 v[102:103], v235 offset:24576\n"
"s_waitcnt lgkmcnt(2)\n"
"v_mfma_f32_16x16x32_bf16 v[132:135], v[96:99], v[108:111], v[132:135]\n"
"v_mfma_f32_16x16x32_bf16 v[164:167], v[96:99], v[112:115], v[164:167]\n"
"v_mfma_f32_16x16x32_bf16 v[196:199], v[96:99], v[116:119], v[196:199]\n"
"ds_read_b64_tr_b16 v[96:97], v234 offset:21504\n"
"ds_read_b64_tr_b16 v[98:99], v234 offset:25600\n"
"ds_read_b64_tr_b16 v[104:105], v234 offset:20992\n"
"ds_read_b64_tr_b16 v[106:107], v234 offset:25088\n"
"s_waitcnt lgkmcnt(4)\n"
"v_mfma_f32_16x16x32_bf16 v[136:139], v[100:103], v[108:111], v[136:139]\n"
"v_mfma_f32_16x16x32_bf16 v[168:171], v[100:103], v[112:115], v[168:171]\n"
"v_mfma_f32_16x16x32_bf16 v[200:203], v[100:103], v[116:119], v[200:203]\n"
"ds_read_b64_tr_b16 v[100:101], v235 offset:21504\n"
"ds_read_b64_tr_b16 v[102:103], v235 offset:25600\n"
"ds_read_b64_tr_b16 v[228:229], v235 offset:20992\n"
"ds_read_b64_tr_b16 v[230:231], v235 offset:25088\n"
"s_waitcnt lgkmcnt(4)\n"
"v_mfma_f32_16x16x32_bf16 v[140:143], v[104:107], v[108:111], v[140:143]\n"
"v_mfma_f32_16x16x32_bf16 v[172:175], v[104:107], v[112:115], v[172:175]\n"
"v_mfma_f32_16x16x32_bf16 v[204:207], v[104:107], v[116:119], v[204:207]\n"
"s_waitcnt lgkmcnt(0)\n"
"ds_read_b64_tr_b16 v[104:105], v234 offset:22016\n"
"ds_read_b64_tr_b16 v[106:107], v234 offset:26112\n"
"v_mfma_f32_16x16x32_bf16 v[144:147], v[228:231], v[108:111], v[144:147]\n"
"v_mfma_f32_16x16x32_bf16 v[176:179], v[228:231], v[112:115], v[176:179]\n"
"v_mfma_f32_16x16x32_bf16 v[208:211], v[228:231], v[116:119], v[208:211]\n"
"ds_read_b64_tr_b16 v[228:229], v235 offset:22016\n"
"ds_read_b64_tr_b16 v[230:231], v235 offset:26112\n"
"v_mfma_f32_16x16x32_bf16 v[148:151], v[96:99], v[108:111], v[148:151]\n"
"v_mfma_f32_16x16x32_bf16 v[180:183], v[96:99], v[112:115], v[180:183]\n"
"v_mfma_f32_16x16x32_bf16 v[212:215], v[96:99], v[116:119], v[212:215]\n"
"v_mfma_f32_16x16x32_bf16 v[152:155], v[100:103], v[108:111], v[152:155]\n"
"v_mfma_f32_16x16x32_bf16 v[184:187], v[100:103], v[112:115], v[184:187]\n"
"v_mfma_f32_16x16x32_bf16 v[216:219], v[100:103], v[116:119], v[216:219]\n"
"s_waitcnt lgkmcnt(2)\n"
"v_mfma_f32_16x16x32_bf16 v[156:159], v[104:107], v[108:111], v[156:159]\n"
"v_mfma_f32_16x16x32_bf16 v[188:191], v[104:107], v[112:115], v[188:191]\n"
"v_mfma_f32_16x16x32_bf16 v[220:223], v[104:107], v[116:119], v[220:223]\n"
"s_waitcnt lgkmcnt(0)\n"
"v_mfma_f32_16x16x32_bf16 v[160:163], v[228:231], v[108:111], v[160:163]\n"
"v_mfma_f32_16x16x32_bf16 v[192:195], v[228:231], v[112:115], v[192:195]\n"
"v_mfma_f32_16x16x32_bf16 v[224:227], v[228:231], v[116:119], v[224:227]\n"
"s_barrier\n"
"ds_read_b128 v[120:123], v254 offset:47616\n"
"ds_read_b128 v[124:127], v254 offset:49664\n"
"ds_read_b128 v[128:131], v254 offset:51712\n"
"ds_read_b64_tr_b16 v[96:97], v232\n"
"ds_read_b64_tr_b16 v[98:99], v232 offset:4096\n"
"ds_read_b64_tr_b16 v[100:101], v233\n"
"ds_read_b64_tr_b16 v[102:103], v233 offset:4096\n"
"s_waitcnt lgkmcnt(2)\n"
"v_mfma_f32_16x16x32_bf16 v[132:135], v[96:99], v[120:123], v[132:135]\n"
"v_mfma_f32_16x16x32_bf16 v[164:167], v[96:99], v[124:127], v[164:167]\n"
"v_mfma_f32_16x16x32_bf16 v[196:199], v[96:99], v[128:131], v[196:199]\n"
"ds_read_b64_tr_b16 v[96:97], v232 offset:1024\n"
"ds_read_b64_tr_b16 v[98:99], v232 offset:5120\n"
"ds_read_b64_tr_b16 v[104:105], v232 offset:512\n"
"ds_read_b64_tr_b16 v[106:107], v232 offset:4608\n"
"s_waitcnt lgkmcnt(4)\n"
"v_mfma_f32_16x16x32_bf16 v[136:139], v[100:103], v[120:123], v[136:139]\n"
"v_mfma_f32_16x16x32_bf16 v[168:171], v[100:103], v[124:127], v[168:171]\n"
"v_mfma_f32_16x16x32_bf16 v[200:203], v[100:103], v[128:131], v[200:203]\n"
"ds_read_b64_tr_b16 v[100:101], v233 offset:1024\n"
"ds_read_b64_tr_b16 v[102:103], v233 offset:5120\n"
"ds_read_b64_tr_b16 v[228:229], v233 offset:512\n"
"ds_read_b64_tr_b16 v[230:231], v233 offset:4608\n"
"s_waitcnt lgkmcnt(4)\n"
"v_mfma_f32_16x16x32_bf16 v[140:143], v[104:107], v[120:123], v[140:143]\n"
"v_mfma_f32_16x16x32_bf16 v[172:175], v[104:107], v[124:127], v[172:175]\n"
"v_mfma_f32_16x16x32_bf16 v[204:207], v[104:107], v[128:131], v[204:207]\n"
"s_waitcnt lgkmcnt(0)\n"
"ds_read_b64_tr_b16 v[104:105], v232 offset:1536\n"
"ds_read_b64_tr_b16 v[106:107], v232 offset:5632\n"
"v_mfma_f32_16x16x32_bf16 v[144:147], v[228:231], v[120:123], v[144:147]\n"
"v_mfma_f32_16x16x32_bf16 v[176:179], v[228:231], v[124:127], v[176:179]\n"
"v_mfma_f32_16x16x32_bf16 v[208:211], v[228:231], v[128:131], v[208:211]\n"
"ds_read_b64_tr_b16 v[228:229], v233 offset:1536\n"
"ds_read_b64_tr_b16 v[230:231], v233 offset:5632\n"
"v_mfma_f32_16x16x32_bf16 v[148:151], v[96:99], v[120:123], v[148:151]\n"
"v_mfma_f32_16x16x32_bf16 v[180:183], v[96:99], v[124:127], v[180:183]\n"
"v_mfma_f32_16x16x32_bf16 v[212:215], v[96:99], v[128:131], v[212:215]\n"
"v_mfma_f32_16x16x32_bf16 v[152:155], v[100:103], v[120:123], v[152:155]\n"
"v_mfma_f32_16x16x32_bf16 v[184:187], v[100:103], v[124:127], v[184:187]\n"
"v_mfma_f32_16x16x32_bf16 v[216:219], v[100:103], v[128:131], v[216:219]\n"
"s_waitcnt lgkmcnt(2)\n"
"v_mfma_f32_16x16x32_bf16 v[156:159], v[104:107], v[120:123], v[156:159]\n"
"v_mfma_f32_16x16x32_bf16 v[188:191], v[104:107], v[124:127], v[188:191]\n"
"v_mfma_f32_16x16x32_bf16 v[220:223], v[104:107], v[128:131], v[220:223]\n"
"s_waitcnt lgkmcnt(0)\n"
"v_mfma_f32_16x16x32_bf16 v[160:163], v[228:231], v[120:123], v[160:163]\n"
"v_mfma_f32_16x16x32_bf16 v[192:195], v[228:231], v[124:127], v[192:195]\n"
"v_mfma_f32_16x16x32_bf16 v[224:227], v[228:231], v[128:131], v[224:227]\n"
"s_barrier\n"
"ds_read_b128 v[108:111], v253 offset:49152\n"
"ds_read_b128 v[112:115], v253 offset:51200\n"
"ds_read_b128 v[116:119], v253 offset:53248\n"
"ds_read_b64_tr_b16 v[96:97], v232 offset:8192\n"
"ds_read_b64_tr_b16 v[98:99], v232 offset:12288\n"
"ds_read_b64_tr_b16 v[100:101], v233 offset:8192\n"
"ds_read_b64_tr_b16 v[102:103], v233 offset:12288\n"
"s_waitcnt lgkmcnt(2)\n"
"v_mfma_f32_16x16x32_bf16 v[132:135], v[96:99], v[108:111], v[132:135]\n"
"v_mfma_f32_16x16x32_bf16 v[164:167], v[96:99], v[112:115], v[164:167]\n"
"v_mfma_f32_16x16x32_bf16 v[196:199], v[96:99], v[116:119], v[196:199]\n"
"ds_read_b64_tr_b16 v[96:97], v232 offset:9216\n"
"ds_read_b64_tr_b16 v[98:99], v232 offset:13312\n"
"ds_read_b64_tr_b16 v[104:105], v232 offset:8704\n"
"ds_read_b64_tr_b16 v[106:107], v232 offset:12800\n"
"s_waitcnt lgkmcnt(4)\n"
"v_mfma_f32_16x16x32_bf16 v[136:139], v[100:103], v[108:111], v[136:139]\n"
"v_mfma_f32_16x16x32_bf16 v[168:171], v[100:103], v[112:115], v[168:171]\n"
"v_mfma_f32_16x16x32_bf16 v[200:203], v[100:103], v[116:119], v[200:203]\n"
"ds_read_b64_tr_b16 v[100:101], v233 offset:9216\n"
"ds_read_b64_tr_b16 v[102:103], v233 offset:13312\n"
"ds_read_b64_tr_b16 v[228:229], v233 offset:8704\n"
"ds_read_b64_tr_b16 v[230:231], v233 offset:12800\n"
"s_waitcnt lgkmcnt(4)\n"
"v_mfma_f32_16x16x32_bf16 v[140:143], v[104:107], v[108:111], v[140:143]\n"
"v_mfma_f32_16x16x32_bf16 v[172:175], v[104:107], v[112:115], v[172:175]\n"
"v_mfma_f32_16x16x32_bf16 v[204:207], v[104:107], v[116:119], v[204:207]\n"
"s_waitcnt lgkmcnt(0)\n"
"ds_read_b64_tr_b16 v[104:105], v232 offset:9728\n"
"ds_read_b64_tr_b16 v[106:107], v232 offset:13824\n"
"v_mfma_f32_16x16x32_bf16 v[144:147], v[228:231], v[108:111], v[144:147]\n"
"v_mfma_f32_16x16x32_bf16 v[176:179], v[228:231], v[112:115], v[176:179]\n"
"v_mfma_f32_16x16x32_bf16 v[208:211], v[228:231], v[116:119], v[208:211]\n"
"ds_read_b64_tr_b16 v[228:229], v233 offset:9728\n"
"ds_read_b64_tr_b16 v[230:231], v233 offset:13824\n"
"v_mfma_f32_16x16x32_bf16 v[148:151], v[96:99], v[108:111], v[148:151]\n"
"v_mfma_f32_16x16x32_bf16 v[180:183], v[96:99], v[112:115], v[180:183]\n"
"v_mfma_f32_16x16x32_bf16 v[212:215], v[96:99], v[116:119], v[212:215]\n"
"v_mfma_f32_16x16x32_bf16 v[152:155], v[100:103], v[108:111], v[152:155]\n"
"v_mfma_f32_16x16x32_bf16 v[184:187], v[100:103], v[112:115], v[184:187]\n"
"v_mfma_f32_16x16x32_bf16 v[216:219], v[100:103], v[116:119], v[216:219]\n"
"s_waitcnt lgkmcnt(2)\n"
"v_mfma_f32_16x16x32_bf16 v[156:159], v[104:107], v[108:111], v[156:159]\n"
"v_mfma_f32_16x16x32_bf16 v[188:191], v[104:107], v[112:115], v[188:191]\n"
"v_mfma_f32_16x16x32_bf16 v[220:223], v[104:107], v[116:119], v[220:223]\n"
"s_waitcnt lgkmcnt(0)\n"
"v_mfma_f32_16x16x32_bf16 v[160:163], v[228:231], v[108:111], v[160:163]\n"
"v_mfma_f32_16x16x32_bf16 v[192:195], v[228:231], v[112:115], v[192:195]\n"
"v_mfma_f32_16x16x32_bf16 v[224:227], v[228:231], v[116:119], v[224:227]\n"
"s_barrier\n"
"ds_read_b128 v[120:123], v253 offset:49664\n"
"ds_read_b128 v[124:127], v253 offset:51712\n"
"ds_read_b128 v[128:131], v253 offset:53760\n"
"ds_read_b64_tr_b16 v[96:97], v234 offset:4096\n"
"ds_read_b64_tr_b16 v[98:99], v234 offset:8192\n"
"ds_read_b64_tr_b16 v[100:101], v235 offset:4096\n"
"ds_read_b64_tr_b16 v[102:103], v235 offset:8192\n"
"s_waitcnt lgkmcnt(2)\n"
"v_mfma_f32_16x16x32_bf16 v[132:135], v[96:99], v[120:123], v[132:135]\n"
"v_mfma_f32_16x16x32_bf16 v[164:167], v[96:99], v[124:127], v[164:167]\n"
"v_mfma_f32_16x16x32_bf16 v[196:199], v[96:99], v[128:131], v[196:199]\n"
"ds_read_b64_tr_b16 v[96:97], v234 offset:5120\n"
"ds_read_b64_tr_b16 v[98:99], v234 offset:9216\n"
"ds_read_b64_tr_b16 v[104:105], v234 offset:4608\n"
"ds_read_b64_tr_b16 v[106:107], v234 offset:8704\n"
"s_waitcnt lgkmcnt(4)\n"
"v_mfma_f32_16x16x32_bf16 v[136:139], v[100:103], v[120:123], v[136:139]\n"
"v_mfma_f32_16x16x32_bf16 v[168:171], v[100:103], v[124:127], v[168:171]\n"
"v_mfma_f32_16x16x32_bf16 v[200:203], v[100:103], v[128:131], v[200:203]\n"
"ds_read_b64_tr_b16 v[100:101], v235 offset:5120\n"
"ds_read_b64_tr_b16 v[102:103], v235 offset:9216\n"
"ds_read_b64_tr_b16 v[228:229], v235 offset:4608\n"
"ds_read_b64_tr_b16 v[230:231], v235 offset:8704\n"
"s_waitcnt lgkmcnt(4)\n"
"v_mfma_f32_16x16x32_bf16 v[140:143], v[104:107], v[120:123], v[140:143]\n"
"v_mfma_f32_16x16x32_bf16 v[172:175], v[104:107], v[124:127], v[172:175]\n"
"v_mfma_f32_16x16x32_bf16 v[204:207], v[104:107], v[128:131], v[204:207]\n"
"s_waitcnt lgkmcnt(0)\n"
"ds_read_b64_tr_b16 v[104:105], v234 offset:5632\n"
"ds_read_b64_tr_b16 v[106:107], v234 offset:9728\n"
"v_mfma_f32_16x16x32_bf16 v[144:147], v[228:231], v[120:123], v[144:147]\n"
"v_mfma_f32_16x16x32_bf16 v[176:179], v[228:231], v[124:127], v[176:179]\n"
"v_mfma_f32_16x16x32_bf16 v[208:211], v[228:231], v[128:131], v[208:211]\n"
"ds_read_b64_tr_b16 v[228:229], v235 offset:5632\n"
"ds_read_b64_tr_b16 v[230:231], v235 offset:9728\n"
"v_mfma_f32_16x16x32_bf16 v[148:151], v[96:99], v[120:123], v[148:151]\n"
"v_mfma_f32_16x16x32_bf16 v[180:183], v[96:99], v[124:127], v[180:183]\n"
"v_mfma_f32_16x16x32_bf16 v[212:215], v[96:99], v[128:131], v[212:215]\n"
"v_mfma_f32_16x16x32_bf16 v[152:155], v[100:103], v[120:123], v[152:155]\n"
"v_mfma_f32_16x16x32_bf16 v[184:187], v[100:103], v[124:127], v[184:187]\n"
"v_mfma_f32_16x16x32_bf16 v[216:219], v[100:103], v[128:131], v[216:219]\n"
"s_waitcnt lgkmcnt(2)\n"
"v_mfma_f32_16x16x32_bf16 v[156:159], v[104:107], v[120:123], v[156:159]\n"
"v_mfma_f32_16x16x32_bf16 v[188:191], v[104:107], v[124:127], v[188:191]\n"
"v_mfma_f32_16x16x32_bf16 v[220:223], v[104:107], v[128:131], v[220:223]\n"
"s_waitcnt lgkmcnt(0)\n"
"v_mfma_f32_16x16x32_bf16 v[160:163], v[228:231], v[120:123], v[160:163]\n"
"v_mfma_f32_16x16x32_bf16 v[192:195], v[228:231], v[124:127], v[192:195]\n"
"v_mfma_f32_16x16x32_bf16 v[224:227], v[228:231], v[128:131], v[224:227]\n"
"s_barrier\n"
"ds_read_b128 v[108:111], v254 offset:28672\n"
"ds_read_b128 v[112:115], v254 offset:30720\n"
"ds_read_b128 v[116:119], v254 offset:32768\n"
"ds_read_b64_tr_b16 v[96:97], v234 offset:12288\n"
"ds_read_b64_tr_b16 v[98:99], v234 offset:16384\n"
"ds_read_b64_tr_b16 v[100:101], v235 offset:12288\n"
"ds_read_b64_tr_b16 v[102:103], v235 offset:16384\n"
"s_waitcnt lgkmcnt(2)\n"
"v_mfma_f32_16x16x32_bf16 v[132:135], v[96:99], v[108:111], v[132:135]\n"
"v_mfma_f32_16x16x32_bf16 v[164:167], v[96:99], v[112:115], v[164:167]\n"
"v_mfma_f32_16x16x32_bf16 v[196:199], v[96:99], v[116:119], v[196:199]\n"
"ds_read_b64_tr_b16 v[96:97], v234 offset:13312\n"
"ds_read_b64_tr_b16 v[98:99], v234 offset:17408\n"
"ds_read_b64_tr_b16 v[104:105], v234 offset:12800\n"
"ds_read_b64_tr_b16 v[106:107], v234 offset:16896\n"
"s_waitcnt lgkmcnt(4)\n"
"v_mfma_f32_16x16x32_bf16 v[136:139], v[100:103], v[108:111], v[136:139]\n"
"v_mfma_f32_16x16x32_bf16 v[168:171], v[100:103], v[112:115], v[168:171]\n"
"v_mfma_f32_16x16x32_bf16 v[200:203], v[100:103], v[116:119], v[200:203]\n"
"ds_read_b64_tr_b16 v[100:101], v235 offset:13312\n"
"ds_read_b64_tr_b16 v[102:103], v235 offset:17408\n"
"ds_read_b64_tr_b16 v[228:229], v235 offset:12800\n"
"ds_read_b64_tr_b16 v[230:231], v235 offset:16896\n"
"s_waitcnt lgkmcnt(4)\n"
"v_mfma_f32_16x16x32_bf16 v[140:143], v[104:107], v[108:111], v[140:143]\n"
"v_mfma_f32_16x16x32_bf16 v[172:175], v[104:107], v[112:115], v[172:175]\n"
"v_mfma_f32_16x16x32_bf16 v[204:207], v[104:107], v[116:119], v[204:207]\n"
"s_waitcnt lgkmcnt(0)\n"
"ds_read_b64_tr_b16 v[104:105], v234 offset:13824\n"
"ds_read_b64_tr_b16 v[106:107], v234 offset:17920\n"
"v_mfma_f32_16x16x32_bf16 v[144:147], v[228:231], v[108:111], v[144:147]\n"
"v_mfma_f32_16x16x32_bf16 v[176:179], v[228:231], v[112:115], v[176:179]\n"
"v_mfma_f32_16x16x32_bf16 v[208:211], v[228:231], v[116:119], v[208:211]\n"
"ds_read_b64_tr_b16 v[228:229], v235 offset:13824\n"
"ds_read_b64_tr_b16 v[230:231], v235 offset:17920\n"
"v_mfma_f32_16x16x32_bf16 v[148:151], v[96:99], v[108:111], v[148:151]\n"
"v_mfma_f32_16x16x32_bf16 v[180:183], v[96:99], v[112:115], v[180:183]\n"
"v_mfma_f32_16x16x32_bf16 v[212:215], v[96:99], v[116:119], v[212:215]\n"
"v_mfma_f32_16x16x32_bf16 v[152:155], v[100:103], v[108:111], v[152:155]\n"
"v_mfma_f32_16x16x32_bf16 v[184:187], v[100:103], v[112:115], v[184:187]\n"
"v_mfma_f32_16x16x32_bf16 v[216:219], v[100:103], v[116:119], v[216:219]\n"
"s_waitcnt lgkmcnt(2)\n"
"v_mfma_f32_16x16x32_bf16 v[156:159], v[104:107], v[108:111], v[156:159]\n"
"v_mfma_f32_16x16x32_bf16 v[188:191], v[104:107], v[112:115], v[188:191]\n"
"v_mfma_f32_16x16x32_bf16 v[220:223], v[104:107], v[116:119], v[220:223]\n"
"s_waitcnt lgkmcnt(0)\n"
"v_mfma_f32_16x16x32_bf16 v[160:163], v[228:231], v[108:111], v[160:163]\n"
"v_mfma_f32_16x16x32_bf16 v[192:195], v[228:231], v[112:115], v[192:195]\n"
"v_mfma_f32_16x16x32_bf16 v[224:227], v[228:231], v[116:119], v[224:227]\n"
"s_barrier\n"
"ds_read_b128 v[120:123], v254 offset:29184\n"
"ds_read_b128 v[124:127], v254 offset:31232\n"
"ds_read_b128 v[128:131], v254 offset:33280\n"
"ds_read_b64_tr_b16 v[96:97], v234 offset:20480\n"
"ds_read_b64_tr_b16 v[98:99], v234 offset:24576\n"
"ds_read_b64_tr_b16 v[100:101], v235 offset:20480\n"
"ds_read_b64_tr_b16 v[102:103], v235 offset:24576\n"
"s_waitcnt lgkmcnt(2)\n"
"v_mfma_f32_16x16x32_bf16 v[132:135], v[96:99], v[120:123], v[132:135]\n"
"v_mfma_f32_16x16x32_bf16 v[164:167], v[96:99], v[124:127], v[164:167]\n"
"v_mfma_f32_16x16x32_bf16 v[196:199], v[96:99], v[128:131], v[196:199]\n"
"ds_read_b64_tr_b16 v[96:97], v234 offset:21504\n"
"ds_read_b64_tr_b16 v[98:99], v234 offset:25600\n"
"ds_read_b64_tr_b16 v[104:105], v234 offset:20992\n"
"ds_read_b64_tr_b16 v[106:107], v234 offset:25088\n"
"s_waitcnt lgkmcnt(4)\n"
"v_mfma_f32_16x16x32_bf16 v[136:139], v[100:103], v[120:123], v[136:139]\n"
"v_mfma_f32_16x16x32_bf16 v[168:171], v[100:103], v[124:127], v[168:171]\n"
"v_mfma_f32_16x16x32_bf16 v[200:203], v[100:103], v[128:131], v[200:203]\n"
"ds_read_b64_tr_b16 v[100:101], v235 offset:21504\n"
"ds_read_b64_tr_b16 v[102:103], v235 offset:25600\n"
"ds_read_b64_tr_b16 v[228:229], v235 offset:20992\n"
"ds_read_b64_tr_b16 v[230:231], v235 offset:25088\n"
"s_waitcnt lgkmcnt(4)\n"
"v_mfma_f32_16x16x32_bf16 v[140:143], v[104:107], v[120:123], v[140:143]\n"
"v_mfma_f32_16x16x32_bf16 v[172:175], v[104:107], v[124:127], v[172:175]\n"
"v_mfma_f32_16x16x32_bf16 v[204:207], v[104:107], v[128:131], v[204:207]\n"
"s_waitcnt lgkmcnt(0)\n"
"ds_read_b64_tr_b16 v[104:105], v234 offset:22016\n"
"ds_read_b64_tr_b16 v[106:107], v234 offset:26112\n"
"v_mfma_f32_16x16x32_bf16 v[144:147], v[228:231], v[120:123], v[144:147]\n"
"v_mfma_f32_16x16x32_bf16 v[176:179], v[228:231], v[124:127], v[176:179]\n"
"v_mfma_f32_16x16x32_bf16 v[208:211], v[228:231], v[128:131], v[208:211]\n"
"ds_read_b64_tr_b16 v[228:229], v235 offset:22016\n"
"ds_read_b64_tr_b16 v[230:231], v235 offset:26112\n"
"v_mfma_f32_16x16x32_bf16 v[148:151], v[96:99], v[120:123], v[148:151]\n"
"v_mfma_f32_16x16x32_bf16 v[180:183], v[96:99], v[124:127], v[180:183]\n"
"v_mfma_f32_16x16x32_bf16 v[212:215], v[96:99], v[128:131], v[212:215]\n"
"v_mfma_f32_16x16x32_bf16 v[152:155], v[100:103], v[120:123], v[152:155]\n"
"v_mfma_f32_16x16x32_bf16 v[184:187], v[100:103], v[124:127], v[184:187]\n"
"v_mfma_f32_16x16x32_bf16 v[216:219], v[100:103], v[128:131], v[216:219]\n"
"s_waitcnt lgkmcnt(2)\n"
"v_mfma_f32_16x16x32_bf16 v[156:159], v[104:107], v[120:123], v[156:159]\n"
"v_mfma_f32_16x16x32_bf16 v[188:191], v[104:107], v[124:127], v[188:191]\n"
"v_mfma_f32_16x16x32_bf16 v[220:223], v[104:107], v[128:131], v[220:223]\n"
"s_waitcnt lgkmcnt(0)\n"
"v_mfma_f32_16x16x32_bf16 v[160:163], v[228:231], v[120:123], v[160:163]\n"
"v_mfma_f32_16x16x32_bf16 v[192:195], v[228:231], v[124:127], v[192:195]\n"
"v_mfma_f32_16x16x32_bf16 v[224:227], v[228:231], v[128:131], v[224:227]\n"
"s_barrier\n"
"ds_read_b128 v[108:111], v254 offset:47104\n"
"ds_read_b128 v[112:115], v254 offset:49152\n"
"ds_read_b128 v[116:119], v254 offset:51200\n"
"ds_read_b64_tr_b16 v[96:97], v232\n"
"ds_read_b64_tr_b16 v[98:99], v232 offset:4096\n"
"ds_read_b64_tr_b16 v[100:101], v233\n"
"ds_read_b64_tr_b16 v[102:103], v233 offset:4096\n"
"s_waitcnt lgkmcnt(2)\n"
"v_mfma_f32_16x16x32_bf16 v[132:135], v[96:99], v[108:111], v[132:135]\n"
"v_mfma_f32_16x16x32_bf16 v[164:167], v[96:99], v[112:115], v[164:167]\n"
"v_mfma_f32_16x16x32_bf16 v[196:199], v[96:99], v[116:119], v[196:199]\n"
"ds_read_b64_tr_b16 v[96:97], v232 offset:1024\n"
"ds_read_b64_tr_b16 v[98:99], v232 offset:5120\n"
"ds_read_b64_tr_b16 v[104:105], v232 offset:512\n"
"ds_read_b64_tr_b16 v[106:107], v232 offset:4608\n"
"s_waitcnt lgkmcnt(4)\n"
"v_mfma_f32_16x16x32_bf16 v[136:139], v[100:103], v[108:111], v[136:139]\n"
"v_mfma_f32_16x16x32_bf16 v[168:171], v[100:103], v[112:115], v[168:171]\n"
"v_mfma_f32_16x16x32_bf16 v[200:203], v[100:103], v[116:119], v[200:203]\n"
"ds_read_b64_tr_b16 v[100:101], v233 offset:1024\n"
"ds_read_b64_tr_b16 v[102:103], v233 offset:5120\n"
"ds_read_b64_tr_b16 v[228:229], v233 offset:512\n"
"ds_read_b64_tr_b16 v[230:231], v233 offset:4608\n"
"s_waitcnt lgkmcnt(4)\n"
"v_mfma_f32_16x16x32_bf16 v[140:143], v[104:107], v[108:111], v[140:143]\n"
"v_mfma_f32_16x16x32_bf16 v[172:175], v[104:107], v[112:115], v[172:175]\n"
"v_mfma_f32_16x16x32_bf16 v[204:207], v[104:107], v[116:119], v[204:207]\n"
"s_waitcnt lgkmcnt(0)\n"
"ds_read_b64_tr_b16 v[104:105], v232 offset:1536\n"
"ds_read_b64_tr_b16 v[106:107], v232 offset:5632\n"
"v_mfma_f32_16x16x32_bf16 v[144:147], v[228:231], v[108:111], v[144:147]\n"
"v_mfma_f32_16x16x32_bf16 v[176:179], v[228:231], v[112:115], v[176:179]\n"
"v_mfma_f32_16x16x32_bf16 v[208:211], v[228:231], v[116:119], v[208:211]\n"
"ds_read_b64_tr_b16 v[228:229], v233 offset:1536\n"
"ds_read_b64_tr_b16 v[230:231], v233 offset:5632\n"
"v_mfma_f32_16x16x32_bf16 v[148:151], v[96:99], v[108:111], v[148:151]\n"
"v_mfma_f32_16x16x32_bf16 v[180:183], v[96:99], v[112:115], v[180:183]\n"
"v_mfma_f32_16x16x32_bf16 v[212:215], v[96:99], v[116:119], v[212:215]\n"
"v_mfma_f32_16x16x32_bf16 v[152:155], v[100:103], v[108:111], v[152:155]\n"
"v_mfma_f32_16x16x32_bf16 v[184:187], v[100:103], v[112:115], v[184:187]\n"
"v_mfma_f32_16x16x32_bf16 v[216:219], v[100:103], v[116:119], v[216:219]\n"
"s_waitcnt lgkmcnt(2)\n"
"v_mfma_f32_16x16x32_bf16 v[156:159], v[104:107], v[108:111], v[156:159]\n"
"v_mfma_f32_16x16x32_bf16 v[188:191], v[104:107], v[112:115], v[188:191]\n"
"v_mfma_f32_16x16x32_bf16 v[220:223], v[104:107], v[116:119], v[220:223]\n"
"s_waitcnt lgkmcnt(0)\n"
"v_mfma_f32_16x16x32_bf16 v[160:163], v[228:231], v[108:111], v[160:163]\n"
"v_mfma_f32_16x16x32_bf16 v[192:195], v[228:231], v[112:115], v[192:195]\n"
"v_mfma_f32_16x16x32_bf16 v[224:227], v[228:231], v[116:119], v[224:227]\n"
"s_barrier\n"
"ds_read_b128 v[120:123], v254 offset:47616\n"
"ds_read_b128 v[124:127], v254 offset:49664\n"
"ds_read_b128 v[128:131], v254 offset:51712\n"
"ds_read_b64_tr_b16 v[96:97], v232 offset:8192\n"
"ds_read_b64_tr_b16 v[98:99], v232 offset:12288\n"
"ds_read_b64_tr_b16 v[100:101], v233 offset:8192\n"
"ds_read_b64_tr_b16 v[102:103], v233 offset:12288\n"
"s_waitcnt lgkmcnt(2)\n"
"v_mfma_f32_16x16x32_bf16 v[132:135], v[96:99], v[120:123], v[132:135]\n"
"v_mfma_f32_16x16x32_bf16 v[164:167], v[96:99], v[124:127], v[164:167]\n"
"v_mfma_f32_16x16x32_bf16 v[196:199], v[96:99], v[128:131], v[196:199]\n"
"ds_read_b64_tr_b16 v[96:97], v232 offset:9216\n"
"ds_read_b64_tr_b16 v[98:99], v232 offset:13312\n"
"ds_read_b64_tr_b16 v[104:105], v232 offset:8704\n"
"ds_read_b64_tr_b16 v[106:107], v232 offset:12800\n"
"s_waitcnt lgkmcnt(4)\n"
"v_mfma_f32_16x16x32_bf16 v[136:139], v[100:103], v[120:123], v[136:139]\n"
"v_mfma_f32_16x16x32_bf16 v[168:171], v[100:103], v[124:127], v[168:171]\n"
"v_mfma_f32_16x16x32_bf16 v[200:203], v[100:103], v[128:131], v[200:203]\n"
"ds_read_b64_tr_b16 v[100:101], v233 offset:9216\n"
"ds_read_b64_tr_b16 v[102:103], v233 offset:13312\n"
"ds_read_b64_tr_b16 v[228:229], v233 offset:8704\n"
"ds_read_b64_tr_b16 v[230:231], v233 offset:12800\n"
"s_waitcnt lgkmcnt(4)\n"
"v_mfma_f32_16x16x32_bf16 v[140:143], v[104:107], v[120:123], v[140:143]\n"
"v_mfma_f32_16x16x32_bf16 v[172:175], v[104:107], v[124:127], v[172:175]\n"
"v_mfma_f32_16x16x32_bf16 v[204:207], v[104:107], v[128:131], v[204:207]\n"
"s_waitcnt lgkmcnt(0)\n"
"ds_read_b64_tr_b16 v[104:105], v232 offset:9728\n"
"ds_read_b64_tr_b16 v[106:107], v232 offset:13824\n"
"v_mfma_f32_16x16x32_bf16 v[144:147], v[228:231], v[120:123], v[144:147]\n"
"v_mfma_f32_16x16x32_bf16 v[176:179], v[228:231], v[124:127], v[176:179]\n"
"v_mfma_f32_16x16x32_bf16 v[208:211], v[228:231], v[128:131], v[208:211]\n"
"ds_read_b64_tr_b16 v[228:229], v233 offset:9728\n"
"ds_read_b64_tr_b16 v[230:231], v233 offset:13824\n"
"v_mfma_f32_16x16x32_bf16 v[148:151], v[96:99], v[120:123], v[148:151]\n"
"v_mfma_f32_16x16x32_bf16 v[180:183], v[96:99], v[124:127], v[180:183]\n"
"v_mfma_f32_16x16x32_bf16 v[212:215], v[96:99], v[128:131], v[212:215]\n"
"v_mfma_f32_16x16x32_bf16 v[152:155], v[100:103], v[120:123], v[152:155]\n"
"v_mfma_f32_16x16x32_bf16 v[184:187], v[100:103], v[124:127], v[184:187]\n"
"v_mfma_f32_16x16x32_bf16 v[216:219], v[100:103], v[128:131], v[216:219]\n"
"s_waitcnt lgkmcnt(2)\n"
"v_mfma_f32_16x16x32_bf16 v[156:159], v[104:107], v[120:123], v[156:159]\n"
"v_mfma_f32_16x16x32_bf16 v[188:191], v[104:107], v[124:127], v[188:191]\n"
"v_mfma_f32_16x16x32_bf16 v[220:223], v[104:107], v[128:131], v[220:223]\n"
"s_waitcnt lgkmcnt(0)\n"
"v_mfma_f32_16x16x32_bf16 v[160:163], v[228:231], v[120:123], v[160:163]\n"
"v_mfma_f32_16x16x32_bf16 v[192:195], v[228:231], v[124:127], v[192:195]\n"
"v_mfma_f32_16x16x32_bf16 v[224:227], v[228:231], v[128:131], v[224:227]\n"
"s_barrier\n"
"ds_read_b128 v[108:111], v253 offset:49152\n"
"ds_read_b128 v[112:115], v253 offset:51200\n"
"ds_read_b128 v[116:119], v253 offset:53248\n"
"ds_read_b64_tr_b16 v[96:97], v234 offset:4096\n"
"ds_read_b64_tr_b16 v[98:99], v234 offset:8192\n"
"ds_read_b64_tr_b16 v[100:101], v235 offset:4096\n"
"ds_read_b64_tr_b16 v[102:103], v235 offset:8192\n"
"s_waitcnt lgkmcnt(2)\n"
"v_mfma_f32_16x16x32_bf16 v[132:135], v[96:99], v[108:111], v[132:135]\n"
"v_mfma_f32_16x16x32_bf16 v[164:167], v[96:99], v[112:115], v[164:167]\n"
"v_mfma_f32_16x16x32_bf16 v[196:199], v[96:99], v[116:119], v[196:199]\n"
"ds_read_b64_tr_b16 v[96:97], v234 offset:5120\n"
"ds_read_b64_tr_b16 v[98:99], v234 offset:9216\n"
"ds_read_b64_tr_b16 v[104:105], v234 offset:4608\n"
"ds_read_b64_tr_b16 v[106:107], v234 offset:8704\n"
"s_waitcnt lgkmcnt(4)\n"
"v_mfma_f32_16x16x32_bf16 v[136:139], v[100:103], v[108:111], v[136:139]\n"
"v_mfma_f32_16x16x32_bf16 v[168:171], v[100:103], v[112:115], v[168:171]\n"
"v_mfma_f32_16x16x32_bf16 v[200:203], v[100:103], v[116:119], v[200:203]\n"
"ds_read_b64_tr_b16 v[100:101], v235 offset:5120\n"
"ds_read_b64_tr_b16 v[102:103], v235 offset:9216\n"
"ds_read_b64_tr_b16 v[228:229], v235 offset:4608\n"
"ds_read_b64_tr_b16 v[230:231], v235 offset:8704\n"
"s_waitcnt lgkmcnt(4)\n"
"v_mfma_f32_16x16x32_bf16 v[140:143], v[104:107], v[108:111], v[140:143]\n"
"v_mfma_f32_16x16x32_bf16 v[172:175], v[104:107], v[112:115], v[172:175]\n"
"v_mfma_f32_16x16x32_bf16 v[204:207], v[104:107], v[116:119], v[204:207]\n"
"s_waitcnt lgkmcnt(0)\n"
"ds_read_b64_tr_b16 v[104:105], v234 offset:5632\n"
"ds_read_b64_tr_b16 v[106:107], v234 offset:9728\n"
"v_mfma_f32_16x16x32_bf16 v[144:147], v[228:231], v[108:111], v[144:147]\n"
"v_mfma_f32_16x16x32_bf16 v[176:179], v[228:231], v[112:115], v[176:179]\n"
"v_mfma_f32_16x16x32_bf16 v[208:211], v[228:231], v[116:119], v[208:211]\n"
"ds_read_b64_tr_b16 v[228:229], v235 offset:5632\n"
"ds_read_b64_tr_b16 v[230:231], v235 offset:9728\n"
"v_mfma_f32_16x16x32_bf16 v[148:151], v[96:99], v[108:111], v[148:151]\n"
"v_mfma_f32_16x16x32_bf16 v[180:183], v[96:99], v[112:115], v[180:183]\n"
"v_mfma_f32_16x16x32_bf16 v[212:215], v[96:99], v[116:119], v[212:215]\n"
"v_mfma_f32_16x16x32_bf16 v[152:155], v[100:103], v[108:111], v[152:155]\n"
"v_mfma_f32_16x16x32_bf16 v[184:187], v[100:103], v[112:115], v[184:187]\n"
"v_mfma_f32_16x16x32_bf16 v[216:219], v[100:103], v[116:119], v[216:219]\n"
"s_waitcnt lgkmcnt(2)\n"
"v_mfma_f32_16x16x32_bf16 v[156:159], v[104:107], v[108:111], v[156:159]\n"
"v_mfma_f32_16x16x32_bf16 v[188:191], v[104:107], v[112:115], v[188:191]\n"
"v_mfma_f32_16x16x32_bf16 v[220:223], v[104:107], v[116:119], v[220:223]\n"
"s_waitcnt lgkmcnt(0)\n"
"v_mfma_f32_16x16x32_bf16 v[160:163], v[228:231], v[108:111], v[160:163]\n"
"v_mfma_f32_16x16x32_bf16 v[192:195], v[228:231], v[112:115], v[192:195]\n"
"v_mfma_f32_16x16x32_bf16 v[224:227], v[228:231], v[116:119], v[224:227]\n"
"s_barrier\n"
"ds_read_b128 v[120:123], v253 offset:49664\n"
"ds_read_b128 v[124:127], v253 offset:51712\n"
"ds_read_b128 v[128:131], v253 offset:53760\n"
"ds_read_b64_tr_b16 v[96:97], v234 offset:12288\n"
"ds_read_b64_tr_b16 v[98:99], v234 offset:16384\n"
"ds_read_b64_tr_b16 v[100:101], v235 offset:12288\n"
"ds_read_b64_tr_b16 v[102:103], v235 offset:16384\n"
"s_waitcnt lgkmcnt(2)\n"
"v_mfma_f32_16x16x32_bf16 v[132:135], v[96:99], v[120:123], v[132:135]\n"
"v_mfma_f32_16x16x32_bf16 v[164:167], v[96:99], v[124:127], v[164:167]\n"
"v_mfma_f32_16x16x32_bf16 v[196:199], v[96:99], v[128:131], v[196:199]\n"
"ds_read_b64_tr_b16 v[96:97], v234 offset:13312\n"
"ds_read_b64_tr_b16 v[98:99], v234 offset:17408\n"
"ds_read_b64_tr_b16 v[104:105], v234 offset:12800\n"
"ds_read_b64_tr_b16 v[106:107], v234 offset:16896\n"
"s_waitcnt lgkmcnt(4)\n"
"v_mfma_f32_16x16x32_bf16 v[136:139], v[100:103], v[120:123], v[136:139]\n"
"v_mfma_f32_16x16x32_bf16 v[168:171], v[100:103], v[124:127], v[168:171]\n"
"v_mfma_f32_16x16x32_bf16 v[200:203], v[100:103], v[128:131], v[200:203]\n"
"ds_read_b64_tr_b16 v[100:101], v235 offset:13312\n"
"ds_read_b64_tr_b16 v[102:103], v235 offset:17408\n"
"ds_read_b64_tr_b16 v[228:229], v235 offset:12800\n"
"ds_read_b64_tr_b16 v[230:231], v235 offset:16896\n"
"s_waitcnt lgkmcnt(4)\n"
"v_mfma_f32_16x16x32_bf16 v[140:143], v[104:107], v[120:123], v[140:143]\n"
"v_mfma_f32_16x16x32_bf16 v[172:175], v[104:107], v[124:127], v[172:175]\n"
"v_mfma_f32_16x16x32_bf16 v[204:207], v[104:107], v[128:131], v[204:207]\n"
"s_waitcnt lgkmcnt(0)\n"
"ds_read_b64_tr_b16 v[104:105], v234 offset:13824\n"
"ds_read_b64_tr_b16 v[106:107], v234 offset:17920\n"
"v_mfma_f32_16x16x32_bf16 v[144:147], v[228:231], v[120:123], v[144:147]\n"
"v_mfma_f32_16x16x32_bf16 v[176:179], v[228:231], v[124:127], v[176:179]\n"
"v_mfma_f32_16x16x32_bf16 v[208:211], v[228:231], v[128:131], v[208:211]\n"
"ds_read_b64_tr_b16 v[228:229], v235 offset:13824\n"
"ds_read_b64_tr_b16 v[230:231], v235 offset:17920\n"
"v_mfma_f32_16x16x32_bf16 v[148:151], v[96:99], v[120:123], v[148:151]\n"
"v_mfma_f32_16x16x32_bf16 v[180:183], v[96:99], v[124:127], v[180:183]\n"
"v_mfma_f32_16x16x32_bf16 v[212:215], v[96:99], v[128:131], v[212:215]\n"
"v_mfma_f32_16x16x32_bf16 v[152:155], v[100:103], v[120:123], v[152:155]\n"
"v_mfma_f32_16x16x32_bf16 v[184:187], v[100:103], v[124:127], v[184:187]\n"
"v_mfma_f32_16x16x32_bf16 v[216:219], v[100:103], v[128:131], v[216:219]\n"
"s_waitcnt lgkmcnt(2)\n"
"v_mfma_f32_16x16x32_bf16 v[156:159], v[104:107], v[120:123], v[156:159]\n"
"v_mfma_f32_16x16x32_bf16 v[188:191], v[104:107], v[124:127], v[188:191]\n"
"v_mfma_f32_16x16x32_bf16 v[220:223], v[104:107], v[128:131], v[220:223]\n"
"s_waitcnt lgkmcnt(0)\n"
"v_mfma_f32_16x16x32_bf16 v[160:163], v[228:231], v[120:123], v[160:163]\n"
"v_mfma_f32_16x16x32_bf16 v[192:195], v[228:231], v[124:127], v[192:195]\n"
"v_mfma_f32_16x16x32_bf16 v[224:227], v[228:231], v[128:131], v[224:227]\n"
"s_barrier\n"
"ds_read_b128 v[108:111], v254 offset:28672\n"
"ds_read_b128 v[112:115], v254 offset:30720\n"
"ds_read_b128 v[116:119], v254 offset:32768\n"
"ds_read_b64_tr_b16 v[96:97], v234 offset:20480\n"
"ds_read_b64_tr_b16 v[98:99], v234 offset:24576\n"
"ds_read_b64_tr_b16 v[100:101], v235 offset:20480\n"
"ds_read_b64_tr_b16 v[102:103], v235 offset:24576\n"
"s_waitcnt lgkmcnt(2)\n"
"v_mfma_f32_16x16x32_bf16 v[132:135], v[96:99], v[108:111], v[132:135]\n"
"v_mfma_f32_16x16x32_bf16 v[164:167], v[96:99], v[112:115], v[164:167]\n"
"v_mfma_f32_16x16x32_bf16 v[196:199], v[96:99], v[116:119], v[196:199]\n"
"ds_read_b64_tr_b16 v[96:97], v234 offset:21504\n"
"ds_read_b64_tr_b16 v[98:99], v234 offset:25600\n"
"ds_read_b64_tr_b16 v[104:105], v234 offset:20992\n"
"ds_read_b64_tr_b16 v[106:107], v234 offset:25088\n"
"s_waitcnt lgkmcnt(4)\n"
"v_mfma_f32_16x16x32_bf16 v[136:139], v[100:103], v[108:111], v[136:139]\n"
"v_mfma_f32_16x16x32_bf16 v[168:171], v[100:103], v[112:115], v[168:171]\n"
"v_mfma_f32_16x16x32_bf16 v[200:203], v[100:103], v[116:119], v[200:203]\n"
"ds_read_b64_tr_b16 v[100:101], v235 offset:21504\n"
"ds_read_b64_tr_b16 v[102:103], v235 offset:25600\n"
"ds_read_b64_tr_b16 v[228:229], v235 offset:20992\n"
"ds_read_b64_tr_b16 v[230:231], v235 offset:25088\n"
"s_waitcnt lgkmcnt(4)\n"
"v_mfma_f32_16x16x32_bf16 v[140:143], v[104:107], v[108:111], v[140:143]\n"
"v_mfma_f32_16x16x32_bf16 v[172:175], v[104:107], v[112:115], v[172:175]\n"
"v_mfma_f32_16x16x32_bf16 v[204:207], v[104:107], v[116:119], v[204:207]\n"
"s_waitcnt lgkmcnt(0)\n"
"ds_read_b64_tr_b16 v[104:105], v234 offset:22016\n"
"ds_read_b64_tr_b16 v[106:107], v234 offset:26112\n"
"v_mfma_f32_16x16x32_bf16 v[144:147], v[228:231], v[108:111], v[144:147]\n"
"v_mfma_f32_16x16x32_bf16 v[176:179], v[228:231], v[112:115], v[176:179]\n"
"v_mfma_f32_16x16x32_bf16 v[208:211], v[228:231], v[116:119], v[208:211]\n"
"ds_read_b64_tr_b16 v[228:229], v235 offset:22016\n"
"ds_read_b64_tr_b16 v[230:231], v235 offset:26112\n"
"v_mfma_f32_16x16x32_bf16 v[148:151], v[96:99], v[108:111], v[148:151]\n"
"v_mfma_f32_16x16x32_bf16 v[180:183], v[96:99], v[112:115], v[180:183]\n"
"v_mfma_f32_16x16x32_bf16 v[212:215], v[96:99], v[116:119], v[212:215]\n"
"v_mfma_f32_16x16x32_bf16 v[152:155], v[100:103], v[108:111], v[152:155]\n"
"v_mfma_f32_16x16x32_bf16 v[184:187], v[100:103], v[112:115], v[184:187]\n"
"v_mfma_f32_16x16x32_bf16 v[216:219], v[100:103], v[116:119], v[216:219]\n"
"s_waitcnt lgkmcnt(2)\n"
"v_mfma_f32_16x16x32_bf16 v[156:159], v[104:107], v[108:111], v[156:159]\n"
"v_mfma_f32_16x16x32_bf16 v[188:191], v[104:107], v[112:115], v[188:191]\n"
"v_mfma_f32_16x16x32_bf16 v[220:223], v[104:107], v[116:119], v[220:223]\n"
"s_waitcnt lgkmcnt(0)\n"
"v_mfma_f32_16x16x32_bf16 v[160:163], v[228:231], v[108:111], v[160:163]\n"
"v_mfma_f32_16x16x32_bf16 v[192:195], v[228:231], v[112:115], v[192:195]\n"
"v_mfma_f32_16x16x32_bf16 v[224:227], v[228:231], v[116:119], v[224:227]\n"
"s_barrier\n"
"ds_read_b128 v[120:123], v254 offset:29184\n"
"ds_read_b128 v[124:127], v254 offset:31232\n"
"ds_read_b128 v[128:131], v254 offset:33280\n"
"ds_read_b64_tr_b16 v[96:97], v232\n"
"ds_read_b64_tr_b16 v[98:99], v232 offset:4096\n"
"ds_read_b64_tr_b16 v[100:101], v233\n"
"ds_read_b64_tr_b16 v[102:103], v233 offset:4096\n"
"s_waitcnt lgkmcnt(2)\n"
"v_mfma_f32_16x16x32_bf16 v[132:135], v[96:99], v[120:123], v[132:135]\n"
"v_mfma_f32_16x16x32_bf16 v[164:167], v[96:99], v[124:127], v[164:167]\n"
"v_mfma_f32_16x16x32_bf16 v[196:199], v[96:99], v[128:131], v[196:199]\n"
"ds_read_b64_tr_b16 v[96:97], v232 offset:1024\n"
"ds_read_b64_tr_b16 v[98:99], v232 offset:5120\n"
"ds_read_b64_tr_b16 v[104:105], v232 offset:512\n"
"ds_read_b64_tr_b16 v[106:107], v232 offset:4608\n"
"s_waitcnt lgkmcnt(4)\n"
"v_mfma_f32_16x16x32_bf16 v[136:139], v[100:103], v[120:123], v[136:139]\n"
"v_mfma_f32_16x16x32_bf16 v[168:171], v[100:103], v[124:127], v[168:171]\n"
"v_mfma_f32_16x16x32_bf16 v[200:203], v[100:103], v[128:131], v[200:203]\n"
"ds_read_b64_tr_b16 v[100:101], v233 offset:1024\n"
"ds_read_b64_tr_b16 v[102:103], v233 offset:5120\n"
"ds_read_b64_tr_b16 v[228:229], v233 offset:512\n"
"ds_read_b64_tr_b16 v[230:231], v233 offset:4608\n"
"s_waitcnt lgkmcnt(4)\n"
"v_mfma_f32_16x16x32_bf16 v[140:143], v[104:107], v[120:123], v[140:143]\n"
"v_mfma_f32_16x16x32_bf16 v[172:175], v[104:107], v[124:127], v[172:175]\n"
"v_mfma_f32_16x16x32_bf16 v[204:207], v[104:107], v[128:131], v[204:207]\n"
"s_waitcnt lgkmcnt(0)\n"
"ds_read_b64_tr_b16 v[104:105], v232 offset:1536\n"
"ds_read_b64_tr_b16 v[106:107], v232 offset:5632\n"
"v_mfma_f32_16x16x32_bf16 v[144:147], v[228:231], v[120:123], v[144:147]\n"
"v_mfma_f32_16x16x32_bf16 v[176:179], v[228:231], v[124:127], v[176:179]\n"
"v_mfma_f32_16x16x32_bf16 v[208:211], v[228:231], v[128:131], v[208:211]\n"
"ds_read_b64_tr_b16 v[228:229], v233 offset:1536\n"
"ds_read_b64_tr_b16 v[230:231], v233 offset:5632\n"
"v_mfma_f32_16x16x32_bf16 v[148:151], v[96:99], v[120:123], v[148:151]\n"
"v_mfma_f32_16x16x32_bf16 v[180:183], v[96:99], v[124:127], v[180:183]\n"
"v_mfma_f32_16x16x32_bf16 v[212:215], v[96:99], v[128:131], v[212:215]\n"
"v_mfma_f32_16x16x32_bf16 v[152:155], v[100:103], v[120:123], v[152:155]\n"
"v_mfma_f32_16x16x32_bf16 v[184:187], v[100:103], v[124:127], v[184:187]\n"
"v_mfma_f32_16x16x32_bf16 v[216:219], v[100:103], v[128:131], v[216:219]\n"
"s_waitcnt lgkmcnt(2)\n"
"v_mfma_f32_16x16x32_bf16 v[156:159], v[104:107], v[120:123], v[156:159]\n"
"v_mfma_f32_16x16x32_bf16 v[188:191], v[104:107], v[124:127], v[188:191]\n"
"v_mfma_f32_16x16x32_bf16 v[220:223], v[104:107], v[128:131], v[220:223]\n"
"s_waitcnt lgkmcnt(0)\n"
"v_mfma_f32_16x16x32_bf16 v[160:163], v[228:231], v[120:123], v[160:163]\n"
"v_mfma_f32_16x16x32_bf16 v[192:195], v[228:231], v[124:127], v[192:195]\n"
"v_mfma_f32_16x16x32_bf16 v[224:227], v[228:231], v[128:131], v[224:227]\n"
"s_barrier\n"
"ds_read_b128 v[108:111], v254 offset:47104\n"
"ds_read_b128 v[112:115], v254 offset:49152\n"
"ds_read_b128 v[116:119], v254 offset:51200\n"
"ds_read_b64_tr_b16 v[96:97], v232 offset:8192\n"
"ds_read_b64_tr_b16 v[98:99], v232 offset:12288\n"
"ds_read_b64_tr_b16 v[100:101], v233 offset:8192\n"
"ds_read_b64_tr_b16 v[102:103], v233 offset:12288\n"
"s_waitcnt lgkmcnt(2)\n"
"v_mfma_f32_16x16x32_bf16 v[132:135], v[96:99], v[108:111], v[132:135]\n"
"v_mfma_f32_16x16x32_bf16 v[164:167], v[96:99], v[112:115], v[164:167]\n"
"v_mfma_f32_16x16x32_bf16 v[196:199], v[96:99], v[116:119], v[196:199]\n"
"ds_read_b64_tr_b16 v[96:97], v232 offset:9216\n"
"ds_read_b64_tr_b16 v[98:99], v232 offset:13312\n"
"ds_read_b64_tr_b16 v[104:105], v232 offset:8704\n"
"ds_read_b64_tr_b16 v[106:107], v232 offset:12800\n"
"s_waitcnt lgkmcnt(4)\n"
"v_mfma_f32_16x16x32_bf16 v[136:139], v[100:103], v[108:111], v[136:139]\n"
"v_mfma_f32_16x16x32_bf16 v[168:171], v[100:103], v[112:115], v[168:171]\n"
"v_mfma_f32_16x16x32_bf16 v[200:203], v[100:103], v[116:119], v[200:203]\n"
"ds_read_b64_tr_b16 v[100:101], v233 offset:9216\n"
"ds_read_b64_tr_b16 v[102:103], v233 offset:13312\n"
"ds_read_b64_tr_b16 v[228:229], v233 offset:8704\n"
"ds_read_b64_tr_b16 v[230:231], v233 offset:12800\n"
"s_waitcnt lgkmcnt(4)\n"
"v_mfma_f32_16x16x32_bf16 v[140:143], v[104:107], v[108:111], v[140:143]\n"
"v_mfma_f32_16x16x32_bf16 v[172:175], v[104:107], v[112:115], v[172:175]\n"
"v_mfma_f32_16x16x32_bf16 v[204:207], v[104:107], v[116:119], v[204:207]\n"
"s_waitcnt lgkmcnt(0)\n"
"ds_read_b64_tr_b16 v[104:105], v232 offset:9728\n"
"ds_read_b64_tr_b16 v[106:107], v232 offset:13824\n"
"v_mfma_f32_16x16x32_bf16 v[144:147], v[228:231], v[108:111], v[144:147]\n"
"v_mfma_f32_16x16x32_bf16 v[176:179], v[228:231], v[112:115], v[176:179]\n"
"v_mfma_f32_16x16x32_bf16 v[208:211], v[228:231], v[116:119], v[208:211]\n"
"ds_read_b64_tr_b16 v[228:229], v233 offset:9728\n"
"ds_read_b64_tr_b16 v[230:231], v233 offset:13824\n"
"v_mfma_f32_16x16x32_bf16 v[148:151], v[96:99], v[108:111], v[148:151]\n"
"v_mfma_f32_16x16x32_bf16 v[180:183], v[96:99], v[112:115], v[180:183]\n"
"v_mfma_f32_16x16x32_bf16 v[212:215], v[96:99], v[116:119], v[212:215]\n"
"v_mfma_f32_16x16x32_bf16 v[152:155], v[100:103], v[108:111], v[152:155]\n"
"v_mfma_f32_16x16x32_bf16 v[184:187], v[100:103], v[112:115], v[184:187]\n"
"v_mfma_f32_16x16x32_bf16 v[216:219], v[100:103], v[116:119], v[216:219]\n"
"s_waitcnt lgkmcnt(2)\n"
"v_mfma_f32_16x16x32_bf16 v[156:159], v[104:107], v[108:111], v[156:159]\n"
"v_mfma_f32_16x16x32_bf16 v[188:191], v[104:107], v[112:115], v[188:191]\n"
"v_mfma_f32_16x16x32_bf16 v[220:223], v[104:107], v[116:119], v[220:223]\n"
"s_waitcnt lgkmcnt(0)\n"
"v_mfma_f32_16x16x32_bf16 v[160:163], v[228:231], v[108:111], v[160:163]\n"
"v_mfma_f32_16x16x32_bf16 v[192:195], v[228:231], v[112:115], v[192:195]\n"
"v_mfma_f32_16x16x32_bf16 v[224:227], v[228:231], v[116:119], v[224:227]\n"
"s_barrier\n"
"ds_read_b128 v[120:123], v254 offset:47616\n"
"ds_read_b128 v[124:127], v254 offset:49664\n"
"ds_read_b128 v[128:131], v254 offset:51712\n"
"ds_read_b64_tr_b16 v[96:97], v234 offset:4096\n"
"ds_read_b64_tr_b16 v[98:99], v234 offset:8192\n"
"ds_read_b64_tr_b16 v[100:101], v235 offset:4096\n"
"ds_read_b64_tr_b16 v[102:103], v235 offset:8192\n"
"s_waitcnt lgkmcnt(2)\n"
"v_mfma_f32_16x16x32_bf16 v[132:135], v[96:99], v[120:123], v[132:135]\n"
"v_mfma_f32_16x16x32_bf16 v[164:167], v[96:99], v[124:127], v[164:167]\n"
"v_mfma_f32_16x16x32_bf16 v[196:199], v[96:99], v[128:131], v[196:199]\n"
"ds_read_b64_tr_b16 v[96:97], v234 offset:5120\n"
"ds_read_b64_tr_b16 v[98:99], v234 offset:9216\n"
"ds_read_b64_tr_b16 v[104:105], v234 offset:4608\n"
"ds_read_b64_tr_b16 v[106:107], v234 offset:8704\n"
"s_waitcnt lgkmcnt(4)\n"
"v_mfma_f32_16x16x32_bf16 v[136:139], v[100:103], v[120:123], v[136:139]\n"
"v_mfma_f32_16x16x32_bf16 v[168:171], v[100:103], v[124:127], v[168:171]\n"
"v_mfma_f32_16x16x32_bf16 v[200:203], v[100:103], v[128:131], v[200:203]\n"
"ds_read_b64_tr_b16 v[100:101], v235 offset:5120\n"
"ds_read_b64_tr_b16 v[102:103], v235 offset:9216\n"
"ds_read_b64_tr_b16 v[228:229], v235 offset:4608\n"
"ds_read_b64_tr_b16 v[230:231], v235 offset:8704\n"
"s_waitcnt lgkmcnt(4)\n"
"v_mfma_f32_16x16x32_bf16 v[140:143], v[104:107], v[120:123], v[140:143]\n"
"v_mfma_f32_16x16x32_bf16 v[172:175], v[104:107], v[124:127], v[172:175]\n"
"v_mfma_f32_16x16x32_bf16 v[204:207], v[104:107], v[128:131], v[204:207]\n"
"s_waitcnt lgkmcnt(0)\n"
"ds_read_b64_tr_b16 v[104:105], v234 offset:5632\n"
"ds_read_b64_tr_b16 v[106:107], v234 offset:9728\n"
"v_mfma_f32_16x16x32_bf16 v[144:147], v[228:231], v[120:123], v[144:147]\n"
"v_mfma_f32_16x16x32_bf16 v[176:179], v[228:231], v[124:127], v[176:179]\n"
"v_mfma_f32_16x16x32_bf16 v[208:211], v[228:231], v[128:131], v[208:211]\n"
"ds_read_b64_tr_b16 v[228:229], v235 offset:5632\n"
"ds_read_b64_tr_b16 v[230:231], v235 offset:9728\n"
"v_mfma_f32_16x16x32_bf16 v[148:151], v[96:99], v[120:123], v[148:151]\n"
"v_mfma_f32_16x16x32_bf16 v[180:183], v[96:99], v[124:127], v[180:183]\n"
"v_mfma_f32_16x16x32_bf16 v[212:215], v[96:99], v[128:131], v[212:215]\n"
"v_mfma_f32_16x16x32_bf16 v[152:155], v[100:103], v[120:123], v[152:155]\n"
"v_mfma_f32_16x16x32_bf16 v[184:187], v[100:103], v[124:127], v[184:187]\n"
"v_mfma_f32_16x16x32_bf16 v[216:219], v[100:103], v[128:131], v[216:219]\n"
"s_waitcnt lgkmcnt(2)\n"
"v_mfma_f32_16x16x32_bf16 v[156:159], v[104:107], v[120:123], v[156:159]\n"
"v_mfma_f32_16x16x32_bf16 v[188:191], v[104:107], v[124:127], v[188:191]\n"
"v_mfma_f32_16x16x32_bf16 v[220:223], v[104:107], v[128:131], v[220:223]\n"
"s_waitcnt lgkmcnt(0)\n"
"v_mfma_f32_16x16x32_bf16 v[160:163], v[228:231], v[120:123], v[160:163]\n"
"v_mfma_f32_16x16x32_bf16 v[192:195], v[228:231], v[124:127], v[192:195]\n"
"v_mfma_f32_16x16x32_bf16 v[224:227], v[228:231], v[128:131], v[224:227]\n"
"s_barrier\n"
"ds_read_b128 v[108:111], v254 offset:28672\n"
"ds_read_b128 v[112:115], v254 offset:30720\n"
"ds_read_b128 v[116:119], v254 offset:32768\n"
"ds_read_b64_tr_b16 v[96:97], v234 offset:12288\n"
"ds_read_b64_tr_b16 v[98:99], v234 offset:16384\n"
"ds_read_b64_tr_b16 v[100:101], v235 offset:12288\n"
"ds_read_b64_tr_b16 v[102:103], v235 offset:16384\n"
"s_waitcnt lgkmcnt(2)\n"
"v_mfma_f32_16x16x32_bf16 v[132:135], v[96:99], v[108:111], v[132:135]\n"
"v_mfma_f32_16x16x32_bf16 v[164:167], v[96:99], v[112:115], v[164:167]\n"
"v_mfma_f32_16x16x32_bf16 v[196:199], v[96:99], v[116:119], v[196:199]\n"
"ds_read_b64_tr_b16 v[96:97], v234 offset:13312\n"
"ds_read_b64_tr_b16 v[98:99], v234 offset:17408\n"
"ds_read_b64_tr_b16 v[104:105], v234 offset:12800\n"
"ds_read_b64_tr_b16 v[106:107], v234 offset:16896\n"
"s_waitcnt lgkmcnt(4)\n"
"v_mfma_f32_16x16x32_bf16 v[136:139], v[100:103], v[108:111], v[136:139]\n"
"v_mfma_f32_16x16x32_bf16 v[168:171], v[100:103], v[112:115], v[168:171]\n"
"v_mfma_f32_16x16x32_bf16 v[200:203], v[100:103], v[116:119], v[200:203]\n"
"ds_read_b64_tr_b16 v[100:101], v235 offset:13312\n"
"ds_read_b64_tr_b16 v[102:103], v235 offset:17408\n"
"ds_read_b64_tr_b16 v[228:229], v235 offset:12800\n"
"ds_read_b64_tr_b16 v[230:231], v235 offset:16896\n"
"s_waitcnt lgkmcnt(4)\n"
"v_mfma_f32_16x16x32_bf16 v[140:143], v[104:107], v[108:111], v[140:143]\n"
"v_mfma_f32_16x16x32_bf16 v[172:175], v[104:107], v[112:115], v[172:175]\n"
"v_mfma_f32_16x16x32_bf16 v[204:207], v[104:107], v[116:119], v[204:207]\n"
"s_waitcnt lgkmcnt(0)\n"
"ds_read_b64_tr_b16 v[104:105], v234 offset:13824\n"
"ds_read_b64_tr_b16 v[106:107], v234 offset:17920\n"
"v_mfma_f32_16x16x32_bf16 v[144:147], v[228:231], v[108:111], v[144:147]\n"
"v_mfma_f32_16x16x32_bf16 v[176:179], v[228:231], v[112:115], v[176:179]\n"
"v_mfma_f32_16x16x32_bf16 v[208:211], v[228:231], v[116:119], v[208:211]\n"
"ds_read_b64_tr_b16 v[228:229], v235 offset:13824\n"
"ds_read_b64_tr_b16 v[230:231], v235 offset:17920\n"
"v_mfma_f32_16x16x32_bf16 v[148:151], v[96:99], v[108:111], v[148:151]\n"
"v_mfma_f32_16x16x32_bf16 v[180:183], v[96:99], v[112:115], v[180:183]\n"
"v_mfma_f32_16x16x32_bf16 v[212:215], v[96:99], v[116:119], v[212:215]\n"
"v_mfma_f32_16x16x32_bf16 v[152:155], v[100:103], v[108:111], v[152:155]\n"
"v_mfma_f32_16x16x32_bf16 v[184:187], v[100:103], v[112:115], v[184:187]\n"
"v_mfma_f32_16x16x32_bf16 v[216:219], v[100:103], v[116:119], v[216:219]\n"
"s_waitcnt lgkmcnt(2)\n"
"v_mfma_f32_16x16x32_bf16 v[156:159], v[104:107], v[108:111], v[156:159]\n"
"v_mfma_f32_16x16x32_bf16 v[188:191], v[104:107], v[112:115], v[188:191]\n"
"v_mfma_f32_16x16x32_bf16 v[220:223], v[104:107], v[116:119], v[220:223]\n"
"s_waitcnt lgkmcnt(0)\n"
"v_mfma_f32_16x16x32_bf16 v[160:163], v[228:231], v[108:111], v[160:163]\n"
"v_mfma_f32_16x16x32_bf16 v[192:195], v[228:231], v[112:115], v[192:195]\n"
"v_mfma_f32_16x16x32_bf16 v[224:227], v[228:231], v[116:119], v[224:227]\n"
"s_barrier\n"
"ds_read_b128 v[120:123], v254 offset:29184\n"
"ds_read_b128 v[124:127], v254 offset:31232\n"
"ds_read_b128 v[128:131], v254 offset:33280\n"
"ds_read_b64_tr_b16 v[96:97], v234 offset:20480\n"
"ds_read_b64_tr_b16 v[98:99], v234 offset:24576\n"
"ds_read_b64_tr_b16 v[100:101], v235 offset:20480\n"
"ds_read_b64_tr_b16 v[102:103], v235 offset:24576\n"
"s_waitcnt lgkmcnt(2)\n"
"v_mfma_f32_16x16x32_bf16 v[132:135], v[96:99], v[120:123], v[132:135]\n"
"v_mfma_f32_16x16x32_bf16 v[164:167], v[96:99], v[124:127], v[164:167]\n"
"v_mfma_f32_16x16x32_bf16 v[196:199], v[96:99], v[128:131], v[196:199]\n"
"ds_read_b64_tr_b16 v[96:97], v234 offset:21504\n"
"ds_read_b64_tr_b16 v[98:99], v234 offset:25600\n"
"ds_read_b64_tr_b16 v[104:105], v234 offset:20992\n"
"ds_read_b64_tr_b16 v[106:107], v234 offset:25088\n"
"s_waitcnt lgkmcnt(4)\n"
"v_mfma_f32_16x16x32_bf16 v[136:139], v[100:103], v[120:123], v[136:139]\n"
"v_mfma_f32_16x16x32_bf16 v[168:171], v[100:103], v[124:127], v[168:171]\n"
"v_mfma_f32_16x16x32_bf16 v[200:203], v[100:103], v[128:131], v[200:203]\n"
"ds_read_b64_tr_b16 v[100:101], v235 offset:21504\n"
"ds_read_b64_tr_b16 v[102:103], v235 offset:25600\n"
"ds_read_b64_tr_b16 v[228:229], v235 offset:20992\n"
"ds_read_b64_tr_b16 v[230:231], v235 offset:25088\n"
"s_waitcnt lgkmcnt(4)\n"
"v_mfma_f32_16x16x32_bf16 v[140:143], v[104:107], v[120:123], v[140:143]\n"
"v_mfma_f32_16x16x32_bf16 v[172:175], v[104:107], v[124:127], v[172:175]\n"
"v_mfma_f32_16x16x32_bf16 v[204:207], v[104:107], v[128:131], v[204:207]\n"
"s_waitcnt lgkmcnt(0)\n"
"ds_read_b64_tr_b16 v[104:105], v234 offset:22016\n"
"ds_read_b64_tr_b16 v[106:107], v234 offset:26112\n"
"v_mfma_f32_16x16x32_bf16 v[144:147], v[228:231], v[120:123], v[144:147]\n"
"v_mfma_f32_16x16x32_bf16 v[176:179], v[228:231], v[124:127], v[176:179]\n"
"v_mfma_f32_16x16x32_bf16 v[208:211], v[228:231], v[128:131], v[208:211]\n"
"ds_read_b64_tr_b16 v[228:229], v235 offset:22016\n"
"ds_read_b64_tr_b16 v[230:231], v235 offset:26112\n"
"v_mfma_f32_16x16x32_bf16 v[148:151], v[96:99], v[120:123], v[148:151]\n"
"v_mfma_f32_16x16x32_bf16 v[180:183], v[96:99], v[124:127], v[180:183]\n"
"v_mfma_f32_16x16x32_bf16 v[212:215], v[96:99], v[128:131], v[212:215]\n"
"v_mfma_f32_16x16x32_bf16 v[152:155], v[100:103], v[120:123], v[152:155]\n"
"v_mfma_f32_16x16x32_bf16 v[184:187], v[100:103], v[124:127], v[184:187]\n"
"v_mfma_f32_16x16x32_bf16 v[216:219], v[100:103], v[128:131], v[216:219]\n"
"s_waitcnt lgkmcnt(2)\n"
"v_mfma_f32_16x16x32_bf16 v[156:159], v[104:107], v[120:123], v[156:159]\n"
"v_mfma_f32_16x16x32_bf16 v[188:191], v[104:107], v[124:127], v[188:191]\n"
"v_mfma_f32_16x16x32_bf16 v[220:223], v[104:107], v[128:131], v[220:223]\n"
"s_waitcnt lgkmcnt(0)\n"
"v_mfma_f32_16x16x32_bf16 v[160:163], v[228:231], v[120:123], v[160:163]\n"
"v_mfma_f32_16x16x32_bf16 v[192:195], v[228:231], v[124:127], v[192:195]\n"
"v_mfma_f32_16x16x32_bf16 v[224:227], v[228:231], v[128:131], v[224:227]\n"
"s_waitcnt vmcnt(0)\n"
"s_nop 7\n"
"v_pk_mul_f32 v[132:133], v[132:133], s[62:63]\n"
"v_pk_mul_f32 v[134:135], v[134:135], s[62:63]\n"
"v_pk_mul_f32 v[136:137], v[136:137], s[62:63]\n"
"v_pk_mul_f32 v[138:139], v[138:139], s[62:63]\n"
"v_cvt_pk_bf16_f32 v96, v132, v133\n"
"v_cvt_pk_bf16_f32 v97, v134, v135\n"
"v_cvt_pk_bf16_f32 v98, v136, v137\n"
"v_cvt_pk_bf16_f32 v99, v138, v139\n"
"v_pk_mul_f32 v[140:141], v[140:141], s[62:63]\n"
"v_pk_mul_f32 v[142:143], v[142:143], s[62:63]\n"
"v_pk_mul_f32 v[144:145], v[144:145], s[62:63]\n"
"v_pk_mul_f32 v[146:147], v[146:147], s[62:63]\n"
"v_cvt_pk_bf16_f32 v100, v140, v141\n"
"v_cvt_pk_bf16_f32 v101, v142, v143\n"
"v_cvt_pk_bf16_f32 v102, v144, v145\n"
"v_cvt_pk_bf16_f32 v103, v146, v147\n"
"v_pk_mul_f32 v[148:149], v[148:149], s[62:63]\n"
"v_pk_mul_f32 v[150:151], v[150:151], s[62:63]\n"
"v_pk_mul_f32 v[152:153], v[152:153], s[62:63]\n"
"v_pk_mul_f32 v[154:155], v[154:155], s[62:63]\n"
"v_cvt_pk_bf16_f32 v104, v148, v149\n"
"v_cvt_pk_bf16_f32 v105, v150, v151\n"
"v_cvt_pk_bf16_f32 v106, v152, v153\n"
"v_cvt_pk_bf16_f32 v107, v154, v155\n"
"v_pk_mul_f32 v[156:157], v[156:157], s[62:63]\n"
"v_pk_mul_f32 v[158:159], v[158:159], s[62:63]\n"
"v_pk_mul_f32 v[160:161], v[160:161], s[62:63]\n"
"v_pk_mul_f32 v[162:163], v[162:163], s[62:63]\n"
"v_cvt_pk_bf16_f32 v108, v156, v157\n"
"v_cvt_pk_bf16_f32 v109, v158, v159\n"
"v_cvt_pk_bf16_f32 v110, v160, v161\n"
"v_cvt_pk_bf16_f32 v111, v162, v163\n"
"v_permlane32_swap_b32 v96, v98\n"
"v_permlane32_swap_b32 v97, v99\n"
"v_permlane32_swap_b32 v100, v102\n"
"v_permlane32_swap_b32 v101, v103\n"
"v_permlane32_swap_b32 v104, v106\n"
"v_permlane32_swap_b32 v105, v107\n"
"v_permlane32_swap_b32 v108, v110\n"
"v_permlane32_swap_b32 v109, v111\n"
"v_permlane16_swap_b32 v96, v98\n"
"v_permlane16_swap_b32 v97, v99\n"
"v_permlane16_swap_b32 v100, v102\n"
"v_permlane16_swap_b32 v101, v103\n"
"v_permlane16_swap_b32 v104, v106\n"
"v_permlane16_swap_b32 v105, v107\n"
"v_permlane16_swap_b32 v108, v110\n"
"v_permlane16_swap_b32 v109, v111\n"
"v_pk_mul_f32 v[164:165], v[164:165], s[62:63]\n"
"v_pk_mul_f32 v[166:167], v[166:167], s[62:63]\n"
"v_pk_mul_f32 v[168:169], v[168:169], s[62:63]\n"
"v_pk_mul_f32 v[170:171], v[170:171], s[62:63]\n"
"v_cvt_pk_bf16_f32 v112, v164, v165\n"
"v_cvt_pk_bf16_f32 v113, v166, v167\n"
"v_cvt_pk_bf16_f32 v114, v168, v169\n"
"v_cvt_pk_bf16_f32 v115, v170, v171\n"
"v_pk_mul_f32 v[172:173], v[172:173], s[62:63]\n"
"v_pk_mul_f32 v[174:175], v[174:175], s[62:63]\n"
"v_pk_mul_f32 v[176:177], v[176:177], s[62:63]\n"
"v_pk_mul_f32 v[178:179], v[178:179], s[62:63]\n"
"v_cvt_pk_bf16_f32 v116, v172, v173\n"
"v_cvt_pk_bf16_f32 v117, v174, v175\n"
"v_cvt_pk_bf16_f32 v118, v176, v177\n"
"v_cvt_pk_bf16_f32 v119, v178, v179\n"
"v_pk_mul_f32 v[180:181], v[180:181], s[62:63]\n"
"v_pk_mul_f32 v[182:183], v[182:183], s[62:63]\n"
"v_pk_mul_f32 v[184:185], v[184:185], s[62:63]\n"
"v_pk_mul_f32 v[186:187], v[186:187], s[62:63]\n"
"v_cvt_pk_bf16_f32 v120, v180, v181\n"
"v_cvt_pk_bf16_f32 v121, v182, v183\n"
"v_cvt_pk_bf16_f32 v122, v184, v185\n"
"v_cvt_pk_bf16_f32 v123, v186, v187\n"
"v_pk_mul_f32 v[188:189], v[188:189], s[62:63]\n"
"v_pk_mul_f32 v[190:191], v[190:191], s[62:63]\n"
"v_pk_mul_f32 v[192:193], v[192:193], s[62:63]\n"
"v_pk_mul_f32 v[194:195], v[194:195], s[62:63]\n"
"v_cvt_pk_bf16_f32 v124, v188, v189\n"
"v_cvt_pk_bf16_f32 v125, v190, v191\n"
"v_cvt_pk_bf16_f32 v126, v192, v193\n"
"v_cvt_pk_bf16_f32 v127, v194, v195\n"
"v_permlane32_swap_b32 v112, v114\n"
"v_permlane32_swap_b32 v113, v115\n"
"v_permlane32_swap_b32 v116, v118\n"
"v_permlane32_swap_b32 v117, v119\n"
"v_permlane32_swap_b32 v120, v122\n"
"v_permlane32_swap_b32 v121, v123\n"
"v_permlane32_swap_b32 v124, v126\n"
"v_permlane32_swap_b32 v125, v127\n"
"v_permlane16_swap_b32 v112, v114\n"
"v_permlane16_swap_b32 v113, v115\n"
"v_permlane16_swap_b32 v116, v118\n"
"v_permlane16_swap_b32 v117, v119\n"
"v_permlane16_swap_b32 v120, v122\n"
"v_permlane16_swap_b32 v121, v123\n"
"v_permlane16_swap_b32 v124, v126\n"
"v_permlane16_swap_b32 v125, v127\n"
"v_pk_mul_f32 v[196:197], v[196:197], s[62:63]\n"
"v_pk_mul_f32 v[198:199], v[198:199], s[62:63]\n"
"v_pk_mul_f32 v[200:201], v[200:201], s[62:63]\n"
"v_pk_mul_f32 v[202:203], v[202:203], s[62:63]\n"
"v_cvt_pk_bf16_f32 v128, v196, v197\n"
"v_cvt_pk_bf16_f32 v129, v198, v199\n"
"v_cvt_pk_bf16_f32 v130, v200, v201\n"
"v_cvt_pk_bf16_f32 v131, v202, v203\n"
"v_pk_mul_f32 v[204:205], v[204:205], s[62:63]\n"
"v_pk_mul_f32 v[206:207], v[206:207], s[62:63]\n"
"v_pk_mul_f32 v[208:209], v[208:209], s[62:63]\n"
"v_pk_mul_f32 v[210:211], v[210:211], s[62:63]\n"
"v_cvt_pk_bf16_f32 v132, v204, v205\n"
"v_cvt_pk_bf16_f32 v133, v206, v207\n"
"v_cvt_pk_bf16_f32 v134, v208, v209\n"
"v_cvt_pk_bf16_f32 v135, v210, v211\n"
"v_pk_mul_f32 v[212:213], v[212:213], s[62:63]\n"
"v_pk_mul_f32 v[214:215], v[214:215], s[62:63]\n"
"v_pk_mul_f32 v[216:217], v[216:217], s[62:63]\n"
"v_pk_mul_f32 v[218:219], v[218:219], s[62:63]\n"
"v_cvt_pk_bf16_f32 v136, v212, v213\n"
"v_cvt_pk_bf16_f32 v137, v214, v215\n"
"v_cvt_pk_bf16_f32 v138, v216, v217\n"
"v_cvt_pk_bf16_f32 v139, v218, v219\n"
"v_pk_mul_f32 v[220:221], v[220:221], s[62:63]\n"
"v_pk_mul_f32 v[222:223], v[222:223], s[62:63]\n"
"v_pk_mul_f32 v[224:225], v[224:225], s[62:63]\n"
"v_pk_mul_f32 v[226:227], v[226:227], s[62:63]\n"
"v_cvt_pk_bf16_f32 v140, v220, v221\n"
"v_cvt_pk_bf16_f32 v141, v222, v223\n"
"v_cvt_pk_bf16_f32 v142, v224, v225\n"
"v_cvt_pk_bf16_f32 v143, v226, v227\n"
"v_permlane32_swap_b32 v128, v130\n"
"v_permlane32_swap_b32 v129, v131\n"
"v_permlane32_swap_b32 v132, v134\n"
"v_permlane32_swap_b32 v133, v135\n"
"v_permlane32_swap_b32 v136, v138\n"
"v_permlane32_swap_b32 v137, v139\n"
"v_permlane32_swap_b32 v140, v142\n"
"v_permlane32_swap_b32 v141, v143\n"
"v_permlane16_swap_b32 v128, v130\n"
"v_permlane16_swap_b32 v129, v131\n"
"v_permlane16_swap_b32 v132, v134\n"
"v_permlane16_swap_b32 v133, v135\n"
"v_permlane16_swap_b32 v136, v138\n"
"v_permlane16_swap_b32 v137, v139\n"
"v_permlane16_swap_b32 v140, v142\n"
"v_permlane16_swap_b32 v141, v143\n"
"ds_read_b128 v[184:187], v248\n"
"ds_read_b128 v[192:195], v248 offset:512\n"
"ds_read_b128 v[200:203], v248 offset:1024\n"
"ds_read_b128 v[220:223], v248 offset:1536\n"
"v_and_b32 v247, 15, v238\n"
"v_cmp_eq_u32 vcc, 0, v247\n"
"v_mov_b32 v247, 0x3f803f80\n"
"s_nop 0\n"
"v_cndmask_b32 v240, 0, v247, vcc\n"
"v_cndmask_b32 v241, 0, v247, vcc\n"
"v_cndmask_b32 v242, 0, v247, vcc\n"
"v_cndmask_b32 v243, 0, v247, vcc\n"
"s_waitcnt lgkmcnt(3)\n"
"v_mfma_f32_16x16x32_bf16 v[160:163], v[184:187], v[96:99], 0\n"
"v_mfma_f32_16x16x32_bf16 v[168:171], v[184:187], v[112:115], 0\n"
"v_mfma_f32_16x16x32_bf16 v[176:179], v[184:187], v[128:131], 0\n"
"s_waitcnt lgkmcnt(2)\n"
"v_mfma_f32_16x16x32_bf16 v[160:163], v[192:195], v[100:103], v[160:163]\n"
"v_mfma_f32_16x16x32_bf16 v[168:171], v[192:195], v[116:119], v[168:171]\n"
"v_mfma_f32_16x16x32_bf16 v[176:179], v[192:195], v[132:135], v[176:179]\n"
"s_waitcnt lgkmcnt(1)\n"
"v_mfma_f32_16x16x32_bf16 v[160:163], v[200:203], v[104:107], v[160:163]\n"
"v_mfma_f32_16x16x32_bf16 v[168:171], v[200:203], v[120:123], v[168:171]\n"
"v_mfma_f32_16x16x32_bf16 v[176:179], v[200:203], v[136:139], v[176:179]\n"
"s_waitcnt lgkmcnt(0)\n"
"v_mfma_f32_16x16x32_bf16 v[160:163], v[220:223], v[108:111], v[160:163]\n"
"v_mfma_f32_16x16x32_bf16 v[168:171], v[220:223], v[124:127], v[168:171]\n"
"v_mfma_f32_16x16x32_bf16 v[176:179], v[220:223], v[140:143], v[176:179]\n"
"v_mov_b32 v156, 0\n"
"v_mov_b32 v157, 0\n"
"v_mov_b32 v158, 0\n"
"v_mov_b32 v159, 0\n"
"v_mov_b32 v164, 0\n"
"v_mov_b32 v165, 0\n"
"v_mov_b32 v166, 0\n"
"v_mov_b32 v167, 0\n"
"v_mov_b32 v172, 0\n"
"v_mov_b32 v173, 0\n"
"v_mov_b32 v174, 0\n"
"v_mov_b32 v175, 0\n"
"v_mov_b32 v180, 0\n"
"v_mov_b32 v181, 0\n"
"v_mov_b32 v182, 0\n"
"v_mov_b32 v183, 0\n"
"v_mov_b32 v188, 0\n"
"v_mov_b32 v189, 0\n"
"v_mov_b32 v190, 0\n"
"v_mov_b32 v191, 0\n"
"v_mov_b32 v196, 0\n"
"v_mov_b32 v197, 0\n"
"v_mov_b32 v198, 0\n"
"v_mov_b32 v199, 0\n"
"v_max3_f32 v228, v160, v161, v162\n"
"v_max_f32 v228, v228, v163\n"
"v_max3_f32 v232, v168, v169, v170\n"
"v_max_f32 v232, v232, v171\n"
"v_max3_f32 v236, v176, v177, v178\n"
"v_max_f32 v236, v236, v179\n"
"v_mov_b32 v204, v228\n"
"v_mov_b32 v205, v228\n"
"s_nop 1\n"
"v_permlane32_swap_b32 v204, v205\n"
"v_max_f32 v228, v204, v205\n"
"v_mov_b32 v204, v228\n"
"v_mov_b32 v205, v228\n"
"s_nop 1\n"
"v_permlane16_swap_b32 v204, v205\n"
"v_max_f32 v228, v204, v205\n"
"v_sub_f32 v144, 0, v228\n"
"v_sub_f32 v145, 0, v228\n"
"v_sub_f32 v146, 0, v228\n"
"v_sub_f32 v147, 0, v228\n"
"v_mov_b32 v228, 0\n"
"v_mov_b32 v204, v232\n"
"v_mov_b32 v205, v232\n"
"s_nop 1\n"
"v_permlane32_swap_b32 v204, v205\n"
"v_max_f32 v232, v204, v205\n"
"v_mov_b32 v204, v232\n"
"v_mov_b32 v205, v232\n"
"s_nop 1\n"
"v_permlane16_swap_b32 v204, v205\n"
"v_max_f32 v232, v204, v205\n"
"v_sub_f32 v148, 0, v232\n"
"v_sub_f32 v149, 0, v232\n"
"v_sub_f32 v150, 0, v232\n"
"v_sub_f32 v151, 0, v232\n"
"v_mov_b32 v232, 0\n"
"v_mov_b32 v204, v236\n"
"v_mov_b32 v205, v236\n"
"s_nop 1\n"
"v_permlane32_swap_b32 v204, v205\n"
"v_max_f32 v236, v204, v205\n"
"v_mov_b32 v204, v236\n"
"v_mov_b32 v205, v236\n"
"s_nop 1\n"
"v_permlane16_swap_b32 v204, v205\n"
"v_max_f32 v236, v204, v205\n"
"v_sub_f32 v152, 0, v236\n"
"v_sub_f32 v153, 0, v236\n"
"v_sub_f32 v154, 0, v236\n"
"v_sub_f32 v155, 0, v236\n"
"v_mov_b32 v236, 0\n"
"v_mov_b32 v216, 0\n"
"v_mov_b32 v217, 0\n"
"v_mov_b32 v218, 0\n"
"v_mov_b32 v219, 0\n"
"v_mov_b32 v220, 0\n"
"v_mov_b32 v221, 0\n"
"v_mov_b32 v222, 0\n"
"v_mov_b32 v223, 0\n"
"v_mov_b32 v224, 0\n"
"v_mov_b32 v225, 0\n"
"v_mov_b32 v226, 0\n"
"v_mov_b32 v227, 0\n"
"s_barrier\n"
"s_branch .Lk1_attn\n"
".Lk1_aux0:\n"
"s_barrier\n"
"s_barrier\n"
"s_barrier\n"
"s_barrier\n"
"s_barrier\n"
"s_barrier\n"
"s_barrier\n"
"s_barrier\n"
"s_barrier\n"
"s_barrier\n"
"s_barrier\n"
"s_barrier\n"
"s_barrier\n"
"s_barrier\n"
"s_barrier\n"
"s_barrier\n"
"s_barrier\n"
"s_barrier\n"
"s_waitcnt vmcnt(0)\n"
"s_barrier\n"
"s_branch .Lk1_gq\n"
".Lk1_w1:\n"
"s_add_u32 s61, s55, 0\n"
"s_add_u32 m0, s59, 0x19000\n"
"s_nop 0\n"
"buffer_load_dwordx4 v209, s[36:39], s61 offen lds\n"
"s_add_u32 m0, s59, 0x19380\n"
"s_nop 0\n"
"buffer_load_dwordx4 v209, s[36:39], s61 offen offset:128 lds\n"
"s_cmp_eq_u32 s44, 0\n"
"s_cbranch_scc0 .Lk1_rx0_0\n"
"s_mov_b32 s58, 0x24000\n"
"s_mov_b32 m0, 0x23000\n"
"s_nop 0\n"
"buffer_load_dwordx4 v210, s[28:31], s58 offen lds\n"
".Lk1_rx0_0:\n"
"s_cmp_eq_u32 s44, 1\n"
"s_cbranch_scc0 .Lk1_rx0_1\n"
"s_mov_b32 s58, 0x24000\n"
"s_mov_b32 m0, 0x23400\n"
"s_nop 0\n"
"buffer_load_dwordx4 v211, s[28:31], s58 offen lds\n"
".Lk1_rx0_1:\n"
"s_add_u32 s58, s57, 0\n"
"s_add_u32 m0, s48, 0x1f000\n"
"s_nop 0\n"
"buffer_load_dwordx4 v210, s[28:31], s58 offen lds\n"
"s_add_u32 m0, s48, 0x1f400\n"
"s_nop 0\n"
"buffer_load_dwordx4 v211, s[28:31], s58 offen lds\n"
"s_add_u32 s58, s57, 0x4800\n"
"s_add_u32 m0, s48, 0x1f800\n"
"s_nop 0\n"
"buffer_load_dwordx4 v210, s[28:31], s58 offen lds\n"
"s_add_u32 m0, s48, 0x1fc00\n"
"s_nop 0\n"
"buffer_load_dwordx4 v211, s[28:31], s58 offen lds\n"
"s_add_u32 s61, s55, 0x40000\n"
"s_add_u32 m0, s59, 0x1b000\n"
"s_nop 0\n"
"buffer_load_dwordx4 v209, s[36:39], s61 offen lds\n"
"s_add_u32 m0, s59, 0x1b380\n"
"s_nop 0\n"
"buffer_load_dwordx4 v209, s[36:39], s61 offen offset:128 lds\n"
"s_add_u32 s61, s55, 0x80000\n"
"s_add_u32 m0, s59, 0x1d000\n"
"s_nop 0\n"
"buffer_load_dwordx4 v209, s[36:39], s61 offen lds\n"
"s_add_u32 m0, s59, 0x1d380\n"
"s_nop 0\n"
"buffer_load_dwordx4 v209, s[36:39], s61 offen offset:128 lds\n"
"s_cmp_eq_u32 s44, 2\n"
"s_cbranch_scc0 .Lk1_rx1_0\n"
"s_mov_b32 s58, 0x24080\n"
"s_mov_b32 m0, 0x27800\n"
"s_nop 0\n"
"buffer_load_dwordx4 v210, s[28:31], s58 offen lds\n"
".Lk1_rx1_0:\n"
"s_cmp_eq_u32 s44, 3\n"
"s_cbranch_scc0 .Lk1_rx1_1\n"
"s_mov_b32 s58, 0x24080\n"
"s_mov_b32 m0, 0x27c00\n"
"s_nop 0\n"
"buffer_load_dwordx4 v211, s[28:31], s58 offen lds\n"
".Lk1_rx1_1:\n"
"s_add_u32 s58, s57, 0x80\n"
"s_add_u32 m0, s48, 0x23800\n"
"s_nop 0\n"
"buffer_load_dwordx4 v210, s[28:31], s58 offen lds\n"
"s_add_u32 m0, s48, 0x23c00\n"
"s_nop 0\n"
"buffer_load_dwordx4 v211, s[28:31], s58 offen lds\n"
"s_add_u32 s58, s57, 0x4880\n"
"s_add_u32 m0, s48, 0x24000\n"
"s_nop 0\n"
"buffer_load_dwordx4 v210, s[28:31], s58 offen lds\n"
"s_add_u32 m0, s48, 0x24400\n"
"s_nop 0\n"
"buffer_load_dwordx4 v211, s[28:31], s58 offen lds\n"
"s_add_u32 s61, s55, 0xc0000\n"
"s_add_u32 m0, s59, 0x8000\n"
"s_nop 0\n"
"buffer_load_dwordx4 v209, s[36:39], s61 offen lds\n"
"s_add_u32 m0, s59, 0x8380\n"
"s_nop 0\n"
"buffer_load_dwordx4 v209, s[36:39], s61 offen offset:128 lds\n"
"s_lshl_b32 s60, s46, 5\n"
"s_add_u32 s60, s60, s47\n"
"s_lshl_b32 s60, s60, 18\n"
"s_add_u32 s20, s10, s60\n"
"s_addc_u32 s21, s11, 0\n"
"s_and_b32 s21, s21, 0xffff\n"
"s_mov_b32 s20, s20\n"
"s_mov_b32 s22, 0x440000\n"
"s_mov_b32 s23, 0x20000\n"
"s_add_u32 s24, s12, s60\n"
"s_addc_u32 s25, s13, 0\n"
"s_and_b32 s25, s25, 0xffff\n"
"s_mov_b32 s24, s24\n"
"s_mov_b32 s26, 0x440000\n"
"s_mov_b32 s27, 0x20000\n"
"s_mul_i32 s60, s46, 0x120000\n"
"s_add_u32 s40, s16, s60\n"
"s_addc_u32 s41, s17, 0\n"
"s_and_b32 s41, s41, 0xffff\n"
"s_mov_b32 s40, s40\n"
"s_mov_b32 s42, 0x120000\n"
"s_mov_b32 s43, 0x20000\n"
"s_add_u32 s84, s16, 0x1f00000\n"
"s_addc_u32 s85, s17, 0\n"
"s_load_dwordx2 s[98:99], s[84:85], 0x0\n"
"s_and_b32 s85, s85, 0xffff\n"
"s_mov_b32 s84, s84\n"
"s_mov_b32 s86, 0x19000\n"
"s_mov_b32 s87, 0x20000\n"
"v_bfe_u32 v247, v238, 2, 3\n"
"v_bfe_u32 v236, v238, 4, 1\n"
"v_lshrrev_b32 v237, 5, v238\n"
"v_lshlrev_b32 v237, 6, v237\n"
"v_and_b32 v239, 3, v238\n"
"v_lshlrev_b32 v237, 5, v237\n"
"v_lshl_add_u32 v237, v247, 6, v237\n"
"v_lshl_add_u32 v237, v236, 5, v237\n"
"v_lshl_add_u32 v237, v239, 3, v237\n"
"s_mov_b32 s71, s45\n"
"s_lshl_b32 s61, s71, 14\n"
"s_add_u32 s60, s61, 0xc000\n"
"v_add_u32 v249, s60, v237\n"
"v_xor_b32 v250, 32, v249\n"
"v_add_u32 v232, 0x8000, v237\n"
"v_xor_b32 v233, 32, v232\n"
"v_add_u32 v234, 0x18000, v237\n"
"v_xor_b32 v235, 32, v234\n"
"v_bfe_u32 v247, v238, 2, 2\n"
"v_sub_u32 v247, 4, v247\n"
"v_lshrrev_b32 v236, 4, v238\n"
"v_xor_b32 v247, v247, v236\n"
"v_and_b32 v247, 3, v247\n"
"v_lshlrev_b32 v247, 4, v247\n"
"v_and_b32 v236, 7, v238\n"
"v_lshl_add_u32 v247, v236, 6, v247\n"
"v_bfe_u32 v236, v238, 3, 1\n"
"v_lshl_add_u32 v237, v236, 10, v247\n"
"v_add_u32 v253, 0x8000, v237\n"
"v_add_u32 v254, 0x18000, v237\n"
"v_lshl_add_u32 v248, v236, 11, v247\n"
"v_add_u32 v248, s61, v248\n"
"s_waitcnt lgkmcnt(0)\n"
"s_mul_i32 s98, s98, 0x19660d\n"
"s_add_u32 s98, s98, 0x3c6ef35f\n"
"s_mul_i32 s99, s99, 0x15a4e35\n"
"s_add_u32 s99, s99, s98\n"
"s_add_u32 s99, s99, 1\n"
"s_waitcnt vmcnt(10)\n"
"s_barrier\n"
"s_cmp_eq_u32 s44, 0\n"
"s_cbranch_scc0 .Lk1_rx2_0\n"
"s_mov_b32 s58, 0x24100\n"
"s_mov_b32 m0, 0x18000\n"
"s_nop 0\n"
"buffer_load_dwordx4 v210, s[28:31], s58 offen lds\n"
".Lk1_rx2_0:\n"
"s_cmp_eq_u32 s44, 1\n"
"s_cbranch_scc0 .Lk1_rx2_1\n"
"s_mov_b32 s58, 0x24100\n"
"s_mov_b32 m0, 0x18400\n"
"s_nop 0\n"
"buffer_load_dwordx4 v211, s[28:31], s58 offen lds\n"
".Lk1_rx2_1:\n"
"s_add_u32 s58, s57, 0x100\n"
"s_add_u32 m0, s48, 0x14000\n"
"s_nop 0\n"
"buffer_load_dwordx4 v210, s[28:31], s58 offen lds\n"
"s_add_u32 m0, s48, 0x14400\n"
"s_nop 0\n"
"buffer_load_dwordx4 v211, s[28:31], s58 offen lds\n"
"s_add_u32 s58, s57, 0x4900\n"
"s_add_u32 m0, s48, 0x14800\n"
"s_nop 0\n"
"buffer_load_dwordx4 v210, s[28:31], s58 offen lds\n"
"s_add_u32 m0, s48, 0x14c00\n"
"s_nop 0\n"
"buffer_load_dwordx4 v211, s[28:31], s58 offen lds\n"
"s_add_u32 s61, s55, 0x100000\n"
"s_add_u32 m0, s59, 0xa000\n"
"s_nop 0\n"
"buffer_load_dwordx4 v209, s[36:39], s61 offen lds\n"
"s_add_u32 m0, s59, 0xa380\n"
"s_nop 0\n"
"buffer_load_dwordx4 v209, s[36:39], s61 offen offset:128 lds\n"
"s_waitcnt vmcnt(14)\n"
"s_barrier\n"
"s_add_u32 s61, s55, 0x140000\n"
"s_add_u32 m0, s59, 0x19000\n"
"s_nop 0\n"
"buffer_load_dwordx4 v209, s[36:39], s61 offen lds\n"
"s_add_u32 m0, s59, 0x19380\n"
"s_nop 0\n"
"buffer_load_dwordx4 v209, s[36:39], s61 offen offset:128 lds\n"
"s_waitcnt vmcnt(10)\n"
"s_barrier\n"
"s_cmp_eq_u32 s44, 2\n"
"s_cbranch_scc0 .Lk1_rx3_0\n"
"s_mov_b32 s58, 0x24180\n"
"s_mov_b32 m0, 0x23000\n"
"s_nop 0\n"
"buffer_load_dwordx4 v210, s[28:31], s58 offen lds\n"
".Lk1_rx3_0:\n"
"s_cmp_eq_u32 s44, 3\n"
"s_cbranch_scc0 .Lk1_rx3_1\n"
"s_mov_b32 s58, 0x24180\n"
"s_mov_b32 m0, 0x23400\n"
"s_nop 0\n"
"buffer_load_dwordx4 v211, s[28:31], s58 offen lds\n"
".Lk1_rx3_1:\n"
"s_add_u32 s58, s57, 0x180\n"
"s_add_u32 m0, s48, 0x1f000\n"
"s_nop 0\n"
"buffer_load_dwordx4 v210, s[28:31], s58 offen lds\n"
"s_add_u32 m0, s48, 0x1f400\n"
"s_nop 0\n"
"buffer_load_dwordx4 v211, s[28:31], s58 offen lds\n"
"s_add_u32 s58, s57, 0x4980\n"
"s_add_u32 m0, s48, 0x1f800\n"
"s_nop 0\n"
"buffer_load_dwordx4 v210, s[28:31], s58 offen lds\n"
"s_add_u32 m0, s48, 0x1fc00\n"
"s_nop 0\n"
"buffer_load_dwordx4 v211, s[28:31], s58 offen lds\n"
"s_add_u32 s61, s55, 0x180000\n"
"s_add_u32 m0, s59, 0x1b000\n"
"s_nop 0\n"
"buffer_load_dwordx4 v209, s[36:39], s61 offen lds\n"
"s_add_u32 m0, s59, 0x1b380\n"
"s_nop 0\n"
"buffer_load_dwordx4 v209, s[36:39], s61 offen offset:128 lds\n"
"s_add_u32 s53, s56, 0\n"
"s_and_b32 s53, s53, 0x3ffff\n"
"s_add_u32 m0, s48, 0\n"
"s_nop 0\n"
"buffer_load_dwordx4 v206, s[20:23], s53 offen nt lds\n"
"buffer_load_dwordx4 v207, s[20:23], s53 offen offset:2048 nt lds\n"
"s_waitcnt vmcnt(16)\n"
"s_barrier\n"
"s_add_u32 s61, s55, 0x1c0000\n"
"s_add_u32 m0, s59, 0x1d000\n"
"s_nop 0\n"
"buffer_load_dwordx4 v209, s[36:39], s61 offen lds\n"
"s_add_u32 m0, s59, 0x1d380\n"
"s_nop 0\n"
"buffer_load_dwordx4 v209, s[36:39], s61 offen offset:128 lds\n"
"s_waitcnt vmcnt(12)\n"
"s_barrier\n"
"s_cmp_eq_u32 s44, 0\n"
"s_cbranch_scc0 .Lk1_rx4_0\n"
"s_mov_b32 s58, 0x24200\n"
"s_mov_b32 m0, 0x27800\n"
"s_nop 0\n"
"buffer_load_dwordx4 v210, s[28:31], s58 offen lds\n"
".Lk1_rx4_0:\n"
"s_cmp_eq_u32 s44, 1\n"
"s_cbranch_scc0 .Lk1_rx4_1\n"
"s_mov_b32 s58, 0x24200\n"
"s_mov_b32 m0, 0x27c00\n"
"s_nop 0\n"
"buffer_load_dwordx4 v211, s[28:31], s58 offen lds\n"
".Lk1_rx4_1:\n"
"s_add_u32 s58, s57, 0x200\n"
"s_add_u32 m0, s48, 0x23800\n"
"s_nop 0\n"
"buffer_load_dwordx4 v210, s[28:31], s58 offen lds\n"
"s_add_u32 m0, s48, 0x23c00\n"
"s_nop 0\n"
"buffer_load_dwordx4 v211, s[28:31], s58 offen lds\n"
"s_add_u32 s58, s57, 0x4a00\n"
"s_add_u32 m0, s48, 0x24000\n"
"s_nop 0\n"
"buffer_load_dwordx4 v210, s[28:31], s58 offen lds\n"
"s_add_u32 m0, s48, 0x24400\n"
"s_nop 0\n"
"buffer_load_dwordx4 v211, s[28:31], s58 offen lds\n"
"s_add_u32 s61, s55, 0x200000\n"
"s_add_u32 m0, s59, 0x8000\n"
"s_nop 0\n"
"buffer_load_dwordx4 v209, s[36:39], s61 offen lds\n"
"s_add_u32 m0, s59, 0x8380\n"
"s_nop 0\n"
"buffer_load_dwordx4 v209, s[36:39], s61 offen offset:128 lds\n"
"s_waitcnt vmcnt(16)\n"
"s_barrier\n"
"s_add_u32 s61, s55, 0x240000\n"
"s_add_u32 m0, s59, 0xa000\n"
"s_nop 0\n"
"buffer_load_dwordx4 v209, s[36:39], s61 offen lds\n"
"s_add_u32 m0, s59, 0xa380\n"
"s_nop 0\n"
"buffer_load_dwordx4 v209, s[36:39], s61 offen offset:128 lds\n"
"s_add_u32 m0, s48, 0x380\n"
"s_nop 0\n"
"buffer_load_dwordx4 v206, s[20:23], s53 offen offset:128 nt lds\n"
"buffer_load_dwordx4 v207, s[20:23], s53 offen offset:2176 nt lds\n"
"s_waitcnt vmcnt(14)\n"
"s_barrier\n"
"s_cmp_eq_u32 s44, 2\n"
"s_cbranch_scc0 .Lk1_rx5_0\n"
"s_mov_b32 s58, 0x24280\n"
"s_mov_b32 m0, 0x18000\n"
"s_nop 0\n"
"buffer_load_dwordx4 v210, s[28:31], s58 offen lds\n"
".Lk1_rx5_0:\n"
"s_cmp_eq_u32 s44, 3\n"
"s_cbranch_scc0 .Lk1_rx5_1\n"
"s_mov_b32 s58, 0x24280\n"
"s_mov_b32 m0, 0x18400\n"
"s_nop 0\n"
"buffer_load_dwordx4 v211, s[28:31], s58 offen lds\n"
".Lk1_rx5_1:\n"
"s_add_u32 s58, s57, 0x280\n"
"s_add_u32 m0, s48, 0x14000\n"
"s_nop 0\n"
"buffer_load_dwordx4 v210, s[28:31], s58 offen lds\n"
"s_add_u32 m0, s48, 0x14400\n"
"s_nop 0\n"
"buffer_load_dwordx4 v211, s[28:31], s58 offen lds\n"
"s_add_u32 s58, s57, 0x4a80\n"
"s_add_u32 m0, s48, 0x14800\n"
"s_nop 0\n"
"buffer_load_dwordx4 v210, s[28:31], s58 offen lds\n"
"s_add_u32 m0, s48, 0x14c00\n"
"s_nop 0\n"
"buffer_load_dwordx4 v211, s[28:31], s58 offen lds\n"
"s_add_u32 s61, s55, 0x280000\n"
"s_add_u32 m0, s59, 0x19000\n"
"s_nop 0\n"
"buffer_load_dwordx4 v209, s[36:39], s61 offen lds\n"
"s_add_u32 m0, s59, 0x19380\n"
"s_nop 0\n"
"buffer_load_dwordx4 v209, s[36:39], s61 offen offset:128 lds\n"
"s_waitcnt vmcnt(16)\n"
"s_barrier\n"
"s_add_u32 s61, s55, 0x2c0000\n"
"s_add_u32 m0, s59, 0x1b000\n"
"s_nop 0\n"
"buffer_load_dwordx4 v209, s[36:39], s61 offen lds\n"
"s_add_u32 m0, s59, 0x1b380\n"
"s_nop 0\n"
"buffer_load_dwordx4 v209, s[36:39], s61 offen offset:128 lds\n"
"s_waitcnt vmcnt(12)\n"
"s_barrier\n"
"s_cmp_eq_u32 s44, 0\n"
"s_cbranch_scc0 .Lk1_rx6_0\n"
"s_mov_b32 s58, 0x24300\n"
"s_mov_b32 m0, 0x23000\n"
"s_nop 0\n"
"buffer_load_dwordx4 v210, s[28:31], s58 offen lds\n"
".Lk1_rx6_0:\n"
"s_cmp_eq_u32 s44, 1\n"
"s_cbranch_scc0 .Lk1_rx6_1\n"
"s_mov_b32 s58, 0x24300\n"
"s_mov_b32 m0, 0x23400\n"
"s_nop 0\n"
"buffer_load_dwordx4 v211, s[28:31], s58 offen lds\n"
".Lk1_rx6_1:\n"
"s_add_u32 s58, s57, 0x300\n"
"s_add_u32 m0, s48, 0x1f000\n"
"s_nop 0\n"
"buffer_load_dwordx4 v210, s[28:31], s58 offen lds\n"
"s_add_u32 m0, s48, 0x1f400\n"
"s_nop 0\n"
"buffer_load_dwordx4 v211, s[28:31], s58 offen lds\n"
"s_add_u32 s58, s57, 0x4b00\n"
"s_add_u32 m0, s48, 0x1f800\n"
"s_nop 0\n"
"buffer_load_dwordx4 v210, s[28:31], s58 offen lds\n"
"s_add_u32 m0, s48, 0x1fc00\n"
"s_nop 0\n"
"buffer_load_dwordx4 v211, s[28:31], s58 offen lds\n"
"s_add_u32 s61, s55, 0x300000\n"
"s_add_u32 m0, s59, 0x1d000\n"
"s_nop 0\n"
"buffer_load_dwordx4 v209, s[36:39], s61 offen lds\n"
"s_add_u32 m0, s59, 0x1d380\n"
"s_nop 0\n"
"buffer_load_dwordx4 v209, s[36:39], s61 offen offset:128 lds\n"
"s_add_u32 s53, s54, 0\n"
"s_and_b32 s53, s53, 0x3ffff\n"
"s_add_u32 m0, s48, 0xc000\n"
"s_nop 0\n"
"buffer_load_dwordx4 v208, s[24:27], s53 offen nt lds\n"
"buffer_load_dwordx4 v208, s[24:27], s53 offen offset:2048 nt lds\n"
"s_waitcnt vmcnt(18)\n"
"s_barrier\n"
"s_add_u32 s61, s55, 0x340000\n"
"s_add_u32 m0, s59, 0x8000\n"
"s_nop 0\n"
"buffer_load_dwordx4 v209, s[36:39], s61 offen lds\n"
"s_add_u32 m0, s59, 0x8380\n"
"s_nop 0\n"
"buffer_load_dwordx4 v209, s[36:39], s61 offen offset:128 lds\n"
"s_waitcnt vmcnt(12)\n"
"s_barrier\n"
"s_cmp_eq_u32 s44, 2\n"
"s_cbranch_scc0 .Lk1_rx7_0\n"
"s_mov_b32 s58, 0x24380\n"
"s_mov_b32 m0, 0x27800\n"
"s_nop 0\n"
"buffer_load_dwordx4 v210, s[28:31], s58 offen lds\n"
".Lk1_rx7_0:\n"
"s_cmp_eq_u32 s44, 3\n"
"s_cbranch_scc0 .Lk1_rx7_1\n"
"s_mov_b32 s58, 0x24380\n"
"s_mov_b32 m0, 0x27c00\n"
"s_nop 0\n"
"buffer_load_dwordx4 v211, s[28:31], s58 offen lds\n"
".Lk1_rx7_1:\n"
"s_add_u32 s58, s57, 0x380\n"
"s_add_u32 m0, s48, 0x23800\n"
"s_nop 0\n"
"buffer_load_dwordx4 v210, s[28:31], s58 offen lds\n"
"s_add_u32 m0, s48, 0x23c00\n"
"s_nop 0\n"
"buffer_load_dwordx4 v211, s[28:31], s58 offen lds\n"
"s_add_u32 s58, s57, 0x4b80\n"
"s_add_u32 m0, s48, 0x24000\n"
"s_nop 0\n"
"buffer_load_dwordx4 v210, s[28:31], s58 offen lds\n"
"s_add_u32 m0, s48, 0x24400\n"
"s_nop 0\n"
"buffer_load_dwordx4 v211, s[28:31], s58 offen lds\n"
"s_add_u32 s61, s55, 0x380000\n"
"s_add_u32 m0, s59, 0xa000\n"
"s_nop 0\n"
"buffer_load_dwordx4 v209, s[36:39], s61 offen lds\n"
"s_add_u32 m0, s59, 0xa380\n"
"s_nop 0\n"
"buffer_load_dwordx4 v209, s[36:39], s61 offen offset:128 lds\n"
"s_waitcnt vmcnt(16)\n"
"s_barrier\n"
"s_add_u32 s61, s55, 0x3c0000\n"
"s_add_u32 m0, s59, 0x19000\n"
"s_nop 0\n"
"buffer_load_dwordx4 v209, s[36:39], s61 offen lds\n"
"s_add_u32 m0, s59, 0x19380\n"
"s_nop 0\n"
"buffer_load_dwordx4 v209, s[36:39], s61 offen offset:128 lds\n"
"s_add_u32 m0, s48, 0xc380\n"
"s_nop 0\n"
"buffer_load_dwordx4 v208, s[24:27], s53 offen offset:128 nt lds\n"
"buffer_load_dwordx4 v208, s[24:27], s53 offen offset:2176 nt lds\n"
"s_waitcnt vmcnt(14)\n"
"s_barrier\n"
"s_add_u32 s61, s55, 0x400000\n"
"s_add_u32 m0, s59, 0x1b000\n"
"s_nop 0\n"
"buffer_load_dwordx4 v209, s[36:39], s61 offen lds\n"
"s_add_u32 m0, s59, 0x1b380\n"
"s_nop 0\n"
"buffer_load_dwordx4 v209, s[36:39], s61 offen offset:128 lds\n"
"s_add_u32 s53, s56, 0x4000\n"
"s_and_b32 s53, s53, 0x3ffff\n"
"s_add_u32 m0, s48, 0x4000\n"
"s_nop 0\n"
"buffer_load_dwordx4 v206, s[20:23], s53 offen nt lds\n"
"buffer_load_dwordx4 v207, s[20:23], s53 offen offset:2048 nt lds\n"
"s_waitcnt vmcnt(14)\n"
"s_barrier\n"
"s_add_u32 s61, s55, 0x440000\n"
"s_add_u32 m0, s59, 0x1d000\n"
"s_nop 0\n"
"buffer_load_dwordx4 v209, s[36:39], s61 offen lds\n"
"s_add_u32 m0, s59, 0x1d380\n"
"s_nop 0\n"
"buffer_load_dwordx4 v209, s[36:39], s61 offen offset:128 lds\n"
"s_add_u32 m0, s48, 0x4380\n"
"s_nop 0\n"
"buffer_load_dwordx4 v206, s[20:23], s53 offen offset:128 nt lds\n"
"buffer_load_dwordx4 v207, s[20:23], s53 offen offset:2176 nt lds\n"
"s_waitcnt vmcnt(12)\n"
"s_barrier\n"
"s_cmp_eq_u32 s44, 0\n"
"s_cbranch_scc0 .Lk1_rx8_0\n"
"s_mov_b32 s58, 0x24400\n"
"s_mov_b32 m0, 0x23000\n"
"s_nop 0\n"
"buffer_load_dwordx4 v210, s[28:31], s58 offen lds\n"
".Lk1_rx8_0:\n"
"s_cmp_eq_u32 s44, 1\n"
"s_cbranch_scc0 .Lk1_rx8_1\n"
"s_mov_b32 s58, 0x24400\n"
"s_mov_b32 m0, 0x23400\n"
"s_nop 0\n"
"buffer_load_dwordx4 v211, s[28:31], s58 offen lds\n"
".Lk1_rx8_1:\n"
"s_add_u32 s58, s57, 0x400\n"
"s_add_u32 m0, s48, 0x1f000\n"
"s_nop 0\n"
"buffer_load_dwordx4 v210, s[28:31], s58 offen lds\n"
"s_add_u32 m0, s48, 0x1f400\n"
"s_nop 0\n"
"buffer_load_dwordx4 v211, s[28:31], s58 offen lds\n"
"s_add_u32 s58, s57, 0x4c00\n"
"s_add_u32 m0, s48, 0x1f800\n"
"s_nop 0\n"
"buffer_load_dwordx4 v210, s[28:31], s58 offen lds\n"
"s_add_u32 m0, s48, 0x1fc00\n"
"s_nop 0\n"
"buffer_load_dwordx4 v211, s[28:31], s58 offen lds\n"
"s_add_u32 s53, s49, 0x4000\n"
"s_and_b32 s53, s53, 0x3ffff\n"
"s_add_u32 m0, s48, 0x10000\n"
"s_nop 0\n"
"buffer_load_dwordx4 v208, s[24:27], s53 offen nt lds\n"
"buffer_load_dwordx4 v208, s[24:27], s53 offen offset:2048 nt lds\n"
"s_add_u32 m0, s48, 0x10380\n"
"s_nop 0\n"
"buffer_load_dwordx4 v208, s[24:27], s53 offen offset:128 nt lds\n"
"buffer_load_dwordx4 v208, s[24:27], s53 offen offset:2176 nt lds\n"
"s_waitcnt vmcnt(18)\n"
"s_barrier\n"
"s_add_u32 s53, s56, 0x8000\n"
"s_and_b32 s53, s53, 0x3ffff\n"
"s_add_u32 m0, s48, 0x8000\n"
"s_nop 0\n"
"buffer_load_dwordx4 v206, s[20:23], s53 offen nt lds\n"
"buffer_load_dwordx4 v207, s[20:23], s53 offen offset:2048 nt lds\n"
"s_add_u32 m0, s48, 0x8380\n"
"s_nop 0\n"
"buffer_load_dwordx4 v206, s[20:23], s53 offen offset:128 nt lds\n"
"buffer_load_dwordx4 v207, s[20:23], s53 offen offset:2176 nt lds\n"
"s_waitcnt vmcnt(8)\n"
"s_barrier\n"
"s_add_u32 s53, s49, 0x8000\n"
"s_and_b32 s53, s53, 0x3ffff\n"
"s_add_u32 m0, s48, 0x14000\n"
"s_nop 0\n"
"buffer_load_dwordx4 v208, s[24:27], s53 offen nt lds\n"
"buffer_load_dwordx4 v208, s[24:27], s53 offen offset:2048 nt lds\n"
"s_add_u32 m0, s48, 0x14380\n"
"s_nop 0\n"
"buffer_load_dwordx4 v208, s[24:27], s53 offen offset:128 nt lds\n"
"buffer_load_dwordx4 v208, s[24:27], s53 offen offset:2176 nt lds\n"
"s_barrier\n"
"s_lshr_b32 s88, s6, 3\n"
"s_lshl_b32 s88, s88, 2\n"
"s_add_u32 s88, s88, s44\n"
"s_and_b32 s60, s6, 3\n"
"s_lshl_b32 s60, s60, 10\n"
"s_add_u32 s60, s60, 0x1000\n"
"s_add_u32 s92, s14, s60\n"
"s_addc_u32 s93, s15, 0\n"
"s_and_b32 s93, s93, 0xffff\n"
"s_mov_b32 s92, s92\n"
"s_mov_b32 s94, 0\n"
"s_mov_b32 s95, 0x20000\n"
"s_sub_u32 s94, 0x480000, s60\n"
"s_lshl_b32 s88, s88, 13\n"
"v_lshlrev_b32 v247, 4, v238\n"
"s_add_u32 s60, s88, 0\n"
"buffer_load_dwordx4 v[240:243], v247, s[92:95], s60 offen\n"
"s_add_u32 s60, s88, 0x100000\n"
"buffer_load_dwordx4 v[240:243], v247, s[92:95], s60 offen\n"
"s_add_u32 s60, s88, 0x200000\n"
"buffer_load_dwordx4 v[240:243], v247, s[92:95], s60 offen\n"
"s_add_u32 s60, s88, 0x300000\n"
"buffer_load_dwordx4 v[240:243], v247, s[92:95], s60 offen\n"
"s_add_u32 s60, s88, 0x400000\n"
"s_cmp_lt_u32 s60, 0x480000\n"
"s_cselect_b32 s60, s60, 0\n"
"buffer_load_dwordx4 v[240:243], v247, s[92:95], s60 offen\n"
"s_barrier\n"
"s_cmp_eq_u32 s44, 3\n"
"s_cbranch_scc1 .Lk1_gq\n"
"s_setprio 1\n"
"v_mov_b32 v0, 0\n"
"v_mov_b32 v1, 0\n"
"v_mov_b32 v2, 0\n"
"v_mov_b32 v3, 0\n"
"v_mov_b32 v4, 0\n"
"v_mov_b32 v5, 0\n"
"v_mov_b32 v6, 0\n"
"v_mov_b32 v7, 0\n"
"v_mov_b32 v8, 0\n"
"v_mov_b32 v9, 0\n"
"v_mov_b32 v10, 0\n"
"v_mov_b32 v11, 0\n"
"v_mov_b32 v12, 0\n"
"v_mov_b32 v13, 0\n"
"v_mov_b32 v14, 0\n"
"v_mov_b32 v15, 0\n"
"v_mov_b32 v16, 0\n"
"v_mov_b32 v17, 0\n"
"v_mov_b32 v18, 0\n"
"v_mov_b32 v19, 0\n"
"v_mov_b32 v20, 0\n"
"v_mov_b32 v21, 0\n"
"v_mov_b32 v22, 0\n"
"v_mov_b32 v23, 0\n"
"v_mov_b32 v24, 0\n"
"v_mov_b32 v25, 0\n"
"v_mov_b32 v26, 0\n"
"v_mov_b32 v27, 0\n"
"v_mov_b32 v28, 0\n"
"v_mov_b32 v29, 0\n"
"v_mov_b32 v30, 0\n"
"v_mov_b32 v31, 0\n"
"v_mov_b32 v32, 0\n"
"v_mov_b32 v33, 0\n"
"v_mov_b32 v34, 0\n"
"v_mov_b32 v35, 0\n"
"v_mov_b32 v36, 0\n"
"v_mov_b32 v37, 0\n"
"v_mov_b32 v38, 0\n"
"v_mov_b32 v39, 0\n"
"v_mov_b32 v40, 0\n"
"v_mov_b32 v41, 0\n"
"v_mov_b32 v42, 0\n"
"v_mov_b32 v43, 0\n"
"v_mov_b32 v44, 0\n"
"v_mov_b32 v45, 0\n"
"v_mov_b32 v46, 0\n"
"v_mov_b32 v47, 0\n"
"v_mov_b32 v48, 0\n"
"v_mov_b32 v49, 0\n"
"v_mov_b32 v50, 0\n"
"v_mov_b32 v51, 0\n"
"v_mov_b32 v52, 0\n"
"v_mov_b32 v53, 0\n"
"v_mov_b32 v54, 0\n"
"v_mov_b32 v55, 0\n"
"v_mov_b32 v56, 0\n"
"v_mov_b32 v57, 0\n"
"v_mov_b32 v58, 0\n"
"v_mov_b32 v59, 0\n"
"v_mov_b32 v60, 0\n"
"v_mov_b32 v61, 0\n"
"v_mov_b32 v62, 0\n"
"v_mov_b32 v63, 0\n"
"v_mov_b32 v64, 0\n"
"v_mov_b32 v65, 0\n"
"v_mov_b32 v66, 0\n"
"v_mov_b32 v67, 0\n"
"v_mov_b32 v68, 0\n"
"v_mov_b32 v69, 0\n"
"v_mov_b32 v70, 0\n"
"v_mov_b32 v71, 0\n"
"v_mov_b32 v72, 0\n"
"v_mov_b32 v73, 0\n"
"v_mov_b32 v74, 0\n"
"v_mov_b32 v75, 0\n"
"v_mov_b32 v76, 0\n"
"v_mov_b32 v77, 0\n"
"v_mov_b32 v78, 0\n"
"v_mov_b32 v79, 0\n"
"v_mov_b32 v80, 0\n"
"v_mov_b32 v81, 0\n"
"v_mov_b32 v82, 0\n"
"v_mov_b32 v83, 0\n"
"v_mov_b32 v84, 0\n"
"v_mov_b32 v85, 0\n"
"v_mov_b32 v86, 0\n"
"v_mov_b32 v87, 0\n"
"v_mov_b32 v88, 0\n"
"v_mov_b32 v89, 0\n"
"v_mov_b32 v90, 0\n"
"v_mov_b32 v91, 0\n"
"v_mov_b32 v92, 0\n"
"v_mov_b32 v93, 0\n"
"v_mov_b32 v94, 0\n"
"v_mov_b32 v95, 0\n"
"v_mov_b32 v244, 0\n"
"v_mov_b32 v245, 0\n"
"v_mov_b32 v246, 0\n"
"s_cmp_eq_u32 s44, 1\n"
"s_cbranch_scc1 .Lk1_ld1\n"
"s_cmp_eq_u32 s44, 2\n"
"s_cbranch_scc1 .Lk1_ld2\n"
".Lk1_ld0:\n"
"s_mov_b32 s61, 0x21000\n"
"s_mov_b32 m0, 0x19800\n"
"s_nop 0\n"
"buffer_load_dwordx4 v209, s[36:39], s61 offen lds\n"
"s_mov_b32 m0, 0x19b80\n"
"s_nop 0\n"
"buffer_load_dwordx4 v209, s[36:39], s61 offen offset:128 lds\n"
"s_mov_b32 s61, 0x41000\n"
"s_mov_b32 m0, 0x1b000\n"
"s_nop 0\n"
"buffer_load_dwordx4 v209, s[36:39], s61 offen lds\n"
"s_mov_b32 m0, 0x1b380\n"
"s_nop 0\n"
"buffer_load_dwordx4 v209, s[36:39], s61 offen offset:128 lds\n"
"s_mov_b32 s61, 0x69000\n"
"s_mov_b32 m0, 0x1c800\n"
"s_nop 0\n"
"buffer_load_dwordx4 v209, s[36:39], s61 offen lds\n"
"s_mov_b32 m0, 0x1cb80\n"
"s_nop 0\n"
"buffer_load_dwordx4 v209, s[36:39], s61 offen offset:128 lds\n"
"s_mov_b32 s58, 0x9000\n"
"s_mov_b32 m0, 0x22000\n"
"s_nop 0\n"
"buffer_load_dwordx4 v210, s[28:31], s58 offen lds\n"
"s_mov_b32 m0, 0x22400\n"
"s_nop 0\n"
"buffer_load_dwordx4 v211, s[28:31], s58 offen lds\n"
"s_mov_b32 s58, 0x16800\n"
"s_mov_b32 m0, 0x23800\n"
"s_nop 0\n"
"buffer_load_dwordx4 v210, s[28:31], s58 offen lds\n"
"s_mov_b32 m0, 0x23c00\n"
"s_nop 0\n"
"buffer_load_dwordx4 v211, s[28:31], s58 offen lds\n"
"s_waitcnt vmcnt(0)\n"
"s_waitcnt lgkmcnt(0)\n"
"s_barrier\n"
"s_mov_b32 s58, 0x1f800\n"
"s_mov_b32 m0, 0x24800\n"
"s_nop 0\n"
"buffer_load_dwordx4 v210, s[28:31], s58 offen lds\n"
"s_mov_b32 m0, 0x24c00\n"
"s_nop 0\n"
"buffer_load_dwordx4 v211, s[28:31], s58 offen lds\n"
"s_mov_b32 s61, 0xa1000\n"
"s_mov_b32 m0, 0x1d800\n"
"s_nop 0\n"
"buffer_load_dwordx4 v209, s[36:39], s61 offen lds\n"
"s_mov_b32 m0, 0x1db80\n"
"s_nop 0\n"
"buffer_load_dwordx4 v209, s[36:39], s61 offen offset:128 lds\n"
"s_mov_b32 s61, 0xc1000\n"
"s_mov_b32 m0, 0x1f000\n"
"s_nop 0\n"
"buffer_load_dwordx4 v209, s[36:39], s61 offen lds\n"
"s_mov_b32 m0, 0x1f380\n"
"s_nop 0\n"
"buffer_load_dwordx4 v209, s[36:39], s61 offen offset:128 lds\n"
"s_mov_b32 s61, 0xe9000\n"
"s_mov_b32 m0, 0x20800\n"
"s_nop 0\n"
"buffer_load_dwordx4 v209, s[36:39], s61 offen lds\n"
"s_mov_b32 m0, 0x20b80\n"
"s_nop 0\n"
"buffer_load_dwordx4 v209, s[36:39], s61 offen offset:128 lds\n"
"s_mov_b32 s58, 0x9080\n"
"s_mov_b32 m0, 0x26800\n"
"s_nop 0\n"
"buffer_load_dwordx4 v210, s[28:31], s58 offen lds\n"
"s_mov_b32 m0, 0x26c00\n"
"s_nop 0\n"
"buffer_load_dwordx4 v211, s[28:31], s58 offen lds\n"
"s_waitcnt lgkmcnt(0)\n"
".Lk1_pc1a:\n"
"s_memrealtime s[72:73]\n"
"s_waitcnt lgkmcnt(0)\n"
"s_sub_u32 s73, s72, s65\n"
"s_cmp_lt_u32 s73, 128\n"
"s_cbranch_scc0 .Lk1_pc1b\n"
"s_sleep 1\n"
"s_branch .Lk1_pc1a\n"
".Lk1_pc1b:\n"
"s_mov_b32 s65, s72\n"
"s_add_u32 s53, s33, 0xa000\n"
"s_and_b32 s53, s53, 0x3ffff\n"
"s_mov_b32 m0, 0xc000\n"
"s_nop 0\n"
"buffer_load_dwordx4 v208, s[24:27], s53 offen nt lds\n"
"buffer_load_dwordx4 v208, s[24:27], s53 offen offset:2048 nt lds\n"
"s_mov_b32 m0, 0xc380\n"
"s_nop 0\n"
"buffer_load_dwordx4 v208, s[24:27], s53 offen offset:128 nt lds\n"
"buffer_load_dwordx4 v208, s[24:27], s53 offen offset:2176 nt lds\n"
"s_add_u32 s53, s33, 0xd000\n"
"s_and_b32 s53, s53, 0x3ffff\n"
"s_mov_b32 m0, 0xf000\n"
"s_nop 0\n"
"buffer_load_dwordx4 v208, s[24:27], s53 offen nt lds\n"
"buffer_load_dwordx4 v208, s[24:27], s53 offen offset:2048 nt lds\n"
"s_mov_b32 m0, 0xf380\n"
"s_nop 0\n"
"buffer_load_dwordx4 v208, s[24:27], s53 offen offset:128 nt lds\n"
"buffer_load_dwordx4 v208, s[24:27], s53 offen offset:2176 nt lds\n"
"s_add_u32 s53, s33, 0xe000\n"
"s_and_b32 s53, s53, 0x3ffff\n"
"s_mov_b32 m0, 0x2000\n"
"s_nop 0\n"
"buffer_load_dwordx4 v206, s[20:23], s53 offen nt lds\n"
"buffer_load_dwordx4 v207, s[20:23], s53 offen offset:2048 nt lds\n"
"s_mov_b32 m0, 0x2380\n"
"s_nop 0\n"
"buffer_load_dwordx4 v206, s[20:23], s53 offen offset:128 nt lds\n"
"buffer_load_dwordx4 v207, s[20:23], s53 offen offset:2176 nt lds\n"
"s_waitcnt vmcnt(12)\n"
"s_waitcnt lgkmcnt(0)\n"
"s_barrier\n"
"s_mov_b32 s58, 0x16880\n"
"s_mov_b32 m0, 0x21000\n"
"s_nop 0\n"
"buffer_load_dwordx4 v210, s[28:31], s58 offen lds\n"
"s_mov_b32 m0, 0x21400\n"
"s_nop 0\n"
"buffer_load_dwordx4 v211, s[28:31], s58 offen lds\n"
"s_mov_b32 s58, 0x24080\n"
"s_mov_b32 m0, 0x22800\n"
"s_nop 0\n"
"buffer_load_dwordx4 v210, s[28:31], s58 offen lds\n"
"s_mov_b32 m0, 0x22c00\n"
"s_nop 0\n"
"buffer_load_dwordx4 v211, s[28:31], s58 offen lds\n"
"s_mov_b32 s61, 0x109000\n"
"s_mov_b32 m0, 0x1a000\n"
"s_nop 0\n"
"buffer_load_dwordx4 v209, s[36:39], s61 offen lds\n"
"s_mov_b32 m0, 0x1a380\n"
"s_nop 0\n"
"buffer_load_dwordx4 v209, s[36:39], s61 offen offset:128 lds\n"
"s_mov_b32 s61, 0x161000\n"
"s_mov_b32 m0, 0x1b800\n"
"s_nop 0\n"
"buffer_load_dwordx4 v209, s[36:39], s61 offen lds\n"
"s_mov_b32 m0, 0x1bb80\n"
"s_nop 0\n"
"buffer_load_dwordx4 v209, s[36:39], s61 offen offset:128 lds\n"
"s_mov_b32 s58, 0x100\n"
"s_mov_b32 m0, 0x23000\n"
"s_nop 0\n"
"buffer_load_dwordx4 v210, s[28:31], s58 offen lds\n"
"s_mov_b32 m0, 0x23400\n"
"s_nop 0\n"
"buffer_load_dwordx4 v211, s[28:31], s58 offen lds\n"
"s_waitcnt lgkmcnt(0)\n"
".Lk1_pc2a:\n"
"s_memrealtime s[72:73]\n"
"s_waitcnt lgkmcnt(0)\n"
"s_sub_u32 s73, s72, s65\n"
"s_cmp_lt_u32 s73, 128\n"
"s_cbranch_scc0 .Lk1_pc2b\n"
"s_sleep 1\n"
"s_branch .Lk1_pc2a\n"
".Lk1_pc2b:\n"
"s_mov_b32 s65, s72\n"
"s_add_u32 s53, s33, 0xe000\n"
"s_and_b32 s53, s53, 0x3ffff\n"
"s_mov_b32 m0, 0x10000\n"
"s_nop 0\n"
"buffer_load_dwordx4 v208, s[24:27], s53 offen nt lds\n"
"buffer_load_dwordx4 v208, s[24:27], s53 offen offset:2048 nt lds\n"
"s_mov_b32 m0, 0x10380\n"
"s_nop 0\n"
"buffer_load_dwordx4 v208, s[24:27], s53 offen offset:128 nt lds\n"
"buffer_load_dwordx4 v208, s[24:27], s53 offen offset:2176 nt lds\n"
"s_add_u32 s53, s33, 0x11000\n"
"s_and_b32 s53, s53, 0x3ffff\n"
"s_mov_b32 m0, 0x13000\n"
"s_nop 0\n"
"buffer_load_dwordx4 v208, s[24:27], s53 offen nt lds\n"
"buffer_load_dwordx4 v208, s[24:27], s53 offen offset:2048 nt lds\n"
"s_mov_b32 m0, 0x13380\n"
"s_nop 0\n"
"buffer_load_dwordx4 v208, s[24:27], s53 offen offset:128 nt lds\n"
"buffer_load_dwordx4 v208, s[24:27], s53 offen offset:2176 nt lds\n"
"s_add_u32 s53, s33, 0x12000\n"
"s_and_b32 s53, s53, 0x3ffff\n"
"s_mov_b32 m0, 0x6000\n"
"s_nop 0\n"
"buffer_load_dwordx4 v206, s[20:23], s53 offen nt lds\n"
"buffer_load_dwordx4 v207, s[20:23], s53 offen offset:2048 nt lds\n"
"s_mov_b32 m0, 0x6380\n"
"s_nop 0\n"
"buffer_load_dwordx4 v206, s[20:23], s53 offen offset:128 nt lds\n"
"buffer_load_dwordx4 v207, s[20:23], s53 offen offset:2176 nt lds\n"
"s_waitcnt vmcnt(12)\n"
"s_waitcnt lgkmcnt(0)\n"
"s_barrier\n"
"s_mov_b32 s58, 0xd900\n"
"s_mov_b32 m0, 0x24800\n"
"s_nop 0\n"
"buffer_load_dwordx4 v210, s[28:31], s58 offen lds\n"
"s_mov_b32 m0, 0x24c00\n"
"s_nop 0\n"
"buffer_load_dwordx4 v211, s[28:31], s58 offen lds\n"
"s_mov_b32 s58, 0x1b100\n"
"s_mov_b32 m0, 0x26000\n"
"s_nop 0\n"
"buffer_load_dwordx4 v210, s[28:31], s58 offen lds\n"
"s_mov_b32 m0, 0x26400\n"
"s_nop 0\n"
"buffer_load_dwordx4 v211, s[28:31], s58 offen lds\n"
"s_waitcnt lgkmcnt(0)\n"
".Lk1_pc3a:\n"
"s_memrealtime s[72:73]\n"
"s_waitcnt lgkmcnt(0)\n"
"s_sub_u32 s73, s72, s65\n"
"s_cmp_lt_u32 s73, 128\n"
"s_cbranch_scc0 .Lk1_pc3b\n"
"s_sleep 1\n"
"s_branch .Lk1_pc3a\n"
".Lk1_pc3b:\n"
"s_mov_b32 s65, s72\n"
"s_add_u32 s53, s33, 0x12000\n"
"s_and_b32 s53, s53, 0x3ffff\n"
"s_mov_b32 m0, 0x14000\n"
"s_nop 0\n"
"buffer_load_dwordx4 v208, s[24:27], s53 offen nt lds\n"
"buffer_load_dwordx4 v208, s[24:27], s53 offen offset:2048 nt lds\n"
"s_mov_b32 m0, 0x14380\n"
"s_nop 0\n"
"buffer_load_dwordx4 v208, s[24:27], s53 offen offset:128 nt lds\n"
"buffer_load_dwordx4 v208, s[24:27], s53 offen offset:2176 nt lds\n"
"s_add_u32 s53, s33, 0x15000\n"
"s_and_b32 s53, s53, 0x3ffff\n"
"s_mov_b32 m0, 0x17000\n"
"s_nop 0\n"
"buffer_load_dwordx4 v208, s[24:27], s53 offen nt lds\n"
"buffer_load_dwordx4 v208, s[24:27], s53 offen offset:2048 nt lds\n"
"s_mov_b32 m0, 0x17380\n"
"s_nop 0\n"
"buffer_load_dwordx4 v208, s[24:27], s53 offen offset:128 nt lds\n"
"buffer_load_dwordx4 v208, s[24:27], s53 offen offset:2176 nt lds\n"
"s_add_u32 s53, s33, 0x16000\n"
"s_and_b32 s53, s53, 0x3ffff\n"
"s_mov_b32 m0, 0xa000\n"
"s_nop 0\n"
"buffer_load_dwordx4 v206, s[20:23], s53 offen nt lds\n"
"buffer_load_dwordx4 v207, s[20:23], s53 offen offset:2048 nt lds\n"
"s_mov_b32 m0, 0xa380\n"
"s_nop 0\n"
"buffer_load_dwordx4 v206, s[20:23], s53 offen offset:128 nt lds\n"
"buffer_load_dwordx4 v207, s[20:23], s53 offen offset:2176 nt lds\n"
"s_waitcnt vmcnt(12)\n"
"s_waitcnt lgkmcnt(0)\n"
"s_barrier\n"
"s_mov_b32 s61, 0x1a1000\n"
"s_mov_b32 m0, 0x1d800\n"
"s_nop 0\n"
"buffer_load_dwordx4 v209, s[36:39], s61 offen lds\n"
"s_mov_b32 m0, 0x1db80\n"
"s_nop 0\n"
"buffer_load_dwordx4 v209, s[36:39], s61 offen offset:128 lds\n"
"s_mov_b32 s61, 0x1c1000\n"
"s_mov_b32 m0, 0x1f000\n"
"s_nop 0\n"
"buffer_load_dwordx4 v209, s[36:39], s61 offen lds\n"
"s_mov_b32 m0, 0x1f380\n"
"s_nop 0\n"
"buffer_load_dwordx4 v209, s[36:39], s61 offen offset:128 lds\n"
"s_mov_b32 s61, 0x1e9000\n"
"s_mov_b32 m0, 0x20800\n"
"s_nop 0\n"
"buffer_load_dwordx4 v209, s[36:39], s61 offen lds\n"
"s_mov_b32 m0, 0x20b80\n"
"s_nop 0\n"
"buffer_load_dwordx4 v209, s[36:39], s61 offen offset:128 lds\n"
"s_mov_b32 s58, 0x9180\n"
"s_mov_b32 m0, 0x21800\n"
"s_nop 0\n"
"buffer_load_dwordx4 v210, s[28:31], s58 offen lds\n"
"s_mov_b32 m0, 0x21c00\n"
"s_nop 0\n"
"buffer_load_dwordx4 v211, s[28:31], s58 offen lds\n"
"s_mov_b32 s58, 0x16980\n"
"s_mov_b32 m0, 0x23000\n"
"s_nop 0\n"
"buffer_load_dwordx4 v210, s[28:31], s58 offen lds\n"
"s_mov_b32 m0, 0x23400\n"
"s_nop 0\n"
"buffer_load_dwordx4 v211, s[28:31], s58 offen lds\n"
"s_waitcnt lgkmcnt(0)\n"
".Lk1_pc4a:\n"
"s_memrealtime s[72:73]\n"
"s_waitcnt lgkmcnt(0)\n"
"s_sub_u32 s73, s72, s65\n"
"s_cmp_lt_u32 s73, 128\n"
"s_cbranch_scc0 .Lk1_pc4b\n"
"s_sleep 1\n"
"s_branch .Lk1_pc4a\n"
".Lk1_pc4b:\n"
"s_mov_b32 s65, s72\n"
"s_add_u32 s53, s33, 0x16000\n"
"s_and_b32 s53, s53, 0x3ffff\n"
"s_mov_b32 m0, 0xc000\n"
"s_nop 0\n"
"buffer_load_dwordx4 v208, s[24:27], s53 offen nt lds\n"
"buffer_load_dwordx4 v208, s[24:27], s53 offen offset:2048 nt lds\n"
"s_mov_b32 m0, 0xc380\n"
"s_nop 0\n"
"buffer_load_dwordx4 v208, s[24:27], s53 offen offset:128 nt lds\n"
"buffer_load_dwordx4 v208, s[24:27], s53 offen offset:2176 nt lds\n"
"s_add_u32 s53, s33, 0x19000\n"
"s_and_b32 s53, s53, 0x3ffff\n"
"s_mov_b32 m0, 0xf000\n"
"s_nop 0\n"
"buffer_load_dwordx4 v208, s[24:27], s53 offen nt lds\n"
"buffer_load_dwordx4 v208, s[24:27], s53 offen offset:2048 nt lds\n"
"s_mov_b32 m0, 0xf380\n"
"s_nop 0\n"
"buffer_load_dwordx4 v208, s[24:27], s53 offen offset:128 nt lds\n"
"buffer_load_dwordx4 v208, s[24:27], s53 offen offset:2176 nt lds\n"
"s_add_u32 s53, s33, 0x1a000\n"
"s_and_b32 s53, s53, 0x3ffff\n"
"s_mov_b32 m0, 0x2000\n"
"s_nop 0\n"
"buffer_load_dwordx4 v206, s[20:23], s53 offen nt lds\n"
"buffer_load_dwordx4 v207, s[20:23], s53 offen offset:2048 nt lds\n"
"s_mov_b32 m0, 0x2380\n"
"s_nop 0\n"
"buffer_load_dwordx4 v206, s[20:23], s53 offen offset:128 nt lds\n"
"buffer_load_dwordx4 v207, s[20:23], s53 offen offset:2176 nt lds\n"
"s_waitcnt vmcnt(12)\n"
"s_waitcnt lgkmcnt(0)\n"
"s_barrier\n"
"s_mov_b32 s58, 0x1f980\n"
"s_mov_b32 m0, 0x24000\n"
"s_nop 0\n"
"buffer_load_dwordx4 v210, s[28:31], s58 offen lds\n"
"s_mov_b32 m0, 0x24400\n"
"s_nop 0\n"
"buffer_load_dwordx4 v211, s[28:31], s58 offen lds\n"
"s_mov_b32 s61, 0x221000\n"
"s_mov_b32 m0, 0x19800\n"
"s_nop 0\n"
"buffer_load_dwordx4 v209, s[36:39], s61 offen lds\n"
"s_mov_b32 m0, 0x19b80\n"
"s_nop 0\n"
"buffer_load_dwordx4 v209, s[36:39], s61 offen offset:128 lds\n"
"s_mov_b32 s61, 0x241000\n"
"s_mov_b32 m0, 0x1b000\n"
"s_nop 0\n"
"buffer_load_dwordx4 v209, s[36:39], s61 offen lds\n"
"s_mov_b32 m0, 0x1b380\n"
"s_nop 0\n"
"buffer_load_dwordx4 v209, s[36:39], s61 offen offset:128 lds\n"
"s_mov_b32 s61, 0x269000\n"
"s_mov_b32 m0, 0x1c800\n"
"s_nop 0\n"
"buffer_load_dwordx4 v209, s[36:39], s61 offen lds\n"
"s_mov_b32 m0, 0x1cb80\n"
"s_nop 0\n"
"buffer_load_dwordx4 v209, s[36:39], s61 offen offset:128 lds\n"
"s_mov_b32 s58, 0x9200\n"
"s_mov_b32 m0, 0x26000\n"
"s_nop 0\n"
"buffer_load_dwordx4 v210, s[28:31], s58 offen lds\n"
"s_mov_b32 m0, 0x26400\n"
"s_nop 0\n"
"buffer_load_dwordx4 v211, s[28:31], s58 offen lds\n"
"s_waitcnt lgkmcnt(0)\n"
".Lk1_pc5a:\n"
"s_memrealtime s[72:73]\n"
"s_waitcnt lgkmcnt(0)\n"
"s_sub_u32 s73, s72, s65\n"
"s_cmp_lt_u32 s73, 128\n"
"s_cbranch_scc0 .Lk1_pc5b\n"
"s_sleep 1\n"
"s_branch .Lk1_pc5a\n"
".Lk1_pc5b:\n"
"s_mov_b32 s65, s72\n"
"s_add_u32 s53, s33, 0x1a000\n"
"s_and_b32 s53, s53, 0x3ffff\n"
"s_mov_b32 m0, 0x10000\n"
"s_nop 0\n"
"buffer_load_dwordx4 v208, s[24:27], s53 offen nt lds\n"
"buffer_load_dwordx4 v208, s[24:27], s53 offen offset:2048 nt lds\n"
"s_mov_b32 m0, 0x10380\n"
"s_nop 0\n"
"buffer_load_dwordx4 v208, s[24:27], s53 offen offset:128 nt lds\n"
"buffer_load_dwordx4 v208, s[24:27], s53 offen offset:2176 nt lds\n"
"s_add_u32 s53, s33, 0x1d000\n"
"s_and_b32 s53, s53, 0x3ffff\n"
"s_mov_b32 m0, 0x13000\n"
"s_nop 0\n"
"buffer_load_dwordx4 v208, s[24:27], s53 offen nt lds\n"
"buffer_load_dwordx4 v208, s[24:27], s53 offen offset:2048 nt lds\n"
"s_mov_b32 m0, 0x13380\n"
"s_nop 0\n"
"buffer_load_dwordx4 v208, s[24:27], s53 offen offset:128 nt lds\n"
"buffer_load_dwordx4 v208, s[24:27], s53 offen offset:2176 nt lds\n"
"s_add_u32 s53, s33, 0x1e000\n"
"s_and_b32 s53, s53, 0x3ffff\n"
"s_mov_b32 m0, 0x6000\n"
"s_nop 0\n"
"buffer_load_dwordx4 v206, s[20:23], s53 offen nt lds\n"
"buffer_load_dwordx4 v207, s[20:23], s53 offen offset:2048 nt lds\n"
"s_mov_b32 m0, 0x6380\n"
"s_nop 0\n"
"buffer_load_dwordx4 v206, s[20:23], s53 offen offset:128 nt lds\n"
"buffer_load_dwordx4 v207, s[20:23], s53 offen offset:2176 nt lds\n"
"s_waitcnt vmcnt(12)\n"
"s_waitcnt lgkmcnt(0)\n"
"s_barrier\n"
"s_mov_b32 s58, 0x16a00\n"
"s_mov_b32 m0, 0x27800\n"
"s_nop 0\n"
"buffer_load_dwordx4 v210, s[28:31], s58 offen lds\n"
"s_mov_b32 m0, 0x27c00\n"
"s_nop 0\n"
"buffer_load_dwordx4 v211, s[28:31], s58 offen lds\n"
"s_mov_b32 s58, 0x24200\n"
"s_mov_b32 m0, 0x22000\n"
"s_nop 0\n"
"buffer_load_dwordx4 v210, s[28:31], s58 offen lds\n"
"s_mov_b32 m0, 0x22400\n"
"s_nop 0\n"
"buffer_load_dwordx4 v211, s[28:31], s58 offen lds\n"
"s_mov_b32 s61, 0x289000\n"
"s_mov_b32 m0, 0x1e000\n"
"s_nop 0\n"
"buffer_load_dwordx4 v209, s[36:39], s61 offen lds\n"
"s_mov_b32 m0, 0x1e380\n"
"s_nop 0\n"
"buffer_load_dwordx4 v209, s[36:39], s61 offen offset:128 lds\n"
"s_mov_b32 s61, 0x2e1000\n"
"s_mov_b32 m0, 0x1f800\n"
"s_nop 0\n"
"buffer_load_dwordx4 v209, s[36:39], s61 offen lds\n"
"s_mov_b32 m0, 0x1fb80\n"
"s_nop 0\n"
"buffer_load_dwordx4 v209, s[36:39], s61 offen offset:128 lds\n"
"s_mov_b32 s58, 0x280\n"
"s_mov_b32 m0, 0x22800\n"
"s_nop 0\n"
"buffer_load_dwordx4 v210, s[28:31], s58 offen lds\n"
"s_mov_b32 m0, 0x22c00\n"
"s_nop 0\n"
"buffer_load_dwordx4 v211, s[28:31], s58 offen lds\n"
"s_waitcnt lgkmcnt(0)\n"
".Lk1_pc6a:\n"
"s_memrealtime s[72:73]\n"
"s_waitcnt lgkmcnt(0)\n"
"s_sub_u32 s73, s72, s65\n"
"s_cmp_lt_u32 s73, 128\n"
"s_cbranch_scc0 .Lk1_pc6b\n"
"s_sleep 1\n"
"s_branch .Lk1_pc6a\n"
".Lk1_pc6b:\n"
"s_mov_b32 s65, s72\n"
"s_add_u32 s53, s33, 0x1e000\n"
"s_and_b32 s53, s53, 0x3ffff\n"
"s_mov_b32 m0, 0x14000\n"
"s_nop 0\n"
"buffer_load_dwordx4 v208, s[24:27], s53 offen nt lds\n"
"buffer_load_dwordx4 v208, s[24:27], s53 offen offset:2048 nt lds\n"
"s_mov_b32 m0, 0x14380\n"
"s_nop 0\n"
"buffer_load_dwordx4 v208, s[24:27], s53 offen offset:128 nt lds\n"
"buffer_load_dwordx4 v208, s[24:27], s53 offen offset:2176 nt lds\n"
"s_add_u32 s53, s33, 0x21000\n"
"s_and_b32 s53, s53, 0x3ffff\n"
"s_mov_b32 m0, 0x17000\n"
"s_nop 0\n"
"buffer_load_dwordx4 v208, s[24:27], s53 offen nt lds\n"
"buffer_load_dwordx4 v208, s[24:27], s53 offen offset:2048 nt lds\n"
"s_mov_b32 m0, 0x17380\n"
"s_nop 0\n"
"buffer_load_dwordx4 v208, s[24:27], s53 offen offset:128 nt lds\n"
"buffer_load_dwordx4 v208, s[24:27], s53 offen offset:2176 nt lds\n"
"s_add_u32 s53, s33, 0x22000\n"
"s_and_b32 s53, s53, 0x3ffff\n"
"s_mov_b32 m0, 0xa000\n"
"s_nop 0\n"
"buffer_load_dwordx4 v206, s[20:23], s53 offen nt lds\n"
"buffer_load_dwordx4 v207, s[20:23], s53 offen offset:2048 nt lds\n"
"s_mov_b32 m0, 0xa380\n"
"s_nop 0\n"
"buffer_load_dwordx4 v206, s[20:23], s53 offen offset:128 nt lds\n"
"buffer_load_dwordx4 v207, s[20:23], s53 offen offset:2176 nt lds\n"
"s_waitcnt vmcnt(12)\n"
"s_waitcnt lgkmcnt(0)\n"
"s_barrier\n"
"s_mov_b32 s58, 0xda80\n"
"s_mov_b32 m0, 0x24000\n"
"s_nop 0\n"
"buffer_load_dwordx4 v210, s[28:31], s58 offen lds\n"
"s_mov_b32 m0, 0x24400\n"
"s_nop 0\n"
"buffer_load_dwordx4 v211, s[28:31], s58 offen lds\n"
"s_mov_b32 s58, 0x1b280\n"
"s_mov_b32 m0, 0x25800\n"
"s_nop 0\n"
"buffer_load_dwordx4 v210, s[28:31], s58 offen lds\n"
"s_mov_b32 m0, 0x25c00\n"
"s_nop 0\n"
"buffer_load_dwordx4 v211, s[28:31], s58 offen lds\n"
"s_waitcnt lgkmcnt(0)\n"
".Lk1_pc7a:\n"
"s_memrealtime s[72:73]\n"
"s_waitcnt lgkmcnt(0)\n"
"s_sub_u32 s73, s72, s65\n"
"s_cmp_lt_u32 s73, 128\n"
"s_cbranch_scc0 .Lk1_pc7b\n"
"s_sleep 1\n"
"s_branch .Lk1_pc7a\n"
".Lk1_pc7b:\n"
"s_mov_b32 s65, s72\n"
"s_add_u32 s53, s33, 0x22000\n"
"s_and_b32 s53, s53, 0x3ffff\n"
"s_mov_b32 m0, 0xc000\n"
"s_nop 0\n"
"buffer_load_dwordx4 v208, s[24:27], s53 offen nt lds\n"
"buffer_load_dwordx4 v208, s[24:27], s53 offen offset:2048 nt lds\n"
"s_mov_b32 m0, 0xc380\n"
"s_nop 0\n"
"buffer_load_dwordx4 v208, s[24:27], s53 offen offset:128 nt lds\n"
"buffer_load_dwordx4 v208, s[24:27], s53 offen offset:2176 nt lds\n"
"s_add_u32 s53, s33, 0x25000\n"
"s_and_b32 s53, s53, 0x3ffff\n"
"s_mov_b32 m0, 0xf000\n"
"s_nop 0\n"
"buffer_load_dwordx4 v208, s[24:27], s53 offen nt lds\n"
"buffer_load_dwordx4 v208, s[24:27], s53 offen offset:2048 nt lds\n"
"s_mov_b32 m0, 0xf380\n"
"s_nop 0\n"
"buffer_load_dwordx4 v208, s[24:27], s53 offen offset:128 nt lds\n"
"buffer_load_dwordx4 v208, s[24:27], s53 offen offset:2176 nt lds\n"
"s_add_u32 s53, s33, 0x26000\n"
"s_and_b32 s53, s53, 0x3ffff\n"
"s_mov_b32 m0, 0x2000\n"
"s_nop 0\n"
"buffer_load_dwordx4 v206, s[20:23], s53 offen nt lds\n"
"buffer_load_dwordx4 v207, s[20:23], s53 offen offset:2048 nt lds\n"
"s_mov_b32 m0, 0x2380\n"
"s_nop 0\n"
"buffer_load_dwordx4 v206, s[20:23], s53 offen offset:128 nt lds\n"
"buffer_load_dwordx4 v207, s[20:23], s53 offen offset:2176 nt lds\n"
"s_waitcnt vmcnt(12)\n"
"s_waitcnt lgkmcnt(0)\n"
"s_barrier\n"
"s_mov_b32 s61, 0x321000\n"
"s_mov_b32 m0, 0x19800\n"
"s_nop 0\n"
"buffer_load_dwordx4 v209, s[36:39], s61 offen lds\n"
"s_mov_b32 m0, 0x19b80\n"
"s_nop 0\n"
"buffer_load_dwordx4 v209, s[36:39], s61 offen offset:128 lds\n"
"s_mov_b32 s61, 0x341000\n"
"s_mov_b32 m0, 0x1b000\n"
"s_nop 0\n"
"buffer_load_dwordx4 v209, s[36:39], s61 offen lds\n"
"s_mov_b32 m0, 0x1b380\n"
"s_nop 0\n"
"buffer_load_dwordx4 v209, s[36:39], s61 offen offset:128 lds\n"
"s_mov_b32 s61, 0x369000\n"
"s_mov_b32 m0, 0x1c800\n"
"s_nop 0\n"
"buffer_load_dwordx4 v209, s[36:39], s61 offen lds\n"
"s_mov_b32 m0, 0x1cb80\n"
"s_nop 0\n"
"buffer_load_dwordx4 v209, s[36:39], s61 offen offset:128 lds\n"
"s_mov_b32 s58, 0x9300\n"
"s_mov_b32 m0, 0x21000\n"
"s_nop 0\n"
"buffer_load_dwordx4 v210, s[28:31], s58 offen lds\n"
"s_mov_b32 m0, 0x21400\n"
"s_nop 0\n"
"buffer_load_dwordx4 v211, s[28:31], s58 offen lds\n"
"s_mov_b32 s58, 0x16b00\n"
"s_mov_b32 m0, 0x22800\n"
"s_nop 0\n"
"buffer_load_dwordx4 v210, s[28:31], s58 offen lds\n"
"s_mov_b32 m0, 0x22c00\n"
"s_nop 0\n"
"buffer_load_dwordx4 v211, s[28:31], s58 offen lds\n"
"s_waitcnt lgkmcnt(0)\n"
".Lk1_pc8a:\n"
"s_memrealtime s[72:73]\n"
"s_waitcnt lgkmcnt(0)\n"
"s_sub_u32 s73, s72, s65\n"
"s_cmp_lt_u32 s73, 128\n"
"s_cbranch_scc0 .Lk1_pc8b\n"
"s_sleep 1\n"
"s_branch .Lk1_pc8a\n"
".Lk1_pc8b:\n"
"s_mov_b32 s65, s72\n"
"s_add_u32 s53, s33, 0x26000\n"
"s_and_b32 s53, s53, 0x3ffff\n"
"s_mov_b32 m0, 0x10000\n"
"s_nop 0\n"
"buffer_load_dwordx4 v208, s[24:27], s53 offen nt lds\n"
"buffer_load_dwordx4 v208, s[24:27], s53 offen offset:2048 nt lds\n"
"s_mov_b32 m0, 0x10380\n"
"s_nop 0\n"
"buffer_load_dwordx4 v208, s[24:27], s53 offen offset:128 nt lds\n"
"buffer_load_dwordx4 v208, s[24:27], s53 offen offset:2176 nt lds\n"
"s_add_u32 s53, s33, 0x29000\n"
"s_and_b32 s53, s53, 0x3ffff\n"
"s_mov_b32 m0, 0x13000\n"
"s_nop 0\n"
"buffer_load_dwordx4 v208, s[24:27], s53 offen nt lds\n"
"buffer_load_dwordx4 v208, s[24:27], s53 offen offset:2048 nt lds\n"
"s_mov_b32 m0, 0x13380\n"
"s_nop 0\n"
"buffer_load_dwordx4 v208, s[24:27], s53 offen offset:128 nt lds\n"
"buffer_load_dwordx4 v208, s[24:27], s53 offen offset:2176 nt lds\n"
"s_add_u32 s53, s33, 0x2a000\n"
"s_and_b32 s53, s53, 0x3ffff\n"
"s_mov_b32 m0, 0x6000\n"
"s_nop 0\n"
"buffer_load_dwordx4 v206, s[20:23], s53 offen nt lds\n"
"buffer_load_dwordx4 v207, s[20:23], s53 offen offset:2048 nt lds\n"
"s_mov_b32 m0, 0x6380\n"
"s_nop 0\n"
"buffer_load_dwordx4 v206, s[20:23], s53 offen offset:128 nt lds\n"
"buffer_load_dwordx4 v207, s[20:23], s53 offen offset:2176 nt lds\n"
"s_waitcnt vmcnt(12)\n"
"s_waitcnt lgkmcnt(0)\n"
"s_barrier\n"
"s_mov_b32 s58, 0x1fb00\n"
"s_mov_b32 m0, 0x23800\n"
"s_nop 0\n"
"buffer_load_dwordx4 v210, s[28:31], s58 offen lds\n"
"s_mov_b32 m0, 0x23c00\n"
"s_nop 0\n"
"buffer_load_dwordx4 v211, s[28:31], s58 offen lds\n"
"s_mov_b32 s61, 0x3a1000\n"
"s_mov_b32 m0, 0x1d800\n"
"s_nop 0\n"
"buffer_load_dwordx4 v209, s[36:39], s61 offen lds\n"
"s_mov_b32 m0, 0x1db80\n"
"s_nop 0\n"
"buffer_load_dwordx4 v209, s[36:39], s61 offen offset:128 lds\n"
"s_mov_b32 s61, 0x3c1000\n"
"s_mov_b32 m0, 0x1f000\n"
"s_nop 0\n"
"buffer_load_dwordx4 v209, s[36:39], s61 offen lds\n"
"s_mov_b32 m0, 0x1f380\n"
"s_nop 0\n"
"buffer_load_dwordx4 v209, s[36:39], s61 offen offset:128 lds\n"
"s_mov_b32 s61, 0x3e9000\n"
"s_mov_b32 m0, 0x20800\n"
"s_nop 0\n"
"buffer_load_dwordx4 v209, s[36:39], s61 offen lds\n"
"s_mov_b32 m0, 0x20b80\n"
"s_nop 0\n"
"buffer_load_dwordx4 v209, s[36:39], s61 offen offset:128 lds\n"
"s_mov_b32 s58, 0x9380\n"
"s_mov_b32 m0, 0x25800\n"
"s_nop 0\n"
"buffer_load_dwordx4 v210, s[28:31], s58 offen lds\n"
"s_mov_b32 m0, 0x25c00\n"
"s_nop 0\n"
"buffer_load_dwordx4 v211, s[28:31], s58 offen lds\n"
"s_waitcnt lgkmcnt(0)\n"
".Lk1_pc9a:\n"
"s_memrealtime s[72:73]\n"
"s_waitcnt lgkmcnt(0)\n"
"s_sub_u32 s73, s72, s65\n"
"s_cmp_lt_u32 s73, 128\n"
"s_cbranch_scc0 .Lk1_pc9b\n"
"s_sleep 1\n"
"s_branch .Lk1_pc9a\n"
".Lk1_pc9b:\n"
"s_mov_b32 s65, s72\n"
"s_add_u32 s53, s33, 0x2a000\n"
"s_and_b32 s53, s53, 0x3ffff\n"
"s_mov_b32 m0, 0x14000\n"
"s_nop 0\n"
"buffer_load_dwordx4 v208, s[24:27], s53 offen nt lds\n"
"buffer_load_dwordx4 v208, s[24:27], s53 offen offset:2048 nt lds\n"
"s_mov_b32 m0, 0x14380\n"
"s_nop 0\n"
"buffer_load_dwordx4 v208, s[24:27], s53 offen offset:128 nt lds\n"
"buffer_load_dwordx4 v208, s[24:27], s53 offen offset:2176 nt lds\n"
"s_add_u32 s53, s33, 0x2d000\n"
"s_and_b32 s53, s53, 0x3ffff\n"
"s_mov_b32 m0, 0x17000\n"
"s_nop 0\n"
"buffer_load_dwordx4 v208, s[24:27], s53 offen nt lds\n"
"buffer_load_dwordx4 v208, s[24:27], s53 offen offset:2048 nt lds\n"
"s_mov_b32 m0, 0x17380\n"
"s_nop 0\n"
"buffer_load_dwordx4 v208, s[24:27], s53 offen offset:128 nt lds\n"
"buffer_load_dwordx4 v208, s[24:27], s53 offen offset:2176 nt lds\n"
"s_add_u32 s53, s33, 0x2e000\n"
"s_and_b32 s53, s53, 0x3ffff\n"
"s_mov_b32 m0, 0xa000\n"
"s_nop 0\n"
"buffer_load_dwordx4 v206, s[20:23], s53 offen nt lds\n"
"buffer_load_dwordx4 v207, s[20:23], s53 offen offset:2048 nt lds\n"
"s_mov_b32 m0, 0xa380\n"
"s_nop 0\n"
"buffer_load_dwordx4 v206, s[20:23], s53 offen offset:128 nt lds\n"
"buffer_load_dwordx4 v207, s[20:23], s53 offen offset:2176 nt lds\n"
"s_waitcnt vmcnt(12)\n"
"s_waitcnt lgkmcnt(0)\n"
"s_barrier\n"
"s_mov_b32 s58, 0x16b80\n"
"s_mov_b32 m0, 0x27000\n"
"s_nop 0\n"
"buffer_load_dwordx4 v210, s[28:31], s58 offen lds\n"
"s_mov_b32 m0, 0x27400\n"
"s_nop 0\n"
"buffer_load_dwordx4 v211, s[28:31], s58 offen lds\n"
"s_mov_b32 s58, 0x24380\n"
"s_mov_b32 m0, 0x21800\n"
"s_nop 0\n"
"buffer_load_dwordx4 v210, s[28:31], s58 offen lds\n"
"s_mov_b32 m0, 0x21c00\n"
"s_nop 0\n"
"buffer_load_dwordx4 v211, s[28:31], s58 offen lds\n"
"s_mov_b32 s61, 0x409000\n"
"s_mov_b32 m0, 0x1a000\n"
"s_nop 0\n"
"buffer_load_dwordx4 v209, s[36:39], s61 offen lds\n"
"s_mov_b32 m0, 0x1a380\n"
"s_nop 0\n"
"buffer_load_dwordx4 v209, s[36:39], s61 offen offset:128 lds\n"
"s_mov_b32 s61, 0x461000\n"
"s_mov_b32 m0, 0x1b800\n"
"s_nop 0\n"
"buffer_load_dwordx4 v209, s[36:39], s61 offen lds\n"
"s_mov_b32 m0, 0x1bb80\n"
"s_nop 0\n"
"buffer_load_dwordx4 v209, s[36:39], s61 offen offset:128 lds\n"
"s_mov_b32 s58, 0x400\n"
"s_mov_b32 m0, 0x22000\n"
"s_nop 0\n"
"buffer_load_dwordx4 v210, s[28:31], s58 offen lds\n"
"s_mov_b32 m0, 0x22400\n"
"s_nop 0\n"
"buffer_load_dwordx4 v211, s[28:31], s58 offen lds\n"
"s_waitcnt lgkmcnt(0)\n"
".Lk1_pc10a:\n"
"s_memrealtime s[72:73]\n"
"s_waitcnt lgkmcnt(0)\n"
"s_sub_u32 s73, s72, s65\n"
"s_cmp_lt_u32 s73, 128\n"
"s_cbranch_scc0 .Lk1_pc10b\n"
"s_sleep 1\n"
"s_branch .Lk1_pc10a\n"
".Lk1_pc10b:\n"
"s_mov_b32 s65, s72\n"
"s_add_u32 s53, s33, 0x2e000\n"
"s_and_b32 s53, s53, 0x3ffff\n"
"s_mov_b32 m0, 0xc000\n"
"s_nop 0\n"
"buffer_load_dwordx4 v208, s[24:27], s53 offen nt lds\n"
"buffer_load_dwordx4 v208, s[24:27], s53 offen offset:2048 nt lds\n"
"s_mov_b32 m0, 0xc380\n"
"s_nop 0\n"
"buffer_load_dwordx4 v208, s[24:27], s53 offen offset:128 nt lds\n"
"buffer_load_dwordx4 v208, s[24:27], s53 offen offset:2176 nt lds\n"
"s_add_u32 s53, s33, 0x31000\n"
"s_and_b32 s53, s53, 0x3ffff\n"
"s_mov_b32 m0, 0xf000\n"
"s_nop 0\n"
"buffer_load_dwordx4 v208, s[24:27], s53 offen nt lds\n"
"buffer_load_dwordx4 v208, s[24:27], s53 offen offset:2048 nt lds\n"
"s_mov_b32 m0, 0xf380\n"
"s_nop 0\n"
"buffer_load_dwordx4 v208, s[24:27], s53 offen offset:128 nt lds\n"
"buffer_load_dwordx4 v208, s[24:27], s53 offen offset:2176 nt lds\n"
"s_add_u32 s53, s33, 0x32000\n"
"s_and_b32 s53, s53, 0x3ffff\n"
"s_mov_b32 m0, 0x2000\n"
"s_nop 0\n"
"buffer_load_dwordx4 v206, s[20:23], s53 offen nt lds\n"
"buffer_load_dwordx4 v207, s[20:23], s53 offen offset:2048 nt lds\n"
"s_mov_b32 m0, 0x2380\n"
"s_nop 0\n"
"buffer_load_dwordx4 v206, s[20:23], s53 offen offset:128 nt lds\n"
"buffer_load_dwordx4 v207, s[20:23], s53 offen offset:2176 nt lds\n"
"s_waitcnt vmcnt(12)\n"
"s_waitcnt lgkmcnt(0)\n"
"s_barrier\n"
"s_mov_b32 s58, 0xdc00\n"
"s_mov_b32 m0, 0x23800\n"
"s_nop 0\n"
"buffer_load_dwordx4 v210, s[28:31], s58 offen lds\n"
"s_mov_b32 m0, 0x23c00\n"
"s_nop 0\n"
"buffer_load_dwordx4 v211, s[28:31], s58 offen lds\n"
"s_mov_b32 s58, 0x1b400\n"
"s_mov_b32 m0, 0x25000\n"
"s_nop 0\n"
"buffer_load_dwordx4 v210, s[28:31], s58 offen lds\n"
"s_mov_b32 m0, 0x25400\n"
"s_nop 0\n"
"buffer_load_dwordx4 v211, s[28:31], s58 offen lds\n"
"s_waitcnt lgkmcnt(0)\n"
".Lk1_pc11a:\n"
"s_memrealtime s[72:73]\n"
"s_waitcnt lgkmcnt(0)\n"
"s_sub_u32 s73, s72, s65\n"
"s_cmp_lt_u32 s73, 128\n"
"s_cbranch_scc0 .Lk1_pc11b\n"
"s_sleep 1\n"
"s_branch .Lk1_pc11a\n"
".Lk1_pc11b:\n"
"s_mov_b32 s65, s72\n"
"s_add_u32 s53, s33, 0x32000\n"
"s_and_b32 s53, s53, 0x3ffff\n"
"s_mov_b32 m0, 0x10000\n"
"s_nop 0\n"
"buffer_load_dwordx4 v208, s[24:27], s53 offen nt lds\n"
"buffer_load_dwordx4 v208, s[24:27], s53 offen offset:2048 nt lds\n"
"s_mov_b32 m0, 0x10380\n"
"s_nop 0\n"
"buffer_load_dwordx4 v208, s[24:27], s53 offen offset:128 nt lds\n"
"buffer_load_dwordx4 v208, s[24:27], s53 offen offset:2176 nt lds\n"
"s_add_u32 s53, s33, 0x35000\n"
"s_and_b32 s53, s53, 0x3ffff\n"
"s_mov_b32 m0, 0x13000\n"
"s_nop 0\n"
"buffer_load_dwordx4 v208, s[24:27], s53 offen nt lds\n"
"buffer_load_dwordx4 v208, s[24:27], s53 offen offset:2048 nt lds\n"
"s_mov_b32 m0, 0x13380\n"
"s_nop 0\n"
"buffer_load_dwordx4 v208, s[24:27], s53 offen offset:128 nt lds\n"
"buffer_load_dwordx4 v208, s[24:27], s53 offen offset:2176 nt lds\n"
"s_add_u32 s53, s33, 0x36000\n"
"s_and_b32 s53, s53, 0x3ffff\n"
"s_mov_b32 m0, 0x6000\n"
"s_nop 0\n"
"buffer_load_dwordx4 v206, s[20:23], s53 offen nt lds\n"
"buffer_load_dwordx4 v207, s[20:23], s53 offen offset:2048 nt lds\n"
"s_mov_b32 m0, 0x6380\n"
"s_nop 0\n"
"buffer_load_dwordx4 v206, s[20:23], s53 offen offset:128 nt lds\n"
"buffer_load_dwordx4 v207, s[20:23], s53 offen offset:2176 nt lds\n"
"s_waitcnt vmcnt(12)\n"
"s_waitcnt lgkmcnt(0)\n"
"s_barrier\n"
"s_waitcnt lgkmcnt(0)\n"
".Lk1_pc12a:\n"
"s_memrealtime s[72:73]\n"
"s_waitcnt lgkmcnt(0)\n"
"s_sub_u32 s73, s72, s65\n"
"s_cmp_lt_u32 s73, 128\n"
"s_cbranch_scc0 .Lk1_pc12b\n"
"s_sleep 1\n"
"s_branch .Lk1_pc12a\n"
".Lk1_pc12b:\n"
"s_mov_b32 s65, s72\n"
"s_add_u32 s53, s33, 0x36000\n"
"s_and_b32 s53, s53, 0x3ffff\n"
"s_mov_b32 m0, 0x14000\n"
"s_nop 0\n"
"buffer_load_dwordx4 v208, s[24:27], s53 offen nt lds\n"
"buffer_load_dwordx4 v208, s[24:27], s53 offen offset:2048 nt lds\n"
"s_mov_b32 m0, 0x14380\n"
"s_nop 0\n"
"buffer_load_dwordx4 v208, s[24:27], s53 offen offset:128 nt lds\n"
"buffer_load_dwordx4 v208, s[24:27], s53 offen offset:2176 nt lds\n"
"s_add_u32 s53, s33, 0x39000\n"
"s_and_b32 s53, s53, 0x3ffff\n"
"s_mov_b32 m0, 0x17000\n"
"s_nop 0\n"
"buffer_load_dwordx4 v208, s[24:27], s53 offen nt lds\n"
"buffer_load_dwordx4 v208, s[24:27], s53 offen offset:2048 nt lds\n"
"s_mov_b32 m0, 0x17380\n"
"s_nop 0\n"
"buffer_load_dwordx4 v208, s[24:27], s53 offen offset:128 nt lds\n"
"buffer_load_dwordx4 v208, s[24:27], s53 offen offset:2176 nt lds\n"
"s_add_u32 s53, s33, 0x3a000\n"
"s_and_b32 s53, s53, 0x3ffff\n"
"s_mov_b32 m0, 0xa000\n"
"s_nop 0\n"
"buffer_load_dwordx4 v206, s[20:23], s53 offen nt lds\n"
"buffer_load_dwordx4 v207, s[20:23], s53 offen offset:2048 nt lds\n"
"s_mov_b32 m0, 0xa380\n"
"s_nop 0\n"
"buffer_load_dwordx4 v206, s[20:23], s53 offen offset:128 nt lds\n"
"buffer_load_dwordx4 v207, s[20:23], s53 offen offset:2176 nt lds\n"
"s_waitcnt vmcnt(12)\n"
"s_waitcnt lgkmcnt(0)\n"
"s_barrier\n"
"s_waitcnt lgkmcnt(0)\n"
".Lk1_pc13a:\n"
"s_memrealtime s[72:73]\n"
"s_waitcnt lgkmcnt(0)\n"
"s_sub_u32 s73, s72, s65\n"
"s_cmp_lt_u32 s73, 128\n"
"s_cbranch_scc0 .Lk1_pc13b\n"
"s_sleep 1\n"
"s_branch .Lk1_pc13a\n"
".Lk1_pc13b:\n"
"s_mov_b32 s65, s72\n"
"s_add_u32 s53, s33, 0x3a000\n"
"s_and_b32 s53, s53, 0x3ffff\n"
"s_mov_b32 m0, 0xc000\n"
"s_nop 0\n"
"buffer_load_dwordx4 v208, s[24:27], s53 offen nt lds\n"
"buffer_load_dwordx4 v208, s[24:27], s53 offen offset:2048 nt lds\n"
"s_mov_b32 m0, 0xc380\n"
"s_nop 0\n"
"buffer_load_dwordx4 v208, s[24:27], s53 offen offset:128 nt lds\n"
"buffer_load_dwordx4 v208, s[24:27], s53 offen offset:2176 nt lds\n"
"s_add_u32 s53, s33, 0x3d000\n"
"s_and_b32 s53, s53, 0x3ffff\n"
"s_mov_b32 m0, 0xf000\n"
"s_nop 0\n"
"buffer_load_dwordx4 v208, s[24:27], s53 offen nt lds\n"
"buffer_load_dwordx4 v208, s[24:27], s53 offen offset:2048 nt lds\n"
"s_mov_b32 m0, 0xf380\n"
"s_nop 0\n"
"buffer_load_dwordx4 v208, s[24:27], s53 offen offset:128 nt lds\n"
"buffer_load_dwordx4 v208, s[24:27], s53 offen offset:2176 nt lds\n"
"s_add_u32 s53, s33, 0x3e000\n"
"s_and_b32 s53, s53, 0x3ffff\n"
"s_mov_b32 m0, 0x2000\n"
"s_nop 0\n"
"buffer_load_dwordx4 v206, s[20:23], s53 offen nt lds\n"
"buffer_load_dwordx4 v207, s[20:23], s53 offen offset:2048 nt lds\n"
"s_mov_b32 m0, 0x2380\n"
"s_nop 0\n"
"buffer_load_dwordx4 v206, s[20:23], s53 offen offset:128 nt lds\n"
"buffer_load_dwordx4 v207, s[20:23], s53 offen offset:2176 nt lds\n"
"s_waitcnt vmcnt(12)\n"
"s_waitcnt lgkmcnt(0)\n"
"s_barrier\n"
"s_waitcnt lgkmcnt(0)\n"
".Lk1_pc14a:\n"
"s_memrealtime s[72:73]\n"
"s_waitcnt lgkmcnt(0)\n"
"s_sub_u32 s73, s72, s65\n"
"s_cmp_lt_u32 s73, 128\n"
"s_cbranch_scc0 .Lk1_pc14b\n"
"s_sleep 1\n"
"s_branch .Lk1_pc14a\n"
".Lk1_pc14b:\n"
"s_mov_b32 s65, s72\n"
"s_add_u32 s53, s33, 0x3e000\n"
"s_and_b32 s53, s53, 0x3ffff\n"
"s_mov_b32 m0, 0x10000\n"
"s_nop 0\n"
"buffer_load_dwordx4 v208, s[24:27], s53 offen nt lds\n"
"buffer_load_dwordx4 v208, s[24:27], s53 offen offset:2048 nt lds\n"
"s_mov_b32 m0, 0x10380\n"
"s_nop 0\n"
"buffer_load_dwordx4 v208, s[24:27], s53 offen offset:128 nt lds\n"
"buffer_load_dwordx4 v208, s[24:27], s53 offen offset:2176 nt lds\n"
"s_add_u32 s53, s33, 0x1000\n"
"s_and_b32 s53, s53, 0x3ffff\n"
"s_add_u32 s53, s53, 0x400000\n"
"s_mov_b32 m0, 0x13000\n"
"s_nop 0\n"
"buffer_load_dwordx4 v208, s[24:27], s53 offen nt lds\n"
"buffer_load_dwordx4 v208, s[24:27], s53 offen offset:2048 nt lds\n"
"s_mov_b32 m0, 0x13380\n"
"s_nop 0\n"
"buffer_load_dwordx4 v208, s[24:27], s53 offen offset:128 nt lds\n"
"buffer_load_dwordx4 v208, s[24:27], s53 offen offset:2176 nt lds\n"
"s_add_u32 s53, s33, 0x2000\n"
"s_and_b32 s53, s53, 0x3ffff\n"
"s_add_u32 s53, s53, 0x400000\n"
"s_mov_b32 m0, 0x6000\n"
"s_nop 0\n"
"buffer_load_dwordx4 v206, s[20:23], s53 offen nt lds\n"
"buffer_load_dwordx4 v207, s[20:23], s53 offen offset:2048 nt lds\n"
"s_mov_b32 m0, 0x6380\n"
"s_nop 0\n"
"buffer_load_dwordx4 v206, s[20:23], s53 offen offset:128 nt lds\n"
"buffer_load_dwordx4 v207, s[20:23], s53 offen offset:2176 nt lds\n"
"v_mov_b32 v200, 0\n"
"v_mov_b32 v201, 0\n"
"v_mov_b32 v202, 0\n"
"v_mov_b32 v203, 0\n"
"v_lshlrev_b32 v247, 4, v238\n"
"v_add_u32 v247, 0x18800, v247\n"
"ds_write_b128 v247, v[200:203]\n"
"s_waitcnt vmcnt(12)\n"
"s_waitcnt lgkmcnt(0)\n"
"s_barrier\n"
"v_lshlrev_b32 v247, 4, v238\n"
"s_mul_i32 s60, s44, 0x3000\n"
"s_add_u32 s60, s60, 0x19000\n"
"v_add_u32 v247, s60, v247\n"
"ds_read_b128 v[96:99], v247\n"
"ds_read_b128 v[100:103], v247 offset:1024\n"
"ds_read_b128 v[104:107], v247 offset:2048\n"
"ds_read_b128 v[108:111], v247 offset:3072\n"
"ds_read_b128 v[112:115], v247 offset:4096\n"
"ds_read_b128 v[116:119], v247 offset:5120\n"
"ds_read_b128 v[120:123], v247 offset:6144\n"
"ds_read_b128 v[124:127], v247 offset:7168\n"
"ds_read_b128 v[128:131], v247 offset:8192\n"
"ds_read_b128 v[132:135], v247 offset:9216\n"
"ds_read_b128 v[136:139], v247 offset:10240\n"
"ds_read_b128 v[140:143], v247 offset:11264\n"
"v_add_u32 v252, 0x2000, v248\n"
"s_waitcnt lgkmcnt(0)\n"
".Lk1_pc15a:\n"
"s_memrealtime s[72:73]\n"
"s_waitcnt lgkmcnt(0)\n"
"s_sub_u32 s73, s72, s65\n"
"s_cmp_lt_u32 s73, 128\n"
"s_cbranch_scc0 .Lk1_pc15b\n"
"s_sleep 1\n"
"s_branch .Lk1_pc15a\n"
".Lk1_pc15b:\n"
"s_mov_b32 s65, s72\n"
"s_add_u32 s53, s33, 0x2000\n"
"s_and_b32 s53, s53, 0x3ffff\n"
"s_add_u32 s53, s53, 0x400000\n"
"s_mov_b32 m0, 0x14000\n"
"s_nop 0\n"
"buffer_load_dwordx4 v208, s[24:27], s53 offen nt lds\n"
"buffer_load_dwordx4 v208, s[24:27], s53 offen offset:2048 nt lds\n"
"s_mov_b32 m0, 0x14380\n"
"s_nop 0\n"
"buffer_load_dwordx4 v208, s[24:27], s53 offen offset:128 nt lds\n"
"buffer_load_dwordx4 v208, s[24:27], s53 offen offset:2176 nt lds\n"
"s_add_u32 s53, s33, 0x5000\n"
"s_and_b32 s53, s53, 0x3ffff\n"
"s_add_u32 s53, s53, 0x400000\n"
"s_mov_b32 m0, 0x17000\n"
"s_nop 0\n"
"buffer_load_dwordx4 v208, s[24:27], s53 offen nt lds\n"
"buffer_load_dwordx4 v208, s[24:27], s53 offen offset:2048 nt lds\n"
"s_mov_b32 m0, 0x17380\n"
"s_nop 0\n"
"buffer_load_dwordx4 v208, s[24:27], s53 offen offset:128 nt lds\n"
"buffer_load_dwordx4 v208, s[24:27], s53 offen offset:2176 nt lds\n"
"s_add_u32 s53, s33, 0x6000\n"
"s_and_b32 s53, s53, 0x3ffff\n"
"s_add_u32 s53, s53, 0x400000\n"
"s_mov_b32 m0, 0xa000\n"
"s_nop 0\n"
"buffer_load_dwordx4 v206, s[20:23], s53 offen nt lds\n"
"buffer_load_dwordx4 v207, s[20:23], s53 offen offset:2048 nt lds\n"
"s_mov_b32 m0, 0xa380\n"
"s_nop 0\n"
"buffer_load_dwordx4 v206, s[20:23], s53 offen offset:128 nt lds\n"
"buffer_load_dwordx4 v207, s[20:23], s53 offen offset:2176 nt lds\n"
"s_waitcnt vmcnt(12)\n"
"ds_read_b128 v[184:187], v252\n"
"ds_read_b128 v[192:195], v252 offset:512\n"
"ds_read_b128 v[200:203], v252 offset:1024\n"
"ds_read_b128 v[220:223], v252 offset:1536\n"
"v_and_b32 v247, 15, v238\n"
"v_cmp_eq_u32 vcc, 0, v247\n"
"v_mov_b32 v247, 0x3f803f80\n"
"s_nop 0\n"
"v_cndmask_b32 v240, 0, v247, vcc\n"
"v_cndmask_b32 v241, 0, v247, vcc\n"
"v_cndmask_b32 v242, 0, v247, vcc\n"
"v_cndmask_b32 v243, 0, v247, vcc\n"
"s_waitcnt lgkmcnt(3)\n"
"v_mfma_f32_16x16x32_bf16 v[160:163], v[184:187], v[96:99], 0\n"
"v_mfma_f32_16x16x32_bf16 v[168:171], v[184:187], v[112:115], 0\n"
"v_mfma_f32_16x16x32_bf16 v[176:179], v[184:187], v[128:131], 0\n"
"s_waitcnt lgkmcnt(2)\n"
"v_mfma_f32_16x16x32_bf16 v[160:163], v[192:195], v[100:103], v[160:163]\n"
"v_mfma_f32_16x16x32_bf16 v[168:171], v[192:195], v[116:119], v[168:171]\n"
"v_mfma_f32_16x16x32_bf16 v[176:179], v[192:195], v[132:135], v[176:179]\n"
"s_waitcnt lgkmcnt(1)\n"
"v_mfma_f32_16x16x32_bf16 v[160:163], v[200:203], v[104:107], v[160:163]\n"
"v_mfma_f32_16x16x32_bf16 v[168:171], v[200:203], v[120:123], v[168:171]\n"
"v_mfma_f32_16x16x32_bf16 v[176:179], v[200:203], v[136:139], v[176:179]\n"
"s_waitcnt lgkmcnt(0)\n"
"v_mfma_f32_16x16x32_bf16 v[160:163], v[220:223], v[108:111], v[160:163]\n"
"v_mfma_f32_16x16x32_bf16 v[168:171], v[220:223], v[124:127], v[168:171]\n"
"v_mfma_f32_16x16x32_bf16 v[176:179], v[220:223], v[140:143], v[176:179]\n"
"v_mov_b32 v156, 0\n"
"v_mov_b32 v157, 0\n"
"v_mov_b32 v158, 0\n"
"v_mov_b32 v159, 0\n"
"v_mov_b32 v164, 0\n"
"v_mov_b32 v165, 0\n"
"v_mov_b32 v166, 0\n"
"v_mov_b32 v167, 0\n"
"v_mov_b32 v172, 0\n"
"v_mov_b32 v173, 0\n"
"v_mov_b32 v174, 0\n"
"v_mov_b32 v175, 0\n"
"v_mov_b32 v180, 0\n"
"v_mov_b32 v181, 0\n"
"v_mov_b32 v182, 0\n"
"v_mov_b32 v183, 0\n"
"v_mov_b32 v188, 0\n"
"v_mov_b32 v189, 0\n"
"v_mov_b32 v190, 0\n"
"v_mov_b32 v191, 0\n"
"v_mov_b32 v196, 0\n"
"v_mov_b32 v197, 0\n"
"v_mov_b32 v198, 0\n"
"v_mov_b32 v199, 0\n"
"v_max3_f32 v228, v160, v161, v162\n"
"v_max_f32 v228, v228, v163\n"
"v_max3_f32 v232, v168, v169, v170\n"
"v_max_f32 v232, v232, v171\n"
"v_max3_f32 v236, v176, v177, v178\n"
"v_max_f32 v236, v236, v179\n"
"v_mov_b32 v204, v228\n"
"v_mov_b32 v205, v228\n"
"s_nop 1\n"
"v_permlane32_swap_b32 v204, v205\n"
"v_max_f32 v228, v204, v205\n"
"v_mov_b32 v204, v228\n"
"v_mov_b32 v205, v228\n"
"s_nop 1\n"
"v_permlane16_swap_b32 v204, v205\n"
"v_max_f32 v228, v204, v205\n"
"v_sub_f32 v144, 0, v228\n"
"v_sub_f32 v145, 0, v228\n"
"v_sub_f32 v146, 0, v228\n"
"v_sub_f32 v147, 0, v228\n"
"v_mov_b32 v228, 0\n"
"v_mov_b32 v204, v232\n"
"v_mov_b32 v205, v232\n"
"s_nop 1\n"
"v_permlane32_swap_b32 v204, v205\n"
"v_max_f32 v232, v204, v205\n"
"v_mov_b32 v204, v232\n"
"v_mov_b32 v205, v232\n"
"s_nop 1\n"
"v_permlane16_swap_b32 v204, v205\n"
"v_max_f32 v232, v204, v205\n"
"v_sub_f32 v148, 0, v232\n"
"v_sub_f32 v149, 0, v232\n"
"v_sub_f32 v150, 0, v232\n"
"v_sub_f32 v151, 0, v232\n"
"v_mov_b32 v232, 0\n"
"v_mov_b32 v204, v236\n"
"v_mov_b32 v205, v236\n"
"s_nop 1\n"
"v_permlane32_swap_b32 v204, v205\n"
"v_max_f32 v236, v204, v205\n"
"v_mov_b32 v204, v236\n"
"v_mov_b32 v205, v236\n"
"s_nop 1\n"
"v_permlane16_swap_b32 v204, v205\n"
"v_max_f32 v236, v204, v205\n"
"v_sub_f32 v152, 0, v236\n"
"v_sub_f32 v153, 0, v236\n"
"v_sub_f32 v154, 0, v236\n"
"v_sub_f32 v155, 0, v236\n"
"v_mov_b32 v236, 0\n"
"v_mov_b32 v216, 0\n"
"v_mov_b32 v217, 0\n"
"v_mov_b32 v218, 0\n"
"v_mov_b32 v219, 0\n"
"v_mov_b32 v220, 0\n"
"v_mov_b32 v221, 0\n"
"v_mov_b32 v222, 0\n"
"v_mov_b32 v223, 0\n"
"v_mov_b32 v224, 0\n"
"v_mov_b32 v225, 0\n"
"v_mov_b32 v226, 0\n"
"v_mov_b32 v227, 0\n"
"s_waitcnt lgkmcnt(0)\n"
"s_barrier\n"
"s_branch .Lk1_attn\n"
".Lk1_ld1:\n"
"s_mov_b32 s61, 0x9000\n"
"s_mov_b32 m0, 0x1a000\n"
"s_nop 0\n"
"buffer_load_dwordx4 v209, s[36:39], s61 offen lds\n"
"s_mov_b32 m0, 0x1a380\n"
"s_nop 0\n"
"buffer_load_dwordx4 v209, s[36:39], s61 offen offset:128 lds\n"
"s_mov_b32 s61, 0x61000\n"
"s_mov_b32 m0, 0x1b800\n"
"s_nop 0\n"
"buffer_load_dwordx4 v209, s[36:39], s61 offen lds\n"
"s_mov_b32 m0, 0x1bb80\n"
"s_nop 0\n"
"buffer_load_dwordx4 v209, s[36:39], s61 offen offset:128 lds\n"
"s_mov_b32 s58, 0\n"
"s_mov_b32 m0, 0x21000\n"
"s_nop 0\n"
"buffer_load_dwordx4 v210, s[28:31], s58 offen lds\n"
"s_mov_b32 m0, 0x21400\n"
"s_nop 0\n"
"buffer_load_dwordx4 v211, s[28:31], s58 offen lds\n"
"s_mov_b32 s58, 0xd800\n"
"s_mov_b32 m0, 0x22800\n"
"s_nop 0\n"
"buffer_load_dwordx4 v210, s[28:31], s58 offen lds\n"
"s_mov_b32 m0, 0x22c00\n"
"s_nop 0\n"
"buffer_load_dwordx4 v211, s[28:31], s58 offen lds\n"
"s_waitcnt vmcnt(0)\n"
"s_waitcnt lgkmcnt(0)\n"
"s_barrier\n"
"s_mov_b32 s58, 0x24000\n"
"s_mov_b32 m0, 0x25000\n"
"s_nop 0\n"
"buffer_load_dwordx4 v210, s[28:31], s58 offen lds\n"
"s_mov_b32 m0, 0x25400\n"
"s_nop 0\n"
"buffer_load_dwordx4 v211, s[28:31], s58 offen lds\n"
"s_mov_b32 s61, 0x89000\n"
"s_mov_b32 m0, 0x1e000\n"
"s_nop 0\n"
"buffer_load_dwordx4 v209, s[36:39], s61 offen lds\n"
"s_mov_b32 m0, 0x1e380\n"
"s_nop 0\n"
"buffer_load_dwordx4 v209, s[36:39], s61 offen offset:128 lds\n"
"s_mov_b32 s61, 0xe1000\n"
"s_mov_b32 m0, 0x1f800\n"
"s_nop 0\n"
"buffer_load_dwordx4 v209, s[36:39], s61 offen lds\n"
"s_mov_b32 m0, 0x1fb80\n"
"s_nop 0\n"
"buffer_load_dwordx4 v209, s[36:39], s61 offen offset:128 lds\n"
"s_mov_b32 s58, 0x80\n"
"s_mov_b32 m0, 0x25800\n"
"s_nop 0\n"
"buffer_load_dwordx4 v210, s[28:31], s58 offen lds\n"
"s_mov_b32 m0, 0x25c00\n"
"s_nop 0\n"
"buffer_load_dwordx4 v211, s[28:31], s58 offen lds\n"
"s_mov_b32 s58, 0xd880\n"
"s_mov_b32 m0, 0x27000\n"
"s_nop 0\n"
"buffer_load_dwordx4 v210, s[28:31], s58 offen lds\n"
"s_mov_b32 m0, 0x27400\n"
"s_nop 0\n"
"buffer_load_dwordx4 v211, s[28:31], s58 offen lds\n"
"s_waitcnt lgkmcnt(0)\n"
".Lk1_pc16a:\n"
"s_memrealtime s[72:73]\n"
"s_waitcnt lgkmcnt(0)\n"
"s_sub_u32 s73, s72, s65\n"
"s_cmp_lt_u32 s73, 128\n"
"s_cbranch_scc0 .Lk1_pc16b\n"
"s_sleep 1\n"
"s_branch .Lk1_pc16a\n"
".Lk1_pc16b:\n"
"s_mov_b32 s65, s72\n"
"s_add_u32 s53, s33, 0xb000\n"
"s_and_b32 s53, s53, 0x3ffff\n"
"s_mov_b32 m0, 0xd000\n"
"s_nop 0\n"
"buffer_load_dwordx4 v208, s[24:27], s53 offen nt lds\n"
"buffer_load_dwordx4 v208, s[24:27], s53 offen offset:2048 nt lds\n"
"s_mov_b32 m0, 0xd380\n"
"s_nop 0\n"
"buffer_load_dwordx4 v208, s[24:27], s53 offen offset:128 nt lds\n"
"buffer_load_dwordx4 v208, s[24:27], s53 offen offset:2176 nt lds\n"
"s_add_u32 s53, s33, 0xc000\n"
"s_and_b32 s53, s53, 0x3ffff\n"
"s_mov_b32 m0, 0\n"
"s_nop 0\n"
"buffer_load_dwordx4 v206, s[20:23], s53 offen nt lds\n"
"buffer_load_dwordx4 v207, s[20:23], s53 offen offset:2048 nt lds\n"
"s_mov_b32 m0, 0x380\n"
"s_nop 0\n"
"buffer_load_dwordx4 v206, s[20:23], s53 offen offset:128 nt lds\n"
"buffer_load_dwordx4 v207, s[20:23], s53 offen offset:2176 nt lds\n"
"s_add_u32 s53, s33, 0xf000\n"
"s_and_b32 s53, s53, 0x3ffff\n"
"s_mov_b32 m0, 0x3000\n"
"s_nop 0\n"
"buffer_load_dwordx4 v206, s[20:23], s53 offen nt lds\n"
"buffer_load_dwordx4 v207, s[20:23], s53 offen offset:2048 nt lds\n"
"s_mov_b32 m0, 0x3380\n"
"s_nop 0\n"
"buffer_load_dwordx4 v206, s[20:23], s53 offen offset:128 nt lds\n"
"buffer_load_dwordx4 v207, s[20:23], s53 offen offset:2176 nt lds\n"
"s_waitcnt vmcnt(12)\n"
"s_waitcnt lgkmcnt(0)\n"
"s_barrier\n"
"s_mov_b32 s58, 0x1b080\n"
"s_mov_b32 m0, 0x21800\n"
"s_nop 0\n"
"buffer_load_dwordx4 v210, s[28:31], s58 offen lds\n"
"s_mov_b32 m0, 0x21c00\n"
"s_nop 0\n"
"buffer_load_dwordx4 v211, s[28:31], s58 offen lds\n"
"s_mov_b32 s61, 0x101000\n"
"s_mov_b32 m0, 0x19000\n"
"s_nop 0\n"
"buffer_load_dwordx4 v209, s[36:39], s61 offen lds\n"
"s_mov_b32 m0, 0x19380\n"
"s_nop 0\n"
"buffer_load_dwordx4 v209, s[36:39], s61 offen offset:128 lds\n"
"s_mov_b32 s61, 0x129000\n"
"s_mov_b32 m0, 0x1a800\n"
"s_nop 0\n"
"buffer_load_dwordx4 v209, s[36:39], s61 offen lds\n"
"s_mov_b32 m0, 0x1ab80\n"
"s_nop 0\n"
"buffer_load_dwordx4 v209, s[36:39], s61 offen offset:128 lds\n"
"s_mov_b32 s61, 0x149000\n"
"s_mov_b32 m0, 0x1c000\n"
"s_nop 0\n"
"buffer_load_dwordx4 v209, s[36:39], s61 offen lds\n"
"s_mov_b32 m0, 0x1c380\n"
"s_nop 0\n"
"buffer_load_dwordx4 v209, s[36:39], s61 offen offset:128 lds\n"
"s_mov_b32 s58, 0x4900\n"
"s_mov_b32 m0, 0x23800\n"
"s_nop 0\n"
"buffer_load_dwordx4 v210, s[28:31], s58 offen lds\n"
"s_mov_b32 m0, 0x23c00\n"
"s_nop 0\n"
"buffer_load_dwordx4 v211, s[28:31], s58 offen lds\n"
"s_waitcnt lgkmcnt(0)\n"
".Lk1_pc17a:\n"
"s_memrealtime s[72:73]\n"
"s_waitcnt lgkmcnt(0)\n"
"s_sub_u32 s73, s72, s65\n"
"s_cmp_lt_u32 s73, 128\n"
"s_cbranch_scc0 .Lk1_pc17b\n"
"s_sleep 1\n"
"s_branch .Lk1_pc17a\n"
".Lk1_pc17b:\n"
"s_mov_b32 s65, s72\n"
"s_add_u32 s53, s33, 0xf000\n"
"s_and_b32 s53, s53, 0x3ffff\n"
"s_mov_b32 m0, 0x11000\n"
"s_nop 0\n"
"buffer_load_dwordx4 v208, s[24:27], s53 offen nt lds\n"
"buffer_load_dwordx4 v208, s[24:27], s53 offen offset:2048 nt lds\n"
"s_mov_b32 m0, 0x11380\n"
"s_nop 0\n"
"buffer_load_dwordx4 v208, s[24:27], s53 offen offset:128 nt lds\n"
"buffer_load_dwordx4 v208, s[24:27], s53 offen offset:2176 nt lds\n"
"s_add_u32 s53, s33, 0x10000\n"
"s_and_b32 s53, s53, 0x3ffff\n"
"s_mov_b32 m0, 0x4000\n"
"s_nop 0\n"
"buffer_load_dwordx4 v206, s[20:23], s53 offen nt lds\n"
"buffer_load_dwordx4 v207, s[20:23], s53 offen offset:2048 nt lds\n"
"s_mov_b32 m0, 0x4380\n"
"s_nop 0\n"
"buffer_load_dwordx4 v206, s[20:23], s53 offen offset:128 nt lds\n"
"buffer_load_dwordx4 v207, s[20:23], s53 offen offset:2176 nt lds\n"
"s_add_u32 s53, s33, 0x13000\n"
"s_and_b32 s53, s53, 0x3ffff\n"
"s_mov_b32 m0, 0x7000\n"
"s_nop 0\n"
"buffer_load_dwordx4 v206, s[20:23], s53 offen nt lds\n"
"buffer_load_dwordx4 v207, s[20:23], s53 offen offset:2048 nt lds\n"
"s_mov_b32 m0, 0x7380\n"
"s_nop 0\n"
"buffer_load_dwordx4 v206, s[20:23], s53 offen offset:128 nt lds\n"
"buffer_load_dwordx4 v207, s[20:23], s53 offen offset:2176 nt lds\n"
"s_waitcnt vmcnt(12)\n"
"s_waitcnt lgkmcnt(0)\n"
"s_barrier\n"
"s_mov_b32 s58, 0x12100\n"
"s_mov_b32 m0, 0x25000\n"
"s_nop 0\n"
"buffer_load_dwordx4 v210, s[28:31], s58 offen lds\n"
"s_mov_b32 m0, 0x25400\n"
"s_nop 0\n"
"buffer_load_dwordx4 v211, s[28:31], s58 offen lds\n"
"s_mov_b32 s58, 0x1f900\n"
"s_mov_b32 m0, 0x26800\n"
"s_nop 0\n"
"buffer_load_dwordx4 v210, s[28:31], s58 offen lds\n"
"s_mov_b32 m0, 0x26c00\n"
"s_nop 0\n"
"buffer_load_dwordx4 v211, s[28:31], s58 offen lds\n"
"s_waitcnt lgkmcnt(0)\n"
".Lk1_pc18a:\n"
"s_memrealtime s[72:73]\n"
"s_waitcnt lgkmcnt(0)\n"
"s_sub_u32 s73, s72, s65\n"
"s_cmp_lt_u32 s73, 128\n"
"s_cbranch_scc0 .Lk1_pc18b\n"
"s_sleep 1\n"
"s_branch .Lk1_pc18a\n"
".Lk1_pc18b:\n"
"s_mov_b32 s65, s72\n"
"s_add_u32 s53, s33, 0x13000\n"
"s_and_b32 s53, s53, 0x3ffff\n"
"s_mov_b32 m0, 0x15000\n"
"s_nop 0\n"
"buffer_load_dwordx4 v208, s[24:27], s53 offen nt lds\n"
"buffer_load_dwordx4 v208, s[24:27], s53 offen offset:2048 nt lds\n"
"s_mov_b32 m0, 0x15380\n"
"s_nop 0\n"
"buffer_load_dwordx4 v208, s[24:27], s53 offen offset:128 nt lds\n"
"buffer_load_dwordx4 v208, s[24:27], s53 offen offset:2176 nt lds\n"
"s_add_u32 s53, s33, 0x14000\n"
"s_and_b32 s53, s53, 0x3ffff\n"
"s_mov_b32 m0, 0x8000\n"
"s_nop 0\n"
"buffer_load_dwordx4 v206, s[20:23], s53 offen nt lds\n"
"buffer_load_dwordx4 v207, s[20:23], s53 offen offset:2048 nt lds\n"
"s_mov_b32 m0, 0x8380\n"
"s_nop 0\n"
"buffer_load_dwordx4 v206, s[20:23], s53 offen offset:128 nt lds\n"
"buffer_load_dwordx4 v207, s[20:23], s53 offen offset:2176 nt lds\n"
"s_add_u32 s53, s33, 0x17000\n"
"s_and_b32 s53, s53, 0x3ffff\n"
"s_mov_b32 m0, 0xb000\n"
"s_nop 0\n"
"buffer_load_dwordx4 v206, s[20:23], s53 offen nt lds\n"
"buffer_load_dwordx4 v207, s[20:23], s53 offen offset:2048 nt lds\n"
"s_mov_b32 m0, 0xb380\n"
"s_nop 0\n"
"buffer_load_dwordx4 v206, s[20:23], s53 offen offset:128 nt lds\n"
"buffer_load_dwordx4 v207, s[20:23], s53 offen offset:2176 nt lds\n"
"s_waitcnt vmcnt(12)\n"
"s_waitcnt lgkmcnt(0)\n"
"s_barrier\n"
"s_mov_b32 s61, 0x189000\n"
"s_mov_b32 m0, 0x1e000\n"
"s_nop 0\n"
"buffer_load_dwordx4 v209, s[36:39], s61 offen lds\n"
"s_mov_b32 m0, 0x1e380\n"
"s_nop 0\n"
"buffer_load_dwordx4 v209, s[36:39], s61 offen offset:128 lds\n"
"s_mov_b32 s61, 0x1e1000\n"
"s_mov_b32 m0, 0x1f800\n"
"s_nop 0\n"
"buffer_load_dwordx4 v209, s[36:39], s61 offen lds\n"
"s_mov_b32 m0, 0x1fb80\n"
"s_nop 0\n"
"buffer_load_dwordx4 v209, s[36:39], s61 offen offset:128 lds\n"
"s_mov_b32 s58, 0x180\n"
"s_mov_b32 m0, 0x27800\n"
"s_nop 0\n"
"buffer_load_dwordx4 v210, s[28:31], s58 offen lds\n"
"s_mov_b32 m0, 0x27c00\n"
"s_nop 0\n"
"buffer_load_dwordx4 v211, s[28:31], s58 offen lds\n"
"s_mov_b32 s58, 0xd980\n"
"s_mov_b32 m0, 0x22000\n"
"s_nop 0\n"
"buffer_load_dwordx4 v210, s[28:31], s58 offen lds\n"
"s_mov_b32 m0, 0x22400\n"
"s_nop 0\n"
"buffer_load_dwordx4 v211, s[28:31], s58 offen lds\n"
"s_waitcnt lgkmcnt(0)\n"
".Lk1_pc19a:\n"
"s_memrealtime s[72:73]\n"
"s_waitcnt lgkmcnt(0)\n"
"s_sub_u32 s73, s72, s65\n"
"s_cmp_lt_u32 s73, 128\n"
"s_cbranch_scc0 .Lk1_pc19b\n"
"s_sleep 1\n"
"s_branch .Lk1_pc19a\n"
".Lk1_pc19b:\n"
"s_mov_b32 s65, s72\n"
"s_add_u32 s53, s33, 0x17000\n"
"s_and_b32 s53, s53, 0x3ffff\n"
"s_mov_b32 m0, 0xd000\n"
"s_nop 0\n"
"buffer_load_dwordx4 v208, s[24:27], s53 offen nt lds\n"
"buffer_load_dwordx4 v208, s[24:27], s53 offen offset:2048 nt lds\n"
"s_mov_b32 m0, 0xd380\n"
"s_nop 0\n"
"buffer_load_dwordx4 v208, s[24:27], s53 offen offset:128 nt lds\n"
"buffer_load_dwordx4 v208, s[24:27], s53 offen offset:2176 nt lds\n"
"s_add_u32 s53, s33, 0x18000\n"
"s_and_b32 s53, s53, 0x3ffff\n"
"s_mov_b32 m0, 0\n"
"s_nop 0\n"
"buffer_load_dwordx4 v206, s[20:23], s53 offen nt lds\n"
"buffer_load_dwordx4 v207, s[20:23], s53 offen offset:2048 nt lds\n"
"s_mov_b32 m0, 0x380\n"
"s_nop 0\n"
"buffer_load_dwordx4 v206, s[20:23], s53 offen offset:128 nt lds\n"
"buffer_load_dwordx4 v207, s[20:23], s53 offen offset:2176 nt lds\n"
"s_add_u32 s53, s33, 0x1b000\n"
"s_and_b32 s53, s53, 0x3ffff\n"
"s_mov_b32 m0, 0x3000\n"
"s_nop 0\n"
"buffer_load_dwordx4 v206, s[20:23], s53 offen nt lds\n"
"buffer_load_dwordx4 v207, s[20:23], s53 offen offset:2048 nt lds\n"
"s_mov_b32 m0, 0x3380\n"
"s_nop 0\n"
"buffer_load_dwordx4 v206, s[20:23], s53 offen offset:128 nt lds\n"
"buffer_load_dwordx4 v207, s[20:23], s53 offen offset:2176 nt lds\n"
"s_waitcnt vmcnt(12)\n"
"s_waitcnt lgkmcnt(0)\n"
"s_barrier\n"
"s_mov_b32 s58, 0x24180\n"
"s_mov_b32 m0, 0x24800\n"
"s_nop 0\n"
"buffer_load_dwordx4 v210, s[28:31], s58 offen lds\n"
"s_mov_b32 m0, 0x24c00\n"
"s_nop 0\n"
"buffer_load_dwordx4 v211, s[28:31], s58 offen lds\n"
"s_mov_b32 s61, 0x209000\n"
"s_mov_b32 m0, 0x1a000\n"
"s_nop 0\n"
"buffer_load_dwordx4 v209, s[36:39], s61 offen lds\n"
"s_mov_b32 m0, 0x1a380\n"
"s_nop 0\n"
"buffer_load_dwordx4 v209, s[36:39], s61 offen offset:128 lds\n"
"s_mov_b32 s61, 0x261000\n"
"s_mov_b32 m0, 0x1b800\n"
"s_nop 0\n"
"buffer_load_dwordx4 v209, s[36:39], s61 offen lds\n"
"s_mov_b32 m0, 0x1bb80\n"
"s_nop 0\n"
"buffer_load_dwordx4 v209, s[36:39], s61 offen offset:128 lds\n"
"s_mov_b32 s58, 0x200\n"
"s_mov_b32 m0, 0x25000\n"
"s_nop 0\n"
"buffer_load_dwordx4 v210, s[28:31], s58 offen lds\n"
"s_mov_b32 m0, 0x25400\n"
"s_nop 0\n"
"buffer_load_dwordx4 v211, s[28:31], s58 offen lds\n"
"s_mov_b32 s58, 0xda00\n"
"s_mov_b32 m0, 0x26800\n"
"s_nop 0\n"
"buffer_load_dwordx4 v210, s[28:31], s58 offen lds\n"
"s_mov_b32 m0, 0x26c00\n"
"s_nop 0\n"
"buffer_load_dwordx4 v211, s[28:31], s58 offen lds\n"
"s_waitcnt lgkmcnt(0)\n"
".Lk1_pc20a:\n"
"s_memrealtime s[72:73]\n"
"s_waitcnt lgkmcnt(0)\n"
"s_sub_u32 s73, s72, s65\n"
"s_cmp_lt_u32 s73, 128\n"
"s_cbranch_scc0 .Lk1_pc20b\n"
"s_sleep 1\n"
"s_branch .Lk1_pc20a\n"
".Lk1_pc20b:\n"
"s_mov_b32 s65, s72\n"
"s_add_u32 s53, s33, 0x1b000\n"
"s_and_b32 s53, s53, 0x3ffff\n"
"s_mov_b32 m0, 0x11000\n"
"s_nop 0\n"
"buffer_load_dwordx4 v208, s[24:27], s53 offen nt lds\n"
"buffer_load_dwordx4 v208, s[24:27], s53 offen offset:2048 nt lds\n"
"s_mov_b32 m0, 0x11380\n"
"s_nop 0\n"
"buffer_load_dwordx4 v208, s[24:27], s53 offen offset:128 nt lds\n"
"buffer_load_dwordx4 v208, s[24:27], s53 offen offset:2176 nt lds\n"
"s_add_u32 s53, s33, 0x1c000\n"
"s_and_b32 s53, s53, 0x3ffff\n"
"s_mov_b32 m0, 0x4000\n"
"s_nop 0\n"
"buffer_load_dwordx4 v206, s[20:23], s53 offen nt lds\n"
"buffer_load_dwordx4 v207, s[20:23], s53 offen offset:2048 nt lds\n"
"s_mov_b32 m0, 0x4380\n"
"s_nop 0\n"
"buffer_load_dwordx4 v206, s[20:23], s53 offen offset:128 nt lds\n"
"buffer_load_dwordx4 v207, s[20:23], s53 offen offset:2176 nt lds\n"
"s_add_u32 s53, s33, 0x1f000\n"
"s_and_b32 s53, s53, 0x3ffff\n"
"s_mov_b32 m0, 0x7000\n"
"s_nop 0\n"
"buffer_load_dwordx4 v206, s[20:23], s53 offen nt lds\n"
"buffer_load_dwordx4 v207, s[20:23], s53 offen offset:2048 nt lds\n"
"s_mov_b32 m0, 0x7380\n"
"s_nop 0\n"
"buffer_load_dwordx4 v206, s[20:23], s53 offen offset:128 nt lds\n"
"buffer_load_dwordx4 v207, s[20:23], s53 offen offset:2176 nt lds\n"
"s_waitcnt vmcnt(12)\n"
"s_waitcnt lgkmcnt(0)\n"
"s_barrier\n"
"s_mov_b32 s58, 0x1b200\n"
"s_mov_b32 m0, 0x21000\n"
"s_nop 0\n"
"buffer_load_dwordx4 v210, s[28:31], s58 offen lds\n"
"s_mov_b32 m0, 0x21400\n"
"s_nop 0\n"
"buffer_load_dwordx4 v211, s[28:31], s58 offen lds\n"
"s_mov_b32 s61, 0x281000\n"
"s_mov_b32 m0, 0x1d000\n"
"s_nop 0\n"
"buffer_load_dwordx4 v209, s[36:39], s61 offen lds\n"
"s_mov_b32 m0, 0x1d380\n"
"s_nop 0\n"
"buffer_load_dwordx4 v209, s[36:39], s61 offen offset:128 lds\n"
"s_mov_b32 s61, 0x2a9000\n"
"s_mov_b32 m0, 0x1e800\n"
"s_nop 0\n"
"buffer_load_dwordx4 v209, s[36:39], s61 offen lds\n"
"s_mov_b32 m0, 0x1eb80\n"
"s_nop 0\n"
"buffer_load_dwordx4 v209, s[36:39], s61 offen offset:128 lds\n"
"s_mov_b32 s61, 0x2c9000\n"
"s_mov_b32 m0, 0x20000\n"
"s_nop 0\n"
"buffer_load_dwordx4 v209, s[36:39], s61 offen lds\n"
"s_mov_b32 m0, 0x20380\n"
"s_nop 0\n"
"buffer_load_dwordx4 v209, s[36:39], s61 offen offset:128 lds\n"
"s_mov_b32 s58, 0x4a80\n"
"s_mov_b32 m0, 0x23000\n"
"s_nop 0\n"
"buffer_load_dwordx4 v210, s[28:31], s58 offen lds\n"
"s_mov_b32 m0, 0x23400\n"
"s_nop 0\n"
"buffer_load_dwordx4 v211, s[28:31], s58 offen lds\n"
"s_waitcnt lgkmcnt(0)\n"
".Lk1_pc21a:\n"
"s_memrealtime s[72:73]\n"
"s_waitcnt lgkmcnt(0)\n"
"s_sub_u32 s73, s72, s65\n"
"s_cmp_lt_u32 s73, 128\n"
"s_cbranch_scc0 .Lk1_pc21b\n"
"s_sleep 1\n"
"s_branch .Lk1_pc21a\n"
".Lk1_pc21b:\n"
"s_mov_b32 s65, s72\n"
"s_add_u32 s53, s33, 0x1f000\n"
"s_and_b32 s53, s53, 0x3ffff\n"
"s_mov_b32 m0, 0x15000\n"
"s_nop 0\n"
"buffer_load_dwordx4 v208, s[24:27], s53 offen nt lds\n"
"buffer_load_dwordx4 v208, s[24:27], s53 offen offset:2048 nt lds\n"
"s_mov_b32 m0, 0x15380\n"
"s_nop 0\n"
"buffer_load_dwordx4 v208, s[24:27], s53 offen offset:128 nt lds\n"
"buffer_load_dwordx4 v208, s[24:27], s53 offen offset:2176 nt lds\n"
"s_add_u32 s53, s33, 0x20000\n"
"s_and_b32 s53, s53, 0x3ffff\n"
"s_mov_b32 m0, 0x8000\n"
"s_nop 0\n"
"buffer_load_dwordx4 v206, s[20:23], s53 offen nt lds\n"
"buffer_load_dwordx4 v207, s[20:23], s53 offen offset:2048 nt lds\n"
"s_mov_b32 m0, 0x8380\n"
"s_nop 0\n"
"buffer_load_dwordx4 v206, s[20:23], s53 offen offset:128 nt lds\n"
"buffer_load_dwordx4 v207, s[20:23], s53 offen offset:2176 nt lds\n"
"s_add_u32 s53, s33, 0x23000\n"
"s_and_b32 s53, s53, 0x3ffff\n"
"s_mov_b32 m0, 0xb000\n"
"s_nop 0\n"
"buffer_load_dwordx4 v206, s[20:23], s53 offen nt lds\n"
"buffer_load_dwordx4 v207, s[20:23], s53 offen offset:2048 nt lds\n"
"s_mov_b32 m0, 0xb380\n"
"s_nop 0\n"
"buffer_load_dwordx4 v206, s[20:23], s53 offen offset:128 nt lds\n"
"buffer_load_dwordx4 v207, s[20:23], s53 offen offset:2176 nt lds\n"
"s_waitcnt vmcnt(12)\n"
"s_waitcnt lgkmcnt(0)\n"
"s_barrier\n"
"s_mov_b32 s58, 0x12280\n"
"s_mov_b32 m0, 0x24800\n"
"s_nop 0\n"
"buffer_load_dwordx4 v210, s[28:31], s58 offen lds\n"
"s_mov_b32 m0, 0x24c00\n"
"s_nop 0\n"
"buffer_load_dwordx4 v211, s[28:31], s58 offen lds\n"
"s_mov_b32 s58, 0x1fa80\n"
"s_mov_b32 m0, 0x26000\n"
"s_nop 0\n"
"buffer_load_dwordx4 v210, s[28:31], s58 offen lds\n"
"s_mov_b32 m0, 0x26400\n"
"s_nop 0\n"
"buffer_load_dwordx4 v211, s[28:31], s58 offen lds\n"
"s_waitcnt lgkmcnt(0)\n"
".Lk1_pc22a:\n"
"s_memrealtime s[72:73]\n"
"s_waitcnt lgkmcnt(0)\n"
"s_sub_u32 s73, s72, s65\n"
"s_cmp_lt_u32 s73, 128\n"
"s_cbranch_scc0 .Lk1_pc22b\n"
"s_sleep 1\n"
"s_branch .Lk1_pc22a\n"
".Lk1_pc22b:\n"
"s_mov_b32 s65, s72\n"
"s_add_u32 s53, s33, 0x23000\n"
"s_and_b32 s53, s53, 0x3ffff\n"
"s_mov_b32 m0, 0xd000\n"
"s_nop 0\n"
"buffer_load_dwordx4 v208, s[24:27], s53 offen nt lds\n"
"buffer_load_dwordx4 v208, s[24:27], s53 offen offset:2048 nt lds\n"
"s_mov_b32 m0, 0xd380\n"
"s_nop 0\n"
"buffer_load_dwordx4 v208, s[24:27], s53 offen offset:128 nt lds\n"
"buffer_load_dwordx4 v208, s[24:27], s53 offen offset:2176 nt lds\n"
"s_add_u32 s53, s33, 0x24000\n"
"s_and_b32 s53, s53, 0x3ffff\n"
"s_mov_b32 m0, 0\n"
"s_nop 0\n"
"buffer_load_dwordx4 v206, s[20:23], s53 offen nt lds\n"
"buffer_load_dwordx4 v207, s[20:23], s53 offen offset:2048 nt lds\n"
"s_mov_b32 m0, 0x380\n"
"s_nop 0\n"
"buffer_load_dwordx4 v206, s[20:23], s53 offen offset:128 nt lds\n"
"buffer_load_dwordx4 v207, s[20:23], s53 offen offset:2176 nt lds\n"
"s_add_u32 s53, s33, 0x27000\n"
"s_and_b32 s53, s53, 0x3ffff\n"
"s_mov_b32 m0, 0x3000\n"
"s_nop 0\n"
"buffer_load_dwordx4 v206, s[20:23], s53 offen nt lds\n"
"buffer_load_dwordx4 v207, s[20:23], s53 offen offset:2048 nt lds\n"
"s_mov_b32 m0, 0x3380\n"
"s_nop 0\n"
"buffer_load_dwordx4 v206, s[20:23], s53 offen offset:128 nt lds\n"
"buffer_load_dwordx4 v207, s[20:23], s53 offen offset:2176 nt lds\n"
"s_waitcnt vmcnt(12)\n"
"s_waitcnt lgkmcnt(0)\n"
"s_barrier\n"
"s_mov_b32 s61, 0x309000\n"
"s_mov_b32 m0, 0x1a000\n"
"s_nop 0\n"
"buffer_load_dwordx4 v209, s[36:39], s61 offen lds\n"
"s_mov_b32 m0, 0x1a380\n"
"s_nop 0\n"
"buffer_load_dwordx4 v209, s[36:39], s61 offen offset:128 lds\n"
"s_mov_b32 s61, 0x361000\n"
"s_mov_b32 m0, 0x1b800\n"
"s_nop 0\n"
"buffer_load_dwordx4 v209, s[36:39], s61 offen lds\n"
"s_mov_b32 m0, 0x1bb80\n"
"s_nop 0\n"
"buffer_load_dwordx4 v209, s[36:39], s61 offen offset:128 lds\n"
"s_mov_b32 s58, 0x300\n"
"s_mov_b32 m0, 0x27000\n"
"s_nop 0\n"
"buffer_load_dwordx4 v210, s[28:31], s58 offen lds\n"
"s_mov_b32 m0, 0x27400\n"
"s_nop 0\n"
"buffer_load_dwordx4 v211, s[28:31], s58 offen lds\n"
"s_mov_b32 s58, 0xdb00\n"
"s_mov_b32 m0, 0x21800\n"
"s_nop 0\n"
"buffer_load_dwordx4 v210, s[28:31], s58 offen lds\n"
"s_mov_b32 m0, 0x21c00\n"
"s_nop 0\n"
"buffer_load_dwordx4 v211, s[28:31], s58 offen lds\n"
"s_waitcnt lgkmcnt(0)\n"
".Lk1_pc23a:\n"
"s_memrealtime s[72:73]\n"
"s_waitcnt lgkmcnt(0)\n"
"s_sub_u32 s73, s72, s65\n"
"s_cmp_lt_u32 s73, 128\n"
"s_cbranch_scc0 .Lk1_pc23b\n"
"s_sleep 1\n"
"s_branch .Lk1_pc23a\n"
".Lk1_pc23b:\n"
"s_mov_b32 s65, s72\n"
"s_add_u32 s53, s33, 0x27000\n"
"s_and_b32 s53, s53, 0x3ffff\n"
"s_mov_b32 m0, 0x11000\n"
"s_nop 0\n"
"buffer_load_dwordx4 v208, s[24:27], s53 offen nt lds\n"
"buffer_load_dwordx4 v208, s[24:27], s53 offen offset:2048 nt lds\n"
"s_mov_b32 m0, 0x11380\n"
"s_nop 0\n"
"buffer_load_dwordx4 v208, s[24:27], s53 offen offset:128 nt lds\n"
"buffer_load_dwordx4 v208, s[24:27], s53 offen offset:2176 nt lds\n"
"s_add_u32 s53, s33, 0x28000\n"
"s_and_b32 s53, s53, 0x3ffff\n"
"s_mov_b32 m0, 0x4000\n"
"s_nop 0\n"
"buffer_load_dwordx4 v206, s[20:23], s53 offen nt lds\n"
"buffer_load_dwordx4 v207, s[20:23], s53 offen offset:2048 nt lds\n"
"s_mov_b32 m0, 0x4380\n"
"s_nop 0\n"
"buffer_load_dwordx4 v206, s[20:23], s53 offen offset:128 nt lds\n"
"buffer_load_dwordx4 v207, s[20:23], s53 offen offset:2176 nt lds\n"
"s_add_u32 s53, s33, 0x2b000\n"
"s_and_b32 s53, s53, 0x3ffff\n"
"s_mov_b32 m0, 0x7000\n"
"s_nop 0\n"
"buffer_load_dwordx4 v206, s[20:23], s53 offen nt lds\n"
"buffer_load_dwordx4 v207, s[20:23], s53 offen offset:2048 nt lds\n"
"s_mov_b32 m0, 0x7380\n"
"s_nop 0\n"
"buffer_load_dwordx4 v206, s[20:23], s53 offen offset:128 nt lds\n"
"buffer_load_dwordx4 v207, s[20:23], s53 offen offset:2176 nt lds\n"
"s_waitcnt vmcnt(12)\n"
"s_waitcnt lgkmcnt(0)\n"
"s_barrier\n"
"s_mov_b32 s58, 0x24300\n"
"s_mov_b32 m0, 0x24000\n"
"s_nop 0\n"
"buffer_load_dwordx4 v210, s[28:31], s58 offen lds\n"
"s_mov_b32 m0, 0x24400\n"
"s_nop 0\n"
"buffer_load_dwordx4 v211, s[28:31], s58 offen lds\n"
"s_mov_b32 s61, 0x389000\n"
"s_mov_b32 m0, 0x1e000\n"
"s_nop 0\n"
"buffer_load_dwordx4 v209, s[36:39], s61 offen lds\n"
"s_mov_b32 m0, 0x1e380\n"
"s_nop 0\n"
"buffer_load_dwordx4 v209, s[36:39], s61 offen offset:128 lds\n"
"s_mov_b32 s61, 0x3e1000\n"
"s_mov_b32 m0, 0x1f800\n"
"s_nop 0\n"
"buffer_load_dwordx4 v209, s[36:39], s61 offen lds\n"
"s_mov_b32 m0, 0x1fb80\n"
"s_nop 0\n"
"buffer_load_dwordx4 v209, s[36:39], s61 offen offset:128 lds\n"
"s_mov_b32 s58, 0x380\n"
"s_mov_b32 m0, 0x24800\n"
"s_nop 0\n"
"buffer_load_dwordx4 v210, s[28:31], s58 offen lds\n"
"s_mov_b32 m0, 0x24c00\n"
"s_nop 0\n"
"buffer_load_dwordx4 v211, s[28:31], s58 offen lds\n"
"s_mov_b32 s58, 0xdb80\n"
"s_mov_b32 m0, 0x26000\n"
"s_nop 0\n"
"buffer_load_dwordx4 v210, s[28:31], s58 offen lds\n"
"s_mov_b32 m0, 0x26400\n"
"s_nop 0\n"
"buffer_load_dwordx4 v211, s[28:31], s58 offen lds\n"
"s_waitcnt lgkmcnt(0)\n"
".Lk1_pc24a:\n"
"s_memrealtime s[72:73]\n"
"s_waitcnt lgkmcnt(0)\n"
"s_sub_u32 s73, s72, s65\n"
"s_cmp_lt_u32 s73, 128\n"
"s_cbranch_scc0 .Lk1_pc24b\n"
"s_sleep 1\n"
"s_branch .Lk1_pc24a\n"
".Lk1_pc24b:\n"
"s_mov_b32 s65, s72\n"
"s_add_u32 s53, s33, 0x2b000\n"
"s_and_b32 s53, s53, 0x3ffff\n"
"s_mov_b32 m0, 0x15000\n"
"s_nop 0\n"
"buffer_load_dwordx4 v208, s[24:27], s53 offen nt lds\n"
"buffer_load_dwordx4 v208, s[24:27], s53 offen offset:2048 nt lds\n"
"s_mov_b32 m0, 0x15380\n"
"s_nop 0\n"
"buffer_load_dwordx4 v208, s[24:27], s53 offen offset:128 nt lds\n"
"buffer_load_dwordx4 v208, s[24:27], s53 offen offset:2176 nt lds\n"
"s_add_u32 s53, s33, 0x2c000\n"
"s_and_b32 s53, s53, 0x3ffff\n"
"s_mov_b32 m0, 0x8000\n"
"s_nop 0\n"
"buffer_load_dwordx4 v206, s[20:23], s53 offen nt lds\n"
"buffer_load_dwordx4 v207, s[20:23], s53 offen offset:2048 nt lds\n"
"s_mov_b32 m0, 0x8380\n"
"s_nop 0\n"
"buffer_load_dwordx4 v206, s[20:23], s53 offen offset:128 nt lds\n"
"buffer_load_dwordx4 v207, s[20:23], s53 offen offset:2176 nt lds\n"
"s_add_u32 s53, s33, 0x2f000\n"
"s_and_b32 s53, s53, 0x3ffff\n"
"s_mov_b32 m0, 0xb000\n"
"s_nop 0\n"
"buffer_load_dwordx4 v206, s[20:23], s53 offen nt lds\n"
"buffer_load_dwordx4 v207, s[20:23], s53 offen offset:2048 nt lds\n"
"s_mov_b32 m0, 0xb380\n"
"s_nop 0\n"
"buffer_load_dwordx4 v206, s[20:23], s53 offen offset:128 nt lds\n"
"buffer_load_dwordx4 v207, s[20:23], s53 offen offset:2176 nt lds\n"
"s_waitcnt vmcnt(12)\n"
"s_waitcnt lgkmcnt(0)\n"
"s_barrier\n"
"s_mov_b32 s58, 0x1b380\n"
"s_mov_b32 m0, 0x27800\n"
"s_nop 0\n"
"buffer_load_dwordx4 v210, s[28:31], s58 offen lds\n"
"s_mov_b32 m0, 0x27c00\n"
"s_nop 0\n"
"buffer_load_dwordx4 v211, s[28:31], s58 offen lds\n"
"s_mov_b32 s61, 0x401000\n"
"s_mov_b32 m0, 0x19000\n"
"s_nop 0\n"
"buffer_load_dwordx4 v209, s[36:39], s61 offen lds\n"
"s_mov_b32 m0, 0x19380\n"
"s_nop 0\n"
"buffer_load_dwordx4 v209, s[36:39], s61 offen offset:128 lds\n"
"s_mov_b32 s61, 0x429000\n"
"s_mov_b32 m0, 0x1a800\n"
"s_nop 0\n"
"buffer_load_dwordx4 v209, s[36:39], s61 offen lds\n"
"s_mov_b32 m0, 0x1ab80\n"
"s_nop 0\n"
"buffer_load_dwordx4 v209, s[36:39], s61 offen offset:128 lds\n"
"s_mov_b32 s61, 0x449000\n"
"s_mov_b32 m0, 0x1c000\n"
"s_nop 0\n"
"buffer_load_dwordx4 v209, s[36:39], s61 offen lds\n"
"s_mov_b32 m0, 0x1c380\n"
"s_nop 0\n"
"buffer_load_dwordx4 v209, s[36:39], s61 offen offset:128 lds\n"
"s_mov_b32 s58, 0x4c00\n"
"s_mov_b32 m0, 0x22800\n"
"s_nop 0\n"
"buffer_load_dwordx4 v210, s[28:31], s58 offen lds\n"
"s_mov_b32 m0, 0x22c00\n"
"s_nop 0\n"
"buffer_load_dwordx4 v211, s[28:31], s58 offen lds\n"
"s_waitcnt lgkmcnt(0)\n"
".Lk1_pc25a:\n"
"s_memrealtime s[72:73]\n"
"s_waitcnt lgkmcnt(0)\n"
"s_sub_u32 s73, s72, s65\n"
"s_cmp_lt_u32 s73, 128\n"
"s_cbranch_scc0 .Lk1_pc25b\n"
"s_sleep 1\n"
"s_branch .Lk1_pc25a\n"
".Lk1_pc25b:\n"
"s_mov_b32 s65, s72\n"
"s_add_u32 s53, s33, 0x2f000\n"
"s_and_b32 s53, s53, 0x3ffff\n"
"s_mov_b32 m0, 0xd000\n"
"s_nop 0\n"
"buffer_load_dwordx4 v208, s[24:27], s53 offen nt lds\n"
"buffer_load_dwordx4 v208, s[24:27], s53 offen offset:2048 nt lds\n"
"s_mov_b32 m0, 0xd380\n"
"s_nop 0\n"
"buffer_load_dwordx4 v208, s[24:27], s53 offen offset:128 nt lds\n"
"buffer_load_dwordx4 v208, s[24:27], s53 offen offset:2176 nt lds\n"
"s_add_u32 s53, s33, 0x30000\n"
"s_and_b32 s53, s53, 0x3ffff\n"
"s_mov_b32 m0, 0\n"
"s_nop 0\n"
"buffer_load_dwordx4 v206, s[20:23], s53 offen nt lds\n"
"buffer_load_dwordx4 v207, s[20:23], s53 offen offset:2048 nt lds\n"
"s_mov_b32 m0, 0x380\n"
"s_nop 0\n"
"buffer_load_dwordx4 v206, s[20:23], s53 offen offset:128 nt lds\n"
"buffer_load_dwordx4 v207, s[20:23], s53 offen offset:2176 nt lds\n"
"s_add_u32 s53, s33, 0x33000\n"
"s_and_b32 s53, s53, 0x3ffff\n"
"s_mov_b32 m0, 0x3000\n"
"s_nop 0\n"
"buffer_load_dwordx4 v206, s[20:23], s53 offen nt lds\n"
"buffer_load_dwordx4 v207, s[20:23], s53 offen offset:2048 nt lds\n"
"s_mov_b32 m0, 0x3380\n"
"s_nop 0\n"
"buffer_load_dwordx4 v206, s[20:23], s53 offen offset:128 nt lds\n"
"buffer_load_dwordx4 v207, s[20:23], s53 offen offset:2176 nt lds\n"
"s_waitcnt vmcnt(12)\n"
"s_waitcnt lgkmcnt(0)\n"
"s_barrier\n"
"s_mov_b32 s58, 0x12400\n"
"s_mov_b32 m0, 0x24000\n"
"s_nop 0\n"
"buffer_load_dwordx4 v210, s[28:31], s58 offen lds\n"
"s_mov_b32 m0, 0x24400\n"
"s_nop 0\n"
"buffer_load_dwordx4 v211, s[28:31], s58 offen lds\n"
"s_mov_b32 s58, 0x1fc00\n"
"s_mov_b32 m0, 0x25800\n"
"s_nop 0\n"
"buffer_load_dwordx4 v210, s[28:31], s58 offen lds\n"
"s_mov_b32 m0, 0x25c00\n"
"s_nop 0\n"
"buffer_load_dwordx4 v211, s[28:31], s58 offen lds\n"
"s_waitcnt lgkmcnt(0)\n"
".Lk1_pc26a:\n"
"s_memrealtime s[72:73]\n"
"s_waitcnt lgkmcnt(0)\n"
"s_sub_u32 s73, s72, s65\n"
"s_cmp_lt_u32 s73, 128\n"
"s_cbranch_scc0 .Lk1_pc26b\n"
"s_sleep 1\n"
"s_branch .Lk1_pc26a\n"
".Lk1_pc26b:\n"
"s_mov_b32 s65, s72\n"
"s_add_u32 s53, s33, 0x33000\n"
"s_and_b32 s53, s53, 0x3ffff\n"
"s_mov_b32 m0, 0x11000\n"
"s_nop 0\n"
"buffer_load_dwordx4 v208, s[24:27], s53 offen nt lds\n"
"buffer_load_dwordx4 v208, s[24:27], s53 offen offset:2048 nt lds\n"
"s_mov_b32 m0, 0x11380\n"
"s_nop 0\n"
"buffer_load_dwordx4 v208, s[24:27], s53 offen offset:128 nt lds\n"
"buffer_load_dwordx4 v208, s[24:27], s53 offen offset:2176 nt lds\n"
"s_add_u32 s53, s33, 0x34000\n"
"s_and_b32 s53, s53, 0x3ffff\n"
"s_mov_b32 m0, 0x4000\n"
"s_nop 0\n"
"buffer_load_dwordx4 v206, s[20:23], s53 offen nt lds\n"
"buffer_load_dwordx4 v207, s[20:23], s53 offen offset:2048 nt lds\n"
"s_mov_b32 m0, 0x4380\n"
"s_nop 0\n"
"buffer_load_dwordx4 v206, s[20:23], s53 offen offset:128 nt lds\n"
"buffer_load_dwordx4 v207, s[20:23], s53 offen offset:2176 nt lds\n"
"s_add_u32 s53, s33, 0x37000\n"
"s_and_b32 s53, s53, 0x3ffff\n"
"s_mov_b32 m0, 0x7000\n"
"s_nop 0\n"
"buffer_load_dwordx4 v206, s[20:23], s53 offen nt lds\n"
"buffer_load_dwordx4 v207, s[20:23], s53 offen offset:2048 nt lds\n"
"s_mov_b32 m0, 0x7380\n"
"s_nop 0\n"
"buffer_load_dwordx4 v206, s[20:23], s53 offen offset:128 nt lds\n"
"buffer_load_dwordx4 v207, s[20:23], s53 offen offset:2176 nt lds\n"
"s_waitcnt vmcnt(12)\n"
"s_waitcnt lgkmcnt(0)\n"
"s_barrier\n"
"s_waitcnt lgkmcnt(0)\n"
".Lk1_pc27a:\n"
"s_memrealtime s[72:73]\n"
"s_waitcnt lgkmcnt(0)\n"
"s_sub_u32 s73, s72, s65\n"
"s_cmp_lt_u32 s73, 128\n"
"s_cbranch_scc0 .Lk1_pc27b\n"
"s_sleep 1\n"
"s_branch .Lk1_pc27a\n"
".Lk1_pc27b:\n"
"s_mov_b32 s65, s72\n"
"s_add_u32 s53, s33, 0x37000\n"
"s_and_b32 s53, s53, 0x3ffff\n"
"s_mov_b32 m0, 0x15000\n"
"s_nop 0\n"
"buffer_load_dwordx4 v208, s[24:27], s53 offen nt lds\n"
"buffer_load_dwordx4 v208, s[24:27], s53 offen offset:2048 nt lds\n"
"s_mov_b32 m0, 0x15380\n"
"s_nop 0\n"
"buffer_load_dwordx4 v208, s[24:27], s53 offen offset:128 nt lds\n"
"buffer_load_dwordx4 v208, s[24:27], s53 offen offset:2176 nt lds\n"
"s_add_u32 s53, s33, 0x38000\n"
"s_and_b32 s53, s53, 0x3ffff\n"
"s_mov_b32 m0, 0x8000\n"
"s_nop 0\n"
"buffer_load_dwordx4 v206, s[20:23], s53 offen nt lds\n"
"buffer_load_dwordx4 v207, s[20:23], s53 offen offset:2048 nt lds\n"
"s_mov_b32 m0, 0x8380\n"
"s_nop 0\n"
"buffer_load_dwordx4 v206, s[20:23], s53 offen offset:128 nt lds\n"
"buffer_load_dwordx4 v207, s[20:23], s53 offen offset:2176 nt lds\n"
"s_add_u32 s53, s33, 0x3b000\n"
"s_and_b32 s53, s53, 0x3ffff\n"
"s_mov_b32 m0, 0xb000\n"
"s_nop 0\n"
"buffer_load_dwordx4 v206, s[20:23], s53 offen nt lds\n"
"buffer_load_dwordx4 v207, s[20:23], s53 offen offset:2048 nt lds\n"
"s_mov_b32 m0, 0xb380\n"
"s_nop 0\n"
"buffer_load_dwordx4 v206, s[20:23], s53 offen offset:128 nt lds\n"
"buffer_load_dwordx4 v207, s[20:23], s53 offen offset:2176 nt lds\n"
"s_waitcnt vmcnt(12)\n"
"s_waitcnt lgkmcnt(0)\n"
"s_barrier\n"
"s_waitcnt lgkmcnt(0)\n"
".Lk1_pc28a:\n"
"s_memrealtime s[72:73]\n"
"s_waitcnt lgkmcnt(0)\n"
"s_sub_u32 s73, s72, s65\n"
"s_cmp_lt_u32 s73, 128\n"
"s_cbranch_scc0 .Lk1_pc28b\n"
"s_sleep 1\n"
"s_branch .Lk1_pc28a\n"
".Lk1_pc28b:\n"
"s_mov_b32 s65, s72\n"
"s_add_u32 s53, s33, 0x3b000\n"
"s_and_b32 s53, s53, 0x3ffff\n"
"s_mov_b32 m0, 0xd000\n"
"s_nop 0\n"
"buffer_load_dwordx4 v208, s[24:27], s53 offen nt lds\n"
"buffer_load_dwordx4 v208, s[24:27], s53 offen offset:2048 nt lds\n"
"s_mov_b32 m0, 0xd380\n"
"s_nop 0\n"
"buffer_load_dwordx4 v208, s[24:27], s53 offen offset:128 nt lds\n"
"buffer_load_dwordx4 v208, s[24:27], s53 offen offset:2176 nt lds\n"
"s_add_u32 s53, s33, 0x3c000\n"
"s_and_b32 s53, s53, 0x3ffff\n"
"s_mov_b32 m0, 0\n"
"s_nop 0\n"
"buffer_load_dwordx4 v206, s[20:23], s53 offen nt lds\n"
"buffer_load_dwordx4 v207, s[20:23], s53 offen offset:2048 nt lds\n"
"s_mov_b32 m0, 0x380\n"
"s_nop 0\n"
"buffer_load_dwordx4 v206, s[20:23], s53 offen offset:128 nt lds\n"
"buffer_load_dwordx4 v207, s[20:23], s53 offen offset:2176 nt lds\n"
"s_add_u32 s53, s33, 0x3f000\n"
"s_and_b32 s53, s53, 0x3ffff\n"
"s_mov_b32 m0, 0x3000\n"
"s_nop 0\n"
"buffer_load_dwordx4 v206, s[20:23], s53 offen nt lds\n"
"buffer_load_dwordx4 v207, s[20:23], s53 offen offset:2048 nt lds\n"
"s_mov_b32 m0, 0x3380\n"
"s_nop 0\n"
"buffer_load_dwordx4 v206, s[20:23], s53 offen offset:128 nt lds\n"
"buffer_load_dwordx4 v207, s[20:23], s53 offen offset:2176 nt lds\n"
"s_waitcnt vmcnt(12)\n"
"s_waitcnt lgkmcnt(0)\n"
"s_barrier\n"
"s_waitcnt lgkmcnt(0)\n"
".Lk1_pc29a:\n"
"s_memrealtime s[72:73]\n"
"s_waitcnt lgkmcnt(0)\n"
"s_sub_u32 s73, s72, s65\n"
"s_cmp_lt_u32 s73, 128\n"
"s_cbranch_scc0 .Lk1_pc29b\n"
"s_sleep 1\n"
"s_branch .Lk1_pc29a\n"
".Lk1_pc29b:\n"
"s_mov_b32 s65, s72\n"
"s_add_u32 s53, s33, 0x3f000\n"
"s_and_b32 s53, s53, 0x3ffff\n"
"s_mov_b32 m0, 0x11000\n"
"s_nop 0\n"
"buffer_load_dwordx4 v208, s[24:27], s53 offen nt lds\n"
"buffer_load_dwordx4 v208, s[24:27], s53 offen offset:2048 nt lds\n"
"s_mov_b32 m0, 0x11380\n"
"s_nop 0\n"
"buffer_load_dwordx4 v208, s[24:27], s53 offen offset:128 nt lds\n"
"buffer_load_dwordx4 v208, s[24:27], s53 offen offset:2176 nt lds\n"
"s_add_u32 s53, s33, 0\n"
"s_and_b32 s53, s53, 0x3ffff\n"
"s_add_u32 s53, s53, 0x400000\n"
"s_mov_b32 m0, 0x4000\n"
"s_nop 0\n"
"buffer_load_dwordx4 v206, s[20:23], s53 offen nt lds\n"
"buffer_load_dwordx4 v207, s[20:23], s53 offen offset:2048 nt lds\n"
"s_mov_b32 m0, 0x4380\n"
"s_nop 0\n"
"buffer_load_dwordx4 v206, s[20:23], s53 offen offset:128 nt lds\n"
"buffer_load_dwordx4 v207, s[20:23], s53 offen offset:2176 nt lds\n"
"s_add_u32 s53, s33, 0x3000\n"
"s_and_b32 s53, s53, 0x3ffff\n"
"s_add_u32 s53, s53, 0x400000\n"
"s_mov_b32 m0, 0x7000\n"
"s_nop 0\n"
"buffer_load_dwordx4 v206, s[20:23], s53 offen nt lds\n"
"buffer_load_dwordx4 v207, s[20:23], s53 offen offset:2048 nt lds\n"
"s_mov_b32 m0, 0x7380\n"
"s_nop 0\n"
"buffer_load_dwordx4 v206, s[20:23], s53 offen offset:128 nt lds\n"
"buffer_load_dwordx4 v207, s[20:23], s53 offen offset:2176 nt lds\n"
"s_waitcnt vmcnt(12)\n"
"s_waitcnt lgkmcnt(0)\n"
"s_barrier\n"
"v_lshlrev_b32 v247, 4, v238\n"
"s_mul_i32 s60, s44, 0x3000\n"
"s_add_u32 s60, s60, 0x19000\n"
"v_add_u32 v247, s60, v247\n"
"ds_read_b128 v[96:99], v247\n"
"ds_read_b128 v[100:103], v247 offset:1024\n"
"ds_read_b128 v[104:107], v247 offset:2048\n"
"ds_read_b128 v[108:111], v247 offset:3072\n"
"ds_read_b128 v[112:115], v247 offset:4096\n"
"ds_read_b128 v[116:119], v247 offset:5120\n"
"ds_read_b128 v[120:123], v247 offset:6144\n"
"ds_read_b128 v[124:127], v247 offset:7168\n"
"ds_read_b128 v[128:131], v247 offset:8192\n"
"ds_read_b128 v[132:135], v247 offset:9216\n"
"ds_read_b128 v[136:139], v247 offset:10240\n"
"ds_read_b128 v[140:143], v247 offset:11264\n"
"v_add_u32 v252, 0, v248\n"
"s_waitcnt lgkmcnt(0)\n"
".Lk1_pc30a:\n"
"s_memrealtime s[72:73]\n"
"s_waitcnt lgkmcnt(0)\n"
"s_sub_u32 s73, s72, s65\n"
"s_cmp_lt_u32 s73, 128\n"
"s_cbranch_scc0 .Lk1_pc30b\n"
"s_sleep 1\n"
"s_branch .Lk1_pc30a\n"
".Lk1_pc30b:\n"
"s_mov_b32 s65, s72\n"
"s_add_u32 s53, s33, 0x3000\n"
"s_and_b32 s53, s53, 0x3ffff\n"
"s_add_u32 s53, s53, 0x400000\n"
"s_mov_b32 m0, 0x15000\n"
"s_nop 0\n"
"buffer_load_dwordx4 v208, s[24:27], s53 offen nt lds\n"
"buffer_load_dwordx4 v208, s[24:27], s53 offen offset:2048 nt lds\n"
"s_mov_b32 m0, 0x15380\n"
"s_nop 0\n"
"buffer_load_dwordx4 v208, s[24:27], s53 offen offset:128 nt lds\n"
"buffer_load_dwordx4 v208, s[24:27], s53 offen offset:2176 nt lds\n"
"s_add_u32 s53, s33, 0x4000\n"
"s_and_b32 s53, s53, 0x3ffff\n"
"s_add_u32 s53, s53, 0x400000\n"
"s_mov_b32 m0, 0x8000\n"
"s_nop 0\n"
"buffer_load_dwordx4 v206, s[20:23], s53 offen nt lds\n"
"buffer_load_dwordx4 v207, s[20:23], s53 offen offset:2048 nt lds\n"
"s_mov_b32 m0, 0x8380\n"
"s_nop 0\n"
"buffer_load_dwordx4 v206, s[20:23], s53 offen offset:128 nt lds\n"
"buffer_load_dwordx4 v207, s[20:23], s53 offen offset:2176 nt lds\n"
"s_add_u32 s53, s33, 0x7000\n"
"s_and_b32 s53, s53, 0x3ffff\n"
"s_add_u32 s53, s53, 0x400000\n"
"s_mov_b32 m0, 0xb000\n"
"s_nop 0\n"
"buffer_load_dwordx4 v206, s[20:23], s53 offen nt lds\n"
"buffer_load_dwordx4 v207, s[20:23], s53 offen offset:2048 nt lds\n"
"s_mov_b32 m0, 0xb380\n"
"s_nop 0\n"
"buffer_load_dwordx4 v206, s[20:23], s53 offen offset:128 nt lds\n"
"buffer_load_dwordx4 v207, s[20:23], s53 offen offset:2176 nt lds\n"
"s_waitcnt vmcnt(12)\n"
"ds_read_b128 v[184:187], v252\n"
"ds_read_b128 v[192:195], v252 offset:512\n"
"ds_read_b128 v[200:203], v252 offset:1024\n"
"ds_read_b128 v[220:223], v252 offset:1536\n"
"v_and_b32 v247, 15, v238\n"
"v_cmp_eq_u32 vcc, 0, v247\n"
"v_mov_b32 v247, 0x3f803f80\n"
"s_nop 0\n"
"v_cndmask_b32 v240, 0, v247, vcc\n"
"v_cndmask_b32 v241, 0, v247, vcc\n"
"v_cndmask_b32 v242, 0, v247, vcc\n"
"v_cndmask_b32 v243, 0, v247, vcc\n"
"s_waitcnt lgkmcnt(3)\n"
"v_mfma_f32_16x16x32_bf16 v[160:163], v[184:187], v[96:99], 0\n"
"v_mfma_f32_16x16x32_bf16 v[168:171], v[184:187], v[112:115], 0\n"
"v_mfma_f32_16x16x32_bf16 v[176:179], v[184:187], v[128:131], 0\n"
"s_waitcnt lgkmcnt(2)\n"
"v_mfma_f32_16x16x32_bf16 v[160:163], v[192:195], v[100:103], v[160:163]\n"
"v_mfma_f32_16x16x32_bf16 v[168:171], v[192:195], v[116:119], v[168:171]\n"
"v_mfma_f32_16x16x32_bf16 v[176:179], v[192:195], v[132:135], v[176:179]\n"
"s_waitcnt lgkmcnt(1)\n"
"v_mfma_f32_16x16x32_bf16 v[160:163], v[200:203], v[104:107], v[160:163]\n"
"v_mfma_f32_16x16x32_bf16 v[168:171], v[200:203], v[120:123], v[168:171]\n"
"v_mfma_f32_16x16x32_bf16 v[176:179], v[200:203], v[136:139], v[176:179]\n"
"s_waitcnt lgkmcnt(0)\n"
"v_mfma_f32_16x16x32_bf16 v[160:163], v[220:223], v[108:111], v[160:163]\n"
"v_mfma_f32_16x16x32_bf16 v[168:171], v[220:223], v[124:127], v[168:171]\n"
"v_mfma_f32_16x16x32_bf16 v[176:179], v[220:223], v[140:143], v[176:179]\n"
"v_mov_b32 v156, 0\n"
"v_mov_b32 v157, 0\n"
"v_mov_b32 v158, 0\n"
"v_mov_b32 v159, 0\n"
"v_mov_b32 v164, 0\n"
"v_mov_b32 v165, 0\n"
"v_mov_b32 v166, 0\n"
"v_mov_b32 v167, 0\n"
"v_mov_b32 v172, 0\n"
"v_mov_b32 v173, 0\n"
"v_mov_b32 v174, 0\n"
"v_mov_b32 v175, 0\n"
"v_mov_b32 v180, 0\n"
"v_mov_b32 v181, 0\n"
"v_mov_b32 v182, 0\n"
"v_mov_b32 v183, 0\n"
"v_mov_b32 v188, 0\n"
"v_mov_b32 v189, 0\n"
"v_mov_b32 v190, 0\n"
"v_mov_b32 v191, 0\n"
"v_mov_b32 v196, 0\n"
"v_mov_b32 v197, 0\n"
"v_mov_b32 v198, 0\n"
"v_mov_b32 v199, 0\n"
"v_max3_f32 v228, v160, v161, v162\n"
"v_max_f32 v228, v228, v163\n"
"v_max3_f32 v232, v168, v169, v170\n"
"v_max_f32 v232, v232, v171\n"
"v_max3_f32 v236, v176, v177, v178\n"
"v_max_f32 v236, v236, v179\n"
"v_mov_b32 v204, v228\n"
"v_mov_b32 v205, v228\n"
"s_nop 1\n"
"v_permlane32_swap_b32 v204, v205\n"
"v_max_f32 v228, v204, v205\n"
"v_mov_b32 v204, v228\n"
"v_mov_b32 v205, v228\n"
"s_nop 1\n"
"v_permlane16_swap_b32 v204, v205\n"
"v_max_f32 v228, v204, v205\n"
"v_sub_f32 v144, 0, v228\n"
"v_sub_f32 v145, 0, v228\n"
"v_sub_f32 v146, 0, v228\n"
"v_sub_f32 v147, 0, v228\n"
"v_mov_b32 v228, 0\n"
"v_mov_b32 v204, v232\n"
"v_mov_b32 v205, v232\n"
"s_nop 1\n"
"v_permlane32_swap_b32 v204, v205\n"
"v_max_f32 v232, v204, v205\n"
"v_mov_b32 v204, v232\n"
"v_mov_b32 v205, v232\n"
"s_nop 1\n"
"v_permlane16_swap_b32 v204, v205\n"
"v_max_f32 v232, v204, v205\n"
"v_sub_f32 v148, 0, v232\n"
"v_sub_f32 v149, 0, v232\n"
"v_sub_f32 v150, 0, v232\n"
"v_sub_f32 v151, 0, v232\n"
"v_mov_b32 v232, 0\n"
"v_mov_b32 v204, v236\n"
"v_mov_b32 v205, v236\n"
"s_nop 1\n"
"v_permlane32_swap_b32 v204, v205\n"
"v_max_f32 v236, v204, v205\n"
"v_mov_b32 v204, v236\n"
"v_mov_b32 v205, v236\n"
"s_nop 1\n"
"v_permlane16_swap_b32 v204, v205\n"
"v_max_f32 v236, v204, v205\n"
"v_sub_f32 v152, 0, v236\n"
"v_sub_f32 v153, 0, v236\n"
"v_sub_f32 v154, 0, v236\n"
"v_sub_f32 v155, 0, v236\n"
"v_mov_b32 v236, 0\n"
"v_mov_b32 v216, 0\n"
"v_mov_b32 v217, 0\n"
"v_mov_b32 v218, 0\n"
"v_mov_b32 v219, 0\n"
"v_mov_b32 v220, 0\n"
"v_mov_b32 v221, 0\n"
"v_mov_b32 v222, 0\n"
"v_mov_b32 v223, 0\n"
"v_mov_b32 v224, 0\n"
"v_mov_b32 v225, 0\n"
"v_mov_b32 v226, 0\n"
"v_mov_b32 v227, 0\n"
"s_waitcnt lgkmcnt(0)\n"
"s_barrier\n"
"s_branch .Lk1_attn\n"
".Lk1_ld2:\n"
"s_mov_b32 s61, 0x1000\n"
"s_mov_b32 m0, 0x19000\n"
"s_nop 0\n"
"buffer_load_dwordx4 v209, s[36:39], s61 offen lds\n"
"s_mov_b32 m0, 0x19380\n"
"s_nop 0\n"
"buffer_load_dwordx4 v209, s[36:39], s61 offen offset:128 lds\n"
"s_mov_b32 s61, 0x29000\n"
"s_mov_b32 m0, 0x1a800\n"
"s_nop 0\n"
"buffer_load_dwordx4 v209, s[36:39], s61 offen lds\n"
"s_mov_b32 m0, 0x1ab80\n"
"s_nop 0\n"
"buffer_load_dwordx4 v209, s[36:39], s61 offen offset:128 lds\n"
"s_mov_b32 s61, 0x49000\n"
"s_mov_b32 m0, 0x1c000\n"
"s_nop 0\n"
"buffer_load_dwordx4 v209, s[36:39], s61 offen lds\n"
"s_mov_b32 m0, 0x1c380\n"
"s_nop 0\n"
"buffer_load_dwordx4 v209, s[36:39], s61 offen offset:128 lds\n"
"s_mov_b32 s58, 0x4800\n"
"s_mov_b32 m0, 0x21800\n"
"s_nop 0\n"
"buffer_load_dwordx4 v210, s[28:31], s58 offen lds\n"
"s_mov_b32 m0, 0x21c00\n"
"s_nop 0\n"
"buffer_load_dwordx4 v211, s[28:31], s58 offen lds\n"
"s_mov_b32 s58, 0x12000\n"
"s_mov_b32 m0, 0x23000\n"
"s_nop 0\n"
"buffer_load_dwordx4 v210, s[28:31], s58 offen lds\n"
"s_mov_b32 m0, 0x23400\n"
"s_nop 0\n"
"buffer_load_dwordx4 v211, s[28:31], s58 offen lds\n"
"s_waitcnt vmcnt(0)\n"
"s_waitcnt lgkmcnt(0)\n"
"s_barrier\n"
"s_mov_b32 s58, 0x1b000\n"
"s_mov_b32 m0, 0x24000\n"
"s_nop 0\n"
"buffer_load_dwordx4 v210, s[28:31], s58 offen lds\n"
"s_mov_b32 m0, 0x24400\n"
"s_nop 0\n"
"buffer_load_dwordx4 v211, s[28:31], s58 offen lds\n"
"s_mov_b32 s61, 0x81000\n"
"s_mov_b32 m0, 0x1d000\n"
"s_nop 0\n"
"buffer_load_dwordx4 v209, s[36:39], s61 offen lds\n"
"s_mov_b32 m0, 0x1d380\n"
"s_nop 0\n"
"buffer_load_dwordx4 v209, s[36:39], s61 offen offset:128 lds\n"
"s_mov_b32 s61, 0xa9000\n"
"s_mov_b32 m0, 0x1e800\n"
"s_nop 0\n"
"buffer_load_dwordx4 v209, s[36:39], s61 offen lds\n"
"s_mov_b32 m0, 0x1eb80\n"
"s_nop 0\n"
"buffer_load_dwordx4 v209, s[36:39], s61 offen offset:128 lds\n"
"s_mov_b32 s61, 0xc9000\n"
"s_mov_b32 m0, 0x20000\n"
"s_nop 0\n"
"buffer_load_dwordx4 v209, s[36:39], s61 offen lds\n"
"s_mov_b32 m0, 0x20380\n"
"s_nop 0\n"
"buffer_load_dwordx4 v209, s[36:39], s61 offen offset:128 lds\n"
"s_mov_b32 s58, 0x4880\n"
"s_mov_b32 m0, 0x26000\n"
"s_nop 0\n"
"buffer_load_dwordx4 v210, s[28:31], s58 offen lds\n"
"s_mov_b32 m0, 0x26400\n"
"s_nop 0\n"
"buffer_load_dwordx4 v211, s[28:31], s58 offen lds\n"
"s_waitcnt lgkmcnt(0)\n"
".Lk1_pc31a:\n"
"s_memrealtime s[72:73]\n"
"s_waitcnt lgkmcnt(0)\n"
"s_sub_u32 s73, s72, s65\n"
"s_cmp_lt_u32 s73, 128\n"
"s_cbranch_scc0 .Lk1_pc31b\n"
"s_sleep 1\n"
"s_branch .Lk1_pc31a\n"
".Lk1_pc31b:\n"
"s_mov_b32 s65, s72\n"
"s_add_u32 s53, s33, 0xc000\n"
"s_and_b32 s53, s53, 0x3ffff\n"
"s_mov_b32 m0, 0xe000\n"
"s_nop 0\n"
"buffer_load_dwordx4 v208, s[24:27], s53 offen nt lds\n"
"buffer_load_dwordx4 v208, s[24:27], s53 offen offset:2048 nt lds\n"
"s_mov_b32 m0, 0xe380\n"
"s_nop 0\n"
"buffer_load_dwordx4 v208, s[24:27], s53 offen offset:128 nt lds\n"
"buffer_load_dwordx4 v208, s[24:27], s53 offen offset:2176 nt lds\n"
"s_add_u32 s53, s33, 0xd000\n"
"s_and_b32 s53, s53, 0x3ffff\n"
"s_mov_b32 m0, 0x1000\n"
"s_nop 0\n"
"buffer_load_dwordx4 v206, s[20:23], s53 offen nt lds\n"
"buffer_load_dwordx4 v207, s[20:23], s53 offen offset:2048 nt lds\n"
"s_mov_b32 m0, 0x1380\n"
"s_nop 0\n"
"buffer_load_dwordx4 v206, s[20:23], s53 offen offset:128 nt lds\n"
"buffer_load_dwordx4 v207, s[20:23], s53 offen offset:2176 nt lds\n"
"s_waitcnt vmcnt(8)\n"
"s_waitcnt lgkmcnt(0)\n"
"s_barrier\n"
"s_mov_b32 s58, 0x12080\n"
"s_mov_b32 m0, 0x27800\n"
"s_nop 0\n"
"buffer_load_dwordx4 v210, s[28:31], s58 offen lds\n"
"s_mov_b32 m0, 0x27c00\n"
"s_nop 0\n"
"buffer_load_dwordx4 v211, s[28:31], s58 offen lds\n"
"s_mov_b32 s58, 0x1f880\n"
"s_mov_b32 m0, 0x22000\n"
"s_nop 0\n"
"buffer_load_dwordx4 v210, s[28:31], s58 offen lds\n"
"s_mov_b32 m0, 0x22400\n"
"s_nop 0\n"
"buffer_load_dwordx4 v211, s[28:31], s58 offen lds\n"
"s_mov_b32 s61, 0x121000\n"
"s_mov_b32 m0, 0x19800\n"
"s_nop 0\n"
"buffer_load_dwordx4 v209, s[36:39], s61 offen lds\n"
"s_mov_b32 m0, 0x19b80\n"
"s_nop 0\n"
"buffer_load_dwordx4 v209, s[36:39], s61 offen offset:128 lds\n"
"s_mov_b32 s61, 0x141000\n"
"s_mov_b32 m0, 0x1b000\n"
"s_nop 0\n"
"buffer_load_dwordx4 v209, s[36:39], s61 offen lds\n"
"s_mov_b32 m0, 0x1b380\n"
"s_nop 0\n"
"buffer_load_dwordx4 v209, s[36:39], s61 offen offset:128 lds\n"
"s_mov_b32 s61, 0x169000\n"
"s_mov_b32 m0, 0x1c800\n"
"s_nop 0\n"
"buffer_load_dwordx4 v209, s[36:39], s61 offen lds\n"
"s_mov_b32 m0, 0x1cb80\n"
"s_nop 0\n"
"buffer_load_dwordx4 v209, s[36:39], s61 offen offset:128 lds\n"
"s_waitcnt lgkmcnt(0)\n"
".Lk1_pc32a:\n"
"s_memrealtime s[72:73]\n"
"s_waitcnt lgkmcnt(0)\n"
"s_sub_u32 s73, s72, s65\n"
"s_cmp_lt_u32 s73, 128\n"
"s_cbranch_scc0 .Lk1_pc32b\n"
"s_sleep 1\n"
"s_branch .Lk1_pc32a\n"
".Lk1_pc32b:\n"
"s_mov_b32 s65, s72\n"
"s_add_u32 s53, s33, 0x10000\n"
"s_and_b32 s53, s53, 0x3ffff\n"
"s_mov_b32 m0, 0x12000\n"
"s_nop 0\n"
"buffer_load_dwordx4 v208, s[24:27], s53 offen nt lds\n"
"buffer_load_dwordx4 v208, s[24:27], s53 offen offset:2048 nt lds\n"
"s_mov_b32 m0, 0x12380\n"
"s_nop 0\n"
"buffer_load_dwordx4 v208, s[24:27], s53 offen offset:128 nt lds\n"
"buffer_load_dwordx4 v208, s[24:27], s53 offen offset:2176 nt lds\n"
"s_add_u32 s53, s33, 0x11000\n"
"s_and_b32 s53, s53, 0x3ffff\n"
"s_mov_b32 m0, 0x5000\n"
"s_nop 0\n"
"buffer_load_dwordx4 v206, s[20:23], s53 offen nt lds\n"
"buffer_load_dwordx4 v207, s[20:23], s53 offen offset:2048 nt lds\n"
"s_mov_b32 m0, 0x5380\n"
"s_nop 0\n"
"buffer_load_dwordx4 v206, s[20:23], s53 offen offset:128 nt lds\n"
"buffer_load_dwordx4 v207, s[20:23], s53 offen offset:2176 nt lds\n"
"s_waitcnt vmcnt(8)\n"
"s_waitcnt lgkmcnt(0)\n"
"s_barrier\n"
"s_mov_b32 s58, 0x9100\n"
"s_mov_b32 m0, 0x24000\n"
"s_nop 0\n"
"buffer_load_dwordx4 v210, s[28:31], s58 offen lds\n"
"s_mov_b32 m0, 0x24400\n"
"s_nop 0\n"
"buffer_load_dwordx4 v211, s[28:31], s58 offen lds\n"
"s_mov_b32 s58, 0x16900\n"
"s_mov_b32 m0, 0x25800\n"
"s_nop 0\n"
"buffer_load_dwordx4 v210, s[28:31], s58 offen lds\n"
"s_mov_b32 m0, 0x25c00\n"
"s_nop 0\n"
"buffer_load_dwordx4 v211, s[28:31], s58 offen lds\n"
"s_mov_b32 s58, 0x24100\n"
"s_mov_b32 m0, 0x27000\n"
"s_nop 0\n"
"buffer_load_dwordx4 v210, s[28:31], s58 offen lds\n"
"s_mov_b32 m0, 0x27400\n"
"s_nop 0\n"
"buffer_load_dwordx4 v211, s[28:31], s58 offen lds\n"
"s_waitcnt lgkmcnt(0)\n"
".Lk1_pc33a:\n"
"s_memrealtime s[72:73]\n"
"s_waitcnt lgkmcnt(0)\n"
"s_sub_u32 s73, s72, s65\n"
"s_cmp_lt_u32 s73, 128\n"
"s_cbranch_scc0 .Lk1_pc33b\n"
"s_sleep 1\n"
"s_branch .Lk1_pc33a\n"
".Lk1_pc33b:\n"
"s_mov_b32 s65, s72\n"
"s_add_u32 s53, s33, 0x14000\n"
"s_and_b32 s53, s53, 0x3ffff\n"
"s_mov_b32 m0, 0x16000\n"
"s_nop 0\n"
"buffer_load_dwordx4 v208, s[24:27], s53 offen nt lds\n"
"buffer_load_dwordx4 v208, s[24:27], s53 offen offset:2048 nt lds\n"
"s_mov_b32 m0, 0x16380\n"
"s_nop 0\n"
"buffer_load_dwordx4 v208, s[24:27], s53 offen offset:128 nt lds\n"
"buffer_load_dwordx4 v208, s[24:27], s53 offen offset:2176 nt lds\n"
"s_add_u32 s53, s33, 0x15000\n"
"s_and_b32 s53, s53, 0x3ffff\n"
"s_mov_b32 m0, 0x9000\n"
"s_nop 0\n"
"buffer_load_dwordx4 v206, s[20:23], s53 offen nt lds\n"
"buffer_load_dwordx4 v207, s[20:23], s53 offen offset:2048 nt lds\n"
"s_mov_b32 m0, 0x9380\n"
"s_nop 0\n"
"buffer_load_dwordx4 v206, s[20:23], s53 offen offset:128 nt lds\n"
"buffer_load_dwordx4 v207, s[20:23], s53 offen offset:2176 nt lds\n"
"s_waitcnt vmcnt(8)\n"
"s_waitcnt lgkmcnt(0)\n"
"s_barrier\n"
"s_mov_b32 s61, 0x181000\n"
"s_mov_b32 m0, 0x1d000\n"
"s_nop 0\n"
"buffer_load_dwordx4 v209, s[36:39], s61 offen lds\n"
"s_mov_b32 m0, 0x1d380\n"
"s_nop 0\n"
"buffer_load_dwordx4 v209, s[36:39], s61 offen offset:128 lds\n"
"s_mov_b32 s61, 0x1a9000\n"
"s_mov_b32 m0, 0x1e800\n"
"s_nop 0\n"
"buffer_load_dwordx4 v209, s[36:39], s61 offen lds\n"
"s_mov_b32 m0, 0x1eb80\n"
"s_nop 0\n"
"buffer_load_dwordx4 v209, s[36:39], s61 offen offset:128 lds\n"
"s_mov_b32 s61, 0x1c9000\n"
"s_mov_b32 m0, 0x20000\n"
"s_nop 0\n"
"buffer_load_dwordx4 v209, s[36:39], s61 offen lds\n"
"s_mov_b32 m0, 0x20380\n"
"s_nop 0\n"
"buffer_load_dwordx4 v209, s[36:39], s61 offen offset:128 lds\n"
"s_mov_b32 s58, 0x4980\n"
"s_mov_b32 m0, 0x21000\n"
"s_nop 0\n"
"buffer_load_dwordx4 v210, s[28:31], s58 offen lds\n"
"s_mov_b32 m0, 0x21400\n"
"s_nop 0\n"
"buffer_load_dwordx4 v211, s[28:31], s58 offen lds\n"
"s_mov_b32 s58, 0x12180\n"
"s_mov_b32 m0, 0x22800\n"
"s_nop 0\n"
"buffer_load_dwordx4 v210, s[28:31], s58 offen lds\n"
"s_mov_b32 m0, 0x22c00\n"
"s_nop 0\n"
"buffer_load_dwordx4 v211, s[28:31], s58 offen lds\n"
"s_waitcnt lgkmcnt(0)\n"
".Lk1_pc34a:\n"
"s_memrealtime s[72:73]\n"
"s_waitcnt lgkmcnt(0)\n"
"s_sub_u32 s73, s72, s65\n"
"s_cmp_lt_u32 s73, 128\n"
"s_cbranch_scc0 .Lk1_pc34b\n"
"s_sleep 1\n"
"s_branch .Lk1_pc34a\n"
".Lk1_pc34b:\n"
"s_mov_b32 s65, s72\n"
"s_add_u32 s53, s33, 0x18000\n"
"s_and_b32 s53, s53, 0x3ffff\n"
"s_mov_b32 m0, 0xe000\n"
"s_nop 0\n"
"buffer_load_dwordx4 v208, s[24:27], s53 offen nt lds\n"
"buffer_load_dwordx4 v208, s[24:27], s53 offen offset:2048 nt lds\n"
"s_mov_b32 m0, 0xe380\n"
"s_nop 0\n"
"buffer_load_dwordx4 v208, s[24:27], s53 offen offset:128 nt lds\n"
"buffer_load_dwordx4 v208, s[24:27], s53 offen offset:2176 nt lds\n"
"s_add_u32 s53, s33, 0x19000\n"
"s_and_b32 s53, s53, 0x3ffff\n"
"s_mov_b32 m0, 0x1000\n"
"s_nop 0\n"
"buffer_load_dwordx4 v206, s[20:23], s53 offen nt lds\n"
"buffer_load_dwordx4 v207, s[20:23], s53 offen offset:2048 nt lds\n"
"s_mov_b32 m0, 0x1380\n"
"s_nop 0\n"
"buffer_load_dwordx4 v206, s[20:23], s53 offen offset:128 nt lds\n"
"buffer_load_dwordx4 v207, s[20:23], s53 offen offset:2176 nt lds\n"
"s_waitcnt vmcnt(8)\n"
"s_waitcnt lgkmcnt(0)\n"
"s_barrier\n"
"s_mov_b32 s58, 0x1b180\n"
"s_mov_b32 m0, 0x23800\n"
"s_nop 0\n"
"buffer_load_dwordx4 v210, s[28:31], s58 offen lds\n"
"s_mov_b32 m0, 0x23c00\n"
"s_nop 0\n"
"buffer_load_dwordx4 v211, s[28:31], s58 offen lds\n"
"s_mov_b32 s61, 0x201000\n"
"s_mov_b32 m0, 0x19000\n"
"s_nop 0\n"
"buffer_load_dwordx4 v209, s[36:39], s61 offen lds\n"
"s_mov_b32 m0, 0x19380\n"
"s_nop 0\n"
"buffer_load_dwordx4 v209, s[36:39], s61 offen offset:128 lds\n"
"s_mov_b32 s61, 0x229000\n"
"s_mov_b32 m0, 0x1a800\n"
"s_nop 0\n"
"buffer_load_dwordx4 v209, s[36:39], s61 offen lds\n"
"s_mov_b32 m0, 0x1ab80\n"
"s_nop 0\n"
"buffer_load_dwordx4 v209, s[36:39], s61 offen offset:128 lds\n"
"s_mov_b32 s61, 0x249000\n"
"s_mov_b32 m0, 0x1c000\n"
"s_nop 0\n"
"buffer_load_dwordx4 v209, s[36:39], s61 offen lds\n"
"s_mov_b32 m0, 0x1c380\n"
"s_nop 0\n"
"buffer_load_dwordx4 v209, s[36:39], s61 offen offset:128 lds\n"
"s_mov_b32 s58, 0x4a00\n"
"s_mov_b32 m0, 0x25800\n"
"s_nop 0\n"
"buffer_load_dwordx4 v210, s[28:31], s58 offen lds\n"
"s_mov_b32 m0, 0x25c00\n"
"s_nop 0\n"
"buffer_load_dwordx4 v211, s[28:31], s58 offen lds\n"
"s_waitcnt lgkmcnt(0)\n"
".Lk1_pc35a:\n"
"s_memrealtime s[72:73]\n"
"s_waitcnt lgkmcnt(0)\n"
"s_sub_u32 s73, s72, s65\n"
"s_cmp_lt_u32 s73, 128\n"
"s_cbranch_scc0 .Lk1_pc35b\n"
"s_sleep 1\n"
"s_branch .Lk1_pc35a\n"
".Lk1_pc35b:\n"
"s_mov_b32 s65, s72\n"
"s_add_u32 s53, s33, 0x1c000\n"
"s_and_b32 s53, s53, 0x3ffff\n"
"s_mov_b32 m0, 0x12000\n"
"s_nop 0\n"
"buffer_load_dwordx4 v208, s[24:27], s53 offen nt lds\n"
"buffer_load_dwordx4 v208, s[24:27], s53 offen offset:2048 nt lds\n"
"s_mov_b32 m0, 0x12380\n"
"s_nop 0\n"
"buffer_load_dwordx4 v208, s[24:27], s53 offen offset:128 nt lds\n"
"buffer_load_dwordx4 v208, s[24:27], s53 offen offset:2176 nt lds\n"
"s_add_u32 s53, s33, 0x1d000\n"
"s_and_b32 s53, s53, 0x3ffff\n"
"s_mov_b32 m0, 0x5000\n"
"s_nop 0\n"
"buffer_load_dwordx4 v206, s[20:23], s53 offen nt lds\n"
"buffer_load_dwordx4 v207, s[20:23], s53 offen offset:2048 nt lds\n"
"s_mov_b32 m0, 0x5380\n"
"s_nop 0\n"
"buffer_load_dwordx4 v206, s[20:23], s53 offen offset:128 nt lds\n"
"buffer_load_dwordx4 v207, s[20:23], s53 offen offset:2176 nt lds\n"
"s_waitcnt vmcnt(8)\n"
"s_waitcnt lgkmcnt(0)\n"
"s_barrier\n"
"s_mov_b32 s58, 0x12200\n"
"s_mov_b32 m0, 0x27000\n"
"s_nop 0\n"
"buffer_load_dwordx4 v210, s[28:31], s58 offen lds\n"
"s_mov_b32 m0, 0x27400\n"
"s_nop 0\n"
"buffer_load_dwordx4 v211, s[28:31], s58 offen lds\n"
"s_mov_b32 s58, 0x1fa00\n"
"s_mov_b32 m0, 0x21800\n"
"s_nop 0\n"
"buffer_load_dwordx4 v210, s[28:31], s58 offen lds\n"
"s_mov_b32 m0, 0x21c00\n"
"s_nop 0\n"
"buffer_load_dwordx4 v211, s[28:31], s58 offen lds\n"
"s_mov_b32 s61, 0x2a1000\n"
"s_mov_b32 m0, 0x1d800\n"
"s_nop 0\n"
"buffer_load_dwordx4 v209, s[36:39], s61 offen lds\n"
"s_mov_b32 m0, 0x1db80\n"
"s_nop 0\n"
"buffer_load_dwordx4 v209, s[36:39], s61 offen offset:128 lds\n"
"s_mov_b32 s61, 0x2c1000\n"
"s_mov_b32 m0, 0x1f000\n"
"s_nop 0\n"
"buffer_load_dwordx4 v209, s[36:39], s61 offen lds\n"
"s_mov_b32 m0, 0x1f380\n"
"s_nop 0\n"
"buffer_load_dwordx4 v209, s[36:39], s61 offen offset:128 lds\n"
"s_mov_b32 s61, 0x2e9000\n"
"s_mov_b32 m0, 0x20800\n"
"s_nop 0\n"
"buffer_load_dwordx4 v209, s[36:39], s61 offen lds\n"
"s_mov_b32 m0, 0x20b80\n"
"s_nop 0\n"
"buffer_load_dwordx4 v209, s[36:39], s61 offen offset:128 lds\n"
"s_waitcnt lgkmcnt(0)\n"
".Lk1_pc36a:\n"
"s_memrealtime s[72:73]\n"
"s_waitcnt lgkmcnt(0)\n"
"s_sub_u32 s73, s72, s65\n"
"s_cmp_lt_u32 s73, 128\n"
"s_cbranch_scc0 .Lk1_pc36b\n"
"s_sleep 1\n"
"s_branch .Lk1_pc36a\n"
".Lk1_pc36b:\n"
"s_mov_b32 s65, s72\n"
"s_add_u32 s53, s33, 0x20000\n"
"s_and_b32 s53, s53, 0x3ffff\n"
"s_mov_b32 m0, 0x16000\n"
"s_nop 0\n"
"buffer_load_dwordx4 v208, s[24:27], s53 offen nt lds\n"
"buffer_load_dwordx4 v208, s[24:27], s53 offen offset:2048 nt lds\n"
"s_mov_b32 m0, 0x16380\n"
"s_nop 0\n"
"buffer_load_dwordx4 v208, s[24:27], s53 offen offset:128 nt lds\n"
"buffer_load_dwordx4 v208, s[24:27], s53 offen offset:2176 nt lds\n"
"s_add_u32 s53, s33, 0x21000\n"
"s_and_b32 s53, s53, 0x3ffff\n"
"s_mov_b32 m0, 0x9000\n"
"s_nop 0\n"
"buffer_load_dwordx4 v206, s[20:23], s53 offen nt lds\n"
"buffer_load_dwordx4 v207, s[20:23], s53 offen offset:2048 nt lds\n"
"s_mov_b32 m0, 0x9380\n"
"s_nop 0\n"
"buffer_load_dwordx4 v206, s[20:23], s53 offen offset:128 nt lds\n"
"buffer_load_dwordx4 v207, s[20:23], s53 offen offset:2176 nt lds\n"
"s_waitcnt vmcnt(8)\n"
"s_waitcnt lgkmcnt(0)\n"
"s_barrier\n"
"s_mov_b32 s58, 0x9280\n"
"s_mov_b32 m0, 0x23800\n"
"s_nop 0\n"
"buffer_load_dwordx4 v210, s[28:31], s58 offen lds\n"
"s_mov_b32 m0, 0x23c00\n"
"s_nop 0\n"
"buffer_load_dwordx4 v211, s[28:31], s58 offen lds\n"
"s_mov_b32 s58, 0x16a80\n"
"s_mov_b32 m0, 0x25000\n"
"s_nop 0\n"
"buffer_load_dwordx4 v210, s[28:31], s58 offen lds\n"
"s_mov_b32 m0, 0x25400\n"
"s_nop 0\n"
"buffer_load_dwordx4 v211, s[28:31], s58 offen lds\n"
"s_mov_b32 s58, 0x24280\n"
"s_mov_b32 m0, 0x26800\n"
"s_nop 0\n"
"buffer_load_dwordx4 v210, s[28:31], s58 offen lds\n"
"s_mov_b32 m0, 0x26c00\n"
"s_nop 0\n"
"buffer_load_dwordx4 v211, s[28:31], s58 offen lds\n"
"s_waitcnt lgkmcnt(0)\n"
".Lk1_pc37a:\n"
"s_memrealtime s[72:73]\n"
"s_waitcnt lgkmcnt(0)\n"
"s_sub_u32 s73, s72, s65\n"
"s_cmp_lt_u32 s73, 128\n"
"s_cbranch_scc0 .Lk1_pc37b\n"
"s_sleep 1\n"
"s_branch .Lk1_pc37a\n"
".Lk1_pc37b:\n"
"s_mov_b32 s65, s72\n"
"s_add_u32 s53, s33, 0x24000\n"
"s_and_b32 s53, s53, 0x3ffff\n"
"s_mov_b32 m0, 0xe000\n"
"s_nop 0\n"
"buffer_load_dwordx4 v208, s[24:27], s53 offen nt lds\n"
"buffer_load_dwordx4 v208, s[24:27], s53 offen offset:2048 nt lds\n"
"s_mov_b32 m0, 0xe380\n"
"s_nop 0\n"
"buffer_load_dwordx4 v208, s[24:27], s53 offen offset:128 nt lds\n"
"buffer_load_dwordx4 v208, s[24:27], s53 offen offset:2176 nt lds\n"
"s_add_u32 s53, s33, 0x25000\n"
"s_and_b32 s53, s53, 0x3ffff\n"
"s_mov_b32 m0, 0x1000\n"
"s_nop 0\n"
"buffer_load_dwordx4 v206, s[20:23], s53 offen nt lds\n"
"buffer_load_dwordx4 v207, s[20:23], s53 offen offset:2048 nt lds\n"
"s_mov_b32 m0, 0x1380\n"
"s_nop 0\n"
"buffer_load_dwordx4 v206, s[20:23], s53 offen offset:128 nt lds\n"
"buffer_load_dwordx4 v207, s[20:23], s53 offen offset:2176 nt lds\n"
"s_waitcnt vmcnt(8)\n"
"s_waitcnt lgkmcnt(0)\n"
"s_barrier\n"
"s_mov_b32 s61, 0x301000\n"
"s_mov_b32 m0, 0x19000\n"
"s_nop 0\n"
"buffer_load_dwordx4 v209, s[36:39], s61 offen lds\n"
"s_mov_b32 m0, 0x19380\n"
"s_nop 0\n"
"buffer_load_dwordx4 v209, s[36:39], s61 offen offset:128 lds\n"
"s_mov_b32 s61, 0x329000\n"
"s_mov_b32 m0, 0x1a800\n"
"s_nop 0\n"
"buffer_load_dwordx4 v209, s[36:39], s61 offen lds\n"
"s_mov_b32 m0, 0x1ab80\n"
"s_nop 0\n"
"buffer_load_dwordx4 v209, s[36:39], s61 offen offset:128 lds\n"
"s_mov_b32 s61, 0x349000\n"
"s_mov_b32 m0, 0x1c000\n"
"s_nop 0\n"
"buffer_load_dwordx4 v209, s[36:39], s61 offen lds\n"
"s_mov_b32 m0, 0x1c380\n"
"s_nop 0\n"
"buffer_load_dwordx4 v209, s[36:39], s61 offen offset:128 lds\n"
"s_mov_b32 s58, 0x4b00\n"
"s_mov_b32 m0, 0x27800\n"
"s_nop 0\n"
"buffer_load_dwordx4 v210, s[28:31], s58 offen lds\n"
"s_mov_b32 m0, 0x27c00\n"
"s_nop 0\n"
"buffer_load_dwordx4 v211, s[28:31], s58 offen lds\n"
"s_mov_b32 s58, 0x12300\n"
"s_mov_b32 m0, 0x22000\n"
"s_nop 0\n"
"buffer_load_dwordx4 v210, s[28:31], s58 offen lds\n"
"s_mov_b32 m0, 0x22400\n"
"s_nop 0\n"
"buffer_load_dwordx4 v211, s[28:31], s58 offen lds\n"
"s_waitcnt lgkmcnt(0)\n"
".Lk1_pc38a:\n"
"s_memrealtime s[72:73]\n"
"s_waitcnt lgkmcnt(0)\n"
"s_sub_u32 s73, s72, s65\n"
"s_cmp_lt_u32 s73, 128\n"
"s_cbranch_scc0 .Lk1_pc38b\n"
"s_sleep 1\n"
"s_branch .Lk1_pc38a\n"
".Lk1_pc38b:\n"
"s_mov_b32 s65, s72\n"
"s_add_u32 s53, s33, 0x28000\n"
"s_and_b32 s53, s53, 0x3ffff\n"
"s_mov_b32 m0, 0x12000\n"
"s_nop 0\n"
"buffer_load_dwordx4 v208, s[24:27], s53 offen nt lds\n"
"buffer_load_dwordx4 v208, s[24:27], s53 offen offset:2048 nt lds\n"
"s_mov_b32 m0, 0x12380\n"
"s_nop 0\n"
"buffer_load_dwordx4 v208, s[24:27], s53 offen offset:128 nt lds\n"
"buffer_load_dwordx4 v208, s[24:27], s53 offen offset:2176 nt lds\n"
"s_add_u32 s53, s33, 0x29000\n"
"s_and_b32 s53, s53, 0x3ffff\n"
"s_mov_b32 m0, 0x5000\n"
"s_nop 0\n"
"buffer_load_dwordx4 v206, s[20:23], s53 offen nt lds\n"
"buffer_load_dwordx4 v207, s[20:23], s53 offen offset:2048 nt lds\n"
"s_mov_b32 m0, 0x5380\n"
"s_nop 0\n"
"buffer_load_dwordx4 v206, s[20:23], s53 offen offset:128 nt lds\n"
"buffer_load_dwordx4 v207, s[20:23], s53 offen offset:2176 nt lds\n"
"s_waitcnt vmcnt(8)\n"
"s_waitcnt lgkmcnt(0)\n"
"s_barrier\n"
"s_mov_b32 s58, 0x1b300\n"
"s_mov_b32 m0, 0x23000\n"
"s_nop 0\n"
"buffer_load_dwordx4 v210, s[28:31], s58 offen lds\n"
"s_mov_b32 m0, 0x23400\n"
"s_nop 0\n"
"buffer_load_dwordx4 v211, s[28:31], s58 offen lds\n"
"s_mov_b32 s61, 0x381000\n"
"s_mov_b32 m0, 0x1d000\n"
"s_nop 0\n"
"buffer_load_dwordx4 v209, s[36:39], s61 offen lds\n"
"s_mov_b32 m0, 0x1d380\n"
"s_nop 0\n"
"buffer_load_dwordx4 v209, s[36:39], s61 offen offset:128 lds\n"
"s_mov_b32 s61, 0x3a9000\n"
"s_mov_b32 m0, 0x1e800\n"
"s_nop 0\n"
"buffer_load_dwordx4 v209, s[36:39], s61 offen lds\n"
"s_mov_b32 m0, 0x1eb80\n"
"s_nop 0\n"
"buffer_load_dwordx4 v209, s[36:39], s61 offen offset:128 lds\n"
"s_mov_b32 s61, 0x3c9000\n"
"s_mov_b32 m0, 0x20000\n"
"s_nop 0\n"
"buffer_load_dwordx4 v209, s[36:39], s61 offen lds\n"
"s_mov_b32 m0, 0x20380\n"
"s_nop 0\n"
"buffer_load_dwordx4 v209, s[36:39], s61 offen offset:128 lds\n"
"s_mov_b32 s58, 0x4b80\n"
"s_mov_b32 m0, 0x25000\n"
"s_nop 0\n"
"buffer_load_dwordx4 v210, s[28:31], s58 offen lds\n"
"s_mov_b32 m0, 0x25400\n"
"s_nop 0\n"
"buffer_load_dwordx4 v211, s[28:31], s58 offen lds\n"
"s_waitcnt lgkmcnt(0)\n"
".Lk1_pc39a:\n"
"s_memrealtime s[72:73]\n"
"s_waitcnt lgkmcnt(0)\n"
"s_sub_u32 s73, s72, s65\n"
"s_cmp_lt_u32 s73, 128\n"
"s_cbranch_scc0 .Lk1_pc39b\n"
"s_sleep 1\n"
"s_branch .Lk1_pc39a\n"
".Lk1_pc39b:\n"
"s_mov_b32 s65, s72\n"
"s_add_u32 s53, s33, 0x2c000\n"
"s_and_b32 s53, s53, 0x3ffff\n"
"s_mov_b32 m0, 0x16000\n"
"s_nop 0\n"
"buffer_load_dwordx4 v208, s[24:27], s53 offen nt lds\n"
"buffer_load_dwordx4 v208, s[24:27], s53 offen offset:2048 nt lds\n"
"s_mov_b32 m0, 0x16380\n"
"s_nop 0\n"
"buffer_load_dwordx4 v208, s[24:27], s53 offen offset:128 nt lds\n"
"buffer_load_dwordx4 v208, s[24:27], s53 offen offset:2176 nt lds\n"
"s_add_u32 s53, s33, 0x2d000\n"
"s_and_b32 s53, s53, 0x3ffff\n"
"s_mov_b32 m0, 0x9000\n"
"s_nop 0\n"
"buffer_load_dwordx4 v206, s[20:23], s53 offen nt lds\n"
"buffer_load_dwordx4 v207, s[20:23], s53 offen offset:2048 nt lds\n"
"s_mov_b32 m0, 0x9380\n"
"s_nop 0\n"
"buffer_load_dwordx4 v206, s[20:23], s53 offen offset:128 nt lds\n"
"buffer_load_dwordx4 v207, s[20:23], s53 offen offset:2176 nt lds\n"
"s_waitcnt vmcnt(8)\n"
"s_waitcnt lgkmcnt(0)\n"
"s_barrier\n"
"s_mov_b32 s58, 0x12380\n"
"s_mov_b32 m0, 0x26800\n"
"s_nop 0\n"
"buffer_load_dwordx4 v210, s[28:31], s58 offen lds\n"
"s_mov_b32 m0, 0x26c00\n"
"s_nop 0\n"
"buffer_load_dwordx4 v211, s[28:31], s58 offen lds\n"
"s_mov_b32 s58, 0x1fb80\n"
"s_mov_b32 m0, 0x21000\n"
"s_nop 0\n"
"buffer_load_dwordx4 v210, s[28:31], s58 offen lds\n"
"s_mov_b32 m0, 0x21400\n"
"s_nop 0\n"
"buffer_load_dwordx4 v211, s[28:31], s58 offen lds\n"
"s_mov_b32 s61, 0x421000\n"
"s_mov_b32 m0, 0x19800\n"
"s_nop 0\n"
"buffer_load_dwordx4 v209, s[36:39], s61 offen lds\n"
"s_mov_b32 m0, 0x19b80\n"
"s_nop 0\n"
"buffer_load_dwordx4 v209, s[36:39], s61 offen offset:128 lds\n"
"s_mov_b32 s61, 0x441000\n"
"s_mov_b32 m0, 0x1b000\n"
"s_nop 0\n"
"buffer_load_dwordx4 v209, s[36:39], s61 offen lds\n"
"s_mov_b32 m0, 0x1b380\n"
"s_nop 0\n"
"buffer_load_dwordx4 v209, s[36:39], s61 offen offset:128 lds\n"
"s_mov_b32 s61, 0x469000\n"
"s_mov_b32 m0, 0x1c800\n"
"s_nop 0\n"
"buffer_load_dwordx4 v209, s[36:39], s61 offen lds\n"
"s_mov_b32 m0, 0x1cb80\n"
"s_nop 0\n"
"buffer_load_dwordx4 v209, s[36:39], s61 offen offset:128 lds\n"
"s_waitcnt lgkmcnt(0)\n"
".Lk1_pc40a:\n"
"s_memrealtime s[72:73]\n"
"s_waitcnt lgkmcnt(0)\n"
"s_sub_u32 s73, s72, s65\n"
"s_cmp_lt_u32 s73, 128\n"
"s_cbranch_scc0 .Lk1_pc40b\n"
"s_sleep 1\n"
"s_branch .Lk1_pc40a\n"
".Lk1_pc40b:\n"
"s_mov_b32 s65, s72\n"
"s_add_u32 s53, s33, 0x30000\n"
"s_and_b32 s53, s53, 0x3ffff\n"
"s_mov_b32 m0, 0xe000\n"
"s_nop 0\n"
"buffer_load_dwordx4 v208, s[24:27], s53 offen nt lds\n"
"buffer_load_dwordx4 v208, s[24:27], s53 offen offset:2048 nt lds\n"
"s_mov_b32 m0, 0xe380\n"
"s_nop 0\n"
"buffer_load_dwordx4 v208, s[24:27], s53 offen offset:128 nt lds\n"
"buffer_load_dwordx4 v208, s[24:27], s53 offen offset:2176 nt lds\n"
"s_add_u32 s53, s33, 0x31000\n"
"s_and_b32 s53, s53, 0x3ffff\n"
"s_mov_b32 m0, 0x1000\n"
"s_nop 0\n"
"buffer_load_dwordx4 v206, s[20:23], s53 offen nt lds\n"
"buffer_load_dwordx4 v207, s[20:23], s53 offen offset:2048 nt lds\n"
"s_mov_b32 m0, 0x1380\n"
"s_nop 0\n"
"buffer_load_dwordx4 v206, s[20:23], s53 offen offset:128 nt lds\n"
"buffer_load_dwordx4 v207, s[20:23], s53 offen offset:2176 nt lds\n"
"s_waitcnt vmcnt(8)\n"
"s_waitcnt lgkmcnt(0)\n"
"s_barrier\n"
"s_mov_b32 s58, 0x9400\n"
"s_mov_b32 m0, 0x23000\n"
"s_nop 0\n"
"buffer_load_dwordx4 v210, s[28:31], s58 offen lds\n"
"s_mov_b32 m0, 0x23400\n"
"s_nop 0\n"
"buffer_load_dwordx4 v211, s[28:31], s58 offen lds\n"
"s_mov_b32 s58, 0x16c00\n"
"s_mov_b32 m0, 0x24800\n"
"s_nop 0\n"
"buffer_load_dwordx4 v210, s[28:31], s58 offen lds\n"
"s_mov_b32 m0, 0x24c00\n"
"s_nop 0\n"
"buffer_load_dwordx4 v211, s[28:31], s58 offen lds\n"
"s_mov_b32 s58, 0x24400\n"
"s_mov_b32 m0, 0x26000\n"
"s_nop 0\n"
"buffer_load_dwordx4 v210, s[28:31], s58 offen lds\n"
"s_mov_b32 m0, 0x26400\n"
"s_nop 0\n"
"buffer_load_dwordx4 v211, s[28:31], s58 offen lds\n"
"s_waitcnt lgkmcnt(0)\n"
".Lk1_pc41a:\n"
"s_memrealtime s[72:73]\n"
"s_waitcnt lgkmcnt(0)\n"
"s_sub_u32 s73, s72, s65\n"
"s_cmp_lt_u32 s73, 128\n"
"s_cbranch_scc0 .Lk1_pc41b\n"
"s_sleep 1\n"
"s_branch .Lk1_pc41a\n"
".Lk1_pc41b:\n"
"s_mov_b32 s65, s72\n"
"s_add_u32 s53, s33, 0x34000\n"
"s_and_b32 s53, s53, 0x3ffff\n"
"s_mov_b32 m0, 0x12000\n"
"s_nop 0\n"
"buffer_load_dwordx4 v208, s[24:27], s53 offen nt lds\n"
"buffer_load_dwordx4 v208, s[24:27], s53 offen offset:2048 nt lds\n"
"s_mov_b32 m0, 0x12380\n"
"s_nop 0\n"
"buffer_load_dwordx4 v208, s[24:27], s53 offen offset:128 nt lds\n"
"buffer_load_dwordx4 v208, s[24:27], s53 offen offset:2176 nt lds\n"
"s_add_u32 s53, s33, 0x35000\n"
"s_and_b32 s53, s53, 0x3ffff\n"
"s_mov_b32 m0, 0x5000\n"
"s_nop 0\n"
"buffer_load_dwordx4 v206, s[20:23], s53 offen nt lds\n"
"buffer_load_dwordx4 v207, s[20:23], s53 offen offset:2048 nt lds\n"
"s_mov_b32 m0, 0x5380\n"
"s_nop 0\n"
"buffer_load_dwordx4 v206, s[20:23], s53 offen offset:128 nt lds\n"
"buffer_load_dwordx4 v207, s[20:23], s53 offen offset:2176 nt lds\n"
"s_waitcnt vmcnt(8)\n"
"s_waitcnt lgkmcnt(0)\n"
"s_barrier\n"
"s_waitcnt lgkmcnt(0)\n"
".Lk1_pc42a:\n"
"s_memrealtime s[72:73]\n"
"s_waitcnt lgkmcnt(0)\n"
"s_sub_u32 s73, s72, s65\n"
"s_cmp_lt_u32 s73, 128\n"
"s_cbranch_scc0 .Lk1_pc42b\n"
"s_sleep 1\n"
"s_branch .Lk1_pc42a\n"
".Lk1_pc42b:\n"
"s_mov_b32 s65, s72\n"
"s_add_u32 s53, s33, 0x38000\n"
"s_and_b32 s53, s53, 0x3ffff\n"
"s_mov_b32 m0, 0x16000\n"
"s_nop 0\n"
"buffer_load_dwordx4 v208, s[24:27], s53 offen nt lds\n"
"buffer_load_dwordx4 v208, s[24:27], s53 offen offset:2048 nt lds\n"
"s_mov_b32 m0, 0x16380\n"
"s_nop 0\n"
"buffer_load_dwordx4 v208, s[24:27], s53 offen offset:128 nt lds\n"
"buffer_load_dwordx4 v208, s[24:27], s53 offen offset:2176 nt lds\n"
"s_add_u32 s53, s33, 0x39000\n"
"s_and_b32 s53, s53, 0x3ffff\n"
"s_mov_b32 m0, 0x9000\n"
"s_nop 0\n"
"buffer_load_dwordx4 v206, s[20:23], s53 offen nt lds\n"
"buffer_load_dwordx4 v207, s[20:23], s53 offen offset:2048 nt lds\n"
"s_mov_b32 m0, 0x9380\n"
"s_nop 0\n"
"buffer_load_dwordx4 v206, s[20:23], s53 offen offset:128 nt lds\n"
"buffer_load_dwordx4 v207, s[20:23], s53 offen offset:2176 nt lds\n"
"s_waitcnt vmcnt(8)\n"
"s_waitcnt lgkmcnt(0)\n"
"s_barrier\n"
"s_waitcnt lgkmcnt(0)\n"
".Lk1_pc43a:\n"
"s_memrealtime s[72:73]\n"
"s_waitcnt lgkmcnt(0)\n"
"s_sub_u32 s73, s72, s65\n"
"s_cmp_lt_u32 s73, 128\n"
"s_cbranch_scc0 .Lk1_pc43b\n"
"s_sleep 1\n"
"s_branch .Lk1_pc43a\n"
".Lk1_pc43b:\n"
"s_mov_b32 s65, s72\n"
"s_add_u32 s53, s33, 0x3c000\n"
"s_and_b32 s53, s53, 0x3ffff\n"
"s_mov_b32 m0, 0xe000\n"
"s_nop 0\n"
"buffer_load_dwordx4 v208, s[24:27], s53 offen nt lds\n"
"buffer_load_dwordx4 v208, s[24:27], s53 offen offset:2048 nt lds\n"
"s_mov_b32 m0, 0xe380\n"
"s_nop 0\n"
"buffer_load_dwordx4 v208, s[24:27], s53 offen offset:128 nt lds\n"
"buffer_load_dwordx4 v208, s[24:27], s53 offen offset:2176 nt lds\n"
"s_add_u32 s53, s33, 0x3d000\n"
"s_and_b32 s53, s53, 0x3ffff\n"
"s_mov_b32 m0, 0x1000\n"
"s_nop 0\n"
"buffer_load_dwordx4 v206, s[20:23], s53 offen nt lds\n"
"buffer_load_dwordx4 v207, s[20:23], s53 offen offset:2048 nt lds\n"
"s_mov_b32 m0, 0x1380\n"
"s_nop 0\n"
"buffer_load_dwordx4 v206, s[20:23], s53 offen offset:128 nt lds\n"
"buffer_load_dwordx4 v207, s[20:23], s53 offen offset:2176 nt lds\n"
"s_waitcnt vmcnt(8)\n"
"s_waitcnt lgkmcnt(0)\n"
"s_barrier\n"
"s_waitcnt lgkmcnt(0)\n"
".Lk1_pc44a:\n"
"s_memrealtime s[72:73]\n"
"s_waitcnt lgkmcnt(0)\n"
"s_sub_u32 s73, s72, s65\n"
"s_cmp_lt_u32 s73, 128\n"
"s_cbranch_scc0 .Lk1_pc44b\n"
"s_sleep 1\n"
"s_branch .Lk1_pc44a\n"
".Lk1_pc44b:\n"
"s_mov_b32 s65, s72\n"
"s_add_u32 s53, s33, 0\n"
"s_and_b32 s53, s53, 0x3ffff\n"
"s_add_u32 s53, s53, 0x400000\n"
"s_mov_b32 m0, 0x12000\n"
"s_nop 0\n"
"buffer_load_dwordx4 v208, s[24:27], s53 offen nt lds\n"
"buffer_load_dwordx4 v208, s[24:27], s53 offen offset:2048 nt lds\n"
"s_mov_b32 m0, 0x12380\n"
"s_nop 0\n"
"buffer_load_dwordx4 v208, s[24:27], s53 offen offset:128 nt lds\n"
"buffer_load_dwordx4 v208, s[24:27], s53 offen offset:2176 nt lds\n"
"s_add_u32 s53, s33, 0x1000\n"
"s_and_b32 s53, s53, 0x3ffff\n"
"s_add_u32 s53, s53, 0x400000\n"
"s_mov_b32 m0, 0x5000\n"
"s_nop 0\n"
"buffer_load_dwordx4 v206, s[20:23], s53 offen nt lds\n"
"buffer_load_dwordx4 v207, s[20:23], s53 offen offset:2048 nt lds\n"
"s_mov_b32 m0, 0x5380\n"
"s_nop 0\n"
"buffer_load_dwordx4 v206, s[20:23], s53 offen offset:128 nt lds\n"
"buffer_load_dwordx4 v207, s[20:23], s53 offen offset:2176 nt lds\n"
"s_waitcnt vmcnt(8)\n"
"s_waitcnt lgkmcnt(0)\n"
"s_barrier\n"
"v_lshlrev_b32 v247, 4, v238\n"
"s_mul_i32 s60, s44, 0x3000\n"
"s_add_u32 s60, s60, 0x19000\n"
"v_add_u32 v247, s60, v247\n"
"ds_read_b128 v[96:99], v247\n"
"ds_read_b128 v[100:103], v247 offset:1024\n"
"ds_read_b128 v[104:107], v247 offset:2048\n"
"ds_read_b128 v[108:111], v247 offset:3072\n"
"ds_read_b128 v[112:115], v247 offset:4096\n"
"ds_read_b128 v[116:119], v247 offset:5120\n"
"ds_read_b128 v[120:123], v247 offset:6144\n"
"ds_read_b128 v[124:127], v247 offset:7168\n"
"ds_read_b128 v[128:131], v247 offset:8192\n"
"ds_read_b128 v[132:135], v247 offset:9216\n"
"ds_read_b128 v[136:139], v247 offset:10240\n"
"ds_read_b128 v[140:143], v247 offset:11264\n"
"v_add_u32 v252, 0x1000, v248\n"
"s_waitcnt lgkmcnt(0)\n"
".Lk1_pc45a:\n"
"s_memrealtime s[72:73]\n"
"s_waitcnt lgkmcnt(0)\n"
"s_sub_u32 s73, s72, s65\n"
"s_cmp_lt_u32 s73, 128\n"
"s_cbranch_scc0 .Lk1_pc45b\n"
"s_sleep 1\n"
"s_branch .Lk1_pc45a\n"
".Lk1_pc45b:\n"
"s_mov_b32 s65, s72\n"
"s_add_u32 s53, s33, 0x4000\n"
"s_and_b32 s53, s53, 0x3ffff\n"
"s_add_u32 s53, s53, 0x400000\n"
"s_mov_b32 m0, 0x16000\n"
"s_nop 0\n"
"buffer_load_dwordx4 v208, s[24:27], s53 offen nt lds\n"
"buffer_load_dwordx4 v208, s[24:27], s53 offen offset:2048 nt lds\n"
"s_mov_b32 m0, 0x16380\n"
"s_nop 0\n"
"buffer_load_dwordx4 v208, s[24:27], s53 offen offset:128 nt lds\n"
"buffer_load_dwordx4 v208, s[24:27], s53 offen offset:2176 nt lds\n"
"s_add_u32 s53, s33, 0x5000\n"
"s_and_b32 s53, s53, 0x3ffff\n"
"s_add_u32 s53, s53, 0x400000\n"
"s_mov_b32 m0, 0x9000\n"
"s_nop 0\n"
"buffer_load_dwordx4 v206, s[20:23], s53 offen nt lds\n"
"buffer_load_dwordx4 v207, s[20:23], s53 offen offset:2048 nt lds\n"
"s_mov_b32 m0, 0x9380\n"
"s_nop 0\n"
"buffer_load_dwordx4 v206, s[20:23], s53 offen offset:128 nt lds\n"
"buffer_load_dwordx4 v207, s[20:23], s53 offen offset:2176 nt lds\n"
"s_waitcnt vmcnt(8)\n"
"ds_read_b128 v[184:187], v252\n"
"ds_read_b128 v[192:195], v252 offset:512\n"
"ds_read_b128 v[200:203], v252 offset:1024\n"
"ds_read_b128 v[220:223], v252 offset:1536\n"
"v_and_b32 v247, 15, v238\n"
"v_cmp_eq_u32 vcc, 0, v247\n"
"v_mov_b32 v247, 0x3f803f80\n"
"s_nop 0\n"
"v_cndmask_b32 v240, 0, v247, vcc\n"
"v_cndmask_b32 v241, 0, v247, vcc\n"
"v_cndmask_b32 v242, 0, v247, vcc\n"
"v_cndmask_b32 v243, 0, v247, vcc\n"
"s_waitcnt lgkmcnt(3)\n"
"v_mfma_f32_16x16x32_bf16 v[160:163], v[184:187], v[96:99], 0\n"
"v_mfma_f32_16x16x32_bf16 v[168:171], v[184:187], v[112:115], 0\n"
"v_mfma_f32_16x16x32_bf16 v[176:179], v[184:187], v[128:131], 0\n"
"s_waitcnt lgkmcnt(2)\n"
"v_mfma_f32_16x16x32_bf16 v[160:163], v[192:195], v[100:103], v[160:163]\n"
"v_mfma_f32_16x16x32_bf16 v[168:171], v[192:195], v[116:119], v[168:171]\n"
"v_mfma_f32_16x16x32_bf16 v[176:179], v[192:195], v[132:135], v[176:179]\n"
"s_waitcnt lgkmcnt(1)\n"
"v_mfma_f32_16x16x32_bf16 v[160:163], v[200:203], v[104:107], v[160:163]\n"
"v_mfma_f32_16x16x32_bf16 v[168:171], v[200:203], v[120:123], v[168:171]\n"
"v_mfma_f32_16x16x32_bf16 v[176:179], v[200:203], v[136:139], v[176:179]\n"
"s_waitcnt lgkmcnt(0)\n"
"v_mfma_f32_16x16x32_bf16 v[160:163], v[220:223], v[108:111], v[160:163]\n"
"v_mfma_f32_16x16x32_bf16 v[168:171], v[220:223], v[124:127], v[168:171]\n"
"v_mfma_f32_16x16x32_bf16 v[176:179], v[220:223], v[140:143], v[176:179]\n"
"v_mov_b32 v156, 0\n"
"v_mov_b32 v157, 0\n"
"v_mov_b32 v158, 0\n"
"v_mov_b32 v159, 0\n"
"v_mov_b32 v164, 0\n"
"v_mov_b32 v165, 0\n"
"v_mov_b32 v166, 0\n"
"v_mov_b32 v167, 0\n"
"v_mov_b32 v172, 0\n"
"v_mov_b32 v173, 0\n"
"v_mov_b32 v174, 0\n"
"v_mov_b32 v175, 0\n"
"v_mov_b32 v180, 0\n"
"v_mov_b32 v181, 0\n"
"v_mov_b32 v182, 0\n"
"v_mov_b32 v183, 0\n"
"v_mov_b32 v188, 0\n"
"v_mov_b32 v189, 0\n"
"v_mov_b32 v190, 0\n"
"v_mov_b32 v191, 0\n"
"v_mov_b32 v196, 0\n"
"v_mov_b32 v197, 0\n"
"v_mov_b32 v198, 0\n"
"v_mov_b32 v199, 0\n"
"v_max3_f32 v228, v160, v161, v162\n"
"v_max_f32 v228, v228, v163\n"
"v_max3_f32 v232, v168, v169, v170\n"
"v_max_f32 v232, v232, v171\n"
"v_max3_f32 v236, v176, v177, v178\n"
"v_max_f32 v236, v236, v179\n"
"v_mov_b32 v204, v228\n"
"v_mov_b32 v205, v228\n"
"s_nop 1\n"
"v_permlane32_swap_b32 v204, v205\n"
"v_max_f32 v228, v204, v205\n"
"v_mov_b32 v204, v228\n"
"v_mov_b32 v205, v228\n"
"s_nop 1\n"
"v_permlane16_swap_b32 v204, v205\n"
"v_max_f32 v228, v204, v205\n"
"v_sub_f32 v144, 0, v228\n"
"v_sub_f32 v145, 0, v228\n"
"v_sub_f32 v146, 0, v228\n"
"v_sub_f32 v147, 0, v228\n"
"v_mov_b32 v228, 0\n"
"v_mov_b32 v204, v232\n"
"v_mov_b32 v205, v232\n"
"s_nop 1\n"
"v_permlane32_swap_b32 v204, v205\n"
"v_max_f32 v232, v204, v205\n"
"v_mov_b32 v204, v232\n"
"v_mov_b32 v205, v232\n"
"s_nop 1\n"
"v_permlane16_swap_b32 v204, v205\n"
"v_max_f32 v232, v204, v205\n"
"v_sub_f32 v148, 0, v232\n"
"v_sub_f32 v149, 0, v232\n"
"v_sub_f32 v150, 0, v232\n"
"v_sub_f32 v151, 0, v232\n"
"v_mov_b32 v232, 0\n"
"v_mov_b32 v204, v236\n"
"v_mov_b32 v205, v236\n"
"s_nop 1\n"
"v_permlane32_swap_b32 v204, v205\n"
"v_max_f32 v236, v204, v205\n"
"v_mov_b32 v204, v236\n"
"v_mov_b32 v205, v236\n"
"s_nop 1\n"
"v_permlane16_swap_b32 v204, v205\n"
"v_max_f32 v236, v204, v205\n"
"v_sub_f32 v152, 0, v236\n"
"v_sub_f32 v153, 0, v236\n"
"v_sub_f32 v154, 0, v236\n"
"v_sub_f32 v155, 0, v236\n"
"v_mov_b32 v236, 0\n"
"v_mov_b32 v216, 0\n"
"v_mov_b32 v217, 0\n"
"v_mov_b32 v218, 0\n"
"v_mov_b32 v219, 0\n"
"v_mov_b32 v220, 0\n"
"v_mov_b32 v221, 0\n"
"v_mov_b32 v222, 0\n"
"v_mov_b32 v223, 0\n"
"v_mov_b32 v224, 0\n"
"v_mov_b32 v225, 0\n"
"v_mov_b32 v226, 0\n"
"v_mov_b32 v227, 0\n"
"s_waitcnt lgkmcnt(0)\n"
"s_barrier\n"
".Lk1_attn:\n"
"s_setprio 0\n"
"s_mov_b32 s50, 0\n"
".Lk1_loop:\n"
"ds_read_b128 v[204:207], v248\n"
"ds_read_b128 v[208:211], v248 offset:512\n"
"v_mfma_f32_16x16x32_bf16 v[28:31], v[224:227], v[156:159], v[28:31]\n"
"ds_read_b128 v[212:215], v248 offset:1024\n"
"v_add_f32 v244, v244, v228\n"
"v_mfma_f32_16x16x32_bf16 v[60:63], v[224:227], v[164:167], v[60:63]\n"
"v_add_f32 v245, v245, v232\n"
"v_add_f32 v246, v246, v236\n"
"s_add_u32 s71, s71, 1\n"
"s_cmp_eq_u32 s71, 3\n"
"s_cselect_b32 s52, 0xffff8000, s70\n"
"s_cselect_b32 s71, 0, s71\n"
"s_add_u32 s50, s50, 1\n"
"v_mfma_f32_16x16x32_bf16 v[92:95], v[224:227], v[172:175], v[92:95]\n"
"s_waitcnt lgkmcnt(2)\n"
"v_mfma_f32_16x16x32_bf16 v[156:159], v[204:207], v[96:99], v[144:147]\n"
"v_mfma_f32_16x16x32_bf16 v[164:167], v[204:207], v[112:115], v[148:151]\n"
"v_mfma_f32_16x16x32_bf16 v[172:175], v[204:207], v[128:131], v[152:155]\n"
"s_waitcnt lgkmcnt(1)\n"
"ds_read_b128 v[204:207], v248 offset:1536\n"
"v_mfma_f32_16x16x32_bf16 v[156:159], v[208:211], v[100:103], v[156:159]\n"
"v_mfma_f32_16x16x32_bf16 v[164:167], v[208:211], v[116:119], v[164:167]\n"
"ds_read_b64_tr_b16 v[216:217], v249\n"
"ds_read_b64_tr_b16 v[218:219], v249 offset:4096\n"
"v_mfma_f32_16x16x32_bf16 v[172:175], v[208:211], v[132:135], v[172:175]\n"
"s_waitcnt lgkmcnt(3)\n"
"ds_read_b128 v[208:211], v248 offset:4096\n"
"v_mfma_f32_16x16x32_bf16 v[156:159], v[212:215], v[104:107], v[156:159]\n"
"v_mfma_f32_16x16x32_bf16 v[164:167], v[212:215], v[120:123], v[164:167]\n"
"ds_read_b64_tr_b16 v[220:221], v250\n"
"ds_read_b64_tr_b16 v[222:223], v250 offset:4096\n"
"v_mfma_f32_16x16x32_bf16 v[172:175], v[212:215], v[136:139], v[172:175]\n"
"s_waitcnt lgkmcnt(5)\n"
"ds_read_b128 v[212:215], v248 offset:4608\n"
"v_mfma_f32_16x16x32_bf16 v[156:159], v[204:207], v[108:111], v[156:159]\n"
"v_mfma_f32_16x16x32_bf16 v[164:167], v[204:207], v[124:127], v[164:167]\n"
"ds_read_b64_tr_b16 v[224:225], v249 offset:512\n"
"ds_read_b64_tr_b16 v[226:227], v249 offset:4608\n"
"v_mfma_f32_16x16x32_bf16 v[172:175], v[204:207], v[140:143], v[172:175]\n"
"s_waitcnt lgkmcnt(5)\n"
"ds_read_b128 v[204:207], v248 offset:5120\n"
"v_mfma_f32_16x16x32_bf16 v[160:163], v[208:211], v[96:99], v[144:147]\n"
"v_mfma_f32_16x16x32_bf16 v[168:171], v[208:211], v[112:115], v[148:151]\n"
"v_exp_f32 v156, v156\n"
"v_mfma_f32_16x16x32_bf16 v[176:179], v[208:211], v[128:131], v[152:155]\n"
"s_waitcnt lgkmcnt(3)\n"
"ds_read_b128 v[208:211], v248 offset:5632\n"
"v_mfma_f32_16x16x32_bf16 v[160:163], v[212:215], v[100:103], v[160:163]\n"
"v_exp_f32 v157, v157\n"
"v_mfma_f32_16x16x32_bf16 v[168:171], v[212:215], v[116:119], v[168:171]\n"
"v_exp_f32 v158, v158\n"
"v_mfma_f32_16x16x32_bf16 v[176:179], v[212:215], v[132:135], v[176:179]\n"
"s_waitcnt lgkmcnt(1)\n"
"v_exp_f32 v159, v159\n"
"v_mfma_f32_16x16x32_bf16 v[160:163], v[204:207], v[104:107], v[160:163]\n"
"v_exp_f32 v164, v164\n"
"v_mfma_f32_16x16x32_bf16 v[168:171], v[204:207], v[120:123], v[168:171]\n"
"v_exp_f32 v165, v165\n"
"v_mfma_f32_16x16x32_bf16 v[176:179], v[204:207], v[136:139], v[176:179]\n"
"s_waitcnt lgkmcnt(0)\n"
"v_exp_f32 v166, v166\n"
"v_mfma_f32_16x16x32_bf16 v[160:163], v[208:211], v[108:111], v[160:163]\n"
"v_exp_f32 v167, v167\n"
"v_mfma_f32_16x16x32_bf16 v[168:171], v[208:211], v[124:127], v[168:171]\n"
"v_cvt_pk_bf16_f32 v156, v156, v157\n"
"v_cvt_pk_bf16_f32 v157, v158, v159\n"
"v_mfma_f32_16x16x32_bf16 v[176:179], v[208:211], v[140:143], v[176:179]\n"
"v_exp_f32 v172, v172\n"
"v_mfma_f32_16x16x32_bf16 v[0:3], v[216:219], v[180:183], v[0:3]\n"
"v_exp_f32 v173, v173\n"
"v_mfma_f32_16x16x32_bf16 v[32:35], v[216:219], v[188:191], v[32:35]\n"
"v_exp_f32 v174, v174\n"
"v_mfma_f32_16x16x32_bf16 v[64:67], v[216:219], v[196:199], v[64:67]\n"
"ds_read_b64_tr_b16 v[216:217], v250 offset:512\n"
"ds_read_b64_tr_b16 v[218:219], v250 offset:4608\n"
"v_mfma_f32_16x16x32_bf16 v[4:7], v[220:223], v[180:183], v[4:7]\n"
"v_exp_f32 v175, v175\n"
"v_mfma_f32_16x16x32_bf16 v[36:39], v[220:223], v[188:191], v[36:39]\n"
"v_cvt_pk_bf16_f32 v164, v164, v165\n"
"v_cvt_pk_bf16_f32 v165, v166, v167\n"
"v_mfma_f32_16x16x32_bf16 v[68:71], v[220:223], v[196:199], v[68:71]\n"
"ds_read_b64_tr_b16 v[220:221], v249 offset:1024\n"
"ds_read_b64_tr_b16 v[222:223], v249 offset:5120\n"
"v_mfma_f32_16x16x32_bf16 v[8:11], v[224:227], v[180:183], v[8:11]\n"
"v_cvt_pk_bf16_f32 v172, v172, v173\n"
"v_cvt_pk_bf16_f32 v173, v174, v175\n"
"v_mfma_f32_16x16x32_bf16 v[40:43], v[224:227], v[188:191], v[40:43]\n"
"v_exp_f32 v160, v160\n"
"v_mfma_f32_16x16x32_bf16 v[72:75], v[224:227], v[196:199], v[72:75]\n"
"s_waitcnt lgkmcnt(2)\n"
"ds_read_b64_tr_b16 v[224:225], v250 offset:1024\n"
"ds_read_b64_tr_b16 v[226:227], v250 offset:5120\n"
"v_mfma_f32_16x16x32_bf16 v[12:15], v[216:219], v[180:183], v[12:15]\n"
"v_exp_f32 v161, v161\n"
"v_mfma_f32_16x16x32_bf16 v[44:47], v[216:219], v[188:191], v[44:47]\n"
"v_exp_f32 v162, v162\n"
"v_mfma_f32_16x16x32_bf16 v[76:79], v[216:219], v[196:199], v[76:79]\n"
"s_waitcnt lgkmcnt(2)\n"
"ds_read_b64_tr_b16 v[216:217], v249 offset:1536\n"
"ds_read_b64_tr_b16 v[218:219], v249 offset:5632\n"
"v_mfma_f32_16x16x32_bf16 v[16:19], v[220:223], v[180:183], v[16:19]\n"
"v_exp_f32 v163, v163\n"
"v_mfma_f32_16x16x32_bf16 v[48:51], v[220:223], v[188:191], v[48:51]\n"
"v_exp_f32 v168, v168\n"
"v_mfma_f32_16x16x32_bf16 v[80:83], v[220:223], v[196:199], v[80:83]\n"
"s_waitcnt lgkmcnt(2)\n"
"ds_read_b64_tr_b16 v[220:221], v250 offset:1536\n"
"ds_read_b64_tr_b16 v[222:223], v250 offset:5632\n"
"v_mfma_f32_16x16x32_bf16 v[20:23], v[224:227], v[180:183], v[20:23]\n"
"ds_read_b128 v[212:215], v248 offset:8192\n"
"v_mfma_f32_16x16x32_bf16 v[52:55], v[224:227], v[188:191], v[52:55]\n"
"v_exp_f32 v169, v169\n"
"v_mfma_f32_16x16x32_bf16 v[84:87], v[224:227], v[196:199], v[84:87]\n"
"s_waitcnt lgkmcnt(3)\n"
"ds_read_b64_tr_b16 v[224:225], v250 offset:8192\n"
"ds_read_b64_tr_b16 v[226:227], v250 offset:12288\n"
"v_mfma_f32_16x16x32_bf16 v[24:27], v[216:219], v[180:183], v[24:27]\n"
"ds_read_b128 v[204:207], v248 offset:8704\n"
"v_mfma_f32_16x16x32_bf16 v[56:59], v[216:219], v[188:191], v[56:59]\n"
"v_exp_f32 v170, v170\n"
"v_mfma_f32_16x16x32_bf16 v[88:91], v[216:219], v[196:199], v[88:91]\n"
"s_waitcnt lgkmcnt(4)\n"
"ds_read_b64_tr_b16 v[216:217], v249 offset:8704\n"
"ds_read_b64_tr_b16 v[218:219], v249 offset:12800\n"
"v_mfma_f32_16x16x32_bf16 v[28:31], v[220:223], v[180:183], v[28:31]\n"
"ds_read_b128 v[208:211], v248 offset:9216\n"
"v_mfma_f32_16x16x32_bf16 v[60:63], v[220:223], v[188:191], v[60:63]\n"
"v_exp_f32 v171, v171\n"
"v_mfma_f32_16x16x32_bf16 v[92:95], v[220:223], v[196:199], v[92:95]\n"
"s_waitcnt lgkmcnt(6)\n"
"ds_read_b64_tr_b16 v[220:221], v249 offset:8192\n"
"ds_read_b64_tr_b16 v[222:223], v249 offset:12288\n"
"v_mfma_f32_16x16x32_bf16 v[180:183], v[212:215], v[96:99], v[144:147]\n"
"v_cvt_pk_bf16_f32 v158, v160, v161\n"
"v_cvt_pk_bf16_f32 v159, v162, v163\n"
"v_mfma_f32_16x16x32_bf16 v[188:191], v[212:215], v[112:115], v[148:151]\n"
"v_exp_f32 v176, v176\n"
"v_mfma_f32_16x16x32_bf16 v[196:199], v[212:215], v[128:131], v[152:155]\n"
"s_waitcnt lgkmcnt(5)\n"
"ds_read_b128 v[212:215], v248 offset:9728\n"
"v_mfma_f32_16x16x32_bf16 v[180:183], v[204:207], v[100:103], v[180:183]\n"
"v_exp_f32 v177, v177\n"
"v_mfma_f32_16x16x32_bf16 v[188:191], v[204:207], v[116:119], v[188:191]\n"
"v_exp_f32 v178, v178\n"
"v_mfma_f32_16x16x32_bf16 v[196:199], v[204:207], v[132:135], v[196:199]\n"
"s_waitcnt lgkmcnt(3)\n"
"ds_read_b128 v[204:207], v248 offset:12288\n"
"v_exp_f32 v179, v179\n"
"v_mfma_f32_16x16x32_bf16 v[180:183], v[208:211], v[104:107], v[180:183]\n"
"v_cvt_pk_bf16_f32 v166, v168, v169\n"
"v_cvt_pk_bf16_f32 v167, v170, v171\n"
"v_mfma_f32_16x16x32_bf16 v[188:191], v[208:211], v[120:123], v[188:191]\n"
"v_cvt_pk_bf16_f32 v174, v176, v177\n"
"v_cvt_pk_bf16_f32 v175, v178, v179\n"
"v_mfma_f32_16x16x32_bf16 v[196:199], v[208:211], v[136:139], v[196:199]\n"
"s_waitcnt lgkmcnt(1)\n"
"ds_read_b128 v[208:211], v248 offset:12800\n"
"v_mfma_f32_16x16x32_bf16 v[180:183], v[212:215], v[108:111], v[180:183]\n"
"v_mfma_f32_16x16x32_bf16 v[188:191], v[212:215], v[124:127], v[188:191]\n"
"v_mfma_f32_16x16x32_bf16 v[196:199], v[212:215], v[140:143], v[196:199]\n"
"s_waitcnt lgkmcnt(1)\n"
"v_mfma_f32_16x16x32_bf16 v[184:187], v[204:207], v[96:99], v[144:147]\n"
"ds_read_b128 v[212:215], v248 offset:13312\n"
"v_mfma_f32_16x16x32_bf16 v[192:195], v[204:207], v[112:115], v[148:151]\n"
"v_mfma_f32_16x16x32_bf16 v[200:203], v[204:207], v[128:131], v[152:155]\n"
"s_waitcnt lgkmcnt(1)\n"
"v_exp_f32 v180, v180\n"
"v_mfma_f32_16x16x32_bf16 v[184:187], v[208:211], v[100:103], v[184:187]\n"
"ds_read_b128 v[204:207], v248 offset:13824\n"
"v_exp_f32 v181, v181\n"
"v_mfma_f32_16x16x32_bf16 v[192:195], v[208:211], v[116:119], v[192:195]\n"
"v_exp_f32 v182, v182\n"
"v_mfma_f32_16x16x32_bf16 v[200:203], v[208:211], v[132:135], v[200:203]\n"
"v_exp_f32 v183, v183\n"
"v_mfma_f32_16x16x32_bf16 v[228:231], v[240:243], v[156:159], 0\n"
"v_exp_f32 v188, v188\n"
"v_mfma_f32_16x16x32_bf16 v[232:235], v[240:243], v[164:167], 0\n"
"v_exp_f32 v189, v189\n"
"v_mfma_f32_16x16x32_bf16 v[236:239], v[240:243], v[172:175], 0\n"
"s_waitcnt lgkmcnt(1)\n"
"v_exp_f32 v190, v190\n"
"v_mfma_f32_16x16x32_bf16 v[184:187], v[212:215], v[104:107], v[184:187]\n"
"v_exp_f32 v191, v191\n"
"v_cvt_pk_bf16_f32 v180, v180, v181\n"
"v_mfma_f32_16x16x32_bf16 v[192:195], v[212:215], v[120:123], v[192:195]\n"
"v_cvt_pk_bf16_f32 v181, v182, v183\n"
"v_exp_f32 v196, v196\n"
"v_mfma_f32_16x16x32_bf16 v[200:203], v[212:215], v[136:139], v[200:203]\n"
"s_waitcnt lgkmcnt(0)\n"
"v_exp_f32 v197, v197\n"
"v_mfma_f32_16x16x32_bf16 v[184:187], v[204:207], v[108:111], v[184:187]\n"
"v_exp_f32 v198, v198\n"
"v_mfma_f32_16x16x32_bf16 v[192:195], v[204:207], v[124:127], v[192:195]\n"
"v_exp_f32 v199, v199\n"
"v_cvt_pk_bf16_f32 v188, v188, v189\n"
"v_mfma_f32_16x16x32_bf16 v[200:203], v[204:207], v[140:143], v[200:203]\n"
"v_cvt_pk_bf16_f32 v189, v190, v191\n"
"v_cvt_pk_bf16_f32 v196, v196, v197\n"
"v_cvt_pk_bf16_f32 v197, v198, v199\n"
"v_max3_f32 v247, v228, v232, v236\n"
"v_cmp_lt_f32 vcc, s64, v247\n"
"s_cbranch_vccnz .Lk1_slow0\n"
".Lk1_ret0:\n"
"v_mfma_f32_16x16x32_bf16 v[0:3], v[220:223], v[156:159], v[0:3]\n"
"v_exp_f32 v184, v184\n"
"v_mfma_f32_16x16x32_bf16 v[32:35], v[220:223], v[164:167], v[32:35]\n"
"v_exp_f32 v185, v185\n"
"v_mfma_f32_16x16x32_bf16 v[64:67], v[220:223], v[172:175], v[64:67]\n"
"ds_read_b64_tr_b16 v[220:221], v250 offset:8704\n"
"ds_read_b64_tr_b16 v[222:223], v250 offset:12800\n"
"v_mfma_f32_16x16x32_bf16 v[4:7], v[224:227], v[156:159], v[4:7]\n"
"v_exp_f32 v186, v186\n"
"v_mfma_f32_16x16x32_bf16 v[36:39], v[224:227], v[164:167], v[36:39]\n"
"v_exp_f32 v187, v187\n"
"v_mfma_f32_16x16x32_bf16 v[68:71], v[224:227], v[172:175], v[68:71]\n"
"ds_read_b64_tr_b16 v[224:225], v249 offset:9216\n"
"ds_read_b64_tr_b16 v[226:227], v249 offset:13312\n"
"v_mfma_f32_16x16x32_bf16 v[8:11], v[216:219], v[156:159], v[8:11]\n"
"v_exp_f32 v192, v192\n"
"v_mfma_f32_16x16x32_bf16 v[40:43], v[216:219], v[164:167], v[40:43]\n"
"v_exp_f32 v193, v193\n"
"v_mfma_f32_16x16x32_bf16 v[72:75], v[216:219], v[172:175], v[72:75]\n"
"s_waitcnt lgkmcnt(2)\n"
"ds_read_b64_tr_b16 v[216:217], v250 offset:9216\n"
"ds_read_b64_tr_b16 v[218:219], v250 offset:13312\n"
"v_mfma_f32_16x16x32_bf16 v[12:15], v[220:223], v[156:159], v[12:15]\n"
"v_exp_f32 v194, v194\n"
"v_mfma_f32_16x16x32_bf16 v[44:47], v[220:223], v[164:167], v[44:47]\n"
"v_exp_f32 v195, v195\n"
"v_cvt_pk_bf16_f32 v182, v184, v185\n"
"v_mfma_f32_16x16x32_bf16 v[76:79], v[220:223], v[172:175], v[76:79]\n"
"s_waitcnt lgkmcnt(2)\n"
"ds_read_b64_tr_b16 v[220:221], v249 offset:9728\n"
"ds_read_b64_tr_b16 v[222:223], v249 offset:13824\n"
"v_cvt_pk_bf16_f32 v183, v186, v187\n"
"v_mfma_f32_16x16x32_bf16 v[16:19], v[224:227], v[156:159], v[16:19]\n"
"v_exp_f32 v200, v200\n"
"v_mfma_f32_16x16x32_bf16 v[48:51], v[224:227], v[164:167], v[48:51]\n"
"v_exp_f32 v201, v201\n"
"v_mfma_f32_16x16x32_bf16 v[80:83], v[224:227], v[172:175], v[80:83]\n"
"s_waitcnt lgkmcnt(2)\n"
"ds_read_b64_tr_b16 v[224:225], v250 offset:9728\n"
"ds_read_b64_tr_b16 v[226:227], v250 offset:13824\n"
"v_mfma_f32_16x16x32_bf16 v[20:23], v[216:219], v[156:159], v[20:23]\n"
"v_exp_f32 v202, v202\n"
"v_exp_f32 v203, v203\n"
"v_mfma_f32_16x16x32_bf16 v[52:55], v[216:219], v[164:167], v[52:55]\n"
"v_cvt_pk_bf16_f32 v190, v192, v193\n"
"v_cvt_pk_bf16_f32 v191, v194, v195\n"
"v_cvt_pk_bf16_f32 v198, v200, v201\n"
"v_cvt_pk_bf16_f32 v199, v202, v203\n"
"v_mfma_f32_16x16x32_bf16 v[84:87], v[216:219], v[172:175], v[84:87]\n"
"v_add_f32 v244, v244, v228\n"
"v_add_f32 v245, v245, v232\n"
"v_add_f32 v246, v246, v236\n"
"v_mfma_f32_16x16x32_bf16 v[228:231], v[240:243], v[180:183], 0\n"
"v_mfma_f32_16x16x32_bf16 v[232:235], v[240:243], v[188:191], 0\n"
"v_mfma_f32_16x16x32_bf16 v[236:239], v[240:243], v[196:199], 0\n"
"s_waitcnt lgkmcnt(2)\n"
"v_add_u32 v252, s52, v248\n"
"v_mfma_f32_16x16x32_bf16 v[24:27], v[220:223], v[156:159], v[24:27]\n"
"v_mfma_f32_16x16x32_bf16 v[56:59], v[220:223], v[164:167], v[56:59]\n"
"v_add_u32 v249, s52, v249\n"
"v_add_u32 v250, s52, v250\n"
"v_mfma_f32_16x16x32_bf16 v[88:91], v[220:223], v[172:175], v[88:91]\n"
"s_nop 0\n"
"v_max3_f32 v247, v228, v232, v236\n"
"v_cmp_lt_f32 vcc, s64, v247\n"
"s_cbranch_vccnz .Lk1_slow1\n"
".Lk1_ret1:\n"
"v_mov_b32 v248, v252\n"
"s_waitcnt vmcnt(0) lgkmcnt(0)\n"
"s_barrier\n"
"s_cmp_lt_u32 s50, 16\n"
"s_cbranch_scc1 .Lk1_loop\n"
"s_setprio 1\n"
"s_cmp_eq_u32 s45, 0\n"
"s_cbranch_scc1 .Lk1_dr\n"
"s_setprio 3\n"
".Lk1_dr:\n"
"ds_read_b64_tr_b16 v[216:217], v249\n"
"ds_read_b64_tr_b16 v[218:219], v249 offset:4096\n"
"ds_read_b64_tr_b16 v[220:221], v250\n"
"ds_read_b64_tr_b16 v[222:223], v250 offset:4096\n"
"v_mfma_f32_16x16x32_bf16 v[28:31], v[224:227], v[156:159], v[28:31]\n"
"v_add_f32 v244, v244, v228\n"
"v_add_f32 v245, v245, v232\n"
"v_mfma_f32_16x16x32_bf16 v[60:63], v[224:227], v[164:167], v[60:63]\n"
"v_add_f32 v246, v246, v236\n"
"v_mfma_f32_16x16x32_bf16 v[92:95], v[224:227], v[172:175], v[92:95]\n"
"s_waitcnt lgkmcnt(2)\n"
"ds_read_b64_tr_b16 v[224:225], v249 offset:512\n"
"ds_read_b64_tr_b16 v[226:227], v249 offset:4608\n"
"v_mfma_f32_16x16x32_bf16 v[0:3], v[216:219], v[180:183], v[0:3]\n"
"v_mfma_f32_16x16x32_bf16 v[32:35], v[216:219], v[188:191], v[32:35]\n"
"v_mfma_f32_16x16x32_bf16 v[64:67], v[216:219], v[196:199], v[64:67]\n"
"s_waitcnt lgkmcnt(2)\n"
"ds_read_b64_tr_b16 v[216:217], v250 offset:512\n"
"ds_read_b64_tr_b16 v[218:219], v250 offset:4608\n"
"v_mfma_f32_16x16x32_bf16 v[4:7], v[220:223], v[180:183], v[4:7]\n"
"v_mfma_f32_16x16x32_bf16 v[36:39], v[220:223], v[188:191], v[36:39]\n"
"v_mfma_f32_16x16x32_bf16 v[68:71], v[220:223], v[196:199], v[68:71]\n"
"s_waitcnt lgkmcnt(2)\n"
"ds_read_b64_tr_b16 v[220:221], v249 offset:1024\n"
"ds_read_b64_tr_b16 v[222:223], v249 offset:5120\n"
"v_mfma_f32_16x16x32_bf16 v[8:11], v[224:227], v[180:183], v[8:11]\n"
"v_mfma_f32_16x16x32_bf16 v[40:43], v[224:227], v[188:191], v[40:43]\n"
"v_mfma_f32_16x16x32_bf16 v[72:75], v[224:227], v[196:199], v[72:75]\n"
"s_waitcnt lgkmcnt(2)\n"
"ds_read_b64_tr_b16 v[224:225], v250 offset:1024\n"
"ds_read_b64_tr_b16 v[226:227], v250 offset:5120\n"
"v_mfma_f32_16x16x32_bf16 v[12:15], v[216:219], v[180:183], v[12:15]\n"
"v_mfma_f32_16x16x32_bf16 v[44:47], v[216:219], v[188:191], v[44:47]\n"
"v_mfma_f32_16x16x32_bf16 v[76:79], v[216:219], v[196:199], v[76:79]\n"
"s_waitcnt lgkmcnt(2)\n"
"ds_read_b64_tr_b16 v[216:217], v249 offset:1536\n"
"ds_read_b64_tr_b16 v[218:219], v249 offset:5632\n"
"v_mfma_f32_16x16x32_bf16 v[16:19], v[220:223], v[180:183], v[16:19]\n"
"v_mfma_f32_16x16x32_bf16 v[48:51], v[220:223], v[188:191], v[48:51]\n"
"v_mfma_f32_16x16x32_bf16 v[80:83], v[220:223], v[196:199], v[80:83]\n"
"s_waitcnt lgkmcnt(2)\n"
"ds_read_b64_tr_b16 v[220:221], v250 offset:1536\n"
"ds_read_b64_tr_b16 v[222:223], v250 offset:5632\n"
"v_mfma_f32_16x16x32_bf16 v[20:23], v[224:227], v[180:183], v[20:23]\n"
"v_mfma_f32_16x16x32_bf16 v[52:55], v[224:227], v[188:191], v[52:55]\n"
"v_mfma_f32_16x16x32_bf16 v[84:87], v[224:227], v[196:199], v[84:87]\n"
"s_waitcnt lgkmcnt(2)\n"
"v_mfma_f32_16x16x32_bf16 v[24:27], v[216:219], v[180:183], v[24:27]\n"
"v_mfma_f32_16x16x32_bf16 v[56:59], v[216:219], v[188:191], v[56:59]\n"
"v_mfma_f32_16x16x32_bf16 v[88:91], v[216:219], v[196:199], v[88:91]\n"
"s_waitcnt lgkmcnt(0)\n"
"v_mfma_f32_16x16x32_bf16 v[28:31], v[220:223], v[180:183], v[28:31]\n"
"v_mfma_f32_16x16x32_bf16 v[60:63], v[220:223], v[188:191], v[60:63]\n"
"v_mfma_f32_16x16x32_bf16 v[92:95], v[220:223], v[196:199], v[92:95]\n"
"v_mbcnt_lo_u32_b32 v238, -1, 0\n"
"v_mbcnt_hi_u32_b32 v238, -1, v238\n"
"v_mov_b32 v224, 0x18800\n"
"v_mov_b32 v225, 1\n"
"v_lshlrev_b32 v237, 4, v238\n"
"s_mul_i32 s80, s44, 0x60000\n"
"s_lshl_b32 s60, s66, 12\n"
"s_add_u32 s80, s80, s60\n"
"s_add_u32 s82, s80, 0x20000\n"
"s_add_u32 s83, s80, 0x40000\n"
"s_lshl_b32 s50, s46, 5\n"
"s_add_u32 s50, s50, s66\n"
"s_lshl_b32 s50, s50, 7\n"
"s_add_u32 s50, s50, 0x1000\n"
"v_mov_b32 v247, v244\n"
"v_mov_b32 v236, v244\n"
"s_nop 1\n"
"v_permlane32_swap_b32 v247, v236\n"
"v_add_f32 v244, v247, v236\n"
"v_mov_b32 v247, v244\n"
"v_mov_b32 v236, v244\n"
"s_nop 1\n"
"v_permlane16_swap_b32 v247, v236\n"
"v_add_f32 v244, v247, v236\n"
"v_rcp_f32 v244, v244\n"
"v_mov_b32 v247, v245\n"
"v_mov_b32 v236, v245\n"
"s_nop 1\n"
"v_permlane32_swap_b32 v247, v236\n"
"v_add_f32 v245, v247, v236\n"
"v_mov_b32 v247, v245\n"
"v_mov_b32 v236, v245\n"
"s_nop 1\n"
"v_permlane16_swap_b32 v247, v236\n"
"v_add_f32 v245, v247, v236\n"
"v_rcp_f32 v245, v245\n"
"v_mov_b32 v247, v246\n"
"v_mov_b32 v236, v246\n"
"s_nop 1\n"
"v_permlane32_swap_b32 v247, v236\n"
"v_add_f32 v246, v247, v236\n"
"v_mov_b32 v247, v246\n"
"v_mov_b32 v236, v246\n"
"s_nop 1\n"
"v_permlane16_swap_b32 v247, v236\n"
"v_add_f32 v246, v247, v236\n"
"v_rcp_f32 v246, v246\n"
"v_mul_f32 v0, v0, v244\n"
"v_mul_f32 v4, v4, v244\n"
"v_mul_f32 v1, v1, v244\n"
"v_mul_f32 v5, v5, v244\n"
"v_mul_f32 v2, v2, v244\n"
"v_mul_f32 v6, v6, v244\n"
"v_mul_f32 v3, v3, v244\n"
"v_mul_f32 v7, v7, v244\n"
"v_cvt_pk_bf16_f32 v96, v0, v1\n"
"v_cvt_pk_bf16_f32 v97, v2, v3\n"
"v_cvt_pk_bf16_f32 v98, v4, v5\n"
"v_cvt_pk_bf16_f32 v99, v6, v7\n"
"v_mul_f32 v8, v8, v244\n"
"v_mul_f32 v12, v12, v244\n"
"v_mul_f32 v9, v9, v244\n"
"v_mul_f32 v13, v13, v244\n"
"v_mul_f32 v10, v10, v244\n"
"v_mul_f32 v14, v14, v244\n"
"v_mul_f32 v11, v11, v244\n"
"v_mul_f32 v15, v15, v244\n"
"v_cvt_pk_bf16_f32 v100, v8, v9\n"
"v_cvt_pk_bf16_f32 v101, v10, v11\n"
"v_cvt_pk_bf16_f32 v102, v12, v13\n"
"v_cvt_pk_bf16_f32 v103, v14, v15\n"
"v_mul_f32 v16, v16, v244\n"
"v_mul_f32 v20, v20, v244\n"
"v_mul_f32 v17, v17, v244\n"
"v_mul_f32 v21, v21, v244\n"
"v_mul_f32 v18, v18, v244\n"
"v_mul_f32 v22, v22, v244\n"
"v_mul_f32 v19, v19, v244\n"
"v_mul_f32 v23, v23, v244\n"
"v_cvt_pk_bf16_f32 v104, v16, v17\n"
"v_cvt_pk_bf16_f32 v105, v18, v19\n"
"v_cvt_pk_bf16_f32 v106, v20, v21\n"
"v_cvt_pk_bf16_f32 v107, v22, v23\n"
"v_mul_f32 v24, v24, v244\n"
"v_mul_f32 v28, v28, v244\n"
"v_mul_f32 v25, v25, v244\n"
"v_mul_f32 v29, v29, v244\n"
"v_mul_f32 v26, v26, v244\n"
"v_mul_f32 v30, v30, v244\n"
"v_mul_f32 v27, v27, v244\n"
"v_mul_f32 v31, v31, v244\n"
"v_cvt_pk_bf16_f32 v108, v24, v25\n"
"v_cvt_pk_bf16_f32 v109, v26, v27\n"
"v_cvt_pk_bf16_f32 v110, v28, v29\n"
"v_cvt_pk_bf16_f32 v111, v30, v31\n"
"s_cmp_eq_u32 s45, 0\n"
"s_cbranch_scc1 .Lk1_es0\n"
"buffer_store_dwordx4 v[96:99], v237, s[40:43], s80 offen sc1\n"
"buffer_store_dwordx4 v[100:103], v237, s[40:43], s80 offen offset:1024 sc1\n"
"buffer_store_dwordx4 v[104:107], v237, s[40:43], s80 offen offset:2048 sc1\n"
"buffer_store_dwordx4 v[108:111], v237, s[40:43], s80 offen offset:3072 sc1\n"
".Lk1_es0:\n"
"v_mul_f32 v32, v32, v245\n"
"v_mul_f32 v36, v36, v245\n"
"v_mul_f32 v33, v33, v245\n"
"v_mul_f32 v37, v37, v245\n"
"v_mul_f32 v34, v34, v245\n"
"v_mul_f32 v38, v38, v245\n"
"v_mul_f32 v35, v35, v245\n"
"v_mul_f32 v39, v39, v245\n"
"v_cvt_pk_bf16_f32 v112, v32, v33\n"
"v_cvt_pk_bf16_f32 v113, v34, v35\n"
"v_cvt_pk_bf16_f32 v114, v36, v37\n"
"v_cvt_pk_bf16_f32 v115, v38, v39\n"
"v_mul_f32 v40, v40, v245\n"
"v_mul_f32 v44, v44, v245\n"
"v_mul_f32 v41, v41, v245\n"
"v_mul_f32 v45, v45, v245\n"
"v_mul_f32 v42, v42, v245\n"
"v_mul_f32 v46, v46, v245\n"
"v_mul_f32 v43, v43, v245\n"
"v_mul_f32 v47, v47, v245\n"
"v_cvt_pk_bf16_f32 v116, v40, v41\n"
"v_cvt_pk_bf16_f32 v117, v42, v43\n"
"v_cvt_pk_bf16_f32 v118, v44, v45\n"
"v_cvt_pk_bf16_f32 v119, v46, v47\n"
"v_mul_f32 v48, v48, v245\n"
"v_mul_f32 v52, v52, v245\n"
"v_mul_f32 v49, v49, v245\n"
"v_mul_f32 v53, v53, v245\n"
"v_mul_f32 v50, v50, v245\n"
"v_mul_f32 v54, v54, v245\n"
"v_mul_f32 v51, v51, v245\n"
"v_mul_f32 v55, v55, v245\n"
"v_cvt_pk_bf16_f32 v120, v48, v49\n"
"v_cvt_pk_bf16_f32 v121, v50, v51\n"
"v_cvt_pk_bf16_f32 v122, v52, v53\n"
"v_cvt_pk_bf16_f32 v123, v54, v55\n"
"v_mul_f32 v56, v56, v245\n"
"v_mul_f32 v60, v60, v245\n"
"v_mul_f32 v57, v57, v245\n"
"v_mul_f32 v61, v61, v245\n"
"v_mul_f32 v58, v58, v245\n"
"v_mul_f32 v62, v62, v245\n"
"v_mul_f32 v59, v59, v245\n"
"v_mul_f32 v63, v63, v245\n"
"v_cvt_pk_bf16_f32 v124, v56, v57\n"
"v_cvt_pk_bf16_f32 v125, v58, v59\n"
"v_cvt_pk_bf16_f32 v126, v60, v61\n"
"v_cvt_pk_bf16_f32 v127, v62, v63\n"
"s_cmp_eq_u32 s45, 0\n"
"s_cbranch_scc1 .Lk1_es1\n"
"buffer_store_dwordx4 v[112:115], v237, s[40:43], s82 offen sc1\n"
"buffer_store_dwordx4 v[116:119], v237, s[40:43], s82 offen offset:1024 sc1\n"
"buffer_store_dwordx4 v[120:123], v237, s[40:43], s82 offen offset:2048 sc1\n"
"buffer_store_dwordx4 v[124:127], v237, s[40:43], s82 offen offset:3072 sc1\n"
".Lk1_es1:\n"
"v_mul_f32 v64, v64, v246\n"
"v_mul_f32 v68, v68, v246\n"
"v_mul_f32 v65, v65, v246\n"
"v_mul_f32 v69, v69, v246\n"
"v_mul_f32 v66, v66, v246\n"
"v_mul_f32 v70, v70, v246\n"
"v_mul_f32 v67, v67, v246\n"
"v_mul_f32 v71, v71, v246\n"
"v_cvt_pk_bf16_f32 v128, v64, v65\n"
"v_cvt_pk_bf16_f32 v129, v66, v67\n"
"v_cvt_pk_bf16_f32 v130, v68, v69\n"
"v_cvt_pk_bf16_f32 v131, v70, v71\n"
"v_mul_f32 v72, v72, v246\n"
"v_mul_f32 v76, v76, v246\n"
"v_mul_f32 v73, v73, v246\n"
"v_mul_f32 v77, v77, v246\n"
"v_mul_f32 v74, v74, v246\n"
"v_mul_f32 v78, v78, v246\n"
"v_mul_f32 v75, v75, v246\n"
"v_mul_f32 v79, v79, v246\n"
"v_cvt_pk_bf16_f32 v132, v72, v73\n"
"v_cvt_pk_bf16_f32 v133, v74, v75\n"
"v_cvt_pk_bf16_f32 v134, v76, v77\n"
"v_cvt_pk_bf16_f32 v135, v78, v79\n"
"v_mul_f32 v80, v80, v246\n"
"v_mul_f32 v84, v84, v246\n"
"v_mul_f32 v81, v81, v246\n"
"v_mul_f32 v85, v85, v246\n"
"v_mul_f32 v82, v82, v246\n"
"v_mul_f32 v86, v86, v246\n"
"v_mul_f32 v83, v83, v246\n"
"v_mul_f32 v87, v87, v246\n"
"v_cvt_pk_bf16_f32 v136, v80, v81\n"
"v_cvt_pk_bf16_f32 v137, v82, v83\n"
"v_cvt_pk_bf16_f32 v138, v84, v85\n"
"v_cvt_pk_bf16_f32 v139, v86, v87\n"
"v_mul_f32 v88, v88, v246\n"
"v_mul_f32 v92, v92, v246\n"
"v_mul_f32 v89, v89, v246\n"
"v_mul_f32 v93, v93, v246\n"
"v_mul_f32 v90, v90, v246\n"
"v_mul_f32 v94, v94, v246\n"
"v_mul_f32 v91, v91, v246\n"
"v_mul_f32 v95, v95, v246\n"
"v_cvt_pk_bf16_f32 v140, v88, v89\n"
"v_cvt_pk_bf16_f32 v141, v90, v91\n"
"v_cvt_pk_bf16_f32 v142, v92, v93\n"
"v_cvt_pk_bf16_f32 v143, v94, v95\n"
"s_cmp_eq_u32 s45, 0\n"
"s_cbranch_scc1 .Lk1_es2\n"
"buffer_store_dwordx4 v[128:131], v237, s[40:43], s83 offen sc1\n"
"buffer_store_dwordx4 v[132:135], v237, s[40:43], s83 offen offset:1024 sc1\n"
"buffer_store_dwordx4 v[136:139], v237, s[40:43], s83 offen offset:2048 sc1\n"
"buffer_store_dwordx4 v[140:143], v237, s[40:43], s83 offen offset:3072 sc1\n"
".Lk1_es2:\n"
"s_cmp_eq_u32 s45, 0\n"
"s_cbranch_scc1 .Lk1_w0r2\n"
"s_waitcnt vmcnt(0)\n"
"s_mov_b64 exec, 1\n"
"ds_add_rtn_u32 v236, v224, v225 offset:8\n"
"s_mov_b64 exec, -1\n"
"s_waitcnt lgkmcnt(0)\n"
"v_readfirstlane_b32 s60, v236\n"
"s_nop 0\n"
"s_cmp_eq_u32 s60, 2\n"
"s_cbranch_scc0 .Lk1_pb_o2\n"
"v_mov_b32 v226, s98\n"
"v_mov_b32 v227, s99\n"
"v_mov_b32 v236, 0\n"
"s_mov_b64 exec, 1\n"
"buffer_store_dwordx2 v[226:227], v236, s[84:87], s50 offen sc1\n"
"s_mov_b64 exec, -1\n"
".Lk1_pb_o2:\n"
"s_branch .Lk1_end\n"
".Lk1_gq:\n"
"s_setprio 0\n"
"s_lshl_b32 s60, s45, 10\n"
"v_add_u32 v234, s60, v234\n"
"v_add_u32 v235, s60, v235\n"
"s_waitcnt vmcnt(13)\n"
"s_waitcnt lgkmcnt(0)\n"
"s_barrier\n"
"ds_read_b128 v[176:179], v254 offset:36864\n"
"ds_read_b128 v[180:183], v254 offset:37376\n"
"ds_read_b64_tr_b16 v[144:145], v234 offset:4096\n"
"ds_read_b64_tr_b16 v[146:147], v234 offset:8192\n"
"ds_read_b64_tr_b16 v[148:149], v235 offset:4096\n"
"ds_read_b64_tr_b16 v[150:151], v235 offset:8192\n"
"ds_read_b64_tr_b16 v[152:153], v234 offset:4608\n"
"ds_read_b64_tr_b16 v[154:155], v234 offset:8704\n"
"ds_read_b64_tr_b16 v[156:157], v235 offset:4608\n"
"ds_read_b64_tr_b16 v[158:159], v235 offset:8704\n"
"ds_read_b64_tr_b16 v[160:161], v234 offset:12288\n"
"ds_read_b64_tr_b16 v[162:163], v234 offset:16384\n"
"ds_read_b64_tr_b16 v[164:165], v235 offset:12288\n"
"ds_read_b64_tr_b16 v[166:167], v235 offset:16384\n"
"s_waitcnt lgkmcnt(10)\n"
"v_mfma_f32_16x16x32_bf16 v[0:3], v[144:147], v[176:179], 0\n"
"s_waitcnt lgkmcnt(8)\n"
"ds_read_b64_tr_b16 v[168:169], v234 offset:12800\n"
"ds_read_b64_tr_b16 v[170:171], v234 offset:16896\n"
"v_mfma_f32_16x16x32_bf16 v[4:7], v[148:151], v[176:179], 0\n"
"s_waitcnt lgkmcnt(8)\n"
"ds_read_b64_tr_b16 v[172:173], v235 offset:12800\n"
"ds_read_b64_tr_b16 v[174:175], v235 offset:16896\n"
"v_mfma_f32_16x16x32_bf16 v[8:11], v[152:155], v[176:179], 0\n"
"s_waitcnt lgkmcnt(8)\n"
"ds_read_b128 v[184:187], v254 offset:38912\n"
"ds_read_b128 v[188:191], v254 offset:39424\n"
"v_mfma_f32_16x16x32_bf16 v[12:15], v[156:159], v[176:179], 0\n"
"s_waitcnt lgkmcnt(8)\n"
"v_mfma_f32_16x16x32_bf16 v[0:3], v[160:163], v[180:183], v[0:3]\n"
"s_waitcnt lgkmcnt(6)\n"
"v_mfma_f32_16x16x32_bf16 v[4:7], v[164:167], v[180:183], v[4:7]\n"
"s_waitcnt lgkmcnt(4)\n"
"v_mfma_f32_16x16x32_bf16 v[8:11], v[168:171], v[180:183], v[8:11]\n"
"s_waitcnt lgkmcnt(2)\n"
"v_mfma_f32_16x16x32_bf16 v[12:15], v[172:175], v[180:183], v[12:15]\n"
"s_waitcnt lgkmcnt(0)\n"
"v_mfma_f32_16x16x32_bf16 v[16:19], v[144:147], v[184:187], 0\n"
"ds_read_b128 v[176:179], v254 offset:40960\n"
"ds_read_b128 v[180:183], v254 offset:41472\n"
"v_mfma_f32_16x16x32_bf16 v[20:23], v[148:151], v[184:187], 0\n"
"v_mfma_f32_16x16x32_bf16 v[24:27], v[152:155], v[184:187], 0\n"
"v_mfma_f32_16x16x32_bf16 v[28:31], v[156:159], v[184:187], 0\n"
"v_mfma_f32_16x16x32_bf16 v[16:19], v[160:163], v[188:191], v[16:19]\n"
"v_mfma_f32_16x16x32_bf16 v[20:23], v[164:167], v[188:191], v[20:23]\n"
"v_mfma_f32_16x16x32_bf16 v[24:27], v[168:171], v[188:191], v[24:27]\n"
"v_mfma_f32_16x16x32_bf16 v[28:31], v[172:175], v[188:191], v[28:31]\n"
"s_waitcnt lgkmcnt(0)\n"
"v_mfma_f32_16x16x32_bf16 v[32:35], v[144:147], v[176:179], 0\n"
"ds_read_b128 v[184:187], v254 offset:43008\n"
"ds_read_b128 v[188:191], v254 offset:43520\n"
"v_mfma_f32_16x16x32_bf16 v[36:39], v[148:151], v[176:179], 0\n"
"v_mfma_f32_16x16x32_bf16 v[40:43], v[152:155], v[176:179], 0\n"
"v_mfma_f32_16x16x32_bf16 v[44:47], v[156:159], v[176:179], 0\n"
"v_mfma_f32_16x16x32_bf16 v[32:35], v[160:163], v[180:183], v[32:35]\n"
"v_mfma_f32_16x16x32_bf16 v[36:39], v[164:167], v[180:183], v[36:39]\n"
"v_mfma_f32_16x16x32_bf16 v[40:43], v[168:171], v[180:183], v[40:43]\n"
"v_mfma_f32_16x16x32_bf16 v[44:47], v[172:175], v[180:183], v[44:47]\n"
"s_waitcnt lgkmcnt(0)\n"
"v_mfma_f32_16x16x32_bf16 v[48:51], v[144:147], v[184:187], 0\n"
"ds_read_b128 v[176:179], v254 offset:45056\n"
"ds_read_b128 v[180:183], v254 offset:45568\n"
"v_mfma_f32_16x16x32_bf16 v[52:55], v[148:151], v[184:187], 0\n"
"v_mfma_f32_16x16x32_bf16 v[56:59], v[152:155], v[184:187], 0\n"
"v_mfma_f32_16x16x32_bf16 v[60:63], v[156:159], v[184:187], 0\n"
"v_mfma_f32_16x16x32_bf16 v[48:51], v[160:163], v[188:191], v[48:51]\n"
"v_mfma_f32_16x16x32_bf16 v[52:55], v[164:167], v[188:191], v[52:55]\n"
"v_mfma_f32_16x16x32_bf16 v[56:59], v[168:171], v[188:191], v[56:59]\n"
"v_mfma_f32_16x16x32_bf16 v[60:63], v[172:175], v[188:191], v[60:63]\n"
"s_waitcnt lgkmcnt(0)\n"
"v_mfma_f32_16x16x32_bf16 v[64:67], v[144:147], v[176:179], 0\n"
"ds_read_b128 v[184:187], v254 offset:47104\n"
"ds_read_b128 v[188:191], v254 offset:47616\n"
"v_mfma_f32_16x16x32_bf16 v[68:71], v[148:151], v[176:179], 0\n"
"v_mfma_f32_16x16x32_bf16 v[72:75], v[152:155], v[176:179], 0\n"
"v_mfma_f32_16x16x32_bf16 v[76:79], v[156:159], v[176:179], 0\n"
"v_mfma_f32_16x16x32_bf16 v[64:67], v[160:163], v[180:183], v[64:67]\n"
"v_mfma_f32_16x16x32_bf16 v[68:71], v[164:167], v[180:183], v[68:71]\n"
"v_mfma_f32_16x16x32_bf16 v[72:75], v[168:171], v[180:183], v[72:75]\n"
"v_mfma_f32_16x16x32_bf16 v[76:79], v[172:175], v[180:183], v[76:79]\n"
"s_waitcnt lgkmcnt(0)\n"
"v_mfma_f32_16x16x32_bf16 v[80:83], v[144:147], v[184:187], 0\n"
"v_mfma_f32_16x16x32_bf16 v[84:87], v[148:151], v[184:187], 0\n"
"v_mfma_f32_16x16x32_bf16 v[88:91], v[152:155], v[184:187], 0\n"
"v_mfma_f32_16x16x32_bf16 v[92:95], v[156:159], v[184:187], 0\n"
"v_mfma_f32_16x16x32_bf16 v[80:83], v[160:163], v[188:191], v[80:83]\n"
"v_mfma_f32_16x16x32_bf16 v[84:87], v[164:167], v[188:191], v[84:87]\n"
"v_mfma_f32_16x16x32_bf16 v[88:91], v[168:171], v[188:191], v[88:91]\n"
"v_mfma_f32_16x16x32_bf16 v[92:95], v[172:175], v[188:191], v[92:95]\n"
"s_waitcnt vmcnt(5)\n"
"s_waitcnt lgkmcnt(0)\n"
"s_barrier\n"
"ds_read_b128 v[176:179], v254 offset:49152\n"
"ds_read_b128 v[180:183], v254 offset:49664\n"
"s_waitcnt lgkmcnt(0)\n"
"v_mfma_f32_16x16x32_bf16 v[96:99], v[144:147], v[176:179], 0\n"
"ds_read_b128 v[184:187], v254 offset:51200\n"
"ds_read_b128 v[188:191], v254 offset:51712\n"
"v_mfma_f32_16x16x32_bf16 v[100:103], v[148:151], v[176:179], 0\n"
"v_mfma_f32_16x16x32_bf16 v[104:107], v[152:155], v[176:179], 0\n"
"v_mfma_f32_16x16x32_bf16 v[108:111], v[156:159], v[176:179], 0\n"
"v_mfma_f32_16x16x32_bf16 v[96:99], v[160:163], v[180:183], v[96:99]\n"
"v_mfma_f32_16x16x32_bf16 v[100:103], v[164:167], v[180:183], v[100:103]\n"
"v_mfma_f32_16x16x32_bf16 v[104:107], v[168:171], v[180:183], v[104:107]\n"
"v_mfma_f32_16x16x32_bf16 v[108:111], v[172:175], v[180:183], v[108:111]\n"
"s_waitcnt lgkmcnt(0)\n"
"v_mfma_f32_16x16x32_bf16 v[112:115], v[144:147], v[184:187], 0\n"
"ds_read_b128 v[176:179], v254 offset:53248\n"
"ds_read_b128 v[180:183], v254 offset:53760\n"
"v_mfma_f32_16x16x32_bf16 v[116:119], v[148:151], v[184:187], 0\n"
"v_mfma_f32_16x16x32_bf16 v[120:123], v[152:155], v[184:187], 0\n"
"v_mfma_f32_16x16x32_bf16 v[124:127], v[156:159], v[184:187], 0\n"
"v_mfma_f32_16x16x32_bf16 v[112:115], v[160:163], v[188:191], v[112:115]\n"
"v_mfma_f32_16x16x32_bf16 v[116:119], v[164:167], v[188:191], v[116:119]\n"
"v_mfma_f32_16x16x32_bf16 v[120:123], v[168:171], v[188:191], v[120:123]\n"
"v_mfma_f32_16x16x32_bf16 v[124:127], v[172:175], v[188:191], v[124:127]\n"
"s_waitcnt lgkmcnt(0)\n"
"v_mfma_f32_16x16x32_bf16 v[128:131], v[144:147], v[176:179], 0\n"
"ds_read_b128 v[184:187], v254 offset:55296\n"
"ds_read_b128 v[188:191], v254 offset:55808\n"
"v_mfma_f32_16x16x32_bf16 v[132:135], v[148:151], v[176:179], 0\n"
"ds_read_b64_tr_b16 v[144:145], v234 offset:20480\n"
"ds_read_b64_tr_b16 v[146:147], v234 offset:24576\n"
"v_mfma_f32_16x16x32_bf16 v[136:139], v[152:155], v[176:179], 0\n"
"ds_read_b64_tr_b16 v[148:149], v235 offset:20480\n"
"ds_read_b64_tr_b16 v[150:151], v235 offset:24576\n"
"v_mfma_f32_16x16x32_bf16 v[140:143], v[156:159], v[176:179], 0\n"
"ds_read_b64_tr_b16 v[152:153], v234 offset:20992\n"
"ds_read_b64_tr_b16 v[154:155], v234 offset:25088\n"
"v_mfma_f32_16x16x32_bf16 v[128:131], v[160:163], v[180:183], v[128:131]\n"
"ds_read_b64_tr_b16 v[156:157], v235 offset:20992\n"
"ds_read_b64_tr_b16 v[158:159], v235 offset:25088\n"
"v_mfma_f32_16x16x32_bf16 v[132:135], v[164:167], v[180:183], v[132:135]\n"
"ds_read_b64_tr_b16 v[160:161], v234 offset:28672\n"
"ds_read_b64_tr_b16 v[162:163], v234 offset:32768\n"
"v_mfma_f32_16x16x32_bf16 v[136:139], v[168:171], v[180:183], v[136:139]\n"
"ds_read_b64_tr_b16 v[164:165], v235 offset:28672\n"
"ds_read_b64_tr_b16 v[166:167], v235 offset:32768\n"
"v_mfma_f32_16x16x32_bf16 v[140:143], v[172:175], v[180:183], v[140:143]\n"
"s_waitcnt lgkmcnt(10)\n"
"ds_read_b64_tr_b16 v[168:169], v234 offset:29184\n"
"ds_read_b64_tr_b16 v[170:171], v234 offset:33280\n"
"v_mfma_f32_16x16x32_bf16 v[0:3], v[144:147], v[184:187], v[0:3]\n"
"s_waitcnt lgkmcnt(10)\n"
"ds_read_b64_tr_b16 v[172:173], v235 offset:29184\n"
"ds_read_b64_tr_b16 v[174:175], v235 offset:33280\n"
"v_mfma_f32_16x16x32_bf16 v[4:7], v[148:151], v[184:187], v[4:7]\n"
"s_waitcnt lgkmcnt(10)\n"
"ds_read_b128 v[176:179], v254 offset:57344\n"
"ds_read_b128 v[180:183], v254 offset:57856\n"
"v_mfma_f32_16x16x32_bf16 v[8:11], v[152:155], v[184:187], v[8:11]\n"
"s_waitcnt lgkmcnt(10)\n"
"v_mfma_f32_16x16x32_bf16 v[12:15], v[156:159], v[184:187], v[12:15]\n"
"s_waitcnt lgkmcnt(8)\n"
"v_mfma_f32_16x16x32_bf16 v[0:3], v[160:163], v[188:191], v[0:3]\n"
"s_waitcnt lgkmcnt(6)\n"
"v_mfma_f32_16x16x32_bf16 v[4:7], v[164:167], v[188:191], v[4:7]\n"
"s_waitcnt lgkmcnt(4)\n"
"v_mfma_f32_16x16x32_bf16 v[8:11], v[168:171], v[188:191], v[8:11]\n"
"s_waitcnt lgkmcnt(2)\n"
"v_mfma_f32_16x16x32_bf16 v[12:15], v[172:175], v[188:191], v[12:15]\n"
"s_waitcnt lgkmcnt(0)\n"
"v_mfma_f32_16x16x32_bf16 v[16:19], v[144:147], v[176:179], v[16:19]\n"
"ds_read_b128 v[184:187], v254 offset:59392\n"
"ds_read_b128 v[188:191], v254 offset:59904\n"
"v_mfma_f32_16x16x32_bf16 v[20:23], v[148:151], v[176:179], v[20:23]\n"
"v_mfma_f32_16x16x32_bf16 v[24:27], v[152:155], v[176:179], v[24:27]\n"
"v_mfma_f32_16x16x32_bf16 v[28:31], v[156:159], v[176:179], v[28:31]\n"
"v_mfma_f32_16x16x32_bf16 v[16:19], v[160:163], v[180:183], v[16:19]\n"
"v_mfma_f32_16x16x32_bf16 v[20:23], v[164:167], v[180:183], v[20:23]\n"
"v_mfma_f32_16x16x32_bf16 v[24:27], v[168:171], v[180:183], v[24:27]\n"
"v_mfma_f32_16x16x32_bf16 v[28:31], v[172:175], v[180:183], v[28:31]\n"
"s_waitcnt lgkmcnt(0)\n"
"v_mfma_f32_16x16x32_bf16 v[32:35], v[144:147], v[184:187], v[32:35]\n"
"ds_read_b128 v[176:179], v254 offset:61440\n"
"ds_read_b128 v[180:183], v254 offset:61952\n"
"v_mfma_f32_16x16x32_bf16 v[36:39], v[148:151], v[184:187], v[36:39]\n"
"v_mfma_f32_16x16x32_bf16 v[40:43], v[152:155], v[184:187], v[40:43]\n"
"v_mfma_f32_16x16x32_bf16 v[44:47], v[156:159], v[184:187], v[44:47]\n"
"v_mfma_f32_16x16x32_bf16 v[32:35], v[160:163], v[188:191], v[32:35]\n"
"v_mfma_f32_16x16x32_bf16 v[36:39], v[164:167], v[188:191], v[36:39]\n"
"v_mfma_f32_16x16x32_bf16 v[40:43], v[168:171], v[188:191], v[40:43]\n"
"v_mfma_f32_16x16x32_bf16 v[44:47], v[172:175], v[188:191], v[44:47]\n"
"s_waitcnt lgkmcnt(0)\n"
"v_mfma_f32_16x16x32_bf16 v[48:51], v[144:147], v[176:179], v[48:51]\n"
"v_mfma_f32_16x16x32_bf16 v[52:55], v[148:151], v[176:179], v[52:55]\n"
"v_mfma_f32_16x16x32_bf16 v[56:59], v[152:155], v[176:179], v[56:59]\n"
"v_mfma_f32_16x16x32_bf16 v[60:63], v[156:159], v[176:179], v[60:63]\n"
"v_mfma_f32_16x16x32_bf16 v[48:51], v[160:163], v[180:183], v[48:51]\n"
"v_mfma_f32_16x16x32_bf16 v[52:55], v[164:167], v[180:183], v[52:55]\n"
"v_mfma_f32_16x16x32_bf16 v[56:59], v[168:171], v[180:183], v[56:59]\n"
"v_mfma_f32_16x16x32_bf16 v[60:63], v[172:175], v[180:183], v[60:63]\n"
"s_waitcnt lgkmcnt(0)\n"
"s_barrier\n"
"ds_read_b128 v[184:187], v254 offset:63488\n"
"ds_read_b128 v[188:191], v254 offset:64000\n"
"s_waitcnt lgkmcnt(0)\n"
"v_mfma_f32_16x16x32_bf16 v[64:67], v[144:147], v[184:187], v[64:67]\n"
"ds_read_b128 v[176:179], v254 offset:36864\n"
"ds_read_b128 v[180:183], v254 offset:37376\n"
"v_mfma_f32_16x16x32_bf16 v[68:71], v[148:151], v[184:187], v[68:71]\n"
"v_mfma_f32_16x16x32_bf16 v[72:75], v[152:155], v[184:187], v[72:75]\n"
"v_mfma_f32_16x16x32_bf16 v[76:79], v[156:159], v[184:187], v[76:79]\n"
"v_mfma_f32_16x16x32_bf16 v[64:67], v[160:163], v[188:191], v[64:67]\n"
"v_mfma_f32_16x16x32_bf16 v[68:71], v[164:167], v[188:191], v[68:71]\n"
"v_mfma_f32_16x16x32_bf16 v[72:75], v[168:171], v[188:191], v[72:75]\n"
"v_mfma_f32_16x16x32_bf16 v[76:79], v[172:175], v[188:191], v[76:79]\n"
"s_waitcnt lgkmcnt(0)\n"
"v_mfma_f32_16x16x32_bf16 v[80:83], v[144:147], v[176:179], v[80:83]\n"
"ds_read_b128 v[184:187], v254 offset:38912\n"
"ds_read_b128 v[188:191], v254 offset:39424\n"
"v_mfma_f32_16x16x32_bf16 v[84:87], v[148:151], v[176:179], v[84:87]\n"
"v_mfma_f32_16x16x32_bf16 v[88:91], v[152:155], v[176:179], v[88:91]\n"
"v_mfma_f32_16x16x32_bf16 v[92:95], v[156:159], v[176:179], v[92:95]\n"
"v_mfma_f32_16x16x32_bf16 v[80:83], v[160:163], v[180:183], v[80:83]\n"
"v_mfma_f32_16x16x32_bf16 v[84:87], v[164:167], v[180:183], v[84:87]\n"
"v_mfma_f32_16x16x32_bf16 v[88:91], v[168:171], v[180:183], v[88:91]\n"
"v_mfma_f32_16x16x32_bf16 v[92:95], v[172:175], v[180:183], v[92:95]\n"
"s_waitcnt lgkmcnt(0)\n"
"v_mfma_f32_16x16x32_bf16 v[96:99], v[144:147], v[184:187], v[96:99]\n"
"ds_read_b128 v[176:179], v254 offset:40960\n"
"ds_read_b128 v[180:183], v254 offset:41472\n"
"v_mfma_f32_16x16x32_bf16 v[100:103], v[148:151], v[184:187], v[100:103]\n"
"v_mfma_f32_16x16x32_bf16 v[104:107], v[152:155], v[184:187], v[104:107]\n"
"v_mfma_f32_16x16x32_bf16 v[108:111], v[156:159], v[184:187], v[108:111]\n"
"v_mfma_f32_16x16x32_bf16 v[96:99], v[160:163], v[188:191], v[96:99]\n"
"v_mfma_f32_16x16x32_bf16 v[100:103], v[164:167], v[188:191], v[100:103]\n"
"v_mfma_f32_16x16x32_bf16 v[104:107], v[168:171], v[188:191], v[104:107]\n"
"v_mfma_f32_16x16x32_bf16 v[108:111], v[172:175], v[188:191], v[108:111]\n"
"s_waitcnt lgkmcnt(0)\n"
"v_mfma_f32_16x16x32_bf16 v[112:115], v[144:147], v[176:179], v[112:115]\n"
"ds_read_b128 v[184:187], v254 offset:43008\n"
"ds_read_b128 v[188:191], v254 offset:43520\n"
"v_mfma_f32_16x16x32_bf16 v[116:119], v[148:151], v[176:179], v[116:119]\n"
"v_mfma_f32_16x16x32_bf16 v[120:123], v[152:155], v[176:179], v[120:123]\n"
"v_mfma_f32_16x16x32_bf16 v[124:127], v[156:159], v[176:179], v[124:127]\n"
"v_mfma_f32_16x16x32_bf16 v[112:115], v[160:163], v[180:183], v[112:115]\n"
"v_mfma_f32_16x16x32_bf16 v[116:119], v[164:167], v[180:183], v[116:119]\n"
"v_mfma_f32_16x16x32_bf16 v[120:123], v[168:171], v[180:183], v[120:123]\n"
"v_mfma_f32_16x16x32_bf16 v[124:127], v[172:175], v[180:183], v[124:127]\n"
"s_waitcnt lgkmcnt(0)\n"
"v_mfma_f32_16x16x32_bf16 v[128:131], v[144:147], v[184:187], v[128:131]\n"
"ds_read_b128 v[176:179], v254 offset:45056\n"
"ds_read_b128 v[180:183], v254 offset:45568\n"
"v_mfma_f32_16x16x32_bf16 v[132:135], v[148:151], v[184:187], v[132:135]\n"
"ds_read_b64_tr_b16 v[144:145], v234 offset:4096\n"
"ds_read_b64_tr_b16 v[146:147], v234 offset:8192\n"
"v_mfma_f32_16x16x32_bf16 v[136:139], v[152:155], v[184:187], v[136:139]\n"
"ds_read_b64_tr_b16 v[148:149], v235 offset:4096\n"
"ds_read_b64_tr_b16 v[150:151], v235 offset:8192\n"
"v_mfma_f32_16x16x32_bf16 v[140:143], v[156:159], v[184:187], v[140:143]\n"
"ds_read_b64_tr_b16 v[152:153], v234 offset:4608\n"
"ds_read_b64_tr_b16 v[154:155], v234 offset:8704\n"
"v_mfma_f32_16x16x32_bf16 v[128:131], v[160:163], v[188:191], v[128:131]\n"
"ds_read_b64_tr_b16 v[156:157], v235 offset:4608\n"
"ds_read_b64_tr_b16 v[158:159], v235 offset:8704\n"
"v_mfma_f32_16x16x32_bf16 v[132:135], v[164:167], v[188:191], v[132:135]\n"
"ds_read_b64_tr_b16 v[160:161], v234 offset:12288\n"
"ds_read_b64_tr_b16 v[162:163], v234 offset:16384\n"
"v_mfma_f32_16x16x32_bf16 v[136:139], v[168:171], v[188:191], v[136:139]\n"
"ds_read_b64_tr_b16 v[164:165], v235 offset:12288\n"
"ds_read_b64_tr_b16 v[166:167], v235 offset:16384\n"
"v_mfma_f32_16x16x32_bf16 v[140:143], v[172:175], v[188:191], v[140:143]\n"
"s_waitcnt lgkmcnt(10)\n"
"ds_read_b64_tr_b16 v[168:169], v234 offset:12800\n"
"ds_read_b64_tr_b16 v[170:171], v234 offset:16896\n"
"v_mfma_f32_16x16x32_bf16 v[0:3], v[144:147], v[176:179], v[0:3]\n"
"s_waitcnt lgkmcnt(10)\n"
"ds_read_b64_tr_b16 v[172:173], v235 offset:12800\n"
"ds_read_b64_tr_b16 v[174:175], v235 offset:16896\n"
"v_mfma_f32_16x16x32_bf16 v[4:7], v[148:151], v[176:179], v[4:7]\n"
"s_waitcnt lgkmcnt(10)\n"
"ds_read_b128 v[184:187], v254 offset:47104\n"
"ds_read_b128 v[188:191], v254 offset:47616\n"
"v_mfma_f32_16x16x32_bf16 v[8:11], v[152:155], v[176:179], v[8:11]\n"
"s_waitcnt lgkmcnt(10)\n"
"v_mfma_f32_16x16x32_bf16 v[12:15], v[156:159], v[176:179], v[12:15]\n"
"s_waitcnt lgkmcnt(8)\n"
"v_mfma_f32_16x16x32_bf16 v[0:3], v[160:163], v[180:183], v[0:3]\n"
"s_waitcnt lgkmcnt(6)\n"
"v_mfma_f32_16x16x32_bf16 v[4:7], v[164:167], v[180:183], v[4:7]\n"
"s_waitcnt lgkmcnt(4)\n"
"v_mfma_f32_16x16x32_bf16 v[8:11], v[168:171], v[180:183], v[8:11]\n"
"s_waitcnt lgkmcnt(2)\n"
"v_mfma_f32_16x16x32_bf16 v[12:15], v[172:175], v[180:183], v[12:15]\n"
"s_waitcnt lgkmcnt(0)\n"
"v_mfma_f32_16x16x32_bf16 v[16:19], v[144:147], v[184:187], v[16:19]\n"
"v_mfma_f32_16x16x32_bf16 v[20:23], v[148:151], v[184:187], v[20:23]\n"
"v_mfma_f32_16x16x32_bf16 v[24:27], v[152:155], v[184:187], v[24:27]\n"
"v_mfma_f32_16x16x32_bf16 v[28:31], v[156:159], v[184:187], v[28:31]\n"
"v_mfma_f32_16x16x32_bf16 v[16:19], v[160:163], v[188:191], v[16:19]\n"
"v_mfma_f32_16x16x32_bf16 v[20:23], v[164:167], v[188:191], v[20:23]\n"
"v_mfma_f32_16x16x32_bf16 v[24:27], v[168:171], v[188:191], v[24:27]\n"
"v_mfma_f32_16x16x32_bf16 v[28:31], v[172:175], v[188:191], v[28:31]\n"
"s_waitcnt lgkmcnt(0)\n"
"s_barrier\n"
"ds_read_b128 v[176:179], v254 offset:49152\n"
"ds_read_b128 v[180:183], v254 offset:49664\n"
"s_waitcnt lgkmcnt(0)\n"
"v_mfma_f32_16x16x32_bf16 v[32:35], v[144:147], v[176:179], v[32:35]\n"
"ds_read_b128 v[184:187], v254 offset:51200\n"
"ds_read_b128 v[188:191], v254 offset:51712\n"
"v_mfma_f32_16x16x32_bf16 v[36:39], v[148:151], v[176:179], v[36:39]\n"
"v_mfma_f32_16x16x32_bf16 v[40:43], v[152:155], v[176:179], v[40:43]\n"
"v_mfma_f32_16x16x32_bf16 v[44:47], v[156:159], v[176:179], v[44:47]\n"
"v_mfma_f32_16x16x32_bf16 v[32:35], v[160:163], v[180:183], v[32:35]\n"
"v_mfma_f32_16x16x32_bf16 v[36:39], v[164:167], v[180:183], v[36:39]\n"
"v_mfma_f32_16x16x32_bf16 v[40:43], v[168:171], v[180:183], v[40:43]\n"
"v_mfma_f32_16x16x32_bf16 v[44:47], v[172:175], v[180:183], v[44:47]\n"
"s_waitcnt lgkmcnt(0)\n"
"v_mfma_f32_16x16x32_bf16 v[48:51], v[144:147], v[184:187], v[48:51]\n"
"ds_read_b128 v[176:179], v254 offset:53248\n"
"ds_read_b128 v[180:183], v254 offset:53760\n"
"v_mfma_f32_16x16x32_bf16 v[52:55], v[148:151], v[184:187], v[52:55]\n"
"v_mfma_f32_16x16x32_bf16 v[56:59], v[152:155], v[184:187], v[56:59]\n"
"v_mfma_f32_16x16x32_bf16 v[60:63], v[156:159], v[184:187], v[60:63]\n"
"v_mfma_f32_16x16x32_bf16 v[48:51], v[160:163], v[188:191], v[48:51]\n"
"v_mfma_f32_16x16x32_bf16 v[52:55], v[164:167], v[188:191], v[52:55]\n"
"v_mfma_f32_16x16x32_bf16 v[56:59], v[168:171], v[188:191], v[56:59]\n"
"v_mfma_f32_16x16x32_bf16 v[60:63], v[172:175], v[188:191], v[60:63]\n"
"s_waitcnt lgkmcnt(0)\n"
"v_mfma_f32_16x16x32_bf16 v[64:67], v[144:147], v[176:179], v[64:67]\n"
"ds_read_b128 v[184:187], v254 offset:55296\n"
"ds_read_b128 v[188:191], v254 offset:55808\n"
"v_mfma_f32_16x16x32_bf16 v[68:71], v[148:151], v[176:179], v[68:71]\n"
"v_mfma_f32_16x16x32_bf16 v[72:75], v[152:155], v[176:179], v[72:75]\n"
"v_mfma_f32_16x16x32_bf16 v[76:79], v[156:159], v[176:179], v[76:79]\n"
"v_mfma_f32_16x16x32_bf16 v[64:67], v[160:163], v[180:183], v[64:67]\n"
"v_mfma_f32_16x16x32_bf16 v[68:71], v[164:167], v[180:183], v[68:71]\n"
"v_mfma_f32_16x16x32_bf16 v[72:75], v[168:171], v[180:183], v[72:75]\n"
"v_mfma_f32_16x16x32_bf16 v[76:79], v[172:175], v[180:183], v[76:79]\n"
"s_waitcnt lgkmcnt(0)\n"
"v_mfma_f32_16x16x32_bf16 v[80:83], v[144:147], v[184:187], v[80:83]\n"
"ds_read_b128 v[176:179], v254 offset:57344\n"
"ds_read_b128 v[180:183], v254 offset:57856\n"
"v_mfma_f32_16x16x32_bf16 v[84:87], v[148:151], v[184:187], v[84:87]\n"
"v_mfma_f32_16x16x32_bf16 v[88:91], v[152:155], v[184:187], v[88:91]\n"
"v_mfma_f32_16x16x32_bf16 v[92:95], v[156:159], v[184:187], v[92:95]\n"
"v_mfma_f32_16x16x32_bf16 v[80:83], v[160:163], v[188:191], v[80:83]\n"
"v_mfma_f32_16x16x32_bf16 v[84:87], v[164:167], v[188:191], v[84:87]\n"
"v_mfma_f32_16x16x32_bf16 v[88:91], v[168:171], v[188:191], v[88:91]\n"
"v_mfma_f32_16x16x32_bf16 v[92:95], v[172:175], v[188:191], v[92:95]\n"
"s_waitcnt lgkmcnt(0)\n"
"v_mfma_f32_16x16x32_bf16 v[96:99], v[144:147], v[176:179], v[96:99]\n"
"ds_read_b128 v[184:187], v254 offset:59392\n"
"ds_read_b128 v[188:191], v254 offset:59904\n"
"v_mfma_f32_16x16x32_bf16 v[100:103], v[148:151], v[176:179], v[100:103]\n"
"v_mfma_f32_16x16x32_bf16 v[104:107], v[152:155], v[176:179], v[104:107]\n"
"v_mfma_f32_16x16x32_bf16 v[108:111], v[156:159], v[176:179], v[108:111]\n"
"v_mfma_f32_16x16x32_bf16 v[96:99], v[160:163], v[180:183], v[96:99]\n"
"v_mfma_f32_16x16x32_bf16 v[100:103], v[164:167], v[180:183], v[100:103]\n"
"v_mfma_f32_16x16x32_bf16 v[104:107], v[168:171], v[180:183], v[104:107]\n"
"v_mfma_f32_16x16x32_bf16 v[108:111], v[172:175], v[180:183], v[108:111]\n"
"s_waitcnt lgkmcnt(0)\n"
"v_mfma_f32_16x16x32_bf16 v[112:115], v[144:147], v[184:187], v[112:115]\n"
"ds_read_b128 v[176:179], v254 offset:61440\n"
"ds_read_b128 v[180:183], v254 offset:61952\n"
"v_mfma_f32_16x16x32_bf16 v[116:119], v[148:151], v[184:187], v[116:119]\n"
"v_mfma_f32_16x16x32_bf16 v[120:123], v[152:155], v[184:187], v[120:123]\n"
"v_mfma_f32_16x16x32_bf16 v[124:127], v[156:159], v[184:187], v[124:127]\n"
"v_mfma_f32_16x16x32_bf16 v[112:115], v[160:163], v[188:191], v[112:115]\n"
"v_mfma_f32_16x16x32_bf16 v[116:119], v[164:167], v[188:191], v[116:119]\n"
"v_mfma_f32_16x16x32_bf16 v[120:123], v[168:171], v[188:191], v[120:123]\n"
"v_mfma_f32_16x16x32_bf16 v[124:127], v[172:175], v[188:191], v[124:127]\n"
"s_waitcnt lgkmcnt(0)\n"
"v_mfma_f32_16x16x32_bf16 v[128:131], v[144:147], v[176:179], v[128:131]\n"
"v_mfma_f32_16x16x32_bf16 v[132:135], v[148:151], v[176:179], v[132:135]\n"
"v_mfma_f32_16x16x32_bf16 v[136:139], v[152:155], v[176:179], v[136:139]\n"
"v_mfma_f32_16x16x32_bf16 v[140:143], v[156:159], v[176:179], v[140:143]\n"
"v_mfma_f32_16x16x32_bf16 v[128:131], v[160:163], v[180:183], v[128:131]\n"
"v_mfma_f32_16x16x32_bf16 v[132:135], v[164:167], v[180:183], v[132:135]\n"
"v_mfma_f32_16x16x32_bf16 v[136:139], v[168:171], v[180:183], v[136:139]\n"
"v_mfma_f32_16x16x32_bf16 v[140:143], v[172:175], v[180:183], v[140:143]\n"
"s_waitcnt lgkmcnt(0)\n"
"s_barrier\n"
"ds_read_b128 v[184:187], v254 offset:63488\n"
"ds_read_b128 v[188:191], v254 offset:64000\n"
"ds_read_b64_tr_b16 v[144:145], v234 offset:20480\n"
"ds_read_b64_tr_b16 v[146:147], v234 offset:24576\n"
"ds_read_b64_tr_b16 v[148:149], v235 offset:20480\n"
"ds_read_b64_tr_b16 v[150:151], v235 offset:24576\n"
"ds_read_b64_tr_b16 v[152:153], v234 offset:20992\n"
"ds_read_b64_tr_b16 v[154:155], v234 offset:25088\n"
"ds_read_b64_tr_b16 v[156:157], v235 offset:20992\n"
"ds_read_b64_tr_b16 v[158:159], v235 offset:25088\n"
"ds_read_b64_tr_b16 v[160:161], v234 offset:28672\n"
"ds_read_b64_tr_b16 v[162:163], v234 offset:32768\n"
"ds_read_b64_tr_b16 v[164:165], v235 offset:28672\n"
"ds_read_b64_tr_b16 v[166:167], v235 offset:32768\n"
"s_waitcnt lgkmcnt(10)\n"
"v_mfma_f32_16x16x32_bf16 v[0:3], v[144:147], v[184:187], v[0:3]\n"
"s_waitcnt lgkmcnt(8)\n"
"ds_read_b64_tr_b16 v[168:169], v234 offset:29184\n"
"ds_read_b64_tr_b16 v[170:171], v234 offset:33280\n"
"v_mfma_f32_16x16x32_bf16 v[4:7], v[148:151], v[184:187], v[4:7]\n"
"s_waitcnt lgkmcnt(8)\n"
"ds_read_b64_tr_b16 v[172:173], v235 offset:29184\n"
"ds_read_b64_tr_b16 v[174:175], v235 offset:33280\n"
"v_mfma_f32_16x16x32_bf16 v[8:11], v[152:155], v[184:187], v[8:11]\n"
"s_waitcnt lgkmcnt(8)\n"
"ds_read_b128 v[176:179], v254 offset:36864\n"
"ds_read_b128 v[180:183], v254 offset:37376\n"
"v_mfma_f32_16x16x32_bf16 v[12:15], v[156:159], v[184:187], v[12:15]\n"
"s_waitcnt lgkmcnt(8)\n"
"v_mfma_f32_16x16x32_bf16 v[0:3], v[160:163], v[188:191], v[0:3]\n"
"s_waitcnt lgkmcnt(6)\n"
"v_mfma_f32_16x16x32_bf16 v[4:7], v[164:167], v[188:191], v[4:7]\n"
"s_waitcnt lgkmcnt(4)\n"
"v_mfma_f32_16x16x32_bf16 v[8:11], v[168:171], v[188:191], v[8:11]\n"
"s_waitcnt lgkmcnt(2)\n"
"v_mfma_f32_16x16x32_bf16 v[12:15], v[172:175], v[188:191], v[12:15]\n"
"s_waitcnt lgkmcnt(0)\n"
"v_mfma_f32_16x16x32_bf16 v[16:19], v[144:147], v[176:179], v[16:19]\n"
"ds_read_b128 v[184:187], v254 offset:38912\n"
"ds_read_b128 v[188:191], v254 offset:39424\n"
"v_mfma_f32_16x16x32_bf16 v[20:23], v[148:151], v[176:179], v[20:23]\n"
"v_mfma_f32_16x16x32_bf16 v[24:27], v[152:155], v[176:179], v[24:27]\n"
"v_mfma_f32_16x16x32_bf16 v[28:31], v[156:159], v[176:179], v[28:31]\n"
"v_mfma_f32_16x16x32_bf16 v[16:19], v[160:163], v[180:183], v[16:19]\n"
"v_mfma_f32_16x16x32_bf16 v[20:23], v[164:167], v[180:183], v[20:23]\n"
"v_mfma_f32_16x16x32_bf16 v[24:27], v[168:171], v[180:183], v[24:27]\n"
"v_mfma_f32_16x16x32_bf16 v[28:31], v[172:175], v[180:183], v[28:31]\n"
"s_waitcnt lgkmcnt(0)\n"
"v_mfma_f32_16x16x32_bf16 v[32:35], v[144:147], v[184:187], v[32:35]\n"
"ds_read_b128 v[176:179], v254 offset:40960\n"
"ds_read_b128 v[180:183], v254 offset:41472\n"
"v_mfma_f32_16x16x32_bf16 v[36:39], v[148:151], v[184:187], v[36:39]\n"
"v_mfma_f32_16x16x32_bf16 v[40:43], v[152:155], v[184:187], v[40:43]\n"
"v_mfma_f32_16x16x32_bf16 v[44:47], v[156:159], v[184:187], v[44:47]\n"
"v_mfma_f32_16x16x32_bf16 v[32:35], v[160:163], v[188:191], v[32:35]\n"
"v_mfma_f32_16x16x32_bf16 v[36:39], v[164:167], v[188:191], v[36:39]\n"
"v_mfma_f32_16x16x32_bf16 v[40:43], v[168:171], v[188:191], v[40:43]\n"
"v_mfma_f32_16x16x32_bf16 v[44:47], v[172:175], v[188:191], v[44:47]\n"
"s_waitcnt lgkmcnt(0)\n"
"v_mfma_f32_16x16x32_bf16 v[48:51], v[144:147], v[176:179], v[48:51]\n"
"ds_read_b128 v[184:187], v254 offset:43008\n"
"ds_read_b128 v[188:191], v254 offset:43520\n"
"v_mfma_f32_16x16x32_bf16 v[52:55], v[148:151], v[176:179], v[52:55]\n"
"v_mfma_f32_16x16x32_bf16 v[56:59], v[152:155], v[176:179], v[56:59]\n"
"v_mfma_f32_16x16x32_bf16 v[60:63], v[156:159], v[176:179], v[60:63]\n"
"v_mfma_f32_16x16x32_bf16 v[48:51], v[160:163], v[180:183], v[48:51]\n"
"v_mfma_f32_16x16x32_bf16 v[52:55], v[164:167], v[180:183], v[52:55]\n"
"v_mfma_f32_16x16x32_bf16 v[56:59], v[168:171], v[180:183], v[56:59]\n"
"v_mfma_f32_16x16x32_bf16 v[60:63], v[172:175], v[180:183], v[60:63]\n"
"s_waitcnt lgkmcnt(0)\n"
"v_mfma_f32_16x16x32_bf16 v[64:67], v[144:147], v[184:187], v[64:67]\n"
"ds_read_b128 v[176:179], v254 offset:45056\n"
"ds_read_b128 v[180:183], v254 offset:45568\n"
"v_mfma_f32_16x16x32_bf16 v[68:71], v[148:151], v[184:187], v[68:71]\n"
"v_mfma_f32_16x16x32_bf16 v[72:75], v[152:155], v[184:187], v[72:75]\n"
"v_mfma_f32_16x16x32_bf16 v[76:79], v[156:159], v[184:187], v[76:79]\n"
"v_mfma_f32_16x16x32_bf16 v[64:67], v[160:163], v[188:191], v[64:67]\n"
"v_mfma_f32_16x16x32_bf16 v[68:71], v[164:167], v[188:191], v[68:71]\n"
"v_mfma_f32_16x16x32_bf16 v[72:75], v[168:171], v[188:191], v[72:75]\n"
"v_mfma_f32_16x16x32_bf16 v[76:79], v[172:175], v[188:191], v[76:79]\n"
"s_waitcnt lgkmcnt(0)\n"
"v_mfma_f32_16x16x32_bf16 v[80:83], v[144:147], v[176:179], v[80:83]\n"
"v_mfma_f32_16x16x32_bf16 v[84:87], v[148:151], v[176:179], v[84:87]\n"
"v_mfma_f32_16x16x32_bf16 v[88:91], v[152:155], v[176:179], v[88:91]\n"
"v_mfma_f32_16x16x32_bf16 v[92:95], v[156:159], v[176:179], v[92:95]\n"
"v_mfma_f32_16x16x32_bf16 v[80:83], v[160:163], v[180:183], v[80:83]\n"
"v_mfma_f32_16x16x32_bf16 v[84:87], v[164:167], v[180:183], v[84:87]\n"
"v_mfma_f32_16x16x32_bf16 v[88:91], v[168:171], v[180:183], v[88:91]\n"
"v_mfma_f32_16x16x32_bf16 v[92:95], v[172:175], v[180:183], v[92:95]\n"
"s_waitcnt lgkmcnt(0)\n"
"s_barrier\n"
"ds_read_b128 v[184:187], v254 offset:47104\n"
"ds_read_b128 v[188:191], v254 offset:47616\n"
"s_waitcnt lgkmcnt(0)\n"
"v_mfma_f32_16x16x32_bf16 v[96:99], v[144:147], v[184:187], v[96:99]\n"
"ds_read_b128 v[176:179], v254 offset:49152\n"
"ds_read_b128 v[180:183], v254 offset:49664\n"
"v_mfma_f32_16x16x32_bf16 v[100:103], v[148:151], v[184:187], v[100:103]\n"
"v_mfma_f32_16x16x32_bf16 v[104:107], v[152:155], v[184:187], v[104:107]\n"
"v_mfma_f32_16x16x32_bf16 v[108:111], v[156:159], v[184:187], v[108:111]\n"
"v_mfma_f32_16x16x32_bf16 v[96:99], v[160:163], v[188:191], v[96:99]\n"
"v_mfma_f32_16x16x32_bf16 v[100:103], v[164:167], v[188:191], v[100:103]\n"
"v_mfma_f32_16x16x32_bf16 v[104:107], v[168:171], v[188:191], v[104:107]\n"
"v_mfma_f32_16x16x32_bf16 v[108:111], v[172:175], v[188:191], v[108:111]\n"
"s_waitcnt lgkmcnt(0)\n"
"v_mfma_f32_16x16x32_bf16 v[112:115], v[144:147], v[176:179], v[112:115]\n"
"ds_read_b128 v[184:187], v254 offset:51200\n"
"ds_read_b128 v[188:191], v254 offset:51712\n"
"v_mfma_f32_16x16x32_bf16 v[116:119], v[148:151], v[176:179], v[116:119]\n"
"v_mfma_f32_16x16x32_bf16 v[120:123], v[152:155], v[176:179], v[120:123]\n"
"v_mfma_f32_16x16x32_bf16 v[124:127], v[156:159], v[176:179], v[124:127]\n"
"v_mfma_f32_16x16x32_bf16 v[112:115], v[160:163], v[180:183], v[112:115]\n"
"v_mfma_f32_16x16x32_bf16 v[116:119], v[164:167], v[180:183], v[116:119]\n"
"v_mfma_f32_16x16x32_bf16 v[120:123], v[168:171], v[180:183], v[120:123]\n"
"v_mfma_f32_16x16x32_bf16 v[124:127], v[172:175], v[180:183], v[124:127]\n"
"s_waitcnt lgkmcnt(0)\n"
"v_mfma_f32_16x16x32_bf16 v[128:131], v[144:147], v[184:187], v[128:131]\n"
"ds_read_b128 v[176:179], v254 offset:53248\n"
"ds_read_b128 v[180:183], v254 offset:53760\n"
"v_mfma_f32_16x16x32_bf16 v[132:135], v[148:151], v[184:187], v[132:135]\n"
"ds_read_b64_tr_b16 v[144:145], v234 offset:4096\n"
"ds_read_b64_tr_b16 v[146:147], v234 offset:8192\n"
"v_mfma_f32_16x16x32_bf16 v[136:139], v[152:155], v[184:187], v[136:139]\n"
"ds_read_b64_tr_b16 v[148:149], v235 offset:4096\n"
"ds_read_b64_tr_b16 v[150:151], v235 offset:8192\n"
"v_mfma_f32_16x16x32_bf16 v[140:143], v[156:159], v[184:187], v[140:143]\n"
"ds_read_b64_tr_b16 v[152:153], v234 offset:4608\n"
"ds_read_b64_tr_b16 v[154:155], v234 offset:8704\n"
"v_mfma_f32_16x16x32_bf16 v[128:131], v[160:163], v[188:191], v[128:131]\n"
"ds_read_b64_tr_b16 v[156:157], v235 offset:4608\n"
"ds_read_b64_tr_b16 v[158:159], v235 offset:8704\n"
"v_mfma_f32_16x16x32_bf16 v[132:135], v[164:167], v[188:191], v[132:135]\n"
"ds_read_b64_tr_b16 v[160:161], v234 offset:12288\n"
"ds_read_b64_tr_b16 v[162:163], v234 offset:16384\n"
"v_mfma_f32_16x16x32_bf16 v[136:139], v[168:171], v[188:191], v[136:139]\n"
"ds_read_b64_tr_b16 v[164:165], v235 offset:12288\n"
"ds_read_b64_tr_b16 v[166:167], v235 offset:16384\n"
"v_mfma_f32_16x16x32_bf16 v[140:143], v[172:175], v[188:191], v[140:143]\n"
"s_waitcnt lgkmcnt(10)\n"
"ds_read_b64_tr_b16 v[168:169], v234 offset:12800\n"
"ds_read_b64_tr_b16 v[170:171], v234 offset:16896\n"
"v_mfma_f32_16x16x32_bf16 v[0:3], v[144:147], v[176:179], v[0:3]\n"
"s_waitcnt lgkmcnt(10)\n"
"ds_read_b64_tr_b16 v[172:173], v235 offset:12800\n"
"ds_read_b64_tr_b16 v[174:175], v235 offset:16896\n"
"v_mfma_f32_16x16x32_bf16 v[4:7], v[148:151], v[176:179], v[4:7]\n"
"s_waitcnt lgkmcnt(10)\n"
"ds_read_b128 v[184:187], v254 offset:55296\n"
"ds_read_b128 v[188:191], v254 offset:55808\n"
"v_mfma_f32_16x16x32_bf16 v[8:11], v[152:155], v[176:179], v[8:11]\n"
"s_waitcnt lgkmcnt(10)\n"
"v_mfma_f32_16x16x32_bf16 v[12:15], v[156:159], v[176:179], v[12:15]\n"
"s_waitcnt lgkmcnt(8)\n"
"v_mfma_f32_16x16x32_bf16 v[0:3], v[160:163], v[180:183], v[0:3]\n"
"s_waitcnt lgkmcnt(6)\n"
"v_mfma_f32_16x16x32_bf16 v[4:7], v[164:167], v[180:183], v[4:7]\n"
"s_waitcnt lgkmcnt(4)\n"
"v_mfma_f32_16x16x32_bf16 v[8:11], v[168:171], v[180:183], v[8:11]\n"
"s_waitcnt lgkmcnt(2)\n"
"v_mfma_f32_16x16x32_bf16 v[12:15], v[172:175], v[180:183], v[12:15]\n"
"s_waitcnt lgkmcnt(0)\n"
"v_mfma_f32_16x16x32_bf16 v[16:19], v[144:147], v[184:187], v[16:19]\n"
"ds_read_b128 v[176:179], v254 offset:57344\n"
"ds_read_b128 v[180:183], v254 offset:57856\n"
"v_mfma_f32_16x16x32_bf16 v[20:23], v[148:151], v[184:187], v[20:23]\n"
"v_mfma_f32_16x16x32_bf16 v[24:27], v[152:155], v[184:187], v[24:27]\n"
"v_mfma_f32_16x16x32_bf16 v[28:31], v[156:159], v[184:187], v[28:31]\n"
"v_mfma_f32_16x16x32_bf16 v[16:19], v[160:163], v[188:191], v[16:19]\n"
"v_mfma_f32_16x16x32_bf16 v[20:23], v[164:167], v[188:191], v[20:23]\n"
"v_mfma_f32_16x16x32_bf16 v[24:27], v[168:171], v[188:191], v[24:27]\n"
"v_mfma_f32_16x16x32_bf16 v[28:31], v[172:175], v[188:191], v[28:31]\n"
"s_waitcnt lgkmcnt(0)\n"
"v_mfma_f32_16x16x32_bf16 v[32:35], v[144:147], v[176:179], v[32:35]\n"
"ds_read_b128 v[184:187], v254 offset:59392\n"
"ds_read_b128 v[188:191], v254 offset:59904\n"
"v_mfma_f32_16x16x32_bf16 v[36:39], v[148:151], v[176:179], v[36:39]\n"
"v_mfma_f32_16x16x32_bf16 v[40:43], v[152:155], v[176:179], v[40:43]\n"
"v_mfma_f32_16x16x32_bf16 v[44:47], v[156:159], v[176:179], v[44:47]\n"
"v_mfma_f32_16x16x32_bf16 v[32:35], v[160:163], v[180:183], v[32:35]\n"
"v_mfma_f32_16x16x32_bf16 v[36:39], v[164:167], v[180:183], v[36:39]\n"
"v_mfma_f32_16x16x32_bf16 v[40:43], v[168:171], v[180:183], v[40:43]\n"
"v_mfma_f32_16x16x32_bf16 v[44:47], v[172:175], v[180:183], v[44:47]\n"
"s_waitcnt lgkmcnt(0)\n"
"v_mfma_f32_16x16x32_bf16 v[48:51], v[144:147], v[184:187], v[48:51]\n"
"v_mfma_f32_16x16x32_bf16 v[52:55], v[148:151], v[184:187], v[52:55]\n"
"v_mfma_f32_16x16x32_bf16 v[56:59], v[152:155], v[184:187], v[56:59]\n"
"v_mfma_f32_16x16x32_bf16 v[60:63], v[156:159], v[184:187], v[60:63]\n"
"v_mfma_f32_16x16x32_bf16 v[48:51], v[160:163], v[188:191], v[48:51]\n"
"v_mfma_f32_16x16x32_bf16 v[52:55], v[164:167], v[188:191], v[52:55]\n"
"v_mfma_f32_16x16x32_bf16 v[56:59], v[168:171], v[188:191], v[56:59]\n"
"v_mfma_f32_16x16x32_bf16 v[60:63], v[172:175], v[188:191], v[60:63]\n"
"s_waitcnt lgkmcnt(0)\n"
"s_barrier\n"
"ds_read_b128 v[176:179], v254 offset:61440\n"
"ds_read_b128 v[180:183], v254 offset:61952\n"
"s_waitcnt lgkmcnt(0)\n"
"v_mfma_f32_16x16x32_bf16 v[64:67], v[144:147], v[176:179], v[64:67]\n"
"ds_read_b128 v[184:187], v254 offset:63488\n"
"ds_read_b128 v[188:191], v254 offset:64000\n"
"v_mfma_f32_16x16x32_bf16 v[68:71], v[148:151], v[176:179], v[68:71]\n"
"v_mfma_f32_16x16x32_bf16 v[72:75], v[152:155], v[176:179], v[72:75]\n"
"v_mfma_f32_16x16x32_bf16 v[76:79], v[156:159], v[176:179], v[76:79]\n"
"v_mfma_f32_16x16x32_bf16 v[64:67], v[160:163], v[180:183], v[64:67]\n"
"v_mfma_f32_16x16x32_bf16 v[68:71], v[164:167], v[180:183], v[68:71]\n"
"v_mfma_f32_16x16x32_bf16 v[72:75], v[168:171], v[180:183], v[72:75]\n"
"v_mfma_f32_16x16x32_bf16 v[76:79], v[172:175], v[180:183], v[76:79]\n"
"s_waitcnt lgkmcnt(0)\n"
"v_mfma_f32_16x16x32_bf16 v[80:83], v[144:147], v[184:187], v[80:83]\n"
"ds_read_b128 v[176:179], v254 offset:36864\n"
"ds_read_b128 v[180:183], v254 offset:37376\n"
"v_mfma_f32_16x16x32_bf16 v[84:87], v[148:151], v[184:187], v[84:87]\n"
"v_mfma_f32_16x16x32_bf16 v[88:91], v[152:155], v[184:187], v[88:91]\n"
"v_mfma_f32_16x16x32_bf16 v[92:95], v[156:159], v[184:187], v[92:95]\n"
"v_mfma_f32_16x16x32_bf16 v[80:83], v[160:163], v[188:191], v[80:83]\n"
"v_mfma_f32_16x16x32_bf16 v[84:87], v[164:167], v[188:191], v[84:87]\n"
"v_mfma_f32_16x16x32_bf16 v[88:91], v[168:171], v[188:191], v[88:91]\n"
"v_mfma_f32_16x16x32_bf16 v[92:95], v[172:175], v[188:191], v[92:95]\n"
"s_waitcnt lgkmcnt(0)\n"
"v_mfma_f32_16x16x32_bf16 v[96:99], v[144:147], v[176:179], v[96:99]\n"
"ds_read_b128 v[184:187], v254 offset:38912\n"
"ds_read_b128 v[188:191], v254 offset:39424\n"
"v_mfma_f32_16x16x32_bf16 v[100:103], v[148:151], v[176:179], v[100:103]\n"
"v_mfma_f32_16x16x32_bf16 v[104:107], v[152:155], v[176:179], v[104:107]\n"
"v_mfma_f32_16x16x32_bf16 v[108:111], v[156:159], v[176:179], v[108:111]\n"
"v_mfma_f32_16x16x32_bf16 v[96:99], v[160:163], v[180:183], v[96:99]\n"
"v_mfma_f32_16x16x32_bf16 v[100:103], v[164:167], v[180:183], v[100:103]\n"
"v_mfma_f32_16x16x32_bf16 v[104:107], v[168:171], v[180:183], v[104:107]\n"
"v_mfma_f32_16x16x32_bf16 v[108:111], v[172:175], v[180:183], v[108:111]\n"
"s_waitcnt lgkmcnt(0)\n"
"v_mfma_f32_16x16x32_bf16 v[112:115], v[144:147], v[184:187], v[112:115]\n"
"ds_read_b128 v[176:179], v254 offset:40960\n"
"ds_read_b128 v[180:183], v254 offset:41472\n"
"v_mfma_f32_16x16x32_bf16 v[116:119], v[148:151], v[184:187], v[116:119]\n"
"v_mfma_f32_16x16x32_bf16 v[120:123], v[152:155], v[184:187], v[120:123]\n"
"v_mfma_f32_16x16x32_bf16 v[124:127], v[156:159], v[184:187], v[124:127]\n"
"v_mfma_f32_16x16x32_bf16 v[112:115], v[160:163], v[188:191], v[112:115]\n"
"v_mfma_f32_16x16x32_bf16 v[116:119], v[164:167], v[188:191], v[116:119]\n"
"v_mfma_f32_16x16x32_bf16 v[120:123], v[168:171], v[188:191], v[120:123]\n"
"v_mfma_f32_16x16x32_bf16 v[124:127], v[172:175], v[188:191], v[124:127]\n"
"s_waitcnt lgkmcnt(0)\n"
"v_mfma_f32_16x16x32_bf16 v[128:131], v[144:147], v[176:179], v[128:131]\n"
"ds_read_b128 v[184:187], v254 offset:43008\n"
"ds_read_b128 v[188:191], v254 offset:43520\n"
"v_mfma_f32_16x16x32_bf16 v[132:135], v[148:151], v[176:179], v[132:135]\n"
"ds_read_b64_tr_b16 v[144:145], v234 offset:20480\n"
"ds_read_b64_tr_b16 v[146:147], v234 offset:24576\n"
"v_mfma_f32_16x16x32_bf16 v[136:139], v[152:155], v[176:179], v[136:139]\n"
"ds_read_b64_tr_b16 v[148:149], v235 offset:20480\n"
"ds_read_b64_tr_b16 v[150:151], v235 offset:24576\n"
"v_mfma_f32_16x16x32_bf16 v[140:143], v[156:159], v[176:179], v[140:143]\n"
"ds_read_b64_tr_b16 v[152:153], v234 offset:20992\n"
"ds_read_b64_tr_b16 v[154:155], v234 offset:25088\n"
"v_mfma_f32_16x16x32_bf16 v[128:131], v[160:163], v[180:183], v[128:131]\n"
"ds_read_b64_tr_b16 v[156:157], v235 offset:20992\n"
"ds_read_b64_tr_b16 v[158:159], v235 offset:25088\n"
"v_mfma_f32_16x16x32_bf16 v[132:135], v[164:167], v[180:183], v[132:135]\n"
"ds_read_b64_tr_b16 v[160:161], v234 offset:28672\n"
"ds_read_b64_tr_b16 v[162:163], v234 offset:32768\n"
"v_mfma_f32_16x16x32_bf16 v[136:139], v[168:171], v[180:183], v[136:139]\n"
"ds_read_b64_tr_b16 v[164:165], v235 offset:28672\n"
"ds_read_b64_tr_b16 v[166:167], v235 offset:32768\n"
"v_mfma_f32_16x16x32_bf16 v[140:143], v[172:175], v[180:183], v[140:143]\n"
"s_waitcnt lgkmcnt(10)\n"
"ds_read_b64_tr_b16 v[168:169], v234 offset:29184\n"
"ds_read_b64_tr_b16 v[170:171], v234 offset:33280\n"
"v_mfma_f32_16x16x32_bf16 v[0:3], v[144:147], v[184:187], v[0:3]\n"
"s_waitcnt lgkmcnt(10)\n"
"ds_read_b64_tr_b16 v[172:173], v235 offset:29184\n"
"ds_read_b64_tr_b16 v[174:175], v235 offset:33280\n"
"v_mfma_f32_16x16x32_bf16 v[4:7], v[148:151], v[184:187], v[4:7]\n"
"s_waitcnt lgkmcnt(10)\n"
"ds_read_b128 v[176:179], v254 offset:45056\n"
"ds_read_b128 v[180:183], v254 offset:45568\n"
"v_mfma_f32_16x16x32_bf16 v[8:11], v[152:155], v[184:187], v[8:11]\n"
"s_waitcnt lgkmcnt(10)\n"
"v_mfma_f32_16x16x32_bf16 v[12:15], v[156:159], v[184:187], v[12:15]\n"
"s_waitcnt lgkmcnt(8)\n"
"v_mfma_f32_16x16x32_bf16 v[0:3], v[160:163], v[188:191], v[0:3]\n"
"s_waitcnt lgkmcnt(6)\n"
"v_mfma_f32_16x16x32_bf16 v[4:7], v[164:167], v[188:191], v[4:7]\n"
"s_waitcnt lgkmcnt(4)\n"
"v_mfma_f32_16x16x32_bf16 v[8:11], v[168:171], v[188:191], v[8:11]\n"
"s_waitcnt lgkmcnt(2)\n"
"v_mfma_f32_16x16x32_bf16 v[12:15], v[172:175], v[188:191], v[12:15]\n"
"s_waitcnt lgkmcnt(0)\n"
"v_mfma_f32_16x16x32_bf16 v[16:19], v[144:147], v[176:179], v[16:19]\n"
"v_mfma_f32_16x16x32_bf16 v[20:23], v[148:151], v[176:179], v[20:23]\n"
"v_mfma_f32_16x16x32_bf16 v[24:27], v[152:155], v[176:179], v[24:27]\n"
"v_mfma_f32_16x16x32_bf16 v[28:31], v[156:159], v[176:179], v[28:31]\n"
"v_mfma_f32_16x16x32_bf16 v[16:19], v[160:163], v[180:183], v[16:19]\n"
"v_mfma_f32_16x16x32_bf16 v[20:23], v[164:167], v[180:183], v[20:23]\n"
"v_mfma_f32_16x16x32_bf16 v[24:27], v[168:171], v[180:183], v[24:27]\n"
"v_mfma_f32_16x16x32_bf16 v[28:31], v[172:175], v[180:183], v[28:31]\n"
"s_waitcnt lgkmcnt(0)\n"
"s_barrier\n"
"ds_read_b128 v[184:187], v254 offset:47104\n"
"ds_read_b128 v[188:191], v254 offset:47616\n"
"s_waitcnt lgkmcnt(0)\n"
"v_mfma_f32_16x16x32_bf16 v[32:35], v[144:147], v[184:187], v[32:35]\n"
"ds_read_b128 v[176:179], v254 offset:49152\n"
"ds_read_b128 v[180:183], v254 offset:49664\n"
"v_mfma_f32_16x16x32_bf16 v[36:39], v[148:151], v[184:187], v[36:39]\n"
"v_mfma_f32_16x16x32_bf16 v[40:43], v[152:155], v[184:187], v[40:43]\n"
"v_mfma_f32_16x16x32_bf16 v[44:47], v[156:159], v[184:187], v[44:47]\n"
"v_mfma_f32_16x16x32_bf16 v[32:35], v[160:163], v[188:191], v[32:35]\n"
"v_mfma_f32_16x16x32_bf16 v[36:39], v[164:167], v[188:191], v[36:39]\n"
"v_mfma_f32_16x16x32_bf16 v[40:43], v[168:171], v[188:191], v[40:43]\n"
"v_mfma_f32_16x16x32_bf16 v[44:47], v[172:175], v[188:191], v[44:47]\n"
"s_waitcnt lgkmcnt(0)\n"
"v_mfma_f32_16x16x32_bf16 v[48:51], v[144:147], v[176:179], v[48:51]\n"
"ds_read_b128 v[184:187], v254 offset:51200\n"
"ds_read_b128 v[188:191], v254 offset:51712\n"
"v_mfma_f32_16x16x32_bf16 v[52:55], v[148:151], v[176:179], v[52:55]\n"
"v_mfma_f32_16x16x32_bf16 v[56:59], v[152:155], v[176:179], v[56:59]\n"
"v_mfma_f32_16x16x32_bf16 v[60:63], v[156:159], v[176:179], v[60:63]\n"
"v_mfma_f32_16x16x32_bf16 v[48:51], v[160:163], v[180:183], v[48:51]\n"
"v_mfma_f32_16x16x32_bf16 v[52:55], v[164:167], v[180:183], v[52:55]\n"
"v_mfma_f32_16x16x32_bf16 v[56:59], v[168:171], v[180:183], v[56:59]\n"
"v_mfma_f32_16x16x32_bf16 v[60:63], v[172:175], v[180:183], v[60:63]\n"
"s_waitcnt lgkmcnt(0)\n"
"v_mfma_f32_16x16x32_bf16 v[64:67], v[144:147], v[184:187], v[64:67]\n"
"ds_read_b128 v[176:179], v254 offset:53248\n"
"ds_read_b128 v[180:183], v254 offset:53760\n"
"v_mfma_f32_16x16x32_bf16 v[68:71], v[148:151], v[184:187], v[68:71]\n"
"v_mfma_f32_16x16x32_bf16 v[72:75], v[152:155], v[184:187], v[72:75]\n"
"v_mfma_f32_16x16x32_bf16 v[76:79], v[156:159], v[184:187], v[76:79]\n"
"v_mfma_f32_16x16x32_bf16 v[64:67], v[160:163], v[188:191], v[64:67]\n"
"v_mfma_f32_16x16x32_bf16 v[68:71], v[164:167], v[188:191], v[68:71]\n"
"v_mfma_f32_16x16x32_bf16 v[72:75], v[168:171], v[188:191], v[72:75]\n"
"v_mfma_f32_16x16x32_bf16 v[76:79], v[172:175], v[188:191], v[76:79]\n"
"s_waitcnt lgkmcnt(0)\n"
"v_mfma_f32_16x16x32_bf16 v[80:83], v[144:147], v[176:179], v[80:83]\n"
"ds_read_b128 v[184:187], v254 offset:55296\n"
"ds_read_b128 v[188:191], v254 offset:55808\n"
"v_mfma_f32_16x16x32_bf16 v[84:87], v[148:151], v[176:179], v[84:87]\n"
"v_mfma_f32_16x16x32_bf16 v[88:91], v[152:155], v[176:179], v[88:91]\n"
"v_mfma_f32_16x16x32_bf16 v[92:95], v[156:159], v[176:179], v[92:95]\n"
"v_mfma_f32_16x16x32_bf16 v[80:83], v[160:163], v[180:183], v[80:83]\n"
"v_mfma_f32_16x16x32_bf16 v[84:87], v[164:167], v[180:183], v[84:87]\n"
"v_mfma_f32_16x16x32_bf16 v[88:91], v[168:171], v[180:183], v[88:91]\n"
"v_mfma_f32_16x16x32_bf16 v[92:95], v[172:175], v[180:183], v[92:95]\n"
"s_waitcnt lgkmcnt(0)\n"
"v_mfma_f32_16x16x32_bf16 v[96:99], v[144:147], v[184:187], v[96:99]\n"
"ds_read_b128 v[176:179], v254 offset:57344\n"
"ds_read_b128 v[180:183], v254 offset:57856\n"
"v_mfma_f32_16x16x32_bf16 v[100:103], v[148:151], v[184:187], v[100:103]\n"
"v_mfma_f32_16x16x32_bf16 v[104:107], v[152:155], v[184:187], v[104:107]\n"
"v_mfma_f32_16x16x32_bf16 v[108:111], v[156:159], v[184:187], v[108:111]\n"
"v_mfma_f32_16x16x32_bf16 v[96:99], v[160:163], v[188:191], v[96:99]\n"
"v_mfma_f32_16x16x32_bf16 v[100:103], v[164:167], v[188:191], v[100:103]\n"
"v_mfma_f32_16x16x32_bf16 v[104:107], v[168:171], v[188:191], v[104:107]\n"
"v_mfma_f32_16x16x32_bf16 v[108:111], v[172:175], v[188:191], v[108:111]\n"
"s_waitcnt lgkmcnt(0)\n"
"v_mfma_f32_16x16x32_bf16 v[112:115], v[144:147], v[176:179], v[112:115]\n"
"ds_read_b128 v[184:187], v254 offset:59392\n"
"ds_read_b128 v[188:191], v254 offset:59904\n"
"v_mfma_f32_16x16x32_bf16 v[116:119], v[148:151], v[176:179], v[116:119]\n"
"v_mfma_f32_16x16x32_bf16 v[120:123], v[152:155], v[176:179], v[120:123]\n"
"v_mfma_f32_16x16x32_bf16 v[124:127], v[156:159], v[176:179], v[124:127]\n"
"v_mfma_f32_16x16x32_bf16 v[112:115], v[160:163], v[180:183], v[112:115]\n"
"v_mfma_f32_16x16x32_bf16 v[116:119], v[164:167], v[180:183], v[116:119]\n"
"v_mfma_f32_16x16x32_bf16 v[120:123], v[168:171], v[180:183], v[120:123]\n"
"v_mfma_f32_16x16x32_bf16 v[124:127], v[172:175], v[180:183], v[124:127]\n"
"s_waitcnt lgkmcnt(0)\n"
"v_mfma_f32_16x16x32_bf16 v[128:131], v[144:147], v[184:187], v[128:131]\n"
"v_mfma_f32_16x16x32_bf16 v[132:135], v[148:151], v[184:187], v[132:135]\n"
"v_mfma_f32_16x16x32_bf16 v[136:139], v[152:155], v[184:187], v[136:139]\n"
"v_mfma_f32_16x16x32_bf16 v[140:143], v[156:159], v[184:187], v[140:143]\n"
"v_mfma_f32_16x16x32_bf16 v[128:131], v[160:163], v[188:191], v[128:131]\n"
"v_mfma_f32_16x16x32_bf16 v[132:135], v[164:167], v[188:191], v[132:135]\n"
"v_mfma_f32_16x16x32_bf16 v[136:139], v[168:171], v[188:191], v[136:139]\n"
"v_mfma_f32_16x16x32_bf16 v[140:143], v[172:175], v[188:191], v[140:143]\n"
"s_waitcnt lgkmcnt(0)\n"
"s_barrier\n"
"ds_read_b128 v[176:179], v254 offset:61440\n"
"ds_read_b128 v[180:183], v254 offset:61952\n"
"ds_read_b64_tr_b16 v[144:145], v234 offset:4096\n"
"ds_read_b64_tr_b16 v[146:147], v234 offset:8192\n"
"ds_read_b64_tr_b16 v[148:149], v235 offset:4096\n"
"ds_read_b64_tr_b16 v[150:151], v235 offset:8192\n"
"ds_read_b64_tr_b16 v[152:153], v234 offset:4608\n"
"ds_read_b64_tr_b16 v[154:155], v234 offset:8704\n"
"ds_read_b64_tr_b16 v[156:157], v235 offset:4608\n"
"ds_read_b64_tr_b16 v[158:159], v235 offset:8704\n"
"ds_read_b64_tr_b16 v[160:161], v234 offset:12288\n"
"ds_read_b64_tr_b16 v[162:163], v234 offset:16384\n"
"ds_read_b64_tr_b16 v[164:165], v235 offset:12288\n"
"ds_read_b64_tr_b16 v[166:167], v235 offset:16384\n"
"s_waitcnt lgkmcnt(10)\n"
"v_mfma_f32_16x16x32_bf16 v[0:3], v[144:147], v[176:179], v[0:3]\n"
"s_waitcnt lgkmcnt(8)\n"
"ds_read_b64_tr_b16 v[168:169], v234 offset:12800\n"
"ds_read_b64_tr_b16 v[170:171], v234 offset:16896\n"
"v_mfma_f32_16x16x32_bf16 v[4:7], v[148:151], v[176:179], v[4:7]\n"
"s_waitcnt lgkmcnt(8)\n"
"ds_read_b64_tr_b16 v[172:173], v235 offset:12800\n"
"ds_read_b64_tr_b16 v[174:175], v235 offset:16896\n"
"v_mfma_f32_16x16x32_bf16 v[8:11], v[152:155], v[176:179], v[8:11]\n"
"s_waitcnt lgkmcnt(8)\n"
"ds_read_b128 v[184:187], v254 offset:63488\n"
"ds_read_b128 v[188:191], v254 offset:64000\n"
"v_mfma_f32_16x16x32_bf16 v[12:15], v[156:159], v[176:179], v[12:15]\n"
"s_waitcnt lgkmcnt(8)\n"
"v_mfma_f32_16x16x32_bf16 v[0:3], v[160:163], v[180:183], v[0:3]\n"
"s_waitcnt lgkmcnt(6)\n"
"v_mfma_f32_16x16x32_bf16 v[4:7], v[164:167], v[180:183], v[4:7]\n"
"s_waitcnt lgkmcnt(4)\n"
"v_mfma_f32_16x16x32_bf16 v[8:11], v[168:171], v[180:183], v[8:11]\n"
"s_waitcnt lgkmcnt(2)\n"
"v_mfma_f32_16x16x32_bf16 v[12:15], v[172:175], v[180:183], v[12:15]\n"
"s_waitcnt lgkmcnt(0)\n"
"v_mfma_f32_16x16x32_bf16 v[16:19], v[144:147], v[184:187], v[16:19]\n"
"ds_read_b128 v[176:179], v254 offset:36864\n"
"ds_read_b128 v[180:183], v254 offset:37376\n"
"v_mfma_f32_16x16x32_bf16 v[20:23], v[148:151], v[184:187], v[20:23]\n"
"v_mfma_f32_16x16x32_bf16 v[24:27], v[152:155], v[184:187], v[24:27]\n"
"v_mfma_f32_16x16x32_bf16 v[28:31], v[156:159], v[184:187], v[28:31]\n"
"v_mfma_f32_16x16x32_bf16 v[16:19], v[160:163], v[188:191], v[16:19]\n"
"v_mfma_f32_16x16x32_bf16 v[20:23], v[164:167], v[188:191], v[20:23]\n"
"v_mfma_f32_16x16x32_bf16 v[24:27], v[168:171], v[188:191], v[24:27]\n"
"v_mfma_f32_16x16x32_bf16 v[28:31], v[172:175], v[188:191], v[28:31]\n"
"s_waitcnt lgkmcnt(0)\n"
"v_mfma_f32_16x16x32_bf16 v[32:35], v[144:147], v[176:179], v[32:35]\n"
"ds_read_b128 v[184:187], v254 offset:38912\n"
"ds_read_b128 v[188:191], v254 offset:39424\n"
"v_mfma_f32_16x16x32_bf16 v[36:39], v[148:151], v[176:179], v[36:39]\n"
"v_mfma_f32_16x16x32_bf16 v[40:43], v[152:155], v[176:179], v[40:43]\n"
"v_mfma_f32_16x16x32_bf16 v[44:47], v[156:159], v[176:179], v[44:47]\n"
"v_mfma_f32_16x16x32_bf16 v[32:35], v[160:163], v[180:183], v[32:35]\n"
"v_mfma_f32_16x16x32_bf16 v[36:39], v[164:167], v[180:183], v[36:39]\n"
"v_mfma_f32_16x16x32_bf16 v[40:43], v[168:171], v[180:183], v[40:43]\n"
"v_mfma_f32_16x16x32_bf16 v[44:47], v[172:175], v[180:183], v[44:47]\n"
"s_waitcnt lgkmcnt(0)\n"
"v_mfma_f32_16x16x32_bf16 v[48:51], v[144:147], v[184:187], v[48:51]\n"
"ds_read_b128 v[176:179], v254 offset:40960\n"
"ds_read_b128 v[180:183], v254 offset:41472\n"
"v_mfma_f32_16x16x32_bf16 v[52:55], v[148:151], v[184:187], v[52:55]\n"
"v_mfma_f32_16x16x32_bf16 v[56:59], v[152:155], v[184:187], v[56:59]\n"
"v_mfma_f32_16x16x32_bf16 v[60:63], v[156:159], v[184:187], v[60:63]\n"
"v_mfma_f32_16x16x32_bf16 v[48:51], v[160:163], v[188:191], v[48:51]\n"
"v_mfma_f32_16x16x32_bf16 v[52:55], v[164:167], v[188:191], v[52:55]\n"
"v_mfma_f32_16x16x32_bf16 v[56:59], v[168:171], v[188:191], v[56:59]\n"
"v_mfma_f32_16x16x32_bf16 v[60:63], v[172:175], v[188:191], v[60:63]\n"
"s_waitcnt lgkmcnt(0)\n"
"v_mfma_f32_16x16x32_bf16 v[64:67], v[144:147], v[176:179], v[64:67]\n"
"ds_read_b128 v[184:187], v254 offset:43008\n"
"ds_read_b128 v[188:191], v254 offset:43520\n"
"v_mfma_f32_16x16x32_bf16 v[68:71], v[148:151], v[176:179], v[68:71]\n"
"v_mfma_f32_16x16x32_bf16 v[72:75], v[152:155], v[176:179], v[72:75]\n"
"v_mfma_f32_16x16x32_bf16 v[76:79], v[156:159], v[176:179], v[76:79]\n"
"v_mfma_f32_16x16x32_bf16 v[64:67], v[160:163], v[180:183], v[64:67]\n"
"v_mfma_f32_16x16x32_bf16 v[68:71], v[164:167], v[180:183], v[68:71]\n"
"v_mfma_f32_16x16x32_bf16 v[72:75], v[168:171], v[180:183], v[72:75]\n"
"v_mfma_f32_16x16x32_bf16 v[76:79], v[172:175], v[180:183], v[76:79]\n"
"s_waitcnt lgkmcnt(0)\n"
"v_mfma_f32_16x16x32_bf16 v[80:83], v[144:147], v[184:187], v[80:83]\n"
"v_mfma_f32_16x16x32_bf16 v[84:87], v[148:151], v[184:187], v[84:87]\n"
"v_mfma_f32_16x16x32_bf16 v[88:91], v[152:155], v[184:187], v[88:91]\n"
"v_mfma_f32_16x16x32_bf16 v[92:95], v[156:159], v[184:187], v[92:95]\n"
"v_mfma_f32_16x16x32_bf16 v[80:83], v[160:163], v[188:191], v[80:83]\n"
"v_mfma_f32_16x16x32_bf16 v[84:87], v[164:167], v[188:191], v[84:87]\n"
"v_mfma_f32_16x16x32_bf16 v[88:91], v[168:171], v[188:191], v[88:91]\n"
"v_mfma_f32_16x16x32_bf16 v[92:95], v[172:175], v[188:191], v[92:95]\n"
"s_waitcnt lgkmcnt(0)\n"
"s_barrier\n"
"ds_read_b128 v[176:179], v254 offset:45056\n"
"ds_read_b128 v[180:183], v254 offset:45568\n"
"s_waitcnt lgkmcnt(0)\n"
"v_mfma_f32_16x16x32_bf16 v[96:99], v[144:147], v[176:179], v[96:99]\n"
"ds_read_b128 v[184:187], v254 offset:47104\n"
"ds_read_b128 v[188:191], v254 offset:47616\n"
"v_mfma_f32_16x16x32_bf16 v[100:103], v[148:151], v[176:179], v[100:103]\n"
"v_mfma_f32_16x16x32_bf16 v[104:107], v[152:155], v[176:179], v[104:107]\n"
"v_mfma_f32_16x16x32_bf16 v[108:111], v[156:159], v[176:179], v[108:111]\n"
"v_mfma_f32_16x16x32_bf16 v[96:99], v[160:163], v[180:183], v[96:99]\n"
"v_mfma_f32_16x16x32_bf16 v[100:103], v[164:167], v[180:183], v[100:103]\n"
"v_mfma_f32_16x16x32_bf16 v[104:107], v[168:171], v[180:183], v[104:107]\n"
"v_mfma_f32_16x16x32_bf16 v[108:111], v[172:175], v[180:183], v[108:111]\n"
"s_waitcnt lgkmcnt(0)\n"
"v_mfma_f32_16x16x32_bf16 v[112:115], v[144:147], v[184:187], v[112:115]\n"
"ds_read_b128 v[176:179], v254 offset:49152\n"
"ds_read_b128 v[180:183], v254 offset:49664\n"
"v_mfma_f32_16x16x32_bf16 v[116:119], v[148:151], v[184:187], v[116:119]\n"
"v_mfma_f32_16x16x32_bf16 v[120:123], v[152:155], v[184:187], v[120:123]\n"
"v_mfma_f32_16x16x32_bf16 v[124:127], v[156:159], v[184:187], v[124:127]\n"
"v_mfma_f32_16x16x32_bf16 v[112:115], v[160:163], v[188:191], v[112:115]\n"
"v_mfma_f32_16x16x32_bf16 v[116:119], v[164:167], v[188:191], v[116:119]\n"
"v_mfma_f32_16x16x32_bf16 v[120:123], v[168:171], v[188:191], v[120:123]\n"
"v_mfma_f32_16x16x32_bf16 v[124:127], v[172:175], v[188:191], v[124:127]\n"
"s_waitcnt lgkmcnt(0)\n"
"v_mfma_f32_16x16x32_bf16 v[128:131], v[144:147], v[176:179], v[128:131]\n"
"ds_read_b128 v[184:187], v254 offset:51200\n"
"ds_read_b128 v[188:191], v254 offset:51712\n"
"v_mfma_f32_16x16x32_bf16 v[132:135], v[148:151], v[176:179], v[132:135]\n"
"ds_read_b64_tr_b16 v[144:145], v234 offset:20480\n"
"ds_read_b64_tr_b16 v[146:147], v234 offset:24576\n"
"v_mfma_f32_16x16x32_bf16 v[136:139], v[152:155], v[176:179], v[136:139]\n"
"ds_read_b64_tr_b16 v[148:149], v235 offset:20480\n"
"ds_read_b64_tr_b16 v[150:151], v235 offset:24576\n"
"v_mfma_f32_16x16x32_bf16 v[140:143], v[156:159], v[176:179], v[140:143]\n"
"ds_read_b64_tr_b16 v[152:153], v234 offset:20992\n"
"ds_read_b64_tr_b16 v[154:155], v234 offset:25088\n"
"v_mfma_f32_16x16x32_bf16 v[128:131], v[160:163], v[180:183], v[128:131]\n"
"ds_read_b64_tr_b16 v[156:157], v235 offset:20992\n"
"ds_read_b64_tr_b16 v[158:159], v235 offset:25088\n"
"v_mfma_f32_16x16x32_bf16 v[132:135], v[164:167], v[180:183], v[132:135]\n"
"ds_read_b64_tr_b16 v[160:161], v234 offset:28672\n"
"ds_read_b64_tr_b16 v[162:163], v234 offset:32768\n"
"v_mfma_f32_16x16x32_bf16 v[136:139], v[168:171], v[180:183], v[136:139]\n"
"ds_read_b64_tr_b16 v[164:165], v235 offset:28672\n"
"ds_read_b64_tr_b16 v[166:167], v235 offset:32768\n"
"v_mfma_f32_16x16x32_bf16 v[140:143], v[172:175], v[180:183], v[140:143]\n"
"s_waitcnt lgkmcnt(10)\n"
"ds_read_b64_tr_b16 v[168:169], v234 offset:29184\n"
"ds_read_b64_tr_b16 v[170:171], v234 offset:33280\n"
"v_mfma_f32_16x16x32_bf16 v[0:3], v[144:147], v[184:187], v[0:3]\n"
"s_waitcnt lgkmcnt(10)\n"
"ds_read_b64_tr_b16 v[172:173], v235 offset:29184\n"
"ds_read_b64_tr_b16 v[174:175], v235 offset:33280\n"
"v_mfma_f32_16x16x32_bf16 v[4:7], v[148:151], v[184:187], v[4:7]\n"
"s_waitcnt lgkmcnt(10)\n"
"ds_read_b128 v[176:179], v254 offset:53248\n"
"ds_read_b128 v[180:183], v254 offset:53760\n"
"v_mfma_f32_16x16x32_bf16 v[8:11], v[152:155], v[184:187], v[8:11]\n"
"s_waitcnt lgkmcnt(10)\n"
"v_mfma_f32_16x16x32_bf16 v[12:15], v[156:159], v[184:187], v[12:15]\n"
"s_waitcnt lgkmcnt(8)\n"
"v_mfma_f32_16x16x32_bf16 v[0:3], v[160:163], v[188:191], v[0:3]\n"
"s_waitcnt lgkmcnt(6)\n"
"v_mfma_f32_16x16x32_bf16 v[4:7], v[164:167], v[188:191], v[4:7]\n"
"s_waitcnt lgkmcnt(4)\n"
"v_mfma_f32_16x16x32_bf16 v[8:11], v[168:171], v[188:191], v[8:11]\n"
"s_waitcnt lgkmcnt(2)\n"
"v_mfma_f32_16x16x32_bf16 v[12:15], v[172:175], v[188:191], v[12:15]\n"
"s_waitcnt lgkmcnt(0)\n"
"v_mfma_f32_16x16x32_bf16 v[16:19], v[144:147], v[176:179], v[16:19]\n"
"ds_read_b128 v[184:187], v254 offset:55296\n"
"ds_read_b128 v[188:191], v254 offset:55808\n"
"v_mfma_f32_16x16x32_bf16 v[20:23], v[148:151], v[176:179], v[20:23]\n"
"v_mfma_f32_16x16x32_bf16 v[24:27], v[152:155], v[176:179], v[24:27]\n"
"v_mfma_f32_16x16x32_bf16 v[28:31], v[156:159], v[176:179], v[28:31]\n"
"v_mfma_f32_16x16x32_bf16 v[16:19], v[160:163], v[180:183], v[16:19]\n"
"v_mfma_f32_16x16x32_bf16 v[20:23], v[164:167], v[180:183], v[20:23]\n"
"v_mfma_f32_16x16x32_bf16 v[24:27], v[168:171], v[180:183], v[24:27]\n"
"v_mfma_f32_16x16x32_bf16 v[28:31], v[172:175], v[180:183], v[28:31]\n"
"s_waitcnt lgkmcnt(0)\n"
"v_mfma_f32_16x16x32_bf16 v[32:35], v[144:147], v[184:187], v[32:35]\n"
"ds_read_b128 v[176:179], v254 offset:57344\n"
"ds_read_b128 v[180:183], v254 offset:57856\n"
"v_mfma_f32_16x16x32_bf16 v[36:39], v[148:151], v[184:187], v[36:39]\n"
"v_mfma_f32_16x16x32_bf16 v[40:43], v[152:155], v[184:187], v[40:43]\n"
"v_mfma_f32_16x16x32_bf16 v[44:47], v[156:159], v[184:187], v[44:47]\n"
"v_mfma_f32_16x16x32_bf16 v[32:35], v[160:163], v[188:191], v[32:35]\n"
"v_mfma_f32_16x16x32_bf16 v[36:39], v[164:167], v[188:191], v[36:39]\n"
"v_mfma_f32_16x16x32_bf16 v[40:43], v[168:171], v[188:191], v[40:43]\n"
"v_mfma_f32_16x16x32_bf16 v[44:47], v[172:175], v[188:191], v[44:47]\n"
"s_waitcnt lgkmcnt(0)\n"
"v_mfma_f32_16x16x32_bf16 v[48:51], v[144:147], v[176:179], v[48:51]\n"
"v_mfma_f32_16x16x32_bf16 v[52:55], v[148:151], v[176:179], v[52:55]\n"
"v_mfma_f32_16x16x32_bf16 v[56:59], v[152:155], v[176:179], v[56:59]\n"
"v_mfma_f32_16x16x32_bf16 v[60:63], v[156:159], v[176:179], v[60:63]\n"
"v_mfma_f32_16x16x32_bf16 v[48:51], v[160:163], v[180:183], v[48:51]\n"
"v_mfma_f32_16x16x32_bf16 v[52:55], v[164:167], v[180:183], v[52:55]\n"
"v_mfma_f32_16x16x32_bf16 v[56:59], v[168:171], v[180:183], v[56:59]\n"
"v_mfma_f32_16x16x32_bf16 v[60:63], v[172:175], v[180:183], v[60:63]\n"
"s_waitcnt lgkmcnt(0)\n"
"s_barrier\n"
"ds_read_b128 v[184:187], v254 offset:59392\n"
"ds_read_b128 v[188:191], v254 offset:59904\n"
"s_waitcnt lgkmcnt(0)\n"
"v_mfma_f32_16x16x32_bf16 v[64:67], v[144:147], v[184:187], v[64:67]\n"
"ds_read_b128 v[176:179], v254 offset:61440\n"
"ds_read_b128 v[180:183], v254 offset:61952\n"
"v_mfma_f32_16x16x32_bf16 v[68:71], v[148:151], v[184:187], v[68:71]\n"
"v_mfma_f32_16x16x32_bf16 v[72:75], v[152:155], v[184:187], v[72:75]\n"
"v_mfma_f32_16x16x32_bf16 v[76:79], v[156:159], v[184:187], v[76:79]\n"
"v_mfma_f32_16x16x32_bf16 v[64:67], v[160:163], v[188:191], v[64:67]\n"
"v_mfma_f32_16x16x32_bf16 v[68:71], v[164:167], v[188:191], v[68:71]\n"
"v_mfma_f32_16x16x32_bf16 v[72:75], v[168:171], v[188:191], v[72:75]\n"
"v_mfma_f32_16x16x32_bf16 v[76:79], v[172:175], v[188:191], v[76:79]\n"
"s_waitcnt lgkmcnt(0)\n"
"v_mfma_f32_16x16x32_bf16 v[80:83], v[144:147], v[176:179], v[80:83]\n"
"ds_read_b128 v[184:187], v254 offset:63488\n"
"ds_read_b128 v[188:191], v254 offset:64000\n"
"v_mfma_f32_16x16x32_bf16 v[84:87], v[148:151], v[176:179], v[84:87]\n"
"v_mfma_f32_16x16x32_bf16 v[88:91], v[152:155], v[176:179], v[88:91]\n"
"v_mfma_f32_16x16x32_bf16 v[92:95], v[156:159], v[176:179], v[92:95]\n"
"v_mfma_f32_16x16x32_bf16 v[80:83], v[160:163], v[180:183], v[80:83]\n"
"v_mfma_f32_16x16x32_bf16 v[84:87], v[164:167], v[180:183], v[84:87]\n"
"v_mfma_f32_16x16x32_bf16 v[88:91], v[168:171], v[180:183], v[88:91]\n"
"v_mfma_f32_16x16x32_bf16 v[92:95], v[172:175], v[180:183], v[92:95]\n"
"s_waitcnt lgkmcnt(0)\n"
"v_mfma_f32_16x16x32_bf16 v[96:99], v[144:147], v[184:187], v[96:99]\n"
"ds_read_b128 v[176:179], v254 offset:36864\n"
"ds_read_b128 v[180:183], v254 offset:37376\n"
"v_mfma_f32_16x16x32_bf16 v[100:103], v[148:151], v[184:187], v[100:103]\n"
"v_mfma_f32_16x16x32_bf16 v[104:107], v[152:155], v[184:187], v[104:107]\n"
"v_mfma_f32_16x16x32_bf16 v[108:111], v[156:159], v[184:187], v[108:111]\n"
"v_mfma_f32_16x16x32_bf16 v[96:99], v[160:163], v[188:191], v[96:99]\n"
"v_mfma_f32_16x16x32_bf16 v[100:103], v[164:167], v[188:191], v[100:103]\n"
"v_mfma_f32_16x16x32_bf16 v[104:107], v[168:171], v[188:191], v[104:107]\n"
"v_mfma_f32_16x16x32_bf16 v[108:111], v[172:175], v[188:191], v[108:111]\n"
"s_waitcnt lgkmcnt(0)\n"
"v_mfma_f32_16x16x32_bf16 v[112:115], v[144:147], v[176:179], v[112:115]\n"
"ds_read_b128 v[184:187], v254 offset:38912\n"
"ds_read_b128 v[188:191], v254 offset:39424\n"
"v_mfma_f32_16x16x32_bf16 v[116:119], v[148:151], v[176:179], v[116:119]\n"
"v_mfma_f32_16x16x32_bf16 v[120:123], v[152:155], v[176:179], v[120:123]\n"
"v_mfma_f32_16x16x32_bf16 v[124:127], v[156:159], v[176:179], v[124:127]\n"
"v_mfma_f32_16x16x32_bf16 v[112:115], v[160:163], v[180:183], v[112:115]\n"
"v_mfma_f32_16x16x32_bf16 v[116:119], v[164:167], v[180:183], v[116:119]\n"
"v_mfma_f32_16x16x32_bf16 v[120:123], v[168:171], v[180:183], v[120:123]\n"
"v_mfma_f32_16x16x32_bf16 v[124:127], v[172:175], v[180:183], v[124:127]\n"
"s_waitcnt lgkmcnt(0)\n"
"v_mfma_f32_16x16x32_bf16 v[128:131], v[144:147], v[184:187], v[128:131]\n"
"ds_read_b128 v[176:179], v254 offset:40960\n"
"ds_read_b128 v[180:183], v254 offset:41472\n"
"v_mfma_f32_16x16x32_bf16 v[132:135], v[148:151], v[184:187], v[132:135]\n"
"ds_read_b64_tr_b16 v[144:145], v234 offset:4096\n"
"ds_read_b64_tr_b16 v[146:147], v234 offset:8192\n"
"v_mfma_f32_16x16x32_bf16 v[136:139], v[152:155], v[184:187], v[136:139]\n"
"ds_read_b64_tr_b16 v[148:149], v235 offset:4096\n"
"ds_read_b64_tr_b16 v[150:151], v235 offset:8192\n"
"v_mfma_f32_16x16x32_bf16 v[140:143], v[156:159], v[184:187], v[140:143]\n"
"ds_read_b64_tr_b16 v[152:153], v234 offset:4608\n"
"ds_read_b64_tr_b16 v[154:155], v234 offset:8704\n"
"v_mfma_f32_16x16x32_bf16 v[128:131], v[160:163], v[188:191], v[128:131]\n"
"ds_read_b64_tr_b16 v[156:157], v235 offset:4608\n"
"ds_read_b64_tr_b16 v[158:159], v235 offset:8704\n"
"v_mfma_f32_16x16x32_bf16 v[132:135], v[164:167], v[188:191], v[132:135]\n"
"ds_read_b64_tr_b16 v[160:161], v234 offset:12288\n"
"ds_read_b64_tr_b16 v[162:163], v234 offset:16384\n"
"v_mfma_f32_16x16x32_bf16 v[136:139], v[168:171], v[188:191], v[136:139]\n"
"ds_read_b64_tr_b16 v[164:165], v235 offset:12288\n"
"ds_read_b64_tr_b16 v[166:167], v235 offset:16384\n"
"v_mfma_f32_16x16x32_bf16 v[140:143], v[172:175], v[188:191], v[140:143]\n"
"s_waitcnt lgkmcnt(10)\n"
"ds_read_b64_tr_b16 v[168:169], v234 offset:12800\n"
"ds_read_b64_tr_b16 v[170:171], v234 offset:16896\n"
"v_mfma_f32_16x16x32_bf16 v[0:3], v[144:147], v[176:179], v[0:3]\n"
"s_waitcnt lgkmcnt(10)\n"
"ds_read_b64_tr_b16 v[172:173], v235 offset:12800\n"
"ds_read_b64_tr_b16 v[174:175], v235 offset:16896\n"
"v_mfma_f32_16x16x32_bf16 v[4:7], v[148:151], v[176:179], v[4:7]\n"
"s_waitcnt lgkmcnt(10)\n"
"ds_read_b128 v[184:187], v254 offset:43008\n"
"ds_read_b128 v[188:191], v254 offset:43520\n"
"v_mfma_f32_16x16x32_bf16 v[8:11], v[152:155], v[176:179], v[8:11]\n"
"s_waitcnt lgkmcnt(10)\n"
"v_mfma_f32_16x16x32_bf16 v[12:15], v[156:159], v[176:179], v[12:15]\n"
"s_waitcnt lgkmcnt(8)\n"
"v_mfma_f32_16x16x32_bf16 v[0:3], v[160:163], v[180:183], v[0:3]\n"
"s_waitcnt lgkmcnt(6)\n"
"v_mfma_f32_16x16x32_bf16 v[4:7], v[164:167], v[180:183], v[4:7]\n"
"s_waitcnt lgkmcnt(4)\n"
"v_mfma_f32_16x16x32_bf16 v[8:11], v[168:171], v[180:183], v[8:11]\n"
"s_waitcnt lgkmcnt(2)\n"
"v_mfma_f32_16x16x32_bf16 v[12:15], v[172:175], v[180:183], v[12:15]\n"
"s_waitcnt lgkmcnt(0)\n"
"v_mfma_f32_16x16x32_bf16 v[16:19], v[144:147], v[184:187], v[16:19]\n"
"v_mfma_f32_16x16x32_bf16 v[20:23], v[148:151], v[184:187], v[20:23]\n"
"v_mfma_f32_16x16x32_bf16 v[24:27], v[152:155], v[184:187], v[24:27]\n"
"v_mfma_f32_16x16x32_bf16 v[28:31], v[156:159], v[184:187], v[28:31]\n"
"v_mfma_f32_16x16x32_bf16 v[16:19], v[160:163], v[188:191], v[16:19]\n"
"v_mfma_f32_16x16x32_bf16 v[20:23], v[164:167], v[188:191], v[20:23]\n"
"v_mfma_f32_16x16x32_bf16 v[24:27], v[168:171], v[188:191], v[24:27]\n"
"v_mfma_f32_16x16x32_bf16 v[28:31], v[172:175], v[188:191], v[28:31]\n"
"s_waitcnt lgkmcnt(0)\n"
"s_barrier\n"
"ds_read_b128 v[176:179], v254 offset:45056\n"
"ds_read_b128 v[180:183], v254 offset:45568\n"
"s_waitcnt lgkmcnt(0)\n"
"v_mfma_f32_16x16x32_bf16 v[32:35], v[144:147], v[176:179], v[32:35]\n"
"ds_read_b128 v[184:187], v254 offset:47104\n"
"ds_read_b128 v[188:191], v254 offset:47616\n"
"v_mfma_f32_16x16x32_bf16 v[36:39], v[148:151], v[176:179], v[36:39]\n"
"v_mfma_f32_16x16x32_bf16 v[40:43], v[152:155], v[176:179], v[40:43]\n"
"v_mfma_f32_16x16x32_bf16 v[44:47], v[156:159], v[176:179], v[44:47]\n"
"v_mfma_f32_16x16x32_bf16 v[32:35], v[160:163], v[180:183], v[32:35]\n"
"v_mfma_f32_16x16x32_bf16 v[36:39], v[164:167], v[180:183], v[36:39]\n"
"v_mfma_f32_16x16x32_bf16 v[40:43], v[168:171], v[180:183], v[40:43]\n"
"v_mfma_f32_16x16x32_bf16 v[44:47], v[172:175], v[180:183], v[44:47]\n"
"s_waitcnt lgkmcnt(0)\n"
"v_mfma_f32_16x16x32_bf16 v[48:51], v[144:147], v[184:187], v[48:51]\n"
"ds_read_b128 v[176:179], v254 offset:49152\n"
"ds_read_b128 v[180:183], v254 offset:49664\n"
"v_mfma_f32_16x16x32_bf16 v[52:55], v[148:151], v[184:187], v[52:55]\n"
"v_mfma_f32_16x16x32_bf16 v[56:59], v[152:155], v[184:187], v[56:59]\n"
"v_mfma_f32_16x16x32_bf16 v[60:63], v[156:159], v[184:187], v[60:63]\n"
"v_mfma_f32_16x16x32_bf16 v[48:51], v[160:163], v[188:191], v[48:51]\n"
"v_mfma_f32_16x16x32_bf16 v[52:55], v[164:167], v[188:191], v[52:55]\n"
"v_mfma_f32_16x16x32_bf16 v[56:59], v[168:171], v[188:191], v[56:59]\n"
"v_mfma_f32_16x16x32_bf16 v[60:63], v[172:175], v[188:191], v[60:63]\n"
"s_waitcnt lgkmcnt(0)\n"
"v_mfma_f32_16x16x32_bf16 v[64:67], v[144:147], v[176:179], v[64:67]\n"
"ds_read_b128 v[184:187], v254 offset:51200\n"
"ds_read_b128 v[188:191], v254 offset:51712\n"
"v_mfma_f32_16x16x32_bf16 v[68:71], v[148:151], v[176:179], v[68:71]\n"
"v_mfma_f32_16x16x32_bf16 v[72:75], v[152:155], v[176:179], v[72:75]\n"
"v_mfma_f32_16x16x32_bf16 v[76:79], v[156:159], v[176:179], v[76:79]\n"
"v_mfma_f32_16x16x32_bf16 v[64:67], v[160:163], v[180:183], v[64:67]\n"
"v_mfma_f32_16x16x32_bf16 v[68:71], v[164:167], v[180:183], v[68:71]\n"
"v_mfma_f32_16x16x32_bf16 v[72:75], v[168:171], v[180:183], v[72:75]\n"
"v_mfma_f32_16x16x32_bf16 v[76:79], v[172:175], v[180:183], v[76:79]\n"
"s_waitcnt lgkmcnt(0)\n"
"v_mfma_f32_16x16x32_bf16 v[80:83], v[144:147], v[184:187], v[80:83]\n"
"ds_read_b128 v[176:179], v254 offset:53248\n"
"ds_read_b128 v[180:183], v254 offset:53760\n"
"v_mfma_f32_16x16x32_bf16 v[84:87], v[148:151], v[184:187], v[84:87]\n"
"v_mfma_f32_16x16x32_bf16 v[88:91], v[152:155], v[184:187], v[88:91]\n"
"v_mfma_f32_16x16x32_bf16 v[92:95], v[156:159], v[184:187], v[92:95]\n"
"v_mfma_f32_16x16x32_bf16 v[80:83], v[160:163], v[188:191], v[80:83]\n"
"v_mfma_f32_16x16x32_bf16 v[84:87], v[164:167], v[188:191], v[84:87]\n"
"v_mfma_f32_16x16x32_bf16 v[88:91], v[168:171], v[188:191], v[88:91]\n"
"v_mfma_f32_16x16x32_bf16 v[92:95], v[172:175], v[188:191], v[92:95]\n"
"s_waitcnt lgkmcnt(0)\n"
"v_mfma_f32_16x16x32_bf16 v[96:99], v[144:147], v[176:179], v[96:99]\n"
"ds_read_b128 v[184:187], v254 offset:55296\n"
"ds_read_b128 v[188:191], v254 offset:55808\n"
"v_mfma_f32_16x16x32_bf16 v[100:103], v[148:151], v[176:179], v[100:103]\n"
"v_mfma_f32_16x16x32_bf16 v[104:107], v[152:155], v[176:179], v[104:107]\n"
"v_mfma_f32_16x16x32_bf16 v[108:111], v[156:159], v[176:179], v[108:111]\n"
"v_mfma_f32_16x16x32_bf16 v[96:99], v[160:163], v[180:183], v[96:99]\n"
"v_mfma_f32_16x16x32_bf16 v[100:103], v[164:167], v[180:183], v[100:103]\n"
"v_mfma_f32_16x16x32_bf16 v[104:107], v[168:171], v[180:183], v[104:107]\n"
"v_mfma_f32_16x16x32_bf16 v[108:111], v[172:175], v[180:183], v[108:111]\n"
"s_waitcnt lgkmcnt(0)\n"
"v_mfma_f32_16x16x32_bf16 v[112:115], v[144:147], v[184:187], v[112:115]\n"
"ds_read_b128 v[176:179], v254 offset:57344\n"
"ds_read_b128 v[180:183], v254 offset:57856\n"
"v_mfma_f32_16x16x32_bf16 v[116:119], v[148:151], v[184:187], v[116:119]\n"
"v_mfma_f32_16x16x32_bf16 v[120:123], v[152:155], v[184:187], v[120:123]\n"
"v_mfma_f32_16x16x32_bf16 v[124:127], v[156:159], v[184:187], v[124:127]\n"
"v_mfma_f32_16x16x32_bf16 v[112:115], v[160:163], v[188:191], v[112:115]\n"
"v_mfma_f32_16x16x32_bf16 v[116:119], v[164:167], v[188:191], v[116:119]\n"
"v_mfma_f32_16x16x32_bf16 v[120:123], v[168:171], v[188:191], v[120:123]\n"
"v_mfma_f32_16x16x32_bf16 v[124:127], v[172:175], v[188:191], v[124:127]\n"
"s_waitcnt lgkmcnt(0)\n"
"v_mfma_f32_16x16x32_bf16 v[128:131], v[144:147], v[176:179], v[128:131]\n"
"v_mfma_f32_16x16x32_bf16 v[132:135], v[148:151], v[176:179], v[132:135]\n"
"v_mfma_f32_16x16x32_bf16 v[136:139], v[152:155], v[176:179], v[136:139]\n"
"v_mfma_f32_16x16x32_bf16 v[140:143], v[156:159], v[176:179], v[140:143]\n"
"v_mfma_f32_16x16x32_bf16 v[128:131], v[160:163], v[180:183], v[128:131]\n"
"v_mfma_f32_16x16x32_bf16 v[132:135], v[164:167], v[180:183], v[132:135]\n"
"v_mfma_f32_16x16x32_bf16 v[136:139], v[168:171], v[180:183], v[136:139]\n"
"v_mfma_f32_16x16x32_bf16 v[140:143], v[172:175], v[180:183], v[140:143]\n"
"s_waitcnt lgkmcnt(0)\n"
"s_barrier\n"
"s_nop 7\n"
"v_pk_mul_f32 v[0:1], v[0:1], s[62:63]\n"
"v_pk_mul_f32 v[2:3], v[2:3], s[62:63]\n"
"v_pk_mul_f32 v[4:5], v[4:5], s[62:63]\n"
"v_pk_mul_f32 v[6:7], v[6:7], s[62:63]\n"
"v_cvt_pk_bf16_f32 v144, v0, v1\n"
"v_cvt_pk_bf16_f32 v145, v2, v3\n"
"v_cvt_pk_bf16_f32 v146, v4, v5\n"
"v_cvt_pk_bf16_f32 v147, v6, v7\n"
"v_pk_mul_f32 v[8:9], v[8:9], s[62:63]\n"
"v_pk_mul_f32 v[10:11], v[10:11], s[62:63]\n"
"v_pk_mul_f32 v[12:13], v[12:13], s[62:63]\n"
"v_pk_mul_f32 v[14:15], v[14:15], s[62:63]\n"
"v_cvt_pk_bf16_f32 v148, v8, v9\n"
"v_cvt_pk_bf16_f32 v149, v10, v11\n"
"v_cvt_pk_bf16_f32 v150, v12, v13\n"
"v_cvt_pk_bf16_f32 v151, v14, v15\n"
"v_pk_mul_f32 v[16:17], v[16:17], s[62:63]\n"
"v_pk_mul_f32 v[18:19], v[18:19], s[62:63]\n"
"v_pk_mul_f32 v[20:21], v[20:21], s[62:63]\n"
"v_pk_mul_f32 v[22:23], v[22:23], s[62:63]\n"
"v_cvt_pk_bf16_f32 v152, v16, v17\n"
"v_cvt_pk_bf16_f32 v153, v18, v19\n"
"v_cvt_pk_bf16_f32 v154, v20, v21\n"
"v_cvt_pk_bf16_f32 v155, v22, v23\n"
"v_pk_mul_f32 v[24:25], v[24:25], s[62:63]\n"
"v_pk_mul_f32 v[26:27], v[26:27], s[62:63]\n"
"v_pk_mul_f32 v[28:29], v[28:29], s[62:63]\n"
"v_pk_mul_f32 v[30:31], v[30:31], s[62:63]\n"
"v_cvt_pk_bf16_f32 v156, v24, v25\n"
"v_cvt_pk_bf16_f32 v157, v26, v27\n"
"v_cvt_pk_bf16_f32 v158, v28, v29\n"
"v_cvt_pk_bf16_f32 v159, v30, v31\n"
"v_pk_mul_f32 v[32:33], v[32:33], s[62:63]\n"
"v_pk_mul_f32 v[34:35], v[34:35], s[62:63]\n"
"v_pk_mul_f32 v[36:37], v[36:37], s[62:63]\n"
"v_pk_mul_f32 v[38:39], v[38:39], s[62:63]\n"
"v_cvt_pk_bf16_f32 v160, v32, v33\n"
"v_cvt_pk_bf16_f32 v161, v34, v35\n"
"v_cvt_pk_bf16_f32 v162, v36, v37\n"
"v_cvt_pk_bf16_f32 v163, v38, v39\n"
"v_pk_mul_f32 v[40:41], v[40:41], s[62:63]\n"
"v_pk_mul_f32 v[42:43], v[42:43], s[62:63]\n"
"v_pk_mul_f32 v[44:45], v[44:45], s[62:63]\n"
"v_pk_mul_f32 v[46:47], v[46:47], s[62:63]\n"
"v_cvt_pk_bf16_f32 v164, v40, v41\n"
"v_cvt_pk_bf16_f32 v165, v42, v43\n"
"v_cvt_pk_bf16_f32 v166, v44, v45\n"
"v_cvt_pk_bf16_f32 v167, v46, v47\n"
"v_permlane32_swap_b32 v144, v146\n"
"v_permlane32_swap_b32 v145, v147\n"
"v_permlane32_swap_b32 v148, v150\n"
"v_permlane32_swap_b32 v149, v151\n"
"v_permlane32_swap_b32 v152, v154\n"
"v_permlane32_swap_b32 v153, v155\n"
"v_permlane32_swap_b32 v156, v158\n"
"v_permlane32_swap_b32 v157, v159\n"
"v_permlane32_swap_b32 v160, v162\n"
"v_permlane32_swap_b32 v161, v163\n"
"v_permlane32_swap_b32 v164, v166\n"
"v_permlane32_swap_b32 v165, v167\n"
"v_permlane16_swap_b32 v144, v146\n"
"v_permlane16_swap_b32 v145, v147\n"
"v_permlane16_swap_b32 v148, v150\n"
"v_permlane16_swap_b32 v149, v151\n"
"v_permlane16_swap_b32 v152, v154\n"
"v_permlane16_swap_b32 v153, v155\n"
"v_permlane16_swap_b32 v156, v158\n"
"v_permlane16_swap_b32 v157, v159\n"
"v_permlane16_swap_b32 v160, v162\n"
"v_permlane16_swap_b32 v161, v163\n"
"v_permlane16_swap_b32 v164, v166\n"
"v_permlane16_swap_b32 v165, v167\n"
"v_pk_mul_f32 v[48:49], v[48:49], s[62:63]\n"
"v_pk_mul_f32 v[50:51], v[50:51], s[62:63]\n"
"v_pk_mul_f32 v[52:53], v[52:53], s[62:63]\n"
"v_pk_mul_f32 v[54:55], v[54:55], s[62:63]\n"
"v_cvt_pk_bf16_f32 v168, v48, v49\n"
"v_cvt_pk_bf16_f32 v169, v50, v51\n"
"v_cvt_pk_bf16_f32 v170, v52, v53\n"
"v_cvt_pk_bf16_f32 v171, v54, v55\n"
"v_pk_mul_f32 v[56:57], v[56:57], s[62:63]\n"
"v_pk_mul_f32 v[58:59], v[58:59], s[62:63]\n"
"v_pk_mul_f32 v[60:61], v[60:61], s[62:63]\n"
"v_pk_mul_f32 v[62:63], v[62:63], s[62:63]\n"
"v_cvt_pk_bf16_f32 v172, v56, v57\n"
"v_cvt_pk_bf16_f32 v173, v58, v59\n"
"v_cvt_pk_bf16_f32 v174, v60, v61\n"
"v_cvt_pk_bf16_f32 v175, v62, v63\n"
"v_pk_mul_f32 v[64:65], v[64:65], s[62:63]\n"
"v_pk_mul_f32 v[66:67], v[66:67], s[62:63]\n"
"v_pk_mul_f32 v[68:69], v[68:69], s[62:63]\n"
"v_pk_mul_f32 v[70:71], v[70:71], s[62:63]\n"
"v_cvt_pk_bf16_f32 v176, v64, v65\n"
"v_cvt_pk_bf16_f32 v177, v66, v67\n"
"v_cvt_pk_bf16_f32 v178, v68, v69\n"
"v_cvt_pk_bf16_f32 v179, v70, v71\n"
"v_pk_mul_f32 v[72:73], v[72:73], s[62:63]\n"
"v_pk_mul_f32 v[74:75], v[74:75], s[62:63]\n"
"v_pk_mul_f32 v[76:77], v[76:77], s[62:63]\n"
"v_pk_mul_f32 v[78:79], v[78:79], s[62:63]\n"
"v_cvt_pk_bf16_f32 v180, v72, v73\n"
"v_cvt_pk_bf16_f32 v181, v74, v75\n"
"v_cvt_pk_bf16_f32 v182, v76, v77\n"
"v_cvt_pk_bf16_f32 v183, v78, v79\n"
"v_pk_mul_f32 v[80:81], v[80:81], s[62:63]\n"
"v_pk_mul_f32 v[82:83], v[82:83], s[62:63]\n"
"v_pk_mul_f32 v[84:85], v[84:85], s[62:63]\n"
"v_pk_mul_f32 v[86:87], v[86:87], s[62:63]\n"
"v_cvt_pk_bf16_f32 v184, v80, v81\n"
"v_cvt_pk_bf16_f32 v185, v82, v83\n"
"v_cvt_pk_bf16_f32 v186, v84, v85\n"
"v_cvt_pk_bf16_f32 v187, v86, v87\n"
"v_pk_mul_f32 v[88:89], v[88:89], s[62:63]\n"
"v_pk_mul_f32 v[90:91], v[90:91], s[62:63]\n"
"v_pk_mul_f32 v[92:93], v[92:93], s[62:63]\n"
"v_pk_mul_f32 v[94:95], v[94:95], s[62:63]\n"
"v_cvt_pk_bf16_f32 v188, v88, v89\n"
"v_cvt_pk_bf16_f32 v189, v90, v91\n"
"v_cvt_pk_bf16_f32 v190, v92, v93\n"
"v_cvt_pk_bf16_f32 v191, v94, v95\n"
"v_permlane32_swap_b32 v168, v170\n"
"v_permlane32_swap_b32 v169, v171\n"
"v_permlane32_swap_b32 v172, v174\n"
"v_permlane32_swap_b32 v173, v175\n"
"v_permlane32_swap_b32 v176, v178\n"
"v_permlane32_swap_b32 v177, v179\n"
"v_permlane32_swap_b32 v180, v182\n"
"v_permlane32_swap_b32 v181, v183\n"
"v_permlane32_swap_b32 v184, v186\n"
"v_permlane32_swap_b32 v185, v187\n"
"v_permlane32_swap_b32 v188, v190\n"
"v_permlane32_swap_b32 v189, v191\n"
"v_permlane16_swap_b32 v168, v170\n"
"v_permlane16_swap_b32 v169, v171\n"
"v_permlane16_swap_b32 v172, v174\n"
"v_permlane16_swap_b32 v173, v175\n"
"v_permlane16_swap_b32 v176, v178\n"
"v_permlane16_swap_b32 v177, v179\n"
"v_permlane16_swap_b32 v180, v182\n"
"v_permlane16_swap_b32 v181, v183\n"
"v_permlane16_swap_b32 v184, v186\n"
"v_permlane16_swap_b32 v185, v187\n"
"v_permlane16_swap_b32 v188, v190\n"
"v_permlane16_swap_b32 v189, v191\n"
"v_lshlrev_b32 v247, 4, v238\n"
"s_lshl_b32 s60, s45, 11\n"
"s_add_u32 s60, s60, 0x19000\n"
"v_add_u32 v247, s60, v247\n"
"ds_write_b128 v247, v[144:147]\n"
"ds_write_b128 v247, v[148:151] offset:1024\n"
"ds_write_b128 v247, v[152:155] offset:4096\n"
"ds_write_b128 v247, v[156:159] offset:5120\n"
"ds_write_b128 v247, v[160:163] offset:8192\n"
"ds_write_b128 v247, v[164:167] offset:9216\n"
"s_waitcnt lgkmcnt(0)\n"
"s_barrier\n"
"s_nop 7\n"
"v_pk_mul_f32 v[96:97], v[96:97], s[62:63]\n"
"v_pk_mul_f32 v[98:99], v[98:99], s[62:63]\n"
"v_pk_mul_f32 v[100:101], v[100:101], s[62:63]\n"
"v_pk_mul_f32 v[102:103], v[102:103], s[62:63]\n"
"v_cvt_pk_bf16_f32 v0, v96, v97\n"
"v_cvt_pk_bf16_f32 v1, v98, v99\n"
"v_cvt_pk_bf16_f32 v2, v100, v101\n"
"v_cvt_pk_bf16_f32 v3, v102, v103\n"
"v_pk_mul_f32 v[104:105], v[104:105], s[62:63]\n"
"v_pk_mul_f32 v[106:107], v[106:107], s[62:63]\n"
"v_pk_mul_f32 v[108:109], v[108:109], s[62:63]\n"
"v_pk_mul_f32 v[110:111], v[110:111], s[62:63]\n"
"v_cvt_pk_bf16_f32 v4, v104, v105\n"
"v_cvt_pk_bf16_f32 v5, v106, v107\n"
"v_cvt_pk_bf16_f32 v6, v108, v109\n"
"v_cvt_pk_bf16_f32 v7, v110, v111\n"
"v_pk_mul_f32 v[112:113], v[112:113], s[62:63]\n"
"v_pk_mul_f32 v[114:115], v[114:115], s[62:63]\n"
"v_pk_mul_f32 v[116:117], v[116:117], s[62:63]\n"
"v_pk_mul_f32 v[118:119], v[118:119], s[62:63]\n"
"v_cvt_pk_bf16_f32 v8, v112, v113\n"
"v_cvt_pk_bf16_f32 v9, v114, v115\n"
"v_cvt_pk_bf16_f32 v10, v116, v117\n"
"v_cvt_pk_bf16_f32 v11, v118, v119\n"
"v_pk_mul_f32 v[120:121], v[120:121], s[62:63]\n"
"v_pk_mul_f32 v[122:123], v[122:123], s[62:63]\n"
"v_pk_mul_f32 v[124:125], v[124:125], s[62:63]\n"
"v_pk_mul_f32 v[126:127], v[126:127], s[62:63]\n"
"v_cvt_pk_bf16_f32 v12, v120, v121\n"
"v_cvt_pk_bf16_f32 v13, v122, v123\n"
"v_cvt_pk_bf16_f32 v14, v124, v125\n"
"v_cvt_pk_bf16_f32 v15, v126, v127\n"
"v_pk_mul_f32 v[128:129], v[128:129], s[62:63]\n"
"v_pk_mul_f32 v[130:131], v[130:131], s[62:63]\n"
"v_pk_mul_f32 v[132:133], v[132:133], s[62:63]\n"
"v_pk_mul_f32 v[134:135], v[134:135], s[62:63]\n"
"v_cvt_pk_bf16_f32 v16, v128, v129\n"
"v_cvt_pk_bf16_f32 v17, v130, v131\n"
"v_cvt_pk_bf16_f32 v18, v132, v133\n"
"v_cvt_pk_bf16_f32 v19, v134, v135\n"
"v_pk_mul_f32 v[136:137], v[136:137], s[62:63]\n"
"v_pk_mul_f32 v[138:139], v[138:139], s[62:63]\n"
"v_pk_mul_f32 v[140:141], v[140:141], s[62:63]\n"
"v_pk_mul_f32 v[142:143], v[142:143], s[62:63]\n"
"v_cvt_pk_bf16_f32 v20, v136, v137\n"
"v_cvt_pk_bf16_f32 v21, v138, v139\n"
"v_cvt_pk_bf16_f32 v22, v140, v141\n"
"v_cvt_pk_bf16_f32 v23, v142, v143\n"
"v_permlane32_swap_b32 v0, v2\n"
"v_permlane32_swap_b32 v1, v3\n"
"v_permlane32_swap_b32 v4, v6\n"
"v_permlane32_swap_b32 v5, v7\n"
"v_permlane32_swap_b32 v8, v10\n"
"v_permlane32_swap_b32 v9, v11\n"
"v_permlane32_swap_b32 v12, v14\n"
"v_permlane32_swap_b32 v13, v15\n"
"v_permlane32_swap_b32 v16, v18\n"
"v_permlane32_swap_b32 v17, v19\n"
"v_permlane32_swap_b32 v20, v22\n"
"v_permlane32_swap_b32 v21, v23\n"
"v_permlane16_swap_b32 v0, v2\n"
"v_permlane16_swap_b32 v1, v3\n"
"v_permlane16_swap_b32 v4, v6\n"
"v_permlane16_swap_b32 v5, v7\n"
"v_permlane16_swap_b32 v8, v10\n"
"v_permlane16_swap_b32 v9, v11\n"
"v_permlane16_swap_b32 v12, v14\n"
"v_permlane16_swap_b32 v13, v15\n"
"v_permlane16_swap_b32 v16, v18\n"
"v_permlane16_swap_b32 v17, v19\n"
"v_permlane16_swap_b32 v20, v22\n"
"v_permlane16_swap_b32 v21, v23\n"
"v_lshlrev_b32 v247, 4, v238\n"
"s_lshl_b32 s60, s45, 11\n"
"s_add_u32 s60, s60, 0x19000\n"
"v_add_u32 v247, s60, v247\n"
"ds_write_b128 v247, v[168:171] offset:12288\n"
"ds_write_b128 v247, v[172:175] offset:13312\n"
"ds_write_b128 v247, v[176:179] offset:16384\n"
"ds_write_b128 v247, v[180:183] offset:17408\n"
"ds_write_b128 v247, v[184:187] offset:20480\n"
"ds_write_b128 v247, v[188:191] offset:21504\n"
"ds_write_b128 v247, v[0:3] offset:24576\n"
"ds_write_b128 v247, v[4:7] offset:25600\n"
"ds_write_b128 v247, v[8:11] offset:28672\n"
"ds_write_b128 v247, v[12:15] offset:29696\n"
"ds_write_b128 v247, v[16:19] offset:32768\n"
"ds_write_b128 v247, v[20:23] offset:33792\n"
"s_waitcnt lgkmcnt(0)\n"
"s_barrier\n"
"s_waitcnt lgkmcnt(0)\n"
"s_barrier\n"
"s_setprio 1\n"
"v_mov_b32 v224, 0x18800\n"
"v_mov_b32 v225, 1\n"
"v_lshlrev_b32 v237, 4, v238\n"
"s_cmp_eq_u32 s45, 0\n"
"s_cbranch_scc1 .Lk1_r2s0\n"
"s_cmp_lt_u32 s6, 0xd8\n"
"s_cbranch_scc1 .Lk1_r2_l11\n"
"s_branch .Lk1_r2_l10\n"
".Lk1_r2s0:\n"
"s_cmp_lt_u32 s6, 0xd8\n"
"s_cbranch_scc0 .Lk1_r2_l00\n"
".Lk1_r2_l01:\n"
"s_waitcnt lgkmcnt(0)\n"
".Lk1_pc46a:\n"
"s_memrealtime s[72:73]\n"
"s_waitcnt lgkmcnt(0)\n"
"s_sub_u32 s73, s72, s65\n"
"s_cmp_lt_u32 s73, 126\n"
"s_cbranch_scc0 .Lk1_pc46b\n"
"s_sleep 1\n"
"s_branch .Lk1_pc46a\n"
".Lk1_pc46b:\n"
"s_mov_b32 s65, s72\n"
"s_add_u32 s53, s33, 0x6000\n"
"s_and_b32 s53, s53, 0x3ffff\n"
"s_add_u32 s53, s53, 0x400000\n"
"s_mov_b32 m0, 0xc000\n"
"s_nop 0\n"
"buffer_load_dwordx4 v208, s[24:27], s53 offen nt lds\n"
"buffer_load_dwordx4 v208, s[24:27], s53 offen offset:2048 nt lds\n"
"s_mov_b32 m0, 0xc380\n"
"s_nop 0\n"
"buffer_load_dwordx4 v208, s[24:27], s53 offen offset:128 nt lds\n"
"buffer_load_dwordx4 v208, s[24:27], s53 offen offset:2176 nt lds\n"
"s_add_u32 s53, s33, 0x8000\n"
"s_and_b32 s53, s53, 0x3ffff\n"
"s_add_u32 s53, s53, 0x400000\n"
"s_mov_b32 m0, 0xe000\n"
"s_nop 0\n"
"buffer_load_dwordx4 v208, s[24:27], s53 offen nt lds\n"
"buffer_load_dwordx4 v208, s[24:27], s53 offen offset:2048 nt lds\n"
"s_mov_b32 m0, 0xe380\n"
"s_nop 0\n"
"buffer_load_dwordx4 v208, s[24:27], s53 offen offset:128 nt lds\n"
"buffer_load_dwordx4 v208, s[24:27], s53 offen offset:2176 nt lds\n"
"s_add_u32 s53, s33, 0x9000\n"
"s_and_b32 s53, s53, 0x3ffff\n"
"s_add_u32 s53, s53, 0x400000\n"
"s_mov_b32 m0, 0x1000\n"
"s_nop 0\n"
"buffer_load_dwordx4 v206, s[20:23], s53 offen nt lds\n"
"buffer_load_dwordx4 v207, s[20:23], s53 offen offset:2048 nt lds\n"
"s_mov_b32 m0, 0x1380\n"
"s_nop 0\n"
"buffer_load_dwordx4 v206, s[20:23], s53 offen offset:128 nt lds\n"
"buffer_load_dwordx4 v207, s[20:23], s53 offen offset:2176 nt lds\n"
"s_add_u32 s53, s33, 0xb000\n"
"s_and_b32 s53, s53, 0x3ffff\n"
"s_add_u32 s53, s53, 0x400000\n"
"s_mov_b32 m0, 0x3000\n"
"s_nop 0\n"
"buffer_load_dwordx4 v206, s[20:23], s53 offen nt lds\n"
"buffer_load_dwordx4 v207, s[20:23], s53 offen offset:2048 nt lds\n"
"s_mov_b32 m0, 0x3380\n"
"s_nop 0\n"
"buffer_load_dwordx4 v206, s[20:23], s53 offen offset:128 nt lds\n"
"buffer_load_dwordx4 v207, s[20:23], s53 offen offset:2176 nt lds\n"
"s_barrier\n"
"s_waitcnt lgkmcnt(0)\n"
".Lk1_pc47a:\n"
"s_memrealtime s[72:73]\n"
"s_waitcnt lgkmcnt(0)\n"
"s_sub_u32 s73, s72, s65\n"
"s_cmp_lt_u32 s73, 126\n"
"s_cbranch_scc0 .Lk1_pc47b\n"
"s_sleep 1\n"
"s_branch .Lk1_pc47a\n"
".Lk1_pc47b:\n"
"s_mov_b32 s65, s72\n"
"s_add_u32 s53, s33, 0xa000\n"
"s_and_b32 s53, s53, 0x3ffff\n"
"s_add_u32 s53, s53, 0x400000\n"
"s_mov_b32 m0, 0x10000\n"
"s_nop 0\n"
"buffer_load_dwordx4 v208, s[24:27], s53 offen nt lds\n"
"buffer_load_dwordx4 v208, s[24:27], s53 offen offset:2048 nt lds\n"
"s_mov_b32 m0, 0x10380\n"
"s_nop 0\n"
"buffer_load_dwordx4 v208, s[24:27], s53 offen offset:128 nt lds\n"
"buffer_load_dwordx4 v208, s[24:27], s53 offen offset:2176 nt lds\n"
"s_add_u32 s53, s33, 0xc000\n"
"s_and_b32 s53, s53, 0x3ffff\n"
"s_add_u32 s53, s53, 0x400000\n"
"s_mov_b32 m0, 0x12000\n"
"s_nop 0\n"
"buffer_load_dwordx4 v208, s[24:27], s53 offen nt lds\n"
"buffer_load_dwordx4 v208, s[24:27], s53 offen offset:2048 nt lds\n"
"s_mov_b32 m0, 0x12380\n"
"s_nop 0\n"
"buffer_load_dwordx4 v208, s[24:27], s53 offen offset:128 nt lds\n"
"buffer_load_dwordx4 v208, s[24:27], s53 offen offset:2176 nt lds\n"
"s_add_u32 s53, s33, 0xd000\n"
"s_and_b32 s53, s53, 0x3ffff\n"
"s_add_u32 s53, s53, 0x400000\n"
"s_mov_b32 m0, 0x5000\n"
"s_nop 0\n"
"buffer_load_dwordx4 v206, s[20:23], s53 offen nt lds\n"
"buffer_load_dwordx4 v207, s[20:23], s53 offen offset:2048 nt lds\n"
"s_mov_b32 m0, 0x5380\n"
"s_nop 0\n"
"buffer_load_dwordx4 v206, s[20:23], s53 offen offset:128 nt lds\n"
"buffer_load_dwordx4 v207, s[20:23], s53 offen offset:2176 nt lds\n"
"s_add_u32 s53, s33, 0xf000\n"
"s_and_b32 s53, s53, 0x3ffff\n"
"s_add_u32 s53, s53, 0x400000\n"
"s_mov_b32 m0, 0x7000\n"
"s_nop 0\n"
"buffer_load_dwordx4 v206, s[20:23], s53 offen nt lds\n"
"buffer_load_dwordx4 v207, s[20:23], s53 offen offset:2048 nt lds\n"
"s_mov_b32 m0, 0x7380\n"
"s_nop 0\n"
"buffer_load_dwordx4 v206, s[20:23], s53 offen offset:128 nt lds\n"
"buffer_load_dwordx4 v207, s[20:23], s53 offen offset:2176 nt lds\n"
"s_waitcnt vmcnt(16)\n"
"s_barrier\n"
"s_waitcnt lgkmcnt(0)\n"
".Lk1_pc48a:\n"
"s_memrealtime s[72:73]\n"
"s_waitcnt lgkmcnt(0)\n"
"s_sub_u32 s73, s72, s65\n"
"s_cmp_lt_u32 s73, 126\n"
"s_cbranch_scc0 .Lk1_pc48b\n"
"s_sleep 1\n"
"s_branch .Lk1_pc48a\n"
".Lk1_pc48b:\n"
"s_mov_b32 s65, s72\n"
"s_add_u32 s53, s33, 0xe000\n"
"s_and_b32 s53, s53, 0x3ffff\n"
"s_add_u32 s53, s53, 0x400000\n"
"s_mov_b32 m0, 0x14000\n"
"s_nop 0\n"
"buffer_load_dwordx4 v208, s[24:27], s53 offen nt lds\n"
"buffer_load_dwordx4 v208, s[24:27], s53 offen offset:2048 nt lds\n"
"s_mov_b32 m0, 0x14380\n"
"s_nop 0\n"
"buffer_load_dwordx4 v208, s[24:27], s53 offen offset:128 nt lds\n"
"buffer_load_dwordx4 v208, s[24:27], s53 offen offset:2176 nt lds\n"
"s_add_u32 s53, s33, 0x10000\n"
"s_and_b32 s53, s53, 0x3ffff\n"
"s_add_u32 s53, s53, 0x400000\n"
"s_mov_b32 m0, 0x16000\n"
"s_nop 0\n"
"buffer_load_dwordx4 v208, s[24:27], s53 offen nt lds\n"
"buffer_load_dwordx4 v208, s[24:27], s53 offen offset:2048 nt lds\n"
"s_mov_b32 m0, 0x16380\n"
"s_nop 0\n"
"buffer_load_dwordx4 v208, s[24:27], s53 offen offset:128 nt lds\n"
"buffer_load_dwordx4 v208, s[24:27], s53 offen offset:2176 nt lds\n"
"s_add_u32 s53, s33, 0x11000\n"
"s_and_b32 s53, s53, 0x3ffff\n"
"s_add_u32 s53, s53, 0x400000\n"
"s_mov_b32 m0, 0x9000\n"
"s_nop 0\n"
"buffer_load_dwordx4 v206, s[20:23], s53 offen nt lds\n"
"buffer_load_dwordx4 v207, s[20:23], s53 offen offset:2048 nt lds\n"
"s_mov_b32 m0, 0x9380\n"
"s_nop 0\n"
"buffer_load_dwordx4 v206, s[20:23], s53 offen offset:128 nt lds\n"
"buffer_load_dwordx4 v207, s[20:23], s53 offen offset:2176 nt lds\n"
"s_add_u32 s53, s33, 0x13000\n"
"s_and_b32 s53, s53, 0x3ffff\n"
"s_add_u32 s53, s53, 0x400000\n"
"s_mov_b32 m0, 0xb000\n"
"s_nop 0\n"
"buffer_load_dwordx4 v206, s[20:23], s53 offen nt lds\n"
"buffer_load_dwordx4 v207, s[20:23], s53 offen offset:2048 nt lds\n"
"s_mov_b32 m0, 0xb380\n"
"s_nop 0\n"
"buffer_load_dwordx4 v206, s[20:23], s53 offen offset:128 nt lds\n"
"buffer_load_dwordx4 v207, s[20:23], s53 offen offset:2176 nt lds\n"
"s_waitcnt vmcnt(16)\n"
"s_barrier\n"
"s_waitcnt lgkmcnt(0)\n"
".Lk1_pc49a:\n"
"s_memrealtime s[72:73]\n"
"s_waitcnt lgkmcnt(0)\n"
"s_sub_u32 s73, s72, s65\n"
"s_cmp_lt_u32 s73, 126\n"
"s_cbranch_scc0 .Lk1_pc49b\n"
"s_sleep 1\n"
"s_branch .Lk1_pc49a\n"
".Lk1_pc49b:\n"
"s_mov_b32 s65, s72\n"
"s_add_u32 s53, s33, 0x12000\n"
"s_and_b32 s53, s53, 0x3ffff\n"
"s_add_u32 s53, s53, 0x400000\n"
"s_mov_b32 m0, 0xc000\n"
"s_nop 0\n"
"buffer_load_dwordx4 v208, s[24:27], s53 offen nt lds\n"
"buffer_load_dwordx4 v208, s[24:27], s53 offen offset:2048 nt lds\n"
"s_mov_b32 m0, 0xc380\n"
"s_nop 0\n"
"buffer_load_dwordx4 v208, s[24:27], s53 offen offset:128 nt lds\n"
"buffer_load_dwordx4 v208, s[24:27], s53 offen offset:2176 nt lds\n"
"s_add_u32 s53, s33, 0x14000\n"
"s_and_b32 s53, s53, 0x3ffff\n"
"s_add_u32 s53, s53, 0x400000\n"
"s_mov_b32 m0, 0xe000\n"
"s_nop 0\n"
"buffer_load_dwordx4 v208, s[24:27], s53 offen nt lds\n"
"buffer_load_dwordx4 v208, s[24:27], s53 offen offset:2048 nt lds\n"
"s_mov_b32 m0, 0xe380\n"
"s_nop 0\n"
"buffer_load_dwordx4 v208, s[24:27], s53 offen offset:128 nt lds\n"
"buffer_load_dwordx4 v208, s[24:27], s53 offen offset:2176 nt lds\n"
"s_add_u32 s53, s33, 0x15000\n"
"s_and_b32 s53, s53, 0x3ffff\n"
"s_add_u32 s53, s53, 0x400000\n"
"s_mov_b32 m0, 0x1000\n"
"s_nop 0\n"
"buffer_load_dwordx4 v206, s[20:23], s53 offen nt lds\n"
"buffer_load_dwordx4 v207, s[20:23], s53 offen offset:2048 nt lds\n"
"s_mov_b32 m0, 0x1380\n"
"s_nop 0\n"
"buffer_load_dwordx4 v206, s[20:23], s53 offen offset:128 nt lds\n"
"buffer_load_dwordx4 v207, s[20:23], s53 offen offset:2176 nt lds\n"
"s_add_u32 s53, s33, 0x17000\n"
"s_and_b32 s53, s53, 0x3ffff\n"
"s_add_u32 s53, s53, 0x400000\n"
"s_mov_b32 m0, 0x3000\n"
"s_nop 0\n"
"buffer_load_dwordx4 v206, s[20:23], s53 offen nt lds\n"
"buffer_load_dwordx4 v207, s[20:23], s53 offen offset:2048 nt lds\n"
"s_mov_b32 m0, 0x3380\n"
"s_nop 0\n"
"buffer_load_dwordx4 v206, s[20:23], s53 offen offset:128 nt lds\n"
"buffer_load_dwordx4 v207, s[20:23], s53 offen offset:2176 nt lds\n"
"s_waitcnt vmcnt(16)\n"
"s_barrier\n"
"s_waitcnt lgkmcnt(0)\n"
".Lk1_pc50a:\n"
"s_memrealtime s[72:73]\n"
"s_waitcnt lgkmcnt(0)\n"
"s_sub_u32 s73, s72, s65\n"
"s_cmp_lt_u32 s73, 126\n"
"s_cbranch_scc0 .Lk1_pc50b\n"
"s_sleep 1\n"
"s_branch .Lk1_pc50a\n"
".Lk1_pc50b:\n"
"s_mov_b32 s65, s72\n"
"s_add_u32 s53, s33, 0x16000\n"
"s_and_b32 s53, s53, 0x3ffff\n"
"s_add_u32 s53, s53, 0x400000\n"
"s_mov_b32 m0, 0x10000\n"
"s_nop 0\n"
"buffer_load_dwordx4 v208, s[24:27], s53 offen nt lds\n"
"buffer_load_dwordx4 v208, s[24:27], s53 offen offset:2048 nt lds\n"
"s_mov_b32 m0, 0x10380\n"
"s_nop 0\n"
"buffer_load_dwordx4 v208, s[24:27], s53 offen offset:128 nt lds\n"
"buffer_load_dwordx4 v208, s[24:27], s53 offen offset:2176 nt lds\n"
"s_add_u32 s53, s33, 0x18000\n"
"s_and_b32 s53, s53, 0x3ffff\n"
"s_add_u32 s53, s53, 0x400000\n"
"s_mov_b32 m0, 0x12000\n"
"s_nop 0\n"
"buffer_load_dwordx4 v208, s[24:27], s53 offen nt lds\n"
"buffer_load_dwordx4 v208, s[24:27], s53 offen offset:2048 nt lds\n"
"s_mov_b32 m0, 0x12380\n"
"s_nop 0\n"
"buffer_load_dwordx4 v208, s[24:27], s53 offen offset:128 nt lds\n"
"buffer_load_dwordx4 v208, s[24:27], s53 offen offset:2176 nt lds\n"
"s_add_u32 s53, s33, 0x19000\n"
"s_and_b32 s53, s53, 0x3ffff\n"
"s_add_u32 s53, s53, 0x400000\n"
"s_mov_b32 m0, 0x5000\n"
"s_nop 0\n"
"buffer_load_dwordx4 v206, s[20:23], s53 offen nt lds\n"
"buffer_load_dwordx4 v207, s[20:23], s53 offen offset:2048 nt lds\n"
"s_mov_b32 m0, 0x5380\n"
"s_nop 0\n"
"buffer_load_dwordx4 v206, s[20:23], s53 offen offset:128 nt lds\n"
"buffer_load_dwordx4 v207, s[20:23], s53 offen offset:2176 nt lds\n"
"s_add_u32 s53, s33, 0x1b000\n"
"s_and_b32 s53, s53, 0x3ffff\n"
"s_add_u32 s53, s53, 0x400000\n"
"s_mov_b32 m0, 0x7000\n"
"s_nop 0\n"
"buffer_load_dwordx4 v206, s[20:23], s53 offen nt lds\n"
"buffer_load_dwordx4 v207, s[20:23], s53 offen offset:2048 nt lds\n"
"s_mov_b32 m0, 0x7380\n"
"s_nop 0\n"
"buffer_load_dwordx4 v206, s[20:23], s53 offen offset:128 nt lds\n"
"buffer_load_dwordx4 v207, s[20:23], s53 offen offset:2176 nt lds\n"
"s_waitcnt vmcnt(16)\n"
"s_barrier\n"
"s_waitcnt lgkmcnt(0)\n"
".Lk1_pc51a:\n"
"s_memrealtime s[72:73]\n"
"s_waitcnt lgkmcnt(0)\n"
"s_sub_u32 s73, s72, s65\n"
"s_cmp_lt_u32 s73, 126\n"
"s_cbranch_scc0 .Lk1_pc51b\n"
"s_sleep 1\n"
"s_branch .Lk1_pc51a\n"
".Lk1_pc51b:\n"
"s_mov_b32 s65, s72\n"
"s_add_u32 s53, s33, 0x1a000\n"
"s_and_b32 s53, s53, 0x3ffff\n"
"s_add_u32 s53, s53, 0x400000\n"
"s_mov_b32 m0, 0x14000\n"
"s_nop 0\n"
"buffer_load_dwordx4 v208, s[24:27], s53 offen nt lds\n"
"buffer_load_dwordx4 v208, s[24:27], s53 offen offset:2048 nt lds\n"
"s_mov_b32 m0, 0x14380\n"
"s_nop 0\n"
"buffer_load_dwordx4 v208, s[24:27], s53 offen offset:128 nt lds\n"
"buffer_load_dwordx4 v208, s[24:27], s53 offen offset:2176 nt lds\n"
"s_add_u32 s53, s33, 0x1c000\n"
"s_and_b32 s53, s53, 0x3ffff\n"
"s_add_u32 s53, s53, 0x400000\n"
"s_mov_b32 m0, 0x16000\n"
"s_nop 0\n"
"buffer_load_dwordx4 v208, s[24:27], s53 offen nt lds\n"
"buffer_load_dwordx4 v208, s[24:27], s53 offen offset:2048 nt lds\n"
"s_mov_b32 m0, 0x16380\n"
"s_nop 0\n"
"buffer_load_dwordx4 v208, s[24:27], s53 offen offset:128 nt lds\n"
"buffer_load_dwordx4 v208, s[24:27], s53 offen offset:2176 nt lds\n"
"s_add_u32 s53, s33, 0x1d000\n"
"s_and_b32 s53, s53, 0x3ffff\n"
"s_add_u32 s53, s53, 0x400000\n"
"s_mov_b32 m0, 0x9000\n"
"s_nop 0\n"
"buffer_load_dwordx4 v206, s[20:23], s53 offen nt lds\n"
"buffer_load_dwordx4 v207, s[20:23], s53 offen offset:2048 nt lds\n"
"s_mov_b32 m0, 0x9380\n"
"s_nop 0\n"
"buffer_load_dwordx4 v206, s[20:23], s53 offen offset:128 nt lds\n"
"buffer_load_dwordx4 v207, s[20:23], s53 offen offset:2176 nt lds\n"
"s_add_u32 s53, s33, 0x1f000\n"
"s_and_b32 s53, s53, 0x3ffff\n"
"s_add_u32 s53, s53, 0x400000\n"
"s_mov_b32 m0, 0xb000\n"
"s_nop 0\n"
"buffer_load_dwordx4 v206, s[20:23], s53 offen nt lds\n"
"buffer_load_dwordx4 v207, s[20:23], s53 offen offset:2048 nt lds\n"
"s_mov_b32 m0, 0xb380\n"
"s_nop 0\n"
"buffer_load_dwordx4 v206, s[20:23], s53 offen offset:128 nt lds\n"
"buffer_load_dwordx4 v207, s[20:23], s53 offen offset:2176 nt lds\n"
"s_waitcnt vmcnt(16)\n"
"s_barrier\n"
"s_waitcnt lgkmcnt(0)\n"
".Lk1_pc52a:\n"
"s_memrealtime s[72:73]\n"
"s_waitcnt lgkmcnt(0)\n"
"s_sub_u32 s73, s72, s65\n"
"s_cmp_lt_u32 s73, 126\n"
"s_cbranch_scc0 .Lk1_pc52b\n"
"s_sleep 1\n"
"s_branch .Lk1_pc52a\n"
".Lk1_pc52b:\n"
"s_mov_b32 s65, s72\n"
"s_add_u32 s53, s33, 0x1e000\n"
"s_and_b32 s53, s53, 0x3ffff\n"
"s_add_u32 s53, s53, 0x400000\n"
"s_mov_b32 m0, 0xc000\n"
"s_nop 0\n"
"buffer_load_dwordx4 v208, s[24:27], s53 offen nt lds\n"
"buffer_load_dwordx4 v208, s[24:27], s53 offen offset:2048 nt lds\n"
"s_mov_b32 m0, 0xc380\n"
"s_nop 0\n"
"buffer_load_dwordx4 v208, s[24:27], s53 offen offset:128 nt lds\n"
"buffer_load_dwordx4 v208, s[24:27], s53 offen offset:2176 nt lds\n"
"s_add_u32 s53, s33, 0x20000\n"
"s_and_b32 s53, s53, 0x3ffff\n"
"s_add_u32 s53, s53, 0x400000\n"
"s_mov_b32 m0, 0xe000\n"
"s_nop 0\n"
"buffer_load_dwordx4 v208, s[24:27], s53 offen nt lds\n"
"buffer_load_dwordx4 v208, s[24:27], s53 offen offset:2048 nt lds\n"
"s_mov_b32 m0, 0xe380\n"
"s_nop 0\n"
"buffer_load_dwordx4 v208, s[24:27], s53 offen offset:128 nt lds\n"
"buffer_load_dwordx4 v208, s[24:27], s53 offen offset:2176 nt lds\n"
"s_add_u32 s53, s33, 0x21000\n"
"s_and_b32 s53, s53, 0x3ffff\n"
"s_add_u32 s53, s53, 0x400000\n"
"s_mov_b32 m0, 0x1000\n"
"s_nop 0\n"
"buffer_load_dwordx4 v206, s[20:23], s53 offen nt lds\n"
"buffer_load_dwordx4 v207, s[20:23], s53 offen offset:2048 nt lds\n"
"s_mov_b32 m0, 0x1380\n"
"s_nop 0\n"
"buffer_load_dwordx4 v206, s[20:23], s53 offen offset:128 nt lds\n"
"buffer_load_dwordx4 v207, s[20:23], s53 offen offset:2176 nt lds\n"
"s_add_u32 s53, s33, 0x23000\n"
"s_and_b32 s53, s53, 0x3ffff\n"
"s_add_u32 s53, s53, 0x400000\n"
"s_mov_b32 m0, 0x3000\n"
"s_nop 0\n"
"buffer_load_dwordx4 v206, s[20:23], s53 offen nt lds\n"
"buffer_load_dwordx4 v207, s[20:23], s53 offen offset:2048 nt lds\n"
"s_mov_b32 m0, 0x3380\n"
"s_nop 0\n"
"buffer_load_dwordx4 v206, s[20:23], s53 offen offset:128 nt lds\n"
"buffer_load_dwordx4 v207, s[20:23], s53 offen offset:2176 nt lds\n"
"s_waitcnt vmcnt(16)\n"
"s_barrier\n"
"s_waitcnt lgkmcnt(0)\n"
".Lk1_pc53a:\n"
"s_memrealtime s[72:73]\n"
"s_waitcnt lgkmcnt(0)\n"
"s_sub_u32 s73, s72, s65\n"
"s_cmp_lt_u32 s73, 126\n"
"s_cbranch_scc0 .Lk1_pc53b\n"
"s_sleep 1\n"
"s_branch .Lk1_pc53a\n"
".Lk1_pc53b:\n"
"s_mov_b32 s65, s72\n"
"s_add_u32 s53, s33, 0x22000\n"
"s_and_b32 s53, s53, 0x3ffff\n"
"s_add_u32 s53, s53, 0x400000\n"
"s_mov_b32 m0, 0x10000\n"
"s_nop 0\n"
"buffer_load_dwordx4 v208, s[24:27], s53 offen nt lds\n"
"buffer_load_dwordx4 v208, s[24:27], s53 offen offset:2048 nt lds\n"
"s_mov_b32 m0, 0x10380\n"
"s_nop 0\n"
"buffer_load_dwordx4 v208, s[24:27], s53 offen offset:128 nt lds\n"
"buffer_load_dwordx4 v208, s[24:27], s53 offen offset:2176 nt lds\n"
"s_add_u32 s53, s33, 0x24000\n"
"s_and_b32 s53, s53, 0x3ffff\n"
"s_add_u32 s53, s53, 0x400000\n"
"s_mov_b32 m0, 0x12000\n"
"s_nop 0\n"
"buffer_load_dwordx4 v208, s[24:27], s53 offen nt lds\n"
"buffer_load_dwordx4 v208, s[24:27], s53 offen offset:2048 nt lds\n"
"s_mov_b32 m0, 0x12380\n"
"s_nop 0\n"
"buffer_load_dwordx4 v208, s[24:27], s53 offen offset:128 nt lds\n"
"buffer_load_dwordx4 v208, s[24:27], s53 offen offset:2176 nt lds\n"
"s_add_u32 s53, s33, 0x25000\n"
"s_and_b32 s53, s53, 0x3ffff\n"
"s_add_u32 s53, s53, 0x400000\n"
"s_mov_b32 m0, 0x5000\n"
"s_nop 0\n"
"buffer_load_dwordx4 v206, s[20:23], s53 offen nt lds\n"
"buffer_load_dwordx4 v207, s[20:23], s53 offen offset:2048 nt lds\n"
"s_mov_b32 m0, 0x5380\n"
"s_nop 0\n"
"buffer_load_dwordx4 v206, s[20:23], s53 offen offset:128 nt lds\n"
"buffer_load_dwordx4 v207, s[20:23], s53 offen offset:2176 nt lds\n"
"s_add_u32 s53, s33, 0x27000\n"
"s_and_b32 s53, s53, 0x3ffff\n"
"s_add_u32 s53, s53, 0x400000\n"
"s_mov_b32 m0, 0x7000\n"
"s_nop 0\n"
"buffer_load_dwordx4 v206, s[20:23], s53 offen nt lds\n"
"buffer_load_dwordx4 v207, s[20:23], s53 offen offset:2048 nt lds\n"
"s_mov_b32 m0, 0x7380\n"
"s_nop 0\n"
"buffer_load_dwordx4 v206, s[20:23], s53 offen offset:128 nt lds\n"
"buffer_load_dwordx4 v207, s[20:23], s53 offen offset:2176 nt lds\n"
"s_and_b32 s67, s6, 3\n"
"s_lshr_b32 s61, s6, 3\n"
"s_mul_i32 s68, s61, 43\n"
"s_lshr_b32 s68, s68, 7\n"
"s_mul_i32 s60, s68, 3\n"
"s_sub_u32 s69, s61, s60\n"
"s_lshr_b32 s60, s6, 2\n"
"s_and_b32 s60, s60, 1\n"
"s_mul_i32 s60, s60, 9\n"
"s_add_u32 s68, s68, s60\n"
"s_lshl_b32 s66, s44, 10\n"
"s_mul_i32 s55, s44, 0x2400\n"
"s_mul_i32 s57, s68, 5\n"
"s_and_b32 s57, s57, 15\n"
"s_mov_b32 s58, s57\n"
"v_bfe_u32 v247, v238, 2, 3\n"
"v_bfe_u32 v236, v238, 4, 1\n"
"v_lshrrev_b32 v243, 5, v238\n"
"v_and_b32 v239, 3, v238\n"
"v_lshlrev_b32 v242, 1, v236\n"
"v_xor_b32 v242, v242, v239\n"
"v_lshlrev_b32 v242, 4, v242\n"
"v_lshl_add_u32 v242, v243, 6, v242\n"
"v_mul_u32_u24 v244, 0x480, v247\n"
"v_add_u32 v242, v242, v244\n"
"v_bfe_u32 v247, v238, 2, 2\n"
"v_lshlrev_b32 v240, 8, v243\n"
"v_lshl_add_u32 v240, v247, 6, v240\n"
"v_xor_b32 v247, v236, v243\n"
"v_lshl_add_u32 v240, v247, 5, v240\n"
"v_lshl_add_u32 v240, v239, 3, v240\n"
"v_add_u32 v241, 0x18000, v240\n"
"v_add_u32 v240, 0x8000, v240\n"
"v_and_b32 v247, 15, v238\n"
"v_lshlrev_b32 v245, 4, v247\n"
"v_lshl_add_u32 v245, v243, 8, v245\n"
"v_lshl_add_u32 v245, v236, 17, v245\n"
"v_and_b32 v247, 31, v238\n"
"v_mul_u32_u24 v244, 0x480, v247\n"
"v_lshl_add_u32 v244, v243, 4, v244\n"
"s_lshl_b32 s60, s68, 3\n"
"s_add_u32 s61, s60, 7\n"
"s_mul_i32 s60, s60, 57\n"
"s_lshr_b32 s60, s60, 9\n"
"s_mul_i32 s61, s61, 57\n"
"s_lshr_b32 s61, s61, 9\n"
"s_sub_u32 s61, s61, s60\n"
"s_lshl_b32 s61, s61, 12\n"
"v_bfe_u32 v247, v238, 2, 1\n"
"v_mul_u32_u24 v247, s61, v247\n"
"s_lshl_b32 s60, s60, 12\n"
"s_lshl_b32 s61, s67, 9\n"
"s_add_u32 s60, s60, s61\n"
"s_add_u32 s60, s60, 0x1000\n"
"v_lshl_add_u32 v243, v239, 7, v247\n"
"v_add_u32 v243, s60, v243\n"
"s_lshl_b32 s60, s68, 2\n"
"s_add_u32 s60, s60, s44\n"
"s_lshl_b32 s60, s60, 18\n"
"s_lshl_b32 s61, s67, 14\n"
"s_add_u32 s60, s60, s61\n"
"s_add_u32 s28, s16, s60\n"
"s_addc_u32 s29, s17, 0\n"
"s_and_b32 s29, s29, 0xffff\n"
"s_mov_b32 s28, s28\n"
"s_mov_b32 s30, 0x34000\n"
"s_mov_b32 s31, 0x20000\n"
"s_mul_i32 s60, s67, 0x90000\n"
"s_mul_i32 s61, s69, 0x180\n"
"s_add_u32 s60, s60, s61\n"
"s_add_u32 s36, s18, s60\n"
"s_addc_u32 s37, s19, 0\n"
"s_and_b32 s37, s37, 0xffff\n"
"s_mov_b32 s36, s36\n"
"s_mov_b32 s38, 0x2cfd00\n"
"s_mov_b32 s39, 0x20000\n"
"s_waitcnt vmcnt(16)\n"
"s_barrier\n"
"s_waitcnt lgkmcnt(0)\n"
".Lk1_pc54a:\n"
"s_memrealtime s[72:73]\n"
"s_waitcnt lgkmcnt(0)\n"
"s_sub_u32 s73, s72, s65\n"
"s_cmp_lt_u32 s73, 126\n"
"s_cbranch_scc0 .Lk1_pc54b\n"
"s_sleep 1\n"
"s_branch .Lk1_pc54a\n"
".Lk1_pc54b:\n"
"s_mov_b32 s65, s72\n"
"s_add_u32 s53, s33, 0x26000\n"
"s_and_b32 s53, s53, 0x3ffff\n"
"s_add_u32 s53, s53, 0x400000\n"
"s_mov_b32 m0, 0x14000\n"
"s_nop 0\n"
"buffer_load_dwordx4 v208, s[24:27], s53 offen nt lds\n"
"buffer_load_dwordx4 v208, s[24:27], s53 offen offset:2048 nt lds\n"
"s_mov_b32 m0, 0x14380\n"
"s_nop 0\n"
"buffer_load_dwordx4 v208, s[24:27], s53 offen offset:128 nt lds\n"
"buffer_load_dwordx4 v208, s[24:27], s53 offen offset:2176 nt lds\n"
"s_add_u32 s53, s33, 0x28000\n"
"s_and_b32 s53, s53, 0x3ffff\n"
"s_add_u32 s53, s53, 0x400000\n"
"s_mov_b32 m0, 0x16000\n"
"s_nop 0\n"
"buffer_load_dwordx4 v208, s[24:27], s53 offen nt lds\n"
"buffer_load_dwordx4 v208, s[24:27], s53 offen offset:2048 nt lds\n"
"s_mov_b32 m0, 0x16380\n"
"s_nop 0\n"
"buffer_load_dwordx4 v208, s[24:27], s53 offen offset:128 nt lds\n"
"buffer_load_dwordx4 v208, s[24:27], s53 offen offset:2176 nt lds\n"
"s_add_u32 s53, s33, 0x29000\n"
"s_and_b32 s53, s53, 0x3ffff\n"
"s_add_u32 s53, s53, 0x400000\n"
"s_mov_b32 m0, 0x9000\n"
"s_nop 0\n"
"buffer_load_dwordx4 v206, s[20:23], s53 offen nt lds\n"
"buffer_load_dwordx4 v207, s[20:23], s53 offen offset:2048 nt lds\n"
"s_mov_b32 m0, 0x9380\n"
"s_nop 0\n"
"buffer_load_dwordx4 v206, s[20:23], s53 offen offset:128 nt lds\n"
"buffer_load_dwordx4 v207, s[20:23], s53 offen offset:2176 nt lds\n"
"s_add_u32 s53, s33, 0x2b000\n"
"s_and_b32 s53, s53, 0x3ffff\n"
"s_add_u32 s53, s53, 0x400000\n"
"s_mov_b32 m0, 0xb000\n"
"s_nop 0\n"
"buffer_load_dwordx4 v206, s[20:23], s53 offen nt lds\n"
"buffer_load_dwordx4 v207, s[20:23], s53 offen offset:2048 nt lds\n"
"s_mov_b32 m0, 0xb380\n"
"s_nop 0\n"
"buffer_load_dwordx4 v206, s[20:23], s53 offen offset:128 nt lds\n"
"buffer_load_dwordx4 v207, s[20:23], s53 offen offset:2176 nt lds\n"
"s_waitcnt vmcnt(16)\n"
"s_barrier\n"
"s_waitcnt lgkmcnt(0)\n"
".Lk1_pc55a:\n"
"s_memrealtime s[72:73]\n"
"s_waitcnt lgkmcnt(0)\n"
"s_sub_u32 s73, s72, s65\n"
"s_cmp_lt_u32 s73, 126\n"
"s_cbranch_scc0 .Lk1_pc55b\n"
"s_sleep 1\n"
"s_branch .Lk1_pc55a\n"
".Lk1_pc55b:\n"
"s_mov_b32 s65, s72\n"
"s_add_u32 s53, s33, 0x2a000\n"
"s_and_b32 s53, s53, 0x3ffff\n"
"s_add_u32 s53, s53, 0x400000\n"
"s_mov_b32 m0, 0xc000\n"
"s_nop 0\n"
"buffer_load_dwordx4 v208, s[24:27], s53 offen nt lds\n"
"buffer_load_dwordx4 v208, s[24:27], s53 offen offset:2048 nt lds\n"
"s_mov_b32 m0, 0xc380\n"
"s_nop 0\n"
"buffer_load_dwordx4 v208, s[24:27], s53 offen offset:128 nt lds\n"
"buffer_load_dwordx4 v208, s[24:27], s53 offen offset:2176 nt lds\n"
"s_add_u32 s53, s33, 0x2c000\n"
"s_and_b32 s53, s53, 0x3ffff\n"
"s_add_u32 s53, s53, 0x400000\n"
"s_mov_b32 m0, 0xe000\n"
"s_nop 0\n"
"buffer_load_dwordx4 v208, s[24:27], s53 offen nt lds\n"
"buffer_load_dwordx4 v208, s[24:27], s53 offen offset:2048 nt lds\n"
"s_mov_b32 m0, 0xe380\n"
"s_nop 0\n"
"buffer_load_dwordx4 v208, s[24:27], s53 offen offset:128 nt lds\n"
"buffer_load_dwordx4 v208, s[24:27], s53 offen offset:2176 nt lds\n"
"s_add_u32 s53, s33, 0x2d000\n"
"s_and_b32 s53, s53, 0x3ffff\n"
"s_add_u32 s53, s53, 0x400000\n"
"s_mov_b32 m0, 0x1000\n"
"s_nop 0\n"
"buffer_load_dwordx4 v206, s[20:23], s53 offen nt lds\n"
"buffer_load_dwordx4 v207, s[20:23], s53 offen offset:2048 nt lds\n"
"s_mov_b32 m0, 0x1380\n"
"s_nop 0\n"
"buffer_load_dwordx4 v206, s[20:23], s53 offen offset:128 nt lds\n"
"buffer_load_dwordx4 v207, s[20:23], s53 offen offset:2176 nt lds\n"
"s_add_u32 s53, s33, 0x2f000\n"
"s_and_b32 s53, s53, 0x3ffff\n"
"s_add_u32 s53, s53, 0x400000\n"
"s_mov_b32 m0, 0x3000\n"
"s_nop 0\n"
"buffer_load_dwordx4 v206, s[20:23], s53 offen nt lds\n"
"buffer_load_dwordx4 v207, s[20:23], s53 offen offset:2048 nt lds\n"
"s_mov_b32 m0, 0x3380\n"
"s_nop 0\n"
"buffer_load_dwordx4 v206, s[20:23], s53 offen offset:128 nt lds\n"
"buffer_load_dwordx4 v207, s[20:23], s53 offen offset:2176 nt lds\n"
"s_waitcnt vmcnt(16)\n"
"s_barrier\n"
"s_waitcnt lgkmcnt(0)\n"
".Lk1_pc56a:\n"
"s_memrealtime s[72:73]\n"
"s_waitcnt lgkmcnt(0)\n"
"s_sub_u32 s73, s72, s65\n"
"s_cmp_lt_u32 s73, 126\n"
"s_cbranch_scc0 .Lk1_pc56b\n"
"s_sleep 1\n"
"s_branch .Lk1_pc56a\n"
".Lk1_pc56b:\n"
"s_mov_b32 s65, s72\n"
"s_add_u32 s53, s33, 0x2e000\n"
"s_and_b32 s53, s53, 0x3ffff\n"
"s_add_u32 s53, s53, 0x400000\n"
"s_mov_b32 m0, 0x10000\n"
"s_nop 0\n"
"buffer_load_dwordx4 v208, s[24:27], s53 offen nt lds\n"
"buffer_load_dwordx4 v208, s[24:27], s53 offen offset:2048 nt lds\n"
"s_mov_b32 m0, 0x10380\n"
"s_nop 0\n"
"buffer_load_dwordx4 v208, s[24:27], s53 offen offset:128 nt lds\n"
"buffer_load_dwordx4 v208, s[24:27], s53 offen offset:2176 nt lds\n"
"s_add_u32 s53, s33, 0x30000\n"
"s_and_b32 s53, s53, 0x3ffff\n"
"s_add_u32 s53, s53, 0x400000\n"
"s_mov_b32 m0, 0x12000\n"
"s_nop 0\n"
"buffer_load_dwordx4 v208, s[24:27], s53 offen nt lds\n"
"buffer_load_dwordx4 v208, s[24:27], s53 offen offset:2048 nt lds\n"
"s_mov_b32 m0, 0x12380\n"
"s_nop 0\n"
"buffer_load_dwordx4 v208, s[24:27], s53 offen offset:128 nt lds\n"
"buffer_load_dwordx4 v208, s[24:27], s53 offen offset:2176 nt lds\n"
"s_add_u32 s53, s33, 0x31000\n"
"s_and_b32 s53, s53, 0x3ffff\n"
"s_add_u32 s53, s53, 0x400000\n"
"s_mov_b32 m0, 0x5000\n"
"s_nop 0\n"
"buffer_load_dwordx4 v206, s[20:23], s53 offen nt lds\n"
"buffer_load_dwordx4 v207, s[20:23], s53 offen offset:2048 nt lds\n"
"s_mov_b32 m0, 0x5380\n"
"s_nop 0\n"
"buffer_load_dwordx4 v206, s[20:23], s53 offen offset:128 nt lds\n"
"buffer_load_dwordx4 v207, s[20:23], s53 offen offset:2176 nt lds\n"
"s_add_u32 s53, s33, 0x33000\n"
"s_and_b32 s53, s53, 0x3ffff\n"
"s_add_u32 s53, s53, 0x400000\n"
"s_mov_b32 m0, 0x7000\n"
"s_nop 0\n"
"buffer_load_dwordx4 v206, s[20:23], s53 offen nt lds\n"
"buffer_load_dwordx4 v207, s[20:23], s53 offen offset:2048 nt lds\n"
"s_mov_b32 m0, 0x7380\n"
"s_nop 0\n"
"buffer_load_dwordx4 v206, s[20:23], s53 offen offset:128 nt lds\n"
"buffer_load_dwordx4 v207, s[20:23], s53 offen offset:2176 nt lds\n"
"s_waitcnt vmcnt(16)\n"
"s_barrier\n"
"s_waitcnt lgkmcnt(0)\n"
".Lk1_pc57a:\n"
"s_memrealtime s[72:73]\n"
"s_waitcnt lgkmcnt(0)\n"
"s_sub_u32 s73, s72, s65\n"
"s_cmp_lt_u32 s73, 126\n"
"s_cbranch_scc0 .Lk1_pc57b\n"
"s_sleep 1\n"
"s_branch .Lk1_pc57a\n"
".Lk1_pc57b:\n"
"s_mov_b32 s65, s72\n"
"s_add_u32 s53, s33, 0x32000\n"
"s_and_b32 s53, s53, 0x3ffff\n"
"s_add_u32 s53, s53, 0x400000\n"
"s_mov_b32 m0, 0x14000\n"
"s_nop 0\n"
"buffer_load_dwordx4 v208, s[24:27], s53 offen nt lds\n"
"buffer_load_dwordx4 v208, s[24:27], s53 offen offset:2048 nt lds\n"
"s_mov_b32 m0, 0x14380\n"
"s_nop 0\n"
"buffer_load_dwordx4 v208, s[24:27], s53 offen offset:128 nt lds\n"
"buffer_load_dwordx4 v208, s[24:27], s53 offen offset:2176 nt lds\n"
"s_add_u32 s53, s33, 0x34000\n"
"s_and_b32 s53, s53, 0x3ffff\n"
"s_add_u32 s53, s53, 0x400000\n"
"s_mov_b32 m0, 0x16000\n"
"s_nop 0\n"
"buffer_load_dwordx4 v208, s[24:27], s53 offen nt lds\n"
"buffer_load_dwordx4 v208, s[24:27], s53 offen offset:2048 nt lds\n"
"s_mov_b32 m0, 0x16380\n"
"s_nop 0\n"
"buffer_load_dwordx4 v208, s[24:27], s53 offen offset:128 nt lds\n"
"buffer_load_dwordx4 v208, s[24:27], s53 offen offset:2176 nt lds\n"
"s_add_u32 s53, s33, 0x35000\n"
"s_and_b32 s53, s53, 0x3ffff\n"
"s_add_u32 s53, s53, 0x400000\n"
"s_mov_b32 m0, 0x9000\n"
"s_nop 0\n"
"buffer_load_dwordx4 v206, s[20:23], s53 offen nt lds\n"
"buffer_load_dwordx4 v207, s[20:23], s53 offen offset:2048 nt lds\n"
"s_mov_b32 m0, 0x9380\n"
"s_nop 0\n"
"buffer_load_dwordx4 v206, s[20:23], s53 offen offset:128 nt lds\n"
"buffer_load_dwordx4 v207, s[20:23], s53 offen offset:2176 nt lds\n"
"s_add_u32 s53, s33, 0x37000\n"
"s_and_b32 s53, s53, 0x3ffff\n"
"s_add_u32 s53, s53, 0x400000\n"
"s_mov_b32 m0, 0xb000\n"
"s_nop 0\n"
"buffer_load_dwordx4 v206, s[20:23], s53 offen nt lds\n"
"buffer_load_dwordx4 v207, s[20:23], s53 offen offset:2048 nt lds\n"
"s_mov_b32 m0, 0xb380\n"
"s_nop 0\n"
"buffer_load_dwordx4 v206, s[20:23], s53 offen offset:128 nt lds\n"
"buffer_load_dwordx4 v207, s[20:23], s53 offen offset:2176 nt lds\n"
"s_waitcnt vmcnt(16)\n"
"s_barrier\n"
"s_waitcnt lgkmcnt(0)\n"
".Lk1_pc58a:\n"
"s_memrealtime s[72:73]\n"
"s_waitcnt lgkmcnt(0)\n"
"s_sub_u32 s73, s72, s65\n"
"s_cmp_lt_u32 s73, 126\n"
"s_cbranch_scc0 .Lk1_pc58b\n"
"s_sleep 1\n"
"s_branch .Lk1_pc58a\n"
".Lk1_pc58b:\n"
"s_mov_b32 s65, s72\n"
"s_add_u32 s53, s33, 0x36000\n"
"s_and_b32 s53, s53, 0x3ffff\n"
"s_add_u32 s53, s53, 0x400000\n"
"s_mov_b32 m0, 0xc000\n"
"s_nop 0\n"
"buffer_load_dwordx4 v208, s[24:27], s53 offen nt lds\n"
"buffer_load_dwordx4 v208, s[24:27], s53 offen offset:2048 nt lds\n"
"s_mov_b32 m0, 0xc380\n"
"s_nop 0\n"
"buffer_load_dwordx4 v208, s[24:27], s53 offen offset:128 nt lds\n"
"buffer_load_dwordx4 v208, s[24:27], s53 offen offset:2176 nt lds\n"
"s_add_u32 s53, s33, 0x38000\n"
"s_and_b32 s53, s53, 0x3ffff\n"
"s_add_u32 s53, s53, 0x400000\n"
"s_mov_b32 m0, 0xe000\n"
"s_nop 0\n"
"buffer_load_dwordx4 v208, s[24:27], s53 offen nt lds\n"
"buffer_load_dwordx4 v208, s[24:27], s53 offen offset:2048 nt lds\n"
"s_mov_b32 m0, 0xe380\n"
"s_nop 0\n"
"buffer_load_dwordx4 v208, s[24:27], s53 offen offset:128 nt lds\n"
"buffer_load_dwordx4 v208, s[24:27], s53 offen offset:2176 nt lds\n"
"s_add_u32 s53, s33, 0x39000\n"
"s_and_b32 s53, s53, 0x3ffff\n"
"s_add_u32 s53, s53, 0x400000\n"
"s_mov_b32 m0, 0x1000\n"
"s_nop 0\n"
"buffer_load_dwordx4 v206, s[20:23], s53 offen nt lds\n"
"buffer_load_dwordx4 v207, s[20:23], s53 offen offset:2048 nt lds\n"
"s_mov_b32 m0, 0x1380\n"
"s_nop 0\n"
"buffer_load_dwordx4 v206, s[20:23], s53 offen offset:128 nt lds\n"
"buffer_load_dwordx4 v207, s[20:23], s53 offen offset:2176 nt lds\n"
"s_add_u32 s53, s33, 0x3b000\n"
"s_and_b32 s53, s53, 0x3ffff\n"
"s_add_u32 s53, s53, 0x400000\n"
"s_mov_b32 m0, 0x3000\n"
"s_nop 0\n"
"buffer_load_dwordx4 v206, s[20:23], s53 offen nt lds\n"
"buffer_load_dwordx4 v207, s[20:23], s53 offen offset:2048 nt lds\n"
"s_mov_b32 m0, 0x3380\n"
"s_nop 0\n"
"buffer_load_dwordx4 v206, s[20:23], s53 offen offset:128 nt lds\n"
"buffer_load_dwordx4 v207, s[20:23], s53 offen offset:2176 nt lds\n"
"s_waitcnt vmcnt(16)\n"
"s_barrier\n"
"buffer_load_dwordx2 v[228:229], v243, s[84:87], 0 offen sc1\n"
"s_waitcnt lgkmcnt(0)\n"
".Lk1_pc59a:\n"
"s_memrealtime s[72:73]\n"
"s_waitcnt lgkmcnt(0)\n"
"s_sub_u32 s73, s72, s65\n"
"s_cmp_lt_u32 s73, 126\n"
"s_cbranch_scc0 .Lk1_pc59b\n"
"s_sleep 1\n"
"s_branch .Lk1_pc59a\n"
".Lk1_pc59b:\n"
"s_mov_b32 s65, s72\n"
"s_add_u32 s53, s33, 0x3a000\n"
"s_and_b32 s53, s53, 0x3ffff\n"
"s_add_u32 s53, s53, 0x400000\n"
"s_mov_b32 m0, 0x10000\n"
"s_nop 0\n"
"buffer_load_dwordx4 v208, s[24:27], s53 offen nt lds\n"
"buffer_load_dwordx4 v208, s[24:27], s53 offen offset:2048 nt lds\n"
"s_mov_b32 m0, 0x10380\n"
"s_nop 0\n"
"buffer_load_dwordx4 v208, s[24:27], s53 offen offset:128 nt lds\n"
"buffer_load_dwordx4 v208, s[24:27], s53 offen offset:2176 nt lds\n"
"s_add_u32 s53, s33, 0x3c000\n"
"s_and_b32 s53, s53, 0x3ffff\n"
"s_add_u32 s53, s53, 0x400000\n"
"s_mov_b32 m0, 0x12000\n"
"s_nop 0\n"
"buffer_load_dwordx4 v208, s[24:27], s53 offen nt lds\n"
"buffer_load_dwordx4 v208, s[24:27], s53 offen offset:2048 nt lds\n"
"s_mov_b32 m0, 0x12380\n"
"s_nop 0\n"
"buffer_load_dwordx4 v208, s[24:27], s53 offen offset:128 nt lds\n"
"buffer_load_dwordx4 v208, s[24:27], s53 offen offset:2176 nt lds\n"
"s_add_u32 s53, s33, 0x3d000\n"
"s_and_b32 s53, s53, 0x3ffff\n"
"s_add_u32 s53, s53, 0x400000\n"
"s_mov_b32 m0, 0x5000\n"
"s_nop 0\n"
"buffer_load_dwordx4 v206, s[20:23], s53 offen nt lds\n"
"buffer_load_dwordx4 v207, s[20:23], s53 offen offset:2048 nt lds\n"
"s_mov_b32 m0, 0x5380\n"
"s_nop 0\n"
"buffer_load_dwordx4 v206, s[20:23], s53 offen offset:128 nt lds\n"
"buffer_load_dwordx4 v207, s[20:23], s53 offen offset:2176 nt lds\n"
"s_add_u32 s53, s33, 0x3f000\n"
"s_and_b32 s53, s53, 0x3ffff\n"
"s_add_u32 s53, s53, 0x400000\n"
"s_mov_b32 m0, 0x7000\n"
"s_nop 0\n"
"buffer_load_dwordx4 v206, s[20:23], s53 offen nt lds\n"
"buffer_load_dwordx4 v207, s[20:23], s53 offen offset:2048 nt lds\n"
"s_mov_b32 m0, 0x7380\n"
"s_nop 0\n"
"buffer_load_dwordx4 v206, s[20:23], s53 offen offset:128 nt lds\n"
"buffer_load_dwordx4 v207, s[20:23], s53 offen offset:2176 nt lds\n"
"s_waitcnt vmcnt(17)\n"
"s_barrier\n"
"s_waitcnt lgkmcnt(0)\n"
".Lk1_pc60a:\n"
"s_memrealtime s[72:73]\n"
"s_waitcnt lgkmcnt(0)\n"
"s_sub_u32 s73, s72, s65\n"
"s_cmp_lt_u32 s73, 126\n"
"s_cbranch_scc0 .Lk1_pc60b\n"
"s_sleep 1\n"
"s_branch .Lk1_pc60a\n"
".Lk1_pc60b:\n"
"s_mov_b32 s65, s72\n"
"s_add_u32 s53, s33, 0x3e000\n"
"s_and_b32 s53, s53, 0x3ffff\n"
"s_add_u32 s53, s53, 0x400000\n"
"s_mov_b32 m0, 0x14000\n"
"s_nop 0\n"
"buffer_load_dwordx4 v208, s[24:27], s53 offen nt lds\n"
"buffer_load_dwordx4 v208, s[24:27], s53 offen offset:2048 nt lds\n"
"s_mov_b32 m0, 0x14380\n"
"s_nop 0\n"
"buffer_load_dwordx4 v208, s[24:27], s53 offen offset:128 nt lds\n"
"buffer_load_dwordx4 v208, s[24:27], s53 offen offset:2176 nt lds\n"
"s_waitcnt vmcnt(4)\n"
"s_barrier\n"
"s_mov_b32 s35, 0\n"
"s_branch .Lk1_plc_o1l01\n"
".Lk1_pl_o1l01:\n"
"s_waitcnt vmcnt(0)\n"
".Lk1_plc_o1l01:\n"
"v_xor_b32 v228, s98, v228\n"
"v_xor_b32 v229, s99, v229\n"
"v_or_b32 v228, v228, v229\n"
"v_cmp_eq_u32 vcc, 0, v228\n"
"s_nop 0\n"
"s_cmp_eq_u64 vcc, -1\n"
"s_cbranch_scc1 .Lk1_pd_o1l01\n"
"s_add_u32 s35, s35, 1\n"
"s_cmp_lt_u32 s35, 0x40000\n"
"s_cbranch_scc0 .Lk1_pd_o1l01\n"
"s_sleep 1\n"
"buffer_load_dwordx2 v[228:229], v243, s[84:87], 0 offen sc1\n"
"s_branch .Lk1_pl_o1l01\n"
".Lk1_pd_o1l01:\n"
"s_lshl_b32 s81, s58, 10\n"
"buffer_load_dwordx4 v[120:123], v245, s[28:31], s81 offen sc1\n"
"buffer_load_dwordx4 v[124:127], v245, s[28:31], s81 offen offset:512 sc1\n"
"s_add_u32 s58, s58, 1\n"
"s_and_b32 s58, s58, 15\n"
"s_lshl_b32 s81, s58, 10\n"
"buffer_load_dwordx4 v[128:131], v245, s[28:31], s81 offen sc1\n"
"buffer_load_dwordx4 v[132:135], v245, s[28:31], s81 offen offset:512 sc1\n"
"s_add_u32 s58, s58, 1\n"
"s_and_b32 s58, s58, 15\n"
"s_lshl_b32 s81, s58, 10\n"
"buffer_load_dwordx4 v[136:139], v245, s[28:31], s81 offen sc1\n"
"buffer_load_dwordx4 v[140:143], v245, s[28:31], s81 offen offset:512 sc1\n"
"s_add_u32 s58, s58, 1\n"
"s_and_b32 s58, s58, 15\n"
"s_lshl_b32 s81, s58, 10\n"
"buffer_load_dwordx4 v[144:147], v245, s[28:31], s81 offen sc1\n"
"buffer_load_dwordx4 v[148:151], v245, s[28:31], s81 offen offset:512 sc1\n"
"s_add_u32 s58, s58, 1\n"
"s_and_b32 s58, s58, 15\n"
"s_lshl_b32 s81, s58, 10\n"
"buffer_load_dwordx4 v[152:155], v245, s[28:31], s81 offen sc1\n"
"buffer_load_dwordx4 v[156:159], v245, s[28:31], s81 offen offset:512 sc1\n"
"s_add_u32 s58, s58, 1\n"
"s_and_b32 s58, s58, 15\n"
"s_lshl_b32 s81, s58, 10\n"
"buffer_load_dwordx4 v[160:163], v245, s[28:31], s81 offen sc1\n"
"buffer_load_dwordx4 v[164:167], v245, s[28:31], s81 offen offset:512 sc1\n"
"s_add_u32 s58, s58, 1\n"
"s_and_b32 s58, s58, 15\n"
"s_lshl_b32 s81, s58, 10\n"
"buffer_load_dwordx4 v[168:171], v245, s[28:31], s81 offen sc1\n"
"buffer_load_dwordx4 v[172:175], v245, s[28:31], s81 offen offset:512 sc1\n"
"s_add_u32 s58, s58, 1\n"
"s_and_b32 s58, s58, 15\n"
"s_lshl_b32 s81, s58, 10\n"
"buffer_load_dwordx4 v[176:179], v245, s[28:31], s81 offen sc1\n"
"buffer_load_dwordx4 v[180:183], v245, s[28:31], s81 offen offset:512 sc1\n"
"s_waitcnt vmcnt(16)\n"
"s_barrier\n"
"s_branch .Lk1_tail\n"
".Lk1_r2_l00:\n"
"s_waitcnt lgkmcnt(0)\n"
".Lk1_pc61a:\n"
"s_memrealtime s[72:73]\n"
"s_waitcnt lgkmcnt(0)\n"
"s_sub_u32 s73, s72, s65\n"
"s_cmp_lt_u32 s73, 126\n"
"s_cbranch_scc0 .Lk1_pc61b\n"
"s_sleep 1\n"
"s_branch .Lk1_pc61a\n"
".Lk1_pc61b:\n"
"s_mov_b32 s65, s72\n"
"s_add_u32 s53, s33, 0x6000\n"
"s_and_b32 s53, s53, 0x3ffff\n"
"s_add_u32 s53, s53, 0x400000\n"
"s_mov_b32 m0, 0xc000\n"
"s_nop 0\n"
"buffer_load_dwordx4 v208, s[24:27], s53 offen nt lds\n"
"buffer_load_dwordx4 v208, s[24:27], s53 offen offset:2048 nt lds\n"
"s_mov_b32 m0, 0xc380\n"
"s_nop 0\n"
"buffer_load_dwordx4 v208, s[24:27], s53 offen offset:128 nt lds\n"
"buffer_load_dwordx4 v208, s[24:27], s53 offen offset:2176 nt lds\n"
"s_add_u32 s53, s33, 0x8000\n"
"s_and_b32 s53, s53, 0x3ffff\n"
"s_add_u32 s53, s53, 0x400000\n"
"s_mov_b32 m0, 0xe000\n"
"s_nop 0\n"
"buffer_load_dwordx4 v208, s[24:27], s53 offen nt lds\n"
"buffer_load_dwordx4 v208, s[24:27], s53 offen offset:2048 nt lds\n"
"s_mov_b32 m0, 0xe380\n"
"s_nop 0\n"
"buffer_load_dwordx4 v208, s[24:27], s53 offen offset:128 nt lds\n"
"buffer_load_dwordx4 v208, s[24:27], s53 offen offset:2176 nt lds\n"
"s_add_u32 s53, s33, 0x9000\n"
"s_and_b32 s53, s53, 0x3ffff\n"
"s_add_u32 s53, s53, 0x400000\n"
"s_mov_b32 m0, 0x1000\n"
"s_nop 0\n"
"buffer_load_dwordx4 v206, s[20:23], s53 offen nt lds\n"
"buffer_load_dwordx4 v207, s[20:23], s53 offen offset:2048 nt lds\n"
"s_mov_b32 m0, 0x1380\n"
"s_nop 0\n"
"buffer_load_dwordx4 v206, s[20:23], s53 offen offset:128 nt lds\n"
"buffer_load_dwordx4 v207, s[20:23], s53 offen offset:2176 nt lds\n"
"s_add_u32 s53, s33, 0xb000\n"
"s_and_b32 s53, s53, 0x3ffff\n"
"s_add_u32 s53, s53, 0x400000\n"
"s_mov_b32 m0, 0x3000\n"
"s_nop 0\n"
"buffer_load_dwordx4 v206, s[20:23], s53 offen nt lds\n"
"buffer_load_dwordx4 v207, s[20:23], s53 offen offset:2048 nt lds\n"
"s_mov_b32 m0, 0x3380\n"
"s_nop 0\n"
"buffer_load_dwordx4 v206, s[20:23], s53 offen offset:128 nt lds\n"
"buffer_load_dwordx4 v207, s[20:23], s53 offen offset:2176 nt lds\n"
"s_barrier\n"
"s_waitcnt lgkmcnt(0)\n"
".Lk1_pc62a:\n"
"s_memrealtime s[72:73]\n"
"s_waitcnt lgkmcnt(0)\n"
"s_sub_u32 s73, s72, s65\n"
"s_cmp_lt_u32 s73, 126\n"
"s_cbranch_scc0 .Lk1_pc62b\n"
"s_sleep 1\n"
"s_branch .Lk1_pc62a\n"
".Lk1_pc62b:\n"
"s_mov_b32 s65, s72\n"
"s_add_u32 s53, s33, 0xa000\n"
"s_and_b32 s53, s53, 0x3ffff\n"
"s_add_u32 s53, s53, 0x400000\n"
"s_mov_b32 m0, 0x10000\n"
"s_nop 0\n"
"buffer_load_dwordx4 v208, s[24:27], s53 offen nt lds\n"
"buffer_load_dwordx4 v208, s[24:27], s53 offen offset:2048 nt lds\n"
"s_mov_b32 m0, 0x10380\n"
"s_nop 0\n"
"buffer_load_dwordx4 v208, s[24:27], s53 offen offset:128 nt lds\n"
"buffer_load_dwordx4 v208, s[24:27], s53 offen offset:2176 nt lds\n"
"s_add_u32 s53, s33, 0xc000\n"
"s_and_b32 s53, s53, 0x3ffff\n"
"s_add_u32 s53, s53, 0x400000\n"
"s_mov_b32 m0, 0x12000\n"
"s_nop 0\n"
"buffer_load_dwordx4 v208, s[24:27], s53 offen nt lds\n"
"buffer_load_dwordx4 v208, s[24:27], s53 offen offset:2048 nt lds\n"
"s_mov_b32 m0, 0x12380\n"
"s_nop 0\n"
"buffer_load_dwordx4 v208, s[24:27], s53 offen offset:128 nt lds\n"
"buffer_load_dwordx4 v208, s[24:27], s53 offen offset:2176 nt lds\n"
"s_add_u32 s53, s33, 0xd000\n"
"s_and_b32 s53, s53, 0x3ffff\n"
"s_add_u32 s53, s53, 0x400000\n"
"s_mov_b32 m0, 0x5000\n"
"s_nop 0\n"
"buffer_load_dwordx4 v206, s[20:23], s53 offen nt lds\n"
"buffer_load_dwordx4 v207, s[20:23], s53 offen offset:2048 nt lds\n"
"s_mov_b32 m0, 0x5380\n"
"s_nop 0\n"
"buffer_load_dwordx4 v206, s[20:23], s53 offen offset:128 nt lds\n"
"buffer_load_dwordx4 v207, s[20:23], s53 offen offset:2176 nt lds\n"
"s_add_u32 s53, s33, 0xf000\n"
"s_and_b32 s53, s53, 0x3ffff\n"
"s_add_u32 s53, s53, 0x400000\n"
"s_mov_b32 m0, 0x7000\n"
"s_nop 0\n"
"buffer_load_dwordx4 v206, s[20:23], s53 offen nt lds\n"
"buffer_load_dwordx4 v207, s[20:23], s53 offen offset:2048 nt lds\n"
"s_mov_b32 m0, 0x7380\n"
"s_nop 0\n"
"buffer_load_dwordx4 v206, s[20:23], s53 offen offset:128 nt lds\n"
"buffer_load_dwordx4 v207, s[20:23], s53 offen offset:2176 nt lds\n"
"s_waitcnt vmcnt(16)\n"
"s_barrier\n"
"s_waitcnt lgkmcnt(0)\n"
".Lk1_pc63a:\n"
"s_memrealtime s[72:73]\n"
"s_waitcnt lgkmcnt(0)\n"
"s_sub_u32 s73, s72, s65\n"
"s_cmp_lt_u32 s73, 126\n"
"s_cbranch_scc0 .Lk1_pc63b\n"
"s_sleep 1\n"
"s_branch .Lk1_pc63a\n"
".Lk1_pc63b:\n"
"s_mov_b32 s65, s72\n"
"s_add_u32 s53, s33, 0xe000\n"
"s_and_b32 s53, s53, 0x3ffff\n"
"s_add_u32 s53, s53, 0x400000\n"
"s_mov_b32 m0, 0x14000\n"
"s_nop 0\n"
"buffer_load_dwordx4 v208, s[24:27], s53 offen nt lds\n"
"buffer_load_dwordx4 v208, s[24:27], s53 offen offset:2048 nt lds\n"
"s_mov_b32 m0, 0x14380\n"
"s_nop 0\n"
"buffer_load_dwordx4 v208, s[24:27], s53 offen offset:128 nt lds\n"
"buffer_load_dwordx4 v208, s[24:27], s53 offen offset:2176 nt lds\n"
"s_add_u32 s53, s33, 0x10000\n"
"s_and_b32 s53, s53, 0x3ffff\n"
"s_add_u32 s53, s53, 0x400000\n"
"s_mov_b32 m0, 0x16000\n"
"s_nop 0\n"
"buffer_load_dwordx4 v208, s[24:27], s53 offen nt lds\n"
"buffer_load_dwordx4 v208, s[24:27], s53 offen offset:2048 nt lds\n"
"s_mov_b32 m0, 0x16380\n"
"s_nop 0\n"
"buffer_load_dwordx4 v208, s[24:27], s53 offen offset:128 nt lds\n"
"buffer_load_dwordx4 v208, s[24:27], s53 offen offset:2176 nt lds\n"
"s_add_u32 s53, s33, 0x11000\n"
"s_and_b32 s53, s53, 0x3ffff\n"
"s_add_u32 s53, s53, 0x400000\n"
"s_mov_b32 m0, 0x9000\n"
"s_nop 0\n"
"buffer_load_dwordx4 v206, s[20:23], s53 offen nt lds\n"
"buffer_load_dwordx4 v207, s[20:23], s53 offen offset:2048 nt lds\n"
"s_mov_b32 m0, 0x9380\n"
"s_nop 0\n"
"buffer_load_dwordx4 v206, s[20:23], s53 offen offset:128 nt lds\n"
"buffer_load_dwordx4 v207, s[20:23], s53 offen offset:2176 nt lds\n"
"s_add_u32 s53, s33, 0x13000\n"
"s_and_b32 s53, s53, 0x3ffff\n"
"s_add_u32 s53, s53, 0x400000\n"
"s_mov_b32 m0, 0xb000\n"
"s_nop 0\n"
"buffer_load_dwordx4 v206, s[20:23], s53 offen nt lds\n"
"buffer_load_dwordx4 v207, s[20:23], s53 offen offset:2048 nt lds\n"
"s_mov_b32 m0, 0xb380\n"
"s_nop 0\n"
"buffer_load_dwordx4 v206, s[20:23], s53 offen offset:128 nt lds\n"
"buffer_load_dwordx4 v207, s[20:23], s53 offen offset:2176 nt lds\n"
"s_waitcnt vmcnt(16)\n"
"s_barrier\n"
"s_waitcnt lgkmcnt(0)\n"
".Lk1_pc64a:\n"
"s_memrealtime s[72:73]\n"
"s_waitcnt lgkmcnt(0)\n"
"s_sub_u32 s73, s72, s65\n"
"s_cmp_lt_u32 s73, 126\n"
"s_cbranch_scc0 .Lk1_pc64b\n"
"s_sleep 1\n"
"s_branch .Lk1_pc64a\n"
".Lk1_pc64b:\n"
"s_mov_b32 s65, s72\n"
"s_add_u32 s53, s33, 0x12000\n"
"s_and_b32 s53, s53, 0x3ffff\n"
"s_add_u32 s53, s53, 0x400000\n"
"s_mov_b32 m0, 0xc000\n"
"s_nop 0\n"
"buffer_load_dwordx4 v208, s[24:27], s53 offen nt lds\n"
"buffer_load_dwordx4 v208, s[24:27], s53 offen offset:2048 nt lds\n"
"s_mov_b32 m0, 0xc380\n"
"s_nop 0\n"
"buffer_load_dwordx4 v208, s[24:27], s53 offen offset:128 nt lds\n"
"buffer_load_dwordx4 v208, s[24:27], s53 offen offset:2176 nt lds\n"
"s_add_u32 s53, s33, 0x14000\n"
"s_and_b32 s53, s53, 0x3ffff\n"
"s_add_u32 s53, s53, 0x400000\n"
"s_mov_b32 m0, 0xe000\n"
"s_nop 0\n"
"buffer_load_dwordx4 v208, s[24:27], s53 offen nt lds\n"
"buffer_load_dwordx4 v208, s[24:27], s53 offen offset:2048 nt lds\n"
"s_mov_b32 m0, 0xe380\n"
"s_nop 0\n"
"buffer_load_dwordx4 v208, s[24:27], s53 offen offset:128 nt lds\n"
"buffer_load_dwordx4 v208, s[24:27], s53 offen offset:2176 nt lds\n"
"s_add_u32 s53, s33, 0x15000\n"
"s_and_b32 s53, s53, 0x3ffff\n"
"s_add_u32 s53, s53, 0x400000\n"
"s_mov_b32 m0, 0x1000\n"
"s_nop 0\n"
"buffer_load_dwordx4 v206, s[20:23], s53 offen nt lds\n"
"buffer_load_dwordx4 v207, s[20:23], s53 offen offset:2048 nt lds\n"
"s_mov_b32 m0, 0x1380\n"
"s_nop 0\n"
"buffer_load_dwordx4 v206, s[20:23], s53 offen offset:128 nt lds\n"
"buffer_load_dwordx4 v207, s[20:23], s53 offen offset:2176 nt lds\n"
"s_add_u32 s53, s33, 0x17000\n"
"s_and_b32 s53, s53, 0x3ffff\n"
"s_add_u32 s53, s53, 0x400000\n"
"s_mov_b32 m0, 0x3000\n"
"s_nop 0\n"
"buffer_load_dwordx4 v206, s[20:23], s53 offen nt lds\n"
"buffer_load_dwordx4 v207, s[20:23], s53 offen offset:2048 nt lds\n"
"s_mov_b32 m0, 0x3380\n"
"s_nop 0\n"
"buffer_load_dwordx4 v206, s[20:23], s53 offen offset:128 nt lds\n"
"buffer_load_dwordx4 v207, s[20:23], s53 offen offset:2176 nt lds\n"
"s_waitcnt vmcnt(16)\n"
"s_barrier\n"
"s_waitcnt lgkmcnt(0)\n"
".Lk1_pc65a:\n"
"s_memrealtime s[72:73]\n"
"s_waitcnt lgkmcnt(0)\n"
"s_sub_u32 s73, s72, s65\n"
"s_cmp_lt_u32 s73, 126\n"
"s_cbranch_scc0 .Lk1_pc65b\n"
"s_sleep 1\n"
"s_branch .Lk1_pc65a\n"
".Lk1_pc65b:\n"
"s_mov_b32 s65, s72\n"
"s_add_u32 s53, s33, 0x16000\n"
"s_and_b32 s53, s53, 0x3ffff\n"
"s_add_u32 s53, s53, 0x400000\n"
"s_mov_b32 m0, 0x10000\n"
"s_nop 0\n"
"buffer_load_dwordx4 v208, s[24:27], s53 offen nt lds\n"
"buffer_load_dwordx4 v208, s[24:27], s53 offen offset:2048 nt lds\n"
"s_mov_b32 m0, 0x10380\n"
"s_nop 0\n"
"buffer_load_dwordx4 v208, s[24:27], s53 offen offset:128 nt lds\n"
"buffer_load_dwordx4 v208, s[24:27], s53 offen offset:2176 nt lds\n"
"s_add_u32 s53, s33, 0x18000\n"
"s_and_b32 s53, s53, 0x3ffff\n"
"s_add_u32 s53, s53, 0x400000\n"
"s_mov_b32 m0, 0x12000\n"
"s_nop 0\n"
"buffer_load_dwordx4 v208, s[24:27], s53 offen nt lds\n"
"buffer_load_dwordx4 v208, s[24:27], s53 offen offset:2048 nt lds\n"
"s_mov_b32 m0, 0x12380\n"
"s_nop 0\n"
"buffer_load_dwordx4 v208, s[24:27], s53 offen offset:128 nt lds\n"
"buffer_load_dwordx4 v208, s[24:27], s53 offen offset:2176 nt lds\n"
"s_add_u32 s53, s33, 0x19000\n"
"s_and_b32 s53, s53, 0x3ffff\n"
"s_add_u32 s53, s53, 0x400000\n"
"s_mov_b32 m0, 0x5000\n"
"s_nop 0\n"
"buffer_load_dwordx4 v206, s[20:23], s53 offen nt lds\n"
"buffer_load_dwordx4 v207, s[20:23], s53 offen offset:2048 nt lds\n"
"s_mov_b32 m0, 0x5380\n"
"s_nop 0\n"
"buffer_load_dwordx4 v206, s[20:23], s53 offen offset:128 nt lds\n"
"buffer_load_dwordx4 v207, s[20:23], s53 offen offset:2176 nt lds\n"
"s_add_u32 s53, s33, 0x1b000\n"
"s_and_b32 s53, s53, 0x3ffff\n"
"s_add_u32 s53, s53, 0x400000\n"
"s_mov_b32 m0, 0x7000\n"
"s_nop 0\n"
"buffer_load_dwordx4 v206, s[20:23], s53 offen nt lds\n"
"buffer_load_dwordx4 v207, s[20:23], s53 offen offset:2048 nt lds\n"
"s_mov_b32 m0, 0x7380\n"
"s_nop 0\n"
"buffer_load_dwordx4 v206, s[20:23], s53 offen offset:128 nt lds\n"
"buffer_load_dwordx4 v207, s[20:23], s53 offen offset:2176 nt lds\n"
"s_waitcnt vmcnt(16)\n"
"s_barrier\n"
"s_waitcnt lgkmcnt(0)\n"
".Lk1_pc66a:\n"
"s_memrealtime s[72:73]\n"
"s_waitcnt lgkmcnt(0)\n"
"s_sub_u32 s73, s72, s65\n"
"s_cmp_lt_u32 s73, 126\n"
"s_cbranch_scc0 .Lk1_pc66b\n"
"s_sleep 1\n"
"s_branch .Lk1_pc66a\n"
".Lk1_pc66b:\n"
"s_mov_b32 s65, s72\n"
"s_add_u32 s53, s33, 0x1a000\n"
"s_and_b32 s53, s53, 0x3ffff\n"
"s_add_u32 s53, s53, 0x400000\n"
"s_mov_b32 m0, 0x14000\n"
"s_nop 0\n"
"buffer_load_dwordx4 v208, s[24:27], s53 offen nt lds\n"
"buffer_load_dwordx4 v208, s[24:27], s53 offen offset:2048 nt lds\n"
"s_mov_b32 m0, 0x14380\n"
"s_nop 0\n"
"buffer_load_dwordx4 v208, s[24:27], s53 offen offset:128 nt lds\n"
"buffer_load_dwordx4 v208, s[24:27], s53 offen offset:2176 nt lds\n"
"s_add_u32 s53, s33, 0x1c000\n"
"s_and_b32 s53, s53, 0x3ffff\n"
"s_add_u32 s53, s53, 0x400000\n"
"s_mov_b32 m0, 0x16000\n"
"s_nop 0\n"
"buffer_load_dwordx4 v208, s[24:27], s53 offen nt lds\n"
"buffer_load_dwordx4 v208, s[24:27], s53 offen offset:2048 nt lds\n"
"s_mov_b32 m0, 0x16380\n"
"s_nop 0\n"
"buffer_load_dwordx4 v208, s[24:27], s53 offen offset:128 nt lds\n"
"buffer_load_dwordx4 v208, s[24:27], s53 offen offset:2176 nt lds\n"
"s_add_u32 s53, s33, 0x1d000\n"
"s_and_b32 s53, s53, 0x3ffff\n"
"s_add_u32 s53, s53, 0x400000\n"
"s_mov_b32 m0, 0x9000\n"
"s_nop 0\n"
"buffer_load_dwordx4 v206, s[20:23], s53 offen nt lds\n"
"buffer_load_dwordx4 v207, s[20:23], s53 offen offset:2048 nt lds\n"
"s_mov_b32 m0, 0x9380\n"
"s_nop 0\n"
"buffer_load_dwordx4 v206, s[20:23], s53 offen offset:128 nt lds\n"
"buffer_load_dwordx4 v207, s[20:23], s53 offen offset:2176 nt lds\n"
"s_add_u32 s53, s33, 0x1f000\n"
"s_and_b32 s53, s53, 0x3ffff\n"
"s_add_u32 s53, s53, 0x400000\n"
"s_mov_b32 m0, 0xb000\n"
"s_nop 0\n"
"buffer_load_dwordx4 v206, s[20:23], s53 offen nt lds\n"
"buffer_load_dwordx4 v207, s[20:23], s53 offen offset:2048 nt lds\n"
"s_mov_b32 m0, 0xb380\n"
"s_nop 0\n"
"buffer_load_dwordx4 v206, s[20:23], s53 offen offset:128 nt lds\n"
"buffer_load_dwordx4 v207, s[20:23], s53 offen offset:2176 nt lds\n"
"s_waitcnt vmcnt(16)\n"
"s_barrier\n"
"s_waitcnt lgkmcnt(0)\n"
".Lk1_pc67a:\n"
"s_memrealtime s[72:73]\n"
"s_waitcnt lgkmcnt(0)\n"
"s_sub_u32 s73, s72, s65\n"
"s_cmp_lt_u32 s73, 126\n"
"s_cbranch_scc0 .Lk1_pc67b\n"
"s_sleep 1\n"
"s_branch .Lk1_pc67a\n"
".Lk1_pc67b:\n"
"s_mov_b32 s65, s72\n"
"s_add_u32 s53, s33, 0x1e000\n"
"s_and_b32 s53, s53, 0x3ffff\n"
"s_add_u32 s53, s53, 0x400000\n"
"s_mov_b32 m0, 0xc000\n"
"s_nop 0\n"
"buffer_load_dwordx4 v208, s[24:27], s53 offen nt lds\n"
"buffer_load_dwordx4 v208, s[24:27], s53 offen offset:2048 nt lds\n"
"s_mov_b32 m0, 0xc380\n"
"s_nop 0\n"
"buffer_load_dwordx4 v208, s[24:27], s53 offen offset:128 nt lds\n"
"buffer_load_dwordx4 v208, s[24:27], s53 offen offset:2176 nt lds\n"
"s_add_u32 s53, s33, 0x20000\n"
"s_and_b32 s53, s53, 0x3ffff\n"
"s_add_u32 s53, s53, 0x400000\n"
"s_mov_b32 m0, 0xe000\n"
"s_nop 0\n"
"buffer_load_dwordx4 v208, s[24:27], s53 offen nt lds\n"
"buffer_load_dwordx4 v208, s[24:27], s53 offen offset:2048 nt lds\n"
"s_mov_b32 m0, 0xe380\n"
"s_nop 0\n"
"buffer_load_dwordx4 v208, s[24:27], s53 offen offset:128 nt lds\n"
"buffer_load_dwordx4 v208, s[24:27], s53 offen offset:2176 nt lds\n"
"s_add_u32 s53, s33, 0x21000\n"
"s_and_b32 s53, s53, 0x3ffff\n"
"s_add_u32 s53, s53, 0x400000\n"
"s_mov_b32 m0, 0x1000\n"
"s_nop 0\n"
"buffer_load_dwordx4 v206, s[20:23], s53 offen nt lds\n"
"buffer_load_dwordx4 v207, s[20:23], s53 offen offset:2048 nt lds\n"
"s_mov_b32 m0, 0x1380\n"
"s_nop 0\n"
"buffer_load_dwordx4 v206, s[20:23], s53 offen offset:128 nt lds\n"
"buffer_load_dwordx4 v207, s[20:23], s53 offen offset:2176 nt lds\n"
"s_add_u32 s53, s33, 0x23000\n"
"s_and_b32 s53, s53, 0x3ffff\n"
"s_add_u32 s53, s53, 0x400000\n"
"s_mov_b32 m0, 0x3000\n"
"s_nop 0\n"
"buffer_load_dwordx4 v206, s[20:23], s53 offen nt lds\n"
"buffer_load_dwordx4 v207, s[20:23], s53 offen offset:2048 nt lds\n"
"s_mov_b32 m0, 0x3380\n"
"s_nop 0\n"
"buffer_load_dwordx4 v206, s[20:23], s53 offen offset:128 nt lds\n"
"buffer_load_dwordx4 v207, s[20:23], s53 offen offset:2176 nt lds\n"
"s_waitcnt vmcnt(16)\n"
"s_barrier\n"
"s_waitcnt lgkmcnt(0)\n"
".Lk1_pc68a:\n"
"s_memrealtime s[72:73]\n"
"s_waitcnt lgkmcnt(0)\n"
"s_sub_u32 s73, s72, s65\n"
"s_cmp_lt_u32 s73, 126\n"
"s_cbranch_scc0 .Lk1_pc68b\n"
"s_sleep 1\n"
"s_branch .Lk1_pc68a\n"
".Lk1_pc68b:\n"
"s_mov_b32 s65, s72\n"
"s_add_u32 s53, s33, 0x22000\n"
"s_and_b32 s53, s53, 0x3ffff\n"
"s_add_u32 s53, s53, 0x400000\n"
"s_mov_b32 m0, 0x10000\n"
"s_nop 0\n"
"buffer_load_dwordx4 v208, s[24:27], s53 offen nt lds\n"
"buffer_load_dwordx4 v208, s[24:27], s53 offen offset:2048 nt lds\n"
"s_mov_b32 m0, 0x10380\n"
"s_nop 0\n"
"buffer_load_dwordx4 v208, s[24:27], s53 offen offset:128 nt lds\n"
"buffer_load_dwordx4 v208, s[24:27], s53 offen offset:2176 nt lds\n"
"s_add_u32 s53, s33, 0x24000\n"
"s_and_b32 s53, s53, 0x3ffff\n"
"s_add_u32 s53, s53, 0x400000\n"
"s_mov_b32 m0, 0x12000\n"
"s_nop 0\n"
"buffer_load_dwordx4 v208, s[24:27], s53 offen nt lds\n"
"buffer_load_dwordx4 v208, s[24:27], s53 offen offset:2048 nt lds\n"
"s_mov_b32 m0, 0x12380\n"
"s_nop 0\n"
"buffer_load_dwordx4 v208, s[24:27], s53 offen offset:128 nt lds\n"
"buffer_load_dwordx4 v208, s[24:27], s53 offen offset:2176 nt lds\n"
"s_add_u32 s53, s33, 0x25000\n"
"s_and_b32 s53, s53, 0x3ffff\n"
"s_add_u32 s53, s53, 0x400000\n"
"s_mov_b32 m0, 0x5000\n"
"s_nop 0\n"
"buffer_load_dwordx4 v206, s[20:23], s53 offen nt lds\n"
"buffer_load_dwordx4 v207, s[20:23], s53 offen offset:2048 nt lds\n"
"s_mov_b32 m0, 0x5380\n"
"s_nop 0\n"
"buffer_load_dwordx4 v206, s[20:23], s53 offen offset:128 nt lds\n"
"buffer_load_dwordx4 v207, s[20:23], s53 offen offset:2176 nt lds\n"
"s_add_u32 s53, s33, 0x27000\n"
"s_and_b32 s53, s53, 0x3ffff\n"
"s_add_u32 s53, s53, 0x400000\n"
"s_mov_b32 m0, 0x7000\n"
"s_nop 0\n"
"buffer_load_dwordx4 v206, s[20:23], s53 offen nt lds\n"
"buffer_load_dwordx4 v207, s[20:23], s53 offen offset:2048 nt lds\n"
"s_mov_b32 m0, 0x7380\n"
"s_nop 0\n"
"buffer_load_dwordx4 v206, s[20:23], s53 offen offset:128 nt lds\n"
"buffer_load_dwordx4 v207, s[20:23], s53 offen offset:2176 nt lds\n"
"s_waitcnt vmcnt(16)\n"
"s_barrier\n"
"s_waitcnt lgkmcnt(0)\n"
".Lk1_pc69a:\n"
"s_memrealtime s[72:73]\n"
"s_waitcnt lgkmcnt(0)\n"
"s_sub_u32 s73, s72, s65\n"
"s_cmp_lt_u32 s73, 126\n"
"s_cbranch_scc0 .Lk1_pc69b\n"
"s_sleep 1\n"
"s_branch .Lk1_pc69a\n"
".Lk1_pc69b:\n"
"s_mov_b32 s65, s72\n"
"s_add_u32 s53, s33, 0x26000\n"
"s_and_b32 s53, s53, 0x3ffff\n"
"s_add_u32 s53, s53, 0x400000\n"
"s_mov_b32 m0, 0x14000\n"
"s_nop 0\n"
"buffer_load_dwordx4 v208, s[24:27], s53 offen nt lds\n"
"buffer_load_dwordx4 v208, s[24:27], s53 offen offset:2048 nt lds\n"
"s_mov_b32 m0, 0x14380\n"
"s_nop 0\n"
"buffer_load_dwordx4 v208, s[24:27], s53 offen offset:128 nt lds\n"
"buffer_load_dwordx4 v208, s[24:27], s53 offen offset:2176 nt lds\n"
"s_add_u32 s53, s33, 0x28000\n"
"s_and_b32 s53, s53, 0x3ffff\n"
"s_add_u32 s53, s53, 0x400000\n"
"s_mov_b32 m0, 0x16000\n"
"s_nop 0\n"
"buffer_load_dwordx4 v208, s[24:27], s53 offen nt lds\n"
"buffer_load_dwordx4 v208, s[24:27], s53 offen offset:2048 nt lds\n"
"s_mov_b32 m0, 0x16380\n"
"s_nop 0\n"
"buffer_load_dwordx4 v208, s[24:27], s53 offen offset:128 nt lds\n"
"buffer_load_dwordx4 v208, s[24:27], s53 offen offset:2176 nt lds\n"
"s_add_u32 s53, s33, 0x29000\n"
"s_and_b32 s53, s53, 0x3ffff\n"
"s_add_u32 s53, s53, 0x400000\n"
"s_mov_b32 m0, 0x9000\n"
"s_nop 0\n"
"buffer_load_dwordx4 v206, s[20:23], s53 offen nt lds\n"
"buffer_load_dwordx4 v207, s[20:23], s53 offen offset:2048 nt lds\n"
"s_mov_b32 m0, 0x9380\n"
"s_nop 0\n"
"buffer_load_dwordx4 v206, s[20:23], s53 offen offset:128 nt lds\n"
"buffer_load_dwordx4 v207, s[20:23], s53 offen offset:2176 nt lds\n"
"s_add_u32 s53, s33, 0x2b000\n"
"s_and_b32 s53, s53, 0x3ffff\n"
"s_add_u32 s53, s53, 0x400000\n"
"s_mov_b32 m0, 0xb000\n"
"s_nop 0\n"
"buffer_load_dwordx4 v206, s[20:23], s53 offen nt lds\n"
"buffer_load_dwordx4 v207, s[20:23], s53 offen offset:2048 nt lds\n"
"s_mov_b32 m0, 0xb380\n"
"s_nop 0\n"
"buffer_load_dwordx4 v206, s[20:23], s53 offen offset:128 nt lds\n"
"buffer_load_dwordx4 v207, s[20:23], s53 offen offset:2176 nt lds\n"
"s_waitcnt vmcnt(16)\n"
"s_barrier\n"
"s_waitcnt lgkmcnt(0)\n"
".Lk1_pc70a:\n"
"s_memrealtime s[72:73]\n"
"s_waitcnt lgkmcnt(0)\n"
"s_sub_u32 s73, s72, s65\n"
"s_cmp_lt_u32 s73, 126\n"
"s_cbranch_scc0 .Lk1_pc70b\n"
"s_sleep 1\n"
"s_branch .Lk1_pc70a\n"
".Lk1_pc70b:\n"
"s_mov_b32 s65, s72\n"
"s_add_u32 s53, s33, 0x2a000\n"
"s_and_b32 s53, s53, 0x3ffff\n"
"s_add_u32 s53, s53, 0x400000\n"
"s_mov_b32 m0, 0xc000\n"
"s_nop 0\n"
"buffer_load_dwordx4 v208, s[24:27], s53 offen nt lds\n"
"buffer_load_dwordx4 v208, s[24:27], s53 offen offset:2048 nt lds\n"
"s_mov_b32 m0, 0xc380\n"
"s_nop 0\n"
"buffer_load_dwordx4 v208, s[24:27], s53 offen offset:128 nt lds\n"
"buffer_load_dwordx4 v208, s[24:27], s53 offen offset:2176 nt lds\n"
"s_add_u32 s53, s33, 0x2c000\n"
"s_and_b32 s53, s53, 0x3ffff\n"
"s_add_u32 s53, s53, 0x400000\n"
"s_mov_b32 m0, 0xe000\n"
"s_nop 0\n"
"buffer_load_dwordx4 v208, s[24:27], s53 offen nt lds\n"
"buffer_load_dwordx4 v208, s[24:27], s53 offen offset:2048 nt lds\n"
"s_mov_b32 m0, 0xe380\n"
"s_nop 0\n"
"buffer_load_dwordx4 v208, s[24:27], s53 offen offset:128 nt lds\n"
"buffer_load_dwordx4 v208, s[24:27], s53 offen offset:2176 nt lds\n"
"s_add_u32 s53, s33, 0x2d000\n"
"s_and_b32 s53, s53, 0x3ffff\n"
"s_add_u32 s53, s53, 0x400000\n"
"s_mov_b32 m0, 0x1000\n"
"s_nop 0\n"
"buffer_load_dwordx4 v206, s[20:23], s53 offen nt lds\n"
"buffer_load_dwordx4 v207, s[20:23], s53 offen offset:2048 nt lds\n"
"s_mov_b32 m0, 0x1380\n"
"s_nop 0\n"
"buffer_load_dwordx4 v206, s[20:23], s53 offen offset:128 nt lds\n"
"buffer_load_dwordx4 v207, s[20:23], s53 offen offset:2176 nt lds\n"
"s_add_u32 s53, s33, 0x2f000\n"
"s_and_b32 s53, s53, 0x3ffff\n"
"s_add_u32 s53, s53, 0x400000\n"
"s_mov_b32 m0, 0x3000\n"
"s_nop 0\n"
"buffer_load_dwordx4 v206, s[20:23], s53 offen nt lds\n"
"buffer_load_dwordx4 v207, s[20:23], s53 offen offset:2048 nt lds\n"
"s_mov_b32 m0, 0x3380\n"
"s_nop 0\n"
"buffer_load_dwordx4 v206, s[20:23], s53 offen offset:128 nt lds\n"
"buffer_load_dwordx4 v207, s[20:23], s53 offen offset:2176 nt lds\n"
"s_waitcnt vmcnt(16)\n"
"s_barrier\n"
"s_waitcnt lgkmcnt(0)\n"
".Lk1_pc71a:\n"
"s_memrealtime s[72:73]\n"
"s_waitcnt lgkmcnt(0)\n"
"s_sub_u32 s73, s72, s65\n"
"s_cmp_lt_u32 s73, 126\n"
"s_cbranch_scc0 .Lk1_pc71b\n"
"s_sleep 1\n"
"s_branch .Lk1_pc71a\n"
".Lk1_pc71b:\n"
"s_mov_b32 s65, s72\n"
"s_add_u32 s53, s33, 0x2e000\n"
"s_and_b32 s53, s53, 0x3ffff\n"
"s_add_u32 s53, s53, 0x400000\n"
"s_mov_b32 m0, 0x10000\n"
"s_nop 0\n"
"buffer_load_dwordx4 v208, s[24:27], s53 offen nt lds\n"
"buffer_load_dwordx4 v208, s[24:27], s53 offen offset:2048 nt lds\n"
"s_mov_b32 m0, 0x10380\n"
"s_nop 0\n"
"buffer_load_dwordx4 v208, s[24:27], s53 offen offset:128 nt lds\n"
"buffer_load_dwordx4 v208, s[24:27], s53 offen offset:2176 nt lds\n"
"s_add_u32 s53, s33, 0x30000\n"
"s_and_b32 s53, s53, 0x3ffff\n"
"s_add_u32 s53, s53, 0x400000\n"
"s_mov_b32 m0, 0x12000\n"
"s_nop 0\n"
"buffer_load_dwordx4 v208, s[24:27], s53 offen nt lds\n"
"buffer_load_dwordx4 v208, s[24:27], s53 offen offset:2048 nt lds\n"
"s_mov_b32 m0, 0x12380\n"
"s_nop 0\n"
"buffer_load_dwordx4 v208, s[24:27], s53 offen offset:128 nt lds\n"
"buffer_load_dwordx4 v208, s[24:27], s53 offen offset:2176 nt lds\n"
"s_add_u32 s53, s33, 0x31000\n"
"s_and_b32 s53, s53, 0x3ffff\n"
"s_add_u32 s53, s53, 0x400000\n"
"s_mov_b32 m0, 0x5000\n"
"s_nop 0\n"
"buffer_load_dwordx4 v206, s[20:23], s53 offen nt lds\n"
"buffer_load_dwordx4 v207, s[20:23], s53 offen offset:2048 nt lds\n"
"s_mov_b32 m0, 0x5380\n"
"s_nop 0\n"
"buffer_load_dwordx4 v206, s[20:23], s53 offen offset:128 nt lds\n"
"buffer_load_dwordx4 v207, s[20:23], s53 offen offset:2176 nt lds\n"
"s_add_u32 s53, s33, 0x33000\n"
"s_and_b32 s53, s53, 0x3ffff\n"
"s_add_u32 s53, s53, 0x400000\n"
"s_mov_b32 m0, 0x7000\n"
"s_nop 0\n"
"buffer_load_dwordx4 v206, s[20:23], s53 offen nt lds\n"
"buffer_load_dwordx4 v207, s[20:23], s53 offen offset:2048 nt lds\n"
"s_mov_b32 m0, 0x7380\n"
"s_nop 0\n"
"buffer_load_dwordx4 v206, s[20:23], s53 offen offset:128 nt lds\n"
"buffer_load_dwordx4 v207, s[20:23], s53 offen offset:2176 nt lds\n"
"s_waitcnt vmcnt(16)\n"
"s_barrier\n"
"s_waitcnt lgkmcnt(0)\n"
".Lk1_pc72a:\n"
"s_memrealtime s[72:73]\n"
"s_waitcnt lgkmcnt(0)\n"
"s_sub_u32 s73, s72, s65\n"
"s_cmp_lt_u32 s73, 126\n"
"s_cbranch_scc0 .Lk1_pc72b\n"
"s_sleep 1\n"
"s_branch .Lk1_pc72a\n"
".Lk1_pc72b:\n"
"s_mov_b32 s65, s72\n"
"s_add_u32 s53, s33, 0x32000\n"
"s_and_b32 s53, s53, 0x3ffff\n"
"s_add_u32 s53, s53, 0x400000\n"
"s_mov_b32 m0, 0x14000\n"
"s_nop 0\n"
"buffer_load_dwordx4 v208, s[24:27], s53 offen nt lds\n"
"buffer_load_dwordx4 v208, s[24:27], s53 offen offset:2048 nt lds\n"
"s_mov_b32 m0, 0x14380\n"
"s_nop 0\n"
"buffer_load_dwordx4 v208, s[24:27], s53 offen offset:128 nt lds\n"
"buffer_load_dwordx4 v208, s[24:27], s53 offen offset:2176 nt lds\n"
"s_add_u32 s53, s33, 0x34000\n"
"s_and_b32 s53, s53, 0x3ffff\n"
"s_add_u32 s53, s53, 0x400000\n"
"s_mov_b32 m0, 0x16000\n"
"s_nop 0\n"
"buffer_load_dwordx4 v208, s[24:27], s53 offen nt lds\n"
"buffer_load_dwordx4 v208, s[24:27], s53 offen offset:2048 nt lds\n"
"s_mov_b32 m0, 0x16380\n"
"s_nop 0\n"
"buffer_load_dwordx4 v208, s[24:27], s53 offen offset:128 nt lds\n"
"buffer_load_dwordx4 v208, s[24:27], s53 offen offset:2176 nt lds\n"
"s_add_u32 s53, s33, 0x35000\n"
"s_and_b32 s53, s53, 0x3ffff\n"
"s_add_u32 s53, s53, 0x400000\n"
"s_mov_b32 m0, 0x9000\n"
"s_nop 0\n"
"buffer_load_dwordx4 v206, s[20:23], s53 offen nt lds\n"
"buffer_load_dwordx4 v207, s[20:23], s53 offen offset:2048 nt lds\n"
"s_mov_b32 m0, 0x9380\n"
"s_nop 0\n"
"buffer_load_dwordx4 v206, s[20:23], s53 offen offset:128 nt lds\n"
"buffer_load_dwordx4 v207, s[20:23], s53 offen offset:2176 nt lds\n"
"s_add_u32 s53, s33, 0x37000\n"
"s_and_b32 s53, s53, 0x3ffff\n"
"s_add_u32 s53, s53, 0x400000\n"
"s_mov_b32 m0, 0xb000\n"
"s_nop 0\n"
"buffer_load_dwordx4 v206, s[20:23], s53 offen nt lds\n"
"buffer_load_dwordx4 v207, s[20:23], s53 offen offset:2048 nt lds\n"
"s_mov_b32 m0, 0xb380\n"
"s_nop 0\n"
"buffer_load_dwordx4 v206, s[20:23], s53 offen offset:128 nt lds\n"
"buffer_load_dwordx4 v207, s[20:23], s53 offen offset:2176 nt lds\n"
"s_waitcnt vmcnt(16)\n"
"s_barrier\n"
"s_waitcnt lgkmcnt(0)\n"
".Lk1_pc73a:\n"
"s_memrealtime s[72:73]\n"
"s_waitcnt lgkmcnt(0)\n"
"s_sub_u32 s73, s72, s65\n"
"s_cmp_lt_u32 s73, 126\n"
"s_cbranch_scc0 .Lk1_pc73b\n"
"s_sleep 1\n"
"s_branch .Lk1_pc73a\n"
".Lk1_pc73b:\n"
"s_mov_b32 s65, s72\n"
"s_add_u32 s53, s33, 0x36000\n"
"s_and_b32 s53, s53, 0x3ffff\n"
"s_add_u32 s53, s53, 0x400000\n"
"s_mov_b32 m0, 0xc000\n"
"s_nop 0\n"
"buffer_load_dwordx4 v208, s[24:27], s53 offen nt lds\n"
"buffer_load_dwordx4 v208, s[24:27], s53 offen offset:2048 nt lds\n"
"s_mov_b32 m0, 0xc380\n"
"s_nop 0\n"
"buffer_load_dwordx4 v208, s[24:27], s53 offen offset:128 nt lds\n"
"buffer_load_dwordx4 v208, s[24:27], s53 offen offset:2176 nt lds\n"
"s_add_u32 s53, s33, 0x38000\n"
"s_and_b32 s53, s53, 0x3ffff\n"
"s_add_u32 s53, s53, 0x400000\n"
"s_mov_b32 m0, 0xe000\n"
"s_nop 0\n"
"buffer_load_dwordx4 v208, s[24:27], s53 offen nt lds\n"
"buffer_load_dwordx4 v208, s[24:27], s53 offen offset:2048 nt lds\n"
"s_mov_b32 m0, 0xe380\n"
"s_nop 0\n"
"buffer_load_dwordx4 v208, s[24:27], s53 offen offset:128 nt lds\n"
"buffer_load_dwordx4 v208, s[24:27], s53 offen offset:2176 nt lds\n"
"s_add_u32 s53, s33, 0x39000\n"
"s_and_b32 s53, s53, 0x3ffff\n"
"s_add_u32 s53, s53, 0x400000\n"
"s_mov_b32 m0, 0x1000\n"
"s_nop 0\n"
"buffer_load_dwordx4 v206, s[20:23], s53 offen nt lds\n"
"buffer_load_dwordx4 v207, s[20:23], s53 offen offset:2048 nt lds\n"
"s_mov_b32 m0, 0x1380\n"
"s_nop 0\n"
"buffer_load_dwordx4 v206, s[20:23], s53 offen offset:128 nt lds\n"
"buffer_load_dwordx4 v207, s[20:23], s53 offen offset:2176 nt lds\n"
"s_add_u32 s53, s33, 0x3b000\n"
"s_and_b32 s53, s53, 0x3ffff\n"
"s_add_u32 s53, s53, 0x400000\n"
"s_mov_b32 m0, 0x3000\n"
"s_nop 0\n"
"buffer_load_dwordx4 v206, s[20:23], s53 offen nt lds\n"
"buffer_load_dwordx4 v207, s[20:23], s53 offen offset:2048 nt lds\n"
"s_mov_b32 m0, 0x3380\n"
"s_nop 0\n"
"buffer_load_dwordx4 v206, s[20:23], s53 offen offset:128 nt lds\n"
"buffer_load_dwordx4 v207, s[20:23], s53 offen offset:2176 nt lds\n"
"s_waitcnt vmcnt(16)\n"
"s_barrier\n"
"s_waitcnt lgkmcnt(0)\n"
".Lk1_pc74a:\n"
"s_memrealtime s[72:73]\n"
"s_waitcnt lgkmcnt(0)\n"
"s_sub_u32 s73, s72, s65\n"
"s_cmp_lt_u32 s73, 126\n"
"s_cbranch_scc0 .Lk1_pc74b\n"
"s_sleep 1\n"
"s_branch .Lk1_pc74a\n"
".Lk1_pc74b:\n"
"s_mov_b32 s65, s72\n"
"s_add_u32 s53, s33, 0x3a000\n"
"s_and_b32 s53, s53, 0x3ffff\n"
"s_add_u32 s53, s53, 0x400000\n"
"s_mov_b32 m0, 0x10000\n"
"s_nop 0\n"
"buffer_load_dwordx4 v208, s[24:27], s53 offen nt lds\n"
"buffer_load_dwordx4 v208, s[24:27], s53 offen offset:2048 nt lds\n"
"s_mov_b32 m0, 0x10380\n"
"s_nop 0\n"
"buffer_load_dwordx4 v208, s[24:27], s53 offen offset:128 nt lds\n"
"buffer_load_dwordx4 v208, s[24:27], s53 offen offset:2176 nt lds\n"
"s_add_u32 s53, s33, 0x3c000\n"
"s_and_b32 s53, s53, 0x3ffff\n"
"s_add_u32 s53, s53, 0x400000\n"
"s_mov_b32 m0, 0x12000\n"
"s_nop 0\n"
"buffer_load_dwordx4 v208, s[24:27], s53 offen nt lds\n"
"buffer_load_dwordx4 v208, s[24:27], s53 offen offset:2048 nt lds\n"
"s_mov_b32 m0, 0x12380\n"
"s_nop 0\n"
"buffer_load_dwordx4 v208, s[24:27], s53 offen offset:128 nt lds\n"
"buffer_load_dwordx4 v208, s[24:27], s53 offen offset:2176 nt lds\n"
"s_add_u32 s53, s33, 0x3d000\n"
"s_and_b32 s53, s53, 0x3ffff\n"
"s_add_u32 s53, s53, 0x400000\n"
"s_mov_b32 m0, 0x5000\n"
"s_nop 0\n"
"buffer_load_dwordx4 v206, s[20:23], s53 offen nt lds\n"
"buffer_load_dwordx4 v207, s[20:23], s53 offen offset:2048 nt lds\n"
"s_mov_b32 m0, 0x5380\n"
"s_nop 0\n"
"buffer_load_dwordx4 v206, s[20:23], s53 offen offset:128 nt lds\n"
"buffer_load_dwordx4 v207, s[20:23], s53 offen offset:2176 nt lds\n"
"s_add_u32 s53, s33, 0x3f000\n"
"s_and_b32 s53, s53, 0x3ffff\n"
"s_add_u32 s53, s53, 0x400000\n"
"s_mov_b32 m0, 0x7000\n"
"s_nop 0\n"
"buffer_load_dwordx4 v206, s[20:23], s53 offen nt lds\n"
"buffer_load_dwordx4 v207, s[20:23], s53 offen offset:2048 nt lds\n"
"s_mov_b32 m0, 0x7380\n"
"s_nop 0\n"
"buffer_load_dwordx4 v206, s[20:23], s53 offen offset:128 nt lds\n"
"buffer_load_dwordx4 v207, s[20:23], s53 offen offset:2176 nt lds\n"
"s_waitcnt vmcnt(16)\n"
"s_barrier\n"
"s_waitcnt lgkmcnt(0)\n"
".Lk1_pc75a:\n"
"s_memrealtime s[72:73]\n"
"s_waitcnt lgkmcnt(0)\n"
"s_sub_u32 s73, s72, s65\n"
"s_cmp_lt_u32 s73, 126\n"
"s_cbranch_scc0 .Lk1_pc75b\n"
"s_sleep 1\n"
"s_branch .Lk1_pc75a\n"
".Lk1_pc75b:\n"
"s_mov_b32 s65, s72\n"
"s_add_u32 s53, s33, 0x3e000\n"
"s_and_b32 s53, s53, 0x3ffff\n"
"s_add_u32 s53, s53, 0x400000\n"
"s_mov_b32 m0, 0x14000\n"
"s_nop 0\n"
"buffer_load_dwordx4 v208, s[24:27], s53 offen nt lds\n"
"buffer_load_dwordx4 v208, s[24:27], s53 offen offset:2048 nt lds\n"
"s_mov_b32 m0, 0x14380\n"
"s_nop 0\n"
"buffer_load_dwordx4 v208, s[24:27], s53 offen offset:128 nt lds\n"
"buffer_load_dwordx4 v208, s[24:27], s53 offen offset:2176 nt lds\n"
"s_waitcnt vmcnt(4)\n"
"s_barrier\n"
"s_waitcnt vmcnt(0)\n"
"s_barrier\n"
"s_branch .Lk1_end\n"
".Lk1_r2_l10:\n"
"s_waitcnt lgkmcnt(0)\n"
".Lk1_pc76a:\n"
"s_memrealtime s[72:73]\n"
"s_waitcnt lgkmcnt(0)\n"
"s_sub_u32 s73, s72, s65\n"
"s_cmp_lt_u32 s73, 126\n"
"s_cbranch_scc0 .Lk1_pc76b\n"
"s_sleep 1\n"
"s_branch .Lk1_pc76a\n"
".Lk1_pc76b:\n"
"s_mov_b32 s65, s72\n"
"s_add_u32 s53, s33, 0x7000\n"
"s_and_b32 s53, s53, 0x3ffff\n"
"s_add_u32 s53, s53, 0x400000\n"
"s_mov_b32 m0, 0xd000\n"
"s_nop 0\n"
"buffer_load_dwordx4 v208, s[24:27], s53 offen nt lds\n"
"buffer_load_dwordx4 v208, s[24:27], s53 offen offset:2048 nt lds\n"
"s_mov_b32 m0, 0xd380\n"
"s_nop 0\n"
"buffer_load_dwordx4 v208, s[24:27], s53 offen offset:128 nt lds\n"
"buffer_load_dwordx4 v208, s[24:27], s53 offen offset:2176 nt lds\n"
"s_add_u32 s53, s33, 0x9000\n"
"s_and_b32 s53, s53, 0x3ffff\n"
"s_add_u32 s53, s53, 0x400000\n"
"s_mov_b32 m0, 0xf000\n"
"s_nop 0\n"
"buffer_load_dwordx4 v208, s[24:27], s53 offen nt lds\n"
"buffer_load_dwordx4 v208, s[24:27], s53 offen offset:2048 nt lds\n"
"s_mov_b32 m0, 0xf380\n"
"s_nop 0\n"
"buffer_load_dwordx4 v208, s[24:27], s53 offen offset:128 nt lds\n"
"buffer_load_dwordx4 v208, s[24:27], s53 offen offset:2176 nt lds\n"
"s_add_u32 s53, s33, 0x8000\n"
"s_and_b32 s53, s53, 0x3ffff\n"
"s_add_u32 s53, s53, 0x400000\n"
"s_mov_b32 m0, 0\n"
"s_nop 0\n"
"buffer_load_dwordx4 v206, s[20:23], s53 offen nt lds\n"
"buffer_load_dwordx4 v207, s[20:23], s53 offen offset:2048 nt lds\n"
"s_mov_b32 m0, 0x380\n"
"s_nop 0\n"
"buffer_load_dwordx4 v206, s[20:23], s53 offen offset:128 nt lds\n"
"buffer_load_dwordx4 v207, s[20:23], s53 offen offset:2176 nt lds\n"
"s_add_u32 s53, s33, 0xa000\n"
"s_and_b32 s53, s53, 0x3ffff\n"
"s_add_u32 s53, s53, 0x400000\n"
"s_mov_b32 m0, 0x2000\n"
"s_nop 0\n"
"buffer_load_dwordx4 v206, s[20:23], s53 offen nt lds\n"
"buffer_load_dwordx4 v207, s[20:23], s53 offen offset:2048 nt lds\n"
"s_mov_b32 m0, 0x2380\n"
"s_nop 0\n"
"buffer_load_dwordx4 v206, s[20:23], s53 offen offset:128 nt lds\n"
"buffer_load_dwordx4 v207, s[20:23], s53 offen offset:2176 nt lds\n"
"s_barrier\n"
"s_waitcnt lgkmcnt(0)\n"
".Lk1_pc77a:\n"
"s_memrealtime s[72:73]\n"
"s_waitcnt lgkmcnt(0)\n"
"s_sub_u32 s73, s72, s65\n"
"s_cmp_lt_u32 s73, 126\n"
"s_cbranch_scc0 .Lk1_pc77b\n"
"s_sleep 1\n"
"s_branch .Lk1_pc77a\n"
".Lk1_pc77b:\n"
"s_mov_b32 s65, s72\n"
"s_add_u32 s53, s33, 0xb000\n"
"s_and_b32 s53, s53, 0x3ffff\n"
"s_add_u32 s53, s53, 0x400000\n"
"s_mov_b32 m0, 0x11000\n"
"s_nop 0\n"
"buffer_load_dwordx4 v208, s[24:27], s53 offen nt lds\n"
"buffer_load_dwordx4 v208, s[24:27], s53 offen offset:2048 nt lds\n"
"s_mov_b32 m0, 0x11380\n"
"s_nop 0\n"
"buffer_load_dwordx4 v208, s[24:27], s53 offen offset:128 nt lds\n"
"buffer_load_dwordx4 v208, s[24:27], s53 offen offset:2176 nt lds\n"
"s_add_u32 s53, s33, 0xd000\n"
"s_and_b32 s53, s53, 0x3ffff\n"
"s_add_u32 s53, s53, 0x400000\n"
"s_mov_b32 m0, 0x13000\n"
"s_nop 0\n"
"buffer_load_dwordx4 v208, s[24:27], s53 offen nt lds\n"
"buffer_load_dwordx4 v208, s[24:27], s53 offen offset:2048 nt lds\n"
"s_mov_b32 m0, 0x13380\n"
"s_nop 0\n"
"buffer_load_dwordx4 v208, s[24:27], s53 offen offset:128 nt lds\n"
"buffer_load_dwordx4 v208, s[24:27], s53 offen offset:2176 nt lds\n"
"s_add_u32 s53, s33, 0xc000\n"
"s_and_b32 s53, s53, 0x3ffff\n"
"s_add_u32 s53, s53, 0x400000\n"
"s_mov_b32 m0, 0x4000\n"
"s_nop 0\n"
"buffer_load_dwordx4 v206, s[20:23], s53 offen nt lds\n"
"buffer_load_dwordx4 v207, s[20:23], s53 offen offset:2048 nt lds\n"
"s_mov_b32 m0, 0x4380\n"
"s_nop 0\n"
"buffer_load_dwordx4 v206, s[20:23], s53 offen offset:128 nt lds\n"
"buffer_load_dwordx4 v207, s[20:23], s53 offen offset:2176 nt lds\n"
"s_add_u32 s53, s33, 0xe000\n"
"s_and_b32 s53, s53, 0x3ffff\n"
"s_add_u32 s53, s53, 0x400000\n"
"s_mov_b32 m0, 0x6000\n"
"s_nop 0\n"
"buffer_load_dwordx4 v206, s[20:23], s53 offen nt lds\n"
"buffer_load_dwordx4 v207, s[20:23], s53 offen offset:2048 nt lds\n"
"s_mov_b32 m0, 0x6380\n"
"s_nop 0\n"
"buffer_load_dwordx4 v206, s[20:23], s53 offen offset:128 nt lds\n"
"buffer_load_dwordx4 v207, s[20:23], s53 offen offset:2176 nt lds\n"
"s_waitcnt vmcnt(16)\n"
"s_barrier\n"
"s_waitcnt lgkmcnt(0)\n"
".Lk1_pc78a:\n"
"s_memrealtime s[72:73]\n"
"s_waitcnt lgkmcnt(0)\n"
"s_sub_u32 s73, s72, s65\n"
"s_cmp_lt_u32 s73, 126\n"
"s_cbranch_scc0 .Lk1_pc78b\n"
"s_sleep 1\n"
"s_branch .Lk1_pc78a\n"
".Lk1_pc78b:\n"
"s_mov_b32 s65, s72\n"
"s_add_u32 s53, s33, 0xf000\n"
"s_and_b32 s53, s53, 0x3ffff\n"
"s_add_u32 s53, s53, 0x400000\n"
"s_mov_b32 m0, 0x15000\n"
"s_nop 0\n"
"buffer_load_dwordx4 v208, s[24:27], s53 offen nt lds\n"
"buffer_load_dwordx4 v208, s[24:27], s53 offen offset:2048 nt lds\n"
"s_mov_b32 m0, 0x15380\n"
"s_nop 0\n"
"buffer_load_dwordx4 v208, s[24:27], s53 offen offset:128 nt lds\n"
"buffer_load_dwordx4 v208, s[24:27], s53 offen offset:2176 nt lds\n"
"s_add_u32 s53, s33, 0x11000\n"
"s_and_b32 s53, s53, 0x3ffff\n"
"s_add_u32 s53, s53, 0x400000\n"
"s_mov_b32 m0, 0x17000\n"
"s_nop 0\n"
"buffer_load_dwordx4 v208, s[24:27], s53 offen nt lds\n"
"buffer_load_dwordx4 v208, s[24:27], s53 offen offset:2048 nt lds\n"
"s_mov_b32 m0, 0x17380\n"
"s_nop 0\n"
"buffer_load_dwordx4 v208, s[24:27], s53 offen offset:128 nt lds\n"
"buffer_load_dwordx4 v208, s[24:27], s53 offen offset:2176 nt lds\n"
"s_add_u32 s53, s33, 0x10000\n"
"s_and_b32 s53, s53, 0x3ffff\n"
"s_add_u32 s53, s53, 0x400000\n"
"s_mov_b32 m0, 0x8000\n"
"s_nop 0\n"
"buffer_load_dwordx4 v206, s[20:23], s53 offen nt lds\n"
"buffer_load_dwordx4 v207, s[20:23], s53 offen offset:2048 nt lds\n"
"s_mov_b32 m0, 0x8380\n"
"s_nop 0\n"
"buffer_load_dwordx4 v206, s[20:23], s53 offen offset:128 nt lds\n"
"buffer_load_dwordx4 v207, s[20:23], s53 offen offset:2176 nt lds\n"
"s_add_u32 s53, s33, 0x12000\n"
"s_and_b32 s53, s53, 0x3ffff\n"
"s_add_u32 s53, s53, 0x400000\n"
"s_mov_b32 m0, 0xa000\n"
"s_nop 0\n"
"buffer_load_dwordx4 v206, s[20:23], s53 offen nt lds\n"
"buffer_load_dwordx4 v207, s[20:23], s53 offen offset:2048 nt lds\n"
"s_mov_b32 m0, 0xa380\n"
"s_nop 0\n"
"buffer_load_dwordx4 v206, s[20:23], s53 offen offset:128 nt lds\n"
"buffer_load_dwordx4 v207, s[20:23], s53 offen offset:2176 nt lds\n"
"s_waitcnt vmcnt(16)\n"
"s_barrier\n"
"s_waitcnt lgkmcnt(0)\n"
".Lk1_pc79a:\n"
"s_memrealtime s[72:73]\n"
"s_waitcnt lgkmcnt(0)\n"
"s_sub_u32 s73, s72, s65\n"
"s_cmp_lt_u32 s73, 126\n"
"s_cbranch_scc0 .Lk1_pc79b\n"
"s_sleep 1\n"
"s_branch .Lk1_pc79a\n"
".Lk1_pc79b:\n"
"s_mov_b32 s65, s72\n"
"s_add_u32 s53, s33, 0x13000\n"
"s_and_b32 s53, s53, 0x3ffff\n"
"s_add_u32 s53, s53, 0x400000\n"
"s_mov_b32 m0, 0xd000\n"
"s_nop 0\n"
"buffer_load_dwordx4 v208, s[24:27], s53 offen nt lds\n"
"buffer_load_dwordx4 v208, s[24:27], s53 offen offset:2048 nt lds\n"
"s_mov_b32 m0, 0xd380\n"
"s_nop 0\n"
"buffer_load_dwordx4 v208, s[24:27], s53 offen offset:128 nt lds\n"
"buffer_load_dwordx4 v208, s[24:27], s53 offen offset:2176 nt lds\n"
"s_add_u32 s53, s33, 0x15000\n"
"s_and_b32 s53, s53, 0x3ffff\n"
"s_add_u32 s53, s53, 0x400000\n"
"s_mov_b32 m0, 0xf000\n"
"s_nop 0\n"
"buffer_load_dwordx4 v208, s[24:27], s53 offen nt lds\n"
"buffer_load_dwordx4 v208, s[24:27], s53 offen offset:2048 nt lds\n"
"s_mov_b32 m0, 0xf380\n"
"s_nop 0\n"
"buffer_load_dwordx4 v208, s[24:27], s53 offen offset:128 nt lds\n"
"buffer_load_dwordx4 v208, s[24:27], s53 offen offset:2176 nt lds\n"
"s_add_u32 s53, s33, 0x14000\n"
"s_and_b32 s53, s53, 0x3ffff\n"
"s_add_u32 s53, s53, 0x400000\n"
"s_mov_b32 m0, 0\n"
"s_nop 0\n"
"buffer_load_dwordx4 v206, s[20:23], s53 offen nt lds\n"
"buffer_load_dwordx4 v207, s[20:23], s53 offen offset:2048 nt lds\n"
"s_mov_b32 m0, 0x380\n"
"s_nop 0\n"
"buffer_load_dwordx4 v206, s[20:23], s53 offen offset:128 nt lds\n"
"buffer_load_dwordx4 v207, s[20:23], s53 offen offset:2176 nt lds\n"
"s_add_u32 s53, s33, 0x16000\n"
"s_and_b32 s53, s53, 0x3ffff\n"
"s_add_u32 s53, s53, 0x400000\n"
"s_mov_b32 m0, 0x2000\n"
"s_nop 0\n"
"buffer_load_dwordx4 v206, s[20:23], s53 offen nt lds\n"
"buffer_load_dwordx4 v207, s[20:23], s53 offen offset:2048 nt lds\n"
"s_mov_b32 m0, 0x2380\n"
"s_nop 0\n"
"buffer_load_dwordx4 v206, s[20:23], s53 offen offset:128 nt lds\n"
"buffer_load_dwordx4 v207, s[20:23], s53 offen offset:2176 nt lds\n"
"s_waitcnt vmcnt(16)\n"
"s_barrier\n"
"s_waitcnt lgkmcnt(0)\n"
".Lk1_pc80a:\n"
"s_memrealtime s[72:73]\n"
"s_waitcnt lgkmcnt(0)\n"
"s_sub_u32 s73, s72, s65\n"
"s_cmp_lt_u32 s73, 126\n"
"s_cbranch_scc0 .Lk1_pc80b\n"
"s_sleep 1\n"
"s_branch .Lk1_pc80a\n"
".Lk1_pc80b:\n"
"s_mov_b32 s65, s72\n"
"s_add_u32 s53, s33, 0x17000\n"
"s_and_b32 s53, s53, 0x3ffff\n"
"s_add_u32 s53, s53, 0x400000\n"
"s_mov_b32 m0, 0x11000\n"
"s_nop 0\n"
"buffer_load_dwordx4 v208, s[24:27], s53 offen nt lds\n"
"buffer_load_dwordx4 v208, s[24:27], s53 offen offset:2048 nt lds\n"
"s_mov_b32 m0, 0x11380\n"
"s_nop 0\n"
"buffer_load_dwordx4 v208, s[24:27], s53 offen offset:128 nt lds\n"
"buffer_load_dwordx4 v208, s[24:27], s53 offen offset:2176 nt lds\n"
"s_add_u32 s53, s33, 0x19000\n"
"s_and_b32 s53, s53, 0x3ffff\n"
"s_add_u32 s53, s53, 0x400000\n"
"s_mov_b32 m0, 0x13000\n"
"s_nop 0\n"
"buffer_load_dwordx4 v208, s[24:27], s53 offen nt lds\n"
"buffer_load_dwordx4 v208, s[24:27], s53 offen offset:2048 nt lds\n"
"s_mov_b32 m0, 0x13380\n"
"s_nop 0\n"
"buffer_load_dwordx4 v208, s[24:27], s53 offen offset:128 nt lds\n"
"buffer_load_dwordx4 v208, s[24:27], s53 offen offset:2176 nt lds\n"
"s_add_u32 s53, s33, 0x18000\n"
"s_and_b32 s53, s53, 0x3ffff\n"
"s_add_u32 s53, s53, 0x400000\n"
"s_mov_b32 m0, 0x4000\n"
"s_nop 0\n"
"buffer_load_dwordx4 v206, s[20:23], s53 offen nt lds\n"
"buffer_load_dwordx4 v207, s[20:23], s53 offen offset:2048 nt lds\n"
"s_mov_b32 m0, 0x4380\n"
"s_nop 0\n"
"buffer_load_dwordx4 v206, s[20:23], s53 offen offset:128 nt lds\n"
"buffer_load_dwordx4 v207, s[20:23], s53 offen offset:2176 nt lds\n"
"s_add_u32 s53, s33, 0x1a000\n"
"s_and_b32 s53, s53, 0x3ffff\n"
"s_add_u32 s53, s53, 0x400000\n"
"s_mov_b32 m0, 0x6000\n"
"s_nop 0\n"
"buffer_load_dwordx4 v206, s[20:23], s53 offen nt lds\n"
"buffer_load_dwordx4 v207, s[20:23], s53 offen offset:2048 nt lds\n"
"s_mov_b32 m0, 0x6380\n"
"s_nop 0\n"
"buffer_load_dwordx4 v206, s[20:23], s53 offen offset:128 nt lds\n"
"buffer_load_dwordx4 v207, s[20:23], s53 offen offset:2176 nt lds\n"
"s_waitcnt vmcnt(16)\n"
"s_barrier\n"
"s_waitcnt lgkmcnt(0)\n"
".Lk1_pc81a:\n"
"s_memrealtime s[72:73]\n"
"s_waitcnt lgkmcnt(0)\n"
"s_sub_u32 s73, s72, s65\n"
"s_cmp_lt_u32 s73, 126\n"
"s_cbranch_scc0 .Lk1_pc81b\n"
"s_sleep 1\n"
"s_branch .Lk1_pc81a\n"
".Lk1_pc81b:\n"
"s_mov_b32 s65, s72\n"
"s_add_u32 s53, s33, 0x1b000\n"
"s_and_b32 s53, s53, 0x3ffff\n"
"s_add_u32 s53, s53, 0x400000\n"
"s_mov_b32 m0, 0x15000\n"
"s_nop 0\n"
"buffer_load_dwordx4 v208, s[24:27], s53 offen nt lds\n"
"buffer_load_dwordx4 v208, s[24:27], s53 offen offset:2048 nt lds\n"
"s_mov_b32 m0, 0x15380\n"
"s_nop 0\n"
"buffer_load_dwordx4 v208, s[24:27], s53 offen offset:128 nt lds\n"
"buffer_load_dwordx4 v208, s[24:27], s53 offen offset:2176 nt lds\n"
"s_add_u32 s53, s33, 0x1d000\n"
"s_and_b32 s53, s53, 0x3ffff\n"
"s_add_u32 s53, s53, 0x400000\n"
"s_mov_b32 m0, 0x17000\n"
"s_nop 0\n"
"buffer_load_dwordx4 v208, s[24:27], s53 offen nt lds\n"
"buffer_load_dwordx4 v208, s[24:27], s53 offen offset:2048 nt lds\n"
"s_mov_b32 m0, 0x17380\n"
"s_nop 0\n"
"buffer_load_dwordx4 v208, s[24:27], s53 offen offset:128 nt lds\n"
"buffer_load_dwordx4 v208, s[24:27], s53 offen offset:2176 nt lds\n"
"s_add_u32 s53, s33, 0x1c000\n"
"s_and_b32 s53, s53, 0x3ffff\n"
"s_add_u32 s53, s53, 0x400000\n"
"s_mov_b32 m0, 0x8000\n"
"s_nop 0\n"
"buffer_load_dwordx4 v206, s[20:23], s53 offen nt lds\n"
"buffer_load_dwordx4 v207, s[20:23], s53 offen offset:2048 nt lds\n"
"s_mov_b32 m0, 0x8380\n"
"s_nop 0\n"
"buffer_load_dwordx4 v206, s[20:23], s53 offen offset:128 nt lds\n"
"buffer_load_dwordx4 v207, s[20:23], s53 offen offset:2176 nt lds\n"
"s_add_u32 s53, s33, 0x1e000\n"
"s_and_b32 s53, s53, 0x3ffff\n"
"s_add_u32 s53, s53, 0x400000\n"
"s_mov_b32 m0, 0xa000\n"
"s_nop 0\n"
"buffer_load_dwordx4 v206, s[20:23], s53 offen nt lds\n"
"buffer_load_dwordx4 v207, s[20:23], s53 offen offset:2048 nt lds\n"
"s_mov_b32 m0, 0xa380\n"
"s_nop 0\n"
"buffer_load_dwordx4 v206, s[20:23], s53 offen offset:128 nt lds\n"
"buffer_load_dwordx4 v207, s[20:23], s53 offen offset:2176 nt lds\n"
"s_waitcnt vmcnt(16)\n"
"s_barrier\n"
"s_waitcnt lgkmcnt(0)\n"
".Lk1_pc82a:\n"
"s_memrealtime s[72:73]\n"
"s_waitcnt lgkmcnt(0)\n"
"s_sub_u32 s73, s72, s65\n"
"s_cmp_lt_u32 s73, 126\n"
"s_cbranch_scc0 .Lk1_pc82b\n"
"s_sleep 1\n"
"s_branch .Lk1_pc82a\n"
".Lk1_pc82b:\n"
"s_mov_b32 s65, s72\n"
"s_add_u32 s53, s33, 0x1f000\n"
"s_and_b32 s53, s53, 0x3ffff\n"
"s_add_u32 s53, s53, 0x400000\n"
"s_mov_b32 m0, 0xd000\n"
"s_nop 0\n"
"buffer_load_dwordx4 v208, s[24:27], s53 offen nt lds\n"
"buffer_load_dwordx4 v208, s[24:27], s53 offen offset:2048 nt lds\n"
"s_mov_b32 m0, 0xd380\n"
"s_nop 0\n"
"buffer_load_dwordx4 v208, s[24:27], s53 offen offset:128 nt lds\n"
"buffer_load_dwordx4 v208, s[24:27], s53 offen offset:2176 nt lds\n"
"s_add_u32 s53, s33, 0x21000\n"
"s_and_b32 s53, s53, 0x3ffff\n"
"s_add_u32 s53, s53, 0x400000\n"
"s_mov_b32 m0, 0xf000\n"
"s_nop 0\n"
"buffer_load_dwordx4 v208, s[24:27], s53 offen nt lds\n"
"buffer_load_dwordx4 v208, s[24:27], s53 offen offset:2048 nt lds\n"
"s_mov_b32 m0, 0xf380\n"
"s_nop 0\n"
"buffer_load_dwordx4 v208, s[24:27], s53 offen offset:128 nt lds\n"
"buffer_load_dwordx4 v208, s[24:27], s53 offen offset:2176 nt lds\n"
"s_add_u32 s53, s33, 0x20000\n"
"s_and_b32 s53, s53, 0x3ffff\n"
"s_add_u32 s53, s53, 0x400000\n"
"s_mov_b32 m0, 0\n"
"s_nop 0\n"
"buffer_load_dwordx4 v206, s[20:23], s53 offen nt lds\n"
"buffer_load_dwordx4 v207, s[20:23], s53 offen offset:2048 nt lds\n"
"s_mov_b32 m0, 0x380\n"
"s_nop 0\n"
"buffer_load_dwordx4 v206, s[20:23], s53 offen offset:128 nt lds\n"
"buffer_load_dwordx4 v207, s[20:23], s53 offen offset:2176 nt lds\n"
"s_add_u32 s53, s33, 0x22000\n"
"s_and_b32 s53, s53, 0x3ffff\n"
"s_add_u32 s53, s53, 0x400000\n"
"s_mov_b32 m0, 0x2000\n"
"s_nop 0\n"
"buffer_load_dwordx4 v206, s[20:23], s53 offen nt lds\n"
"buffer_load_dwordx4 v207, s[20:23], s53 offen offset:2048 nt lds\n"
"s_mov_b32 m0, 0x2380\n"
"s_nop 0\n"
"buffer_load_dwordx4 v206, s[20:23], s53 offen offset:128 nt lds\n"
"buffer_load_dwordx4 v207, s[20:23], s53 offen offset:2176 nt lds\n"
"s_waitcnt vmcnt(16)\n"
"s_barrier\n"
"s_waitcnt lgkmcnt(0)\n"
".Lk1_pc83a:\n"
"s_memrealtime s[72:73]\n"
"s_waitcnt lgkmcnt(0)\n"
"s_sub_u32 s73, s72, s65\n"
"s_cmp_lt_u32 s73, 126\n"
"s_cbranch_scc0 .Lk1_pc83b\n"
"s_sleep 1\n"
"s_branch .Lk1_pc83a\n"
".Lk1_pc83b:\n"
"s_mov_b32 s65, s72\n"
"s_add_u32 s53, s33, 0x23000\n"
"s_and_b32 s53, s53, 0x3ffff\n"
"s_add_u32 s53, s53, 0x400000\n"
"s_mov_b32 m0, 0x11000\n"
"s_nop 0\n"
"buffer_load_dwordx4 v208, s[24:27], s53 offen nt lds\n"
"buffer_load_dwordx4 v208, s[24:27], s53 offen offset:2048 nt lds\n"
"s_mov_b32 m0, 0x11380\n"
"s_nop 0\n"
"buffer_load_dwordx4 v208, s[24:27], s53 offen offset:128 nt lds\n"
"buffer_load_dwordx4 v208, s[24:27], s53 offen offset:2176 nt lds\n"
"s_add_u32 s53, s33, 0x25000\n"
"s_and_b32 s53, s53, 0x3ffff\n"
"s_add_u32 s53, s53, 0x400000\n"
"s_mov_b32 m0, 0x13000\n"
"s_nop 0\n"
"buffer_load_dwordx4 v208, s[24:27], s53 offen nt lds\n"
"buffer_load_dwordx4 v208, s[24:27], s53 offen offset:2048 nt lds\n"
"s_mov_b32 m0, 0x13380\n"
"s_nop 0\n"
"buffer_load_dwordx4 v208, s[24:27], s53 offen offset:128 nt lds\n"
"buffer_load_dwordx4 v208, s[24:27], s53 offen offset:2176 nt lds\n"
"s_add_u32 s53, s33, 0x24000\n"
"s_and_b32 s53, s53, 0x3ffff\n"
"s_add_u32 s53, s53, 0x400000\n"
"s_mov_b32 m0, 0x4000\n"
"s_nop 0\n"
"buffer_load_dwordx4 v206, s[20:23], s53 offen nt lds\n"
"buffer_load_dwordx4 v207, s[20:23], s53 offen offset:2048 nt lds\n"
"s_mov_b32 m0, 0x4380\n"
"s_nop 0\n"
"buffer_load_dwordx4 v206, s[20:23], s53 offen offset:128 nt lds\n"
"buffer_load_dwordx4 v207, s[20:23], s53 offen offset:2176 nt lds\n"
"s_add_u32 s53, s33, 0x26000\n"
"s_and_b32 s53, s53, 0x3ffff\n"
"s_add_u32 s53, s53, 0x400000\n"
"s_mov_b32 m0, 0x6000\n"
"s_nop 0\n"
"buffer_load_dwordx4 v206, s[20:23], s53 offen nt lds\n"
"buffer_load_dwordx4 v207, s[20:23], s53 offen offset:2048 nt lds\n"
"s_mov_b32 m0, 0x6380\n"
"s_nop 0\n"
"buffer_load_dwordx4 v206, s[20:23], s53 offen offset:128 nt lds\n"
"buffer_load_dwordx4 v207, s[20:23], s53 offen offset:2176 nt lds\n"
"s_waitcnt vmcnt(16)\n"
"s_barrier\n"
"s_waitcnt lgkmcnt(0)\n"
".Lk1_pc84a:\n"
"s_memrealtime s[72:73]\n"
"s_waitcnt lgkmcnt(0)\n"
"s_sub_u32 s73, s72, s65\n"
"s_cmp_lt_u32 s73, 126\n"
"s_cbranch_scc0 .Lk1_pc84b\n"
"s_sleep 1\n"
"s_branch .Lk1_pc84a\n"
".Lk1_pc84b:\n"
"s_mov_b32 s65, s72\n"
"s_add_u32 s53, s33, 0x27000\n"
"s_and_b32 s53, s53, 0x3ffff\n"
"s_add_u32 s53, s53, 0x400000\n"
"s_mov_b32 m0, 0x15000\n"
"s_nop 0\n"
"buffer_load_dwordx4 v208, s[24:27], s53 offen nt lds\n"
"buffer_load_dwordx4 v208, s[24:27], s53 offen offset:2048 nt lds\n"
"s_mov_b32 m0, 0x15380\n"
"s_nop 0\n"
"buffer_load_dwordx4 v208, s[24:27], s53 offen offset:128 nt lds\n"
"buffer_load_dwordx4 v208, s[24:27], s53 offen offset:2176 nt lds\n"
"s_add_u32 s53, s33, 0x29000\n"
"s_and_b32 s53, s53, 0x3ffff\n"
"s_add_u32 s53, s53, 0x400000\n"
"s_mov_b32 m0, 0x17000\n"
"s_nop 0\n"
"buffer_load_dwordx4 v208, s[24:27], s53 offen nt lds\n"
"buffer_load_dwordx4 v208, s[24:27], s53 offen offset:2048 nt lds\n"
"s_mov_b32 m0, 0x17380\n"
"s_nop 0\n"
"buffer_load_dwordx4 v208, s[24:27], s53 offen offset:128 nt lds\n"
"buffer_load_dwordx4 v208, s[24:27], s53 offen offset:2176 nt lds\n"
"s_add_u32 s53, s33, 0x28000\n"
"s_and_b32 s53, s53, 0x3ffff\n"
"s_add_u32 s53, s53, 0x400000\n"
"s_mov_b32 m0, 0x8000\n"
"s_nop 0\n"
"buffer_load_dwordx4 v206, s[20:23], s53 offen nt lds\n"
"buffer_load_dwordx4 v207, s[20:23], s53 offen offset:2048 nt lds\n"
"s_mov_b32 m0, 0x8380\n"
"s_nop 0\n"
"buffer_load_dwordx4 v206, s[20:23], s53 offen offset:128 nt lds\n"
"buffer_load_dwordx4 v207, s[20:23], s53 offen offset:2176 nt lds\n"
"s_add_u32 s53, s33, 0x2a000\n"
"s_and_b32 s53, s53, 0x3ffff\n"
"s_add_u32 s53, s53, 0x400000\n"
"s_mov_b32 m0, 0xa000\n"
"s_nop 0\n"
"buffer_load_dwordx4 v206, s[20:23], s53 offen nt lds\n"
"buffer_load_dwordx4 v207, s[20:23], s53 offen offset:2048 nt lds\n"
"s_mov_b32 m0, 0xa380\n"
"s_nop 0\n"
"buffer_load_dwordx4 v206, s[20:23], s53 offen offset:128 nt lds\n"
"buffer_load_dwordx4 v207, s[20:23], s53 offen offset:2176 nt lds\n"
"s_waitcnt vmcnt(16)\n"
"s_barrier\n"
"s_waitcnt lgkmcnt(0)\n"
".Lk1_pc85a:\n"
"s_memrealtime s[72:73]\n"
"s_waitcnt lgkmcnt(0)\n"
"s_sub_u32 s73, s72, s65\n"
"s_cmp_lt_u32 s73, 126\n"
"s_cbranch_scc0 .Lk1_pc85b\n"
"s_sleep 1\n"
"s_branch .Lk1_pc85a\n"
".Lk1_pc85b:\n"
"s_mov_b32 s65, s72\n"
"s_add_u32 s53, s33, 0x2b000\n"
"s_and_b32 s53, s53, 0x3ffff\n"
"s_add_u32 s53, s53, 0x400000\n"
"s_mov_b32 m0, 0xd000\n"
"s_nop 0\n"
"buffer_load_dwordx4 v208, s[24:27], s53 offen nt lds\n"
"buffer_load_dwordx4 v208, s[24:27], s53 offen offset:2048 nt lds\n"
"s_mov_b32 m0, 0xd380\n"
"s_nop 0\n"
"buffer_load_dwordx4 v208, s[24:27], s53 offen offset:128 nt lds\n"
"buffer_load_dwordx4 v208, s[24:27], s53 offen offset:2176 nt lds\n"
"s_add_u32 s53, s33, 0x2d000\n"
"s_and_b32 s53, s53, 0x3ffff\n"
"s_add_u32 s53, s53, 0x400000\n"
"s_mov_b32 m0, 0xf000\n"
"s_nop 0\n"
"buffer_load_dwordx4 v208, s[24:27], s53 offen nt lds\n"
"buffer_load_dwordx4 v208, s[24:27], s53 offen offset:2048 nt lds\n"
"s_mov_b32 m0, 0xf380\n"
"s_nop 0\n"
"buffer_load_dwordx4 v208, s[24:27], s53 offen offset:128 nt lds\n"
"buffer_load_dwordx4 v208, s[24:27], s53 offen offset:2176 nt lds\n"
"s_add_u32 s53, s33, 0x2c000\n"
"s_and_b32 s53, s53, 0x3ffff\n"
"s_add_u32 s53, s53, 0x400000\n"
"s_mov_b32 m0, 0\n"
"s_nop 0\n"
"buffer_load_dwordx4 v206, s[20:23], s53 offen nt lds\n"
"buffer_load_dwordx4 v207, s[20:23], s53 offen offset:2048 nt lds\n"
"s_mov_b32 m0, 0x380\n"
"s_nop 0\n"
"buffer_load_dwordx4 v206, s[20:23], s53 offen offset:128 nt lds\n"
"buffer_load_dwordx4 v207, s[20:23], s53 offen offset:2176 nt lds\n"
"s_add_u32 s53, s33, 0x2e000\n"
"s_and_b32 s53, s53, 0x3ffff\n"
"s_add_u32 s53, s53, 0x400000\n"
"s_mov_b32 m0, 0x2000\n"
"s_nop 0\n"
"buffer_load_dwordx4 v206, s[20:23], s53 offen nt lds\n"
"buffer_load_dwordx4 v207, s[20:23], s53 offen offset:2048 nt lds\n"
"s_mov_b32 m0, 0x2380\n"
"s_nop 0\n"
"buffer_load_dwordx4 v206, s[20:23], s53 offen offset:128 nt lds\n"
"buffer_load_dwordx4 v207, s[20:23], s53 offen offset:2176 nt lds\n"
"s_waitcnt vmcnt(16)\n"
"s_barrier\n"
"s_waitcnt lgkmcnt(0)\n"
".Lk1_pc86a:\n"
"s_memrealtime s[72:73]\n"
"s_waitcnt lgkmcnt(0)\n"
"s_sub_u32 s73, s72, s65\n"
"s_cmp_lt_u32 s73, 126\n"
"s_cbranch_scc0 .Lk1_pc86b\n"
"s_sleep 1\n"
"s_branch .Lk1_pc86a\n"
".Lk1_pc86b:\n"
"s_mov_b32 s65, s72\n"
"s_add_u32 s53, s33, 0x2f000\n"
"s_and_b32 s53, s53, 0x3ffff\n"
"s_add_u32 s53, s53, 0x400000\n"
"s_mov_b32 m0, 0x11000\n"
"s_nop 0\n"
"buffer_load_dwordx4 v208, s[24:27], s53 offen nt lds\n"
"buffer_load_dwordx4 v208, s[24:27], s53 offen offset:2048 nt lds\n"
"s_mov_b32 m0, 0x11380\n"
"s_nop 0\n"
"buffer_load_dwordx4 v208, s[24:27], s53 offen offset:128 nt lds\n"
"buffer_load_dwordx4 v208, s[24:27], s53 offen offset:2176 nt lds\n"
"s_add_u32 s53, s33, 0x31000\n"
"s_and_b32 s53, s53, 0x3ffff\n"
"s_add_u32 s53, s53, 0x400000\n"
"s_mov_b32 m0, 0x13000\n"
"s_nop 0\n"
"buffer_load_dwordx4 v208, s[24:27], s53 offen nt lds\n"
"buffer_load_dwordx4 v208, s[24:27], s53 offen offset:2048 nt lds\n"
"s_mov_b32 m0, 0x13380\n"
"s_nop 0\n"
"buffer_load_dwordx4 v208, s[24:27], s53 offen offset:128 nt lds\n"
"buffer_load_dwordx4 v208, s[24:27], s53 offen offset:2176 nt lds\n"
"s_add_u32 s53, s33, 0x30000\n"
"s_and_b32 s53, s53, 0x3ffff\n"
"s_add_u32 s53, s53, 0x400000\n"
"s_mov_b32 m0, 0x4000\n"
"s_nop 0\n"
"buffer_load_dwordx4 v206, s[20:23], s53 offen nt lds\n"
"buffer_load_dwordx4 v207, s[20:23], s53 offen offset:2048 nt lds\n"
"s_mov_b32 m0, 0x4380\n"
"s_nop 0\n"
"buffer_load_dwordx4 v206, s[20:23], s53 offen offset:128 nt lds\n"
"buffer_load_dwordx4 v207, s[20:23], s53 offen offset:2176 nt lds\n"
"s_add_u32 s53, s33, 0x32000\n"
"s_and_b32 s53, s53, 0x3ffff\n"
"s_add_u32 s53, s53, 0x400000\n"
"s_mov_b32 m0, 0x6000\n"
"s_nop 0\n"
"buffer_load_dwordx4 v206, s[20:23], s53 offen nt lds\n"
"buffer_load_dwordx4 v207, s[20:23], s53 offen offset:2048 nt lds\n"
"s_mov_b32 m0, 0x6380\n"
"s_nop 0\n"
"buffer_load_dwordx4 v206, s[20:23], s53 offen offset:128 nt lds\n"
"buffer_load_dwordx4 v207, s[20:23], s53 offen offset:2176 nt lds\n"
"s_waitcnt vmcnt(16)\n"
"s_barrier\n"
"s_waitcnt lgkmcnt(0)\n"
".Lk1_pc87a:\n"
"s_memrealtime s[72:73]\n"
"s_waitcnt lgkmcnt(0)\n"
"s_sub_u32 s73, s72, s65\n"
"s_cmp_lt_u32 s73, 126\n"
"s_cbranch_scc0 .Lk1_pc87b\n"
"s_sleep 1\n"
"s_branch .Lk1_pc87a\n"
".Lk1_pc87b:\n"
"s_mov_b32 s65, s72\n"
"s_add_u32 s53, s33, 0x33000\n"
"s_and_b32 s53, s53, 0x3ffff\n"
"s_add_u32 s53, s53, 0x400000\n"
"s_mov_b32 m0, 0x15000\n"
"s_nop 0\n"
"buffer_load_dwordx4 v208, s[24:27], s53 offen nt lds\n"
"buffer_load_dwordx4 v208, s[24:27], s53 offen offset:2048 nt lds\n"
"s_mov_b32 m0, 0x15380\n"
"s_nop 0\n"
"buffer_load_dwordx4 v208, s[24:27], s53 offen offset:128 nt lds\n"
"buffer_load_dwordx4 v208, s[24:27], s53 offen offset:2176 nt lds\n"
"s_add_u32 s53, s33, 0x35000\n"
"s_and_b32 s53, s53, 0x3ffff\n"
"s_add_u32 s53, s53, 0x400000\n"
"s_mov_b32 m0, 0x17000\n"
"s_nop 0\n"
"buffer_load_dwordx4 v208, s[24:27], s53 offen nt lds\n"
"buffer_load_dwordx4 v208, s[24:27], s53 offen offset:2048 nt lds\n"
"s_mov_b32 m0, 0x17380\n"
"s_nop 0\n"
"buffer_load_dwordx4 v208, s[24:27], s53 offen offset:128 nt lds\n"
"buffer_load_dwordx4 v208, s[24:27], s53 offen offset:2176 nt lds\n"
"s_add_u32 s53, s33, 0x34000\n"
"s_and_b32 s53, s53, 0x3ffff\n"
"s_add_u32 s53, s53, 0x400000\n"
"s_mov_b32 m0, 0x8000\n"
"s_nop 0\n"
"buffer_load_dwordx4 v206, s[20:23], s53 offen nt lds\n"
"buffer_load_dwordx4 v207, s[20:23], s53 offen offset:2048 nt lds\n"
"s_mov_b32 m0, 0x8380\n"
"s_nop 0\n"
"buffer_load_dwordx4 v206, s[20:23], s53 offen offset:128 nt lds\n"
"buffer_load_dwordx4 v207, s[20:23], s53 offen offset:2176 nt lds\n"
"s_add_u32 s53, s33, 0x36000\n"
"s_and_b32 s53, s53, 0x3ffff\n"
"s_add_u32 s53, s53, 0x400000\n"
"s_mov_b32 m0, 0xa000\n"
"s_nop 0\n"
"buffer_load_dwordx4 v206, s[20:23], s53 offen nt lds\n"
"buffer_load_dwordx4 v207, s[20:23], s53 offen offset:2048 nt lds\n"
"s_mov_b32 m0, 0xa380\n"
"s_nop 0\n"
"buffer_load_dwordx4 v206, s[20:23], s53 offen offset:128 nt lds\n"
"buffer_load_dwordx4 v207, s[20:23], s53 offen offset:2176 nt lds\n"
"s_waitcnt vmcnt(16)\n"
"s_barrier\n"
"s_waitcnt lgkmcnt(0)\n"
".Lk1_pc88a:\n"
"s_memrealtime s[72:73]\n"
"s_waitcnt lgkmcnt(0)\n"
"s_sub_u32 s73, s72, s65\n"
"s_cmp_lt_u32 s73, 126\n"
"s_cbranch_scc0 .Lk1_pc88b\n"
"s_sleep 1\n"
"s_branch .Lk1_pc88a\n"
".Lk1_pc88b:\n"
"s_mov_b32 s65, s72\n"
"s_add_u32 s53, s33, 0x37000\n"
"s_and_b32 s53, s53, 0x3ffff\n"
"s_add_u32 s53, s53, 0x400000\n"
"s_mov_b32 m0, 0xd000\n"
"s_nop 0\n"
"buffer_load_dwordx4 v208, s[24:27], s53 offen nt lds\n"
"buffer_load_dwordx4 v208, s[24:27], s53 offen offset:2048 nt lds\n"
"s_mov_b32 m0, 0xd380\n"
"s_nop 0\n"
"buffer_load_dwordx4 v208, s[24:27], s53 offen offset:128 nt lds\n"
"buffer_load_dwordx4 v208, s[24:27], s53 offen offset:2176 nt lds\n"
"s_add_u32 s53, s33, 0x39000\n"
"s_and_b32 s53, s53, 0x3ffff\n"
"s_add_u32 s53, s53, 0x400000\n"
"s_mov_b32 m0, 0xf000\n"
"s_nop 0\n"
"buffer_load_dwordx4 v208, s[24:27], s53 offen nt lds\n"
"buffer_load_dwordx4 v208, s[24:27], s53 offen offset:2048 nt lds\n"
"s_mov_b32 m0, 0xf380\n"
"s_nop 0\n"
"buffer_load_dwordx4 v208, s[24:27], s53 offen offset:128 nt lds\n"
"buffer_load_dwordx4 v208, s[24:27], s53 offen offset:2176 nt lds\n"
"s_add_u32 s53, s33, 0x38000\n"
"s_and_b32 s53, s53, 0x3ffff\n"
"s_add_u32 s53, s53, 0x400000\n"
"s_mov_b32 m0, 0\n"
"s_nop 0\n"
"buffer_load_dwordx4 v206, s[20:23], s53 offen nt lds\n"
"buffer_load_dwordx4 v207, s[20:23], s53 offen offset:2048 nt lds\n"
"s_mov_b32 m0, 0x380\n"
"s_nop 0\n"
"buffer_load_dwordx4 v206, s[20:23], s53 offen offset:128 nt lds\n"
"buffer_load_dwordx4 v207, s[20:23], s53 offen offset:2176 nt lds\n"
"s_add_u32 s53, s33, 0x3a000\n"
"s_and_b32 s53, s53, 0x3ffff\n"
"s_add_u32 s53, s53, 0x400000\n"
"s_mov_b32 m0, 0x2000\n"
"s_nop 0\n"
"buffer_load_dwordx4 v206, s[20:23], s53 offen nt lds\n"
"buffer_load_dwordx4 v207, s[20:23], s53 offen offset:2048 nt lds\n"
"s_mov_b32 m0, 0x2380\n"
"s_nop 0\n"
"buffer_load_dwordx4 v206, s[20:23], s53 offen offset:128 nt lds\n"
"buffer_load_dwordx4 v207, s[20:23], s53 offen offset:2176 nt lds\n"
"s_waitcnt vmcnt(16)\n"
"s_barrier\n"
"s_waitcnt lgkmcnt(0)\n"
".Lk1_pc89a:\n"
"s_memrealtime s[72:73]\n"
"s_waitcnt lgkmcnt(0)\n"
"s_sub_u32 s73, s72, s65\n"
"s_cmp_lt_u32 s73, 126\n"
"s_cbranch_scc0 .Lk1_pc89b\n"
"s_sleep 1\n"
"s_branch .Lk1_pc89a\n"
".Lk1_pc89b:\n"
"s_mov_b32 s65, s72\n"
"s_add_u32 s53, s33, 0x3b000\n"
"s_and_b32 s53, s53, 0x3ffff\n"
"s_add_u32 s53, s53, 0x400000\n"
"s_mov_b32 m0, 0x11000\n"
"s_nop 0\n"
"buffer_load_dwordx4 v208, s[24:27], s53 offen nt lds\n"
"buffer_load_dwordx4 v208, s[24:27], s53 offen offset:2048 nt lds\n"
"s_mov_b32 m0, 0x11380\n"
"s_nop 0\n"
"buffer_load_dwordx4 v208, s[24:27], s53 offen offset:128 nt lds\n"
"buffer_load_dwordx4 v208, s[24:27], s53 offen offset:2176 nt lds\n"
"s_add_u32 s53, s33, 0x3d000\n"
"s_and_b32 s53, s53, 0x3ffff\n"
"s_add_u32 s53, s53, 0x400000\n"
"s_mov_b32 m0, 0x13000\n"
"s_nop 0\n"
"buffer_load_dwordx4 v208, s[24:27], s53 offen nt lds\n"
"buffer_load_dwordx4 v208, s[24:27], s53 offen offset:2048 nt lds\n"
"s_mov_b32 m0, 0x13380\n"
"s_nop 0\n"
"buffer_load_dwordx4 v208, s[24:27], s53 offen offset:128 nt lds\n"
"buffer_load_dwordx4 v208, s[24:27], s53 offen offset:2176 nt lds\n"
"s_add_u32 s53, s33, 0x3c000\n"
"s_and_b32 s53, s53, 0x3ffff\n"
"s_add_u32 s53, s53, 0x400000\n"
"s_mov_b32 m0, 0x4000\n"
"s_nop 0\n"
"buffer_load_dwordx4 v206, s[20:23], s53 offen nt lds\n"
"buffer_load_dwordx4 v207, s[20:23], s53 offen offset:2048 nt lds\n"
"s_mov_b32 m0, 0x4380\n"
"s_nop 0\n"
"buffer_load_dwordx4 v206, s[20:23], s53 offen offset:128 nt lds\n"
"buffer_load_dwordx4 v207, s[20:23], s53 offen offset:2176 nt lds\n"
"s_add_u32 s53, s33, 0x3e000\n"
"s_and_b32 s53, s53, 0x3ffff\n"
"s_add_u32 s53, s53, 0x400000\n"
"s_mov_b32 m0, 0x6000\n"
"s_nop 0\n"
"buffer_load_dwordx4 v206, s[20:23], s53 offen nt lds\n"
"buffer_load_dwordx4 v207, s[20:23], s53 offen offset:2048 nt lds\n"
"s_mov_b32 m0, 0x6380\n"
"s_nop 0\n"
"buffer_load_dwordx4 v206, s[20:23], s53 offen offset:128 nt lds\n"
"buffer_load_dwordx4 v207, s[20:23], s53 offen offset:2176 nt lds\n"
"s_waitcnt vmcnt(16)\n"
"s_barrier\n"
"s_waitcnt lgkmcnt(0)\n"
".Lk1_pc90a:\n"
"s_memrealtime s[72:73]\n"
"s_waitcnt lgkmcnt(0)\n"
"s_sub_u32 s73, s72, s65\n"
"s_cmp_lt_u32 s73, 126\n"
"s_cbranch_scc0 .Lk1_pc90b\n"
"s_sleep 1\n"
"s_branch .Lk1_pc90a\n"
".Lk1_pc90b:\n"
"s_mov_b32 s65, s72\n"
"s_add_u32 s53, s33, 0x3f000\n"
"s_and_b32 s53, s53, 0x3ffff\n"
"s_add_u32 s53, s53, 0x400000\n"
"s_mov_b32 m0, 0x15000\n"
"s_nop 0\n"
"buffer_load_dwordx4 v208, s[24:27], s53 offen nt lds\n"
"buffer_load_dwordx4 v208, s[24:27], s53 offen offset:2048 nt lds\n"
"s_mov_b32 m0, 0x15380\n"
"s_nop 0\n"
"buffer_load_dwordx4 v208, s[24:27], s53 offen offset:128 nt lds\n"
"buffer_load_dwordx4 v208, s[24:27], s53 offen offset:2176 nt lds\n"
"s_waitcnt vmcnt(4)\n"
"s_barrier\n"
"s_waitcnt vmcnt(0)\n"
"s_barrier\n"
"s_branch .Lk1_end\n"
".Lk1_r2_l11:\n"
"s_waitcnt lgkmcnt(0)\n"
".Lk1_pc91a:\n"
"s_memrealtime s[72:73]\n"
"s_waitcnt lgkmcnt(0)\n"
"s_sub_u32 s73, s72, s65\n"
"s_cmp_lt_u32 s73, 126\n"
"s_cbranch_scc0 .Lk1_pc91b\n"
"s_sleep 1\n"
"s_branch .Lk1_pc91a\n"
".Lk1_pc91b:\n"
"s_mov_b32 s65, s72\n"
"s_add_u32 s53, s33, 0x7000\n"
"s_and_b32 s53, s53, 0x3ffff\n"
"s_add_u32 s53, s53, 0x400000\n"
"s_mov_b32 m0, 0xd000\n"
"s_nop 0\n"
"buffer_load_dwordx4 v208, s[24:27], s53 offen nt lds\n"
"buffer_load_dwordx4 v208, s[24:27], s53 offen offset:2048 nt lds\n"
"s_mov_b32 m0, 0xd380\n"
"s_nop 0\n"
"buffer_load_dwordx4 v208, s[24:27], s53 offen offset:128 nt lds\n"
"buffer_load_dwordx4 v208, s[24:27], s53 offen offset:2176 nt lds\n"
"s_add_u32 s53, s33, 0x9000\n"
"s_and_b32 s53, s53, 0x3ffff\n"
"s_add_u32 s53, s53, 0x400000\n"
"s_mov_b32 m0, 0xf000\n"
"s_nop 0\n"
"buffer_load_dwordx4 v208, s[24:27], s53 offen nt lds\n"
"buffer_load_dwordx4 v208, s[24:27], s53 offen offset:2048 nt lds\n"
"s_mov_b32 m0, 0xf380\n"
"s_nop 0\n"
"buffer_load_dwordx4 v208, s[24:27], s53 offen offset:128 nt lds\n"
"buffer_load_dwordx4 v208, s[24:27], s53 offen offset:2176 nt lds\n"
"s_add_u32 s53, s33, 0x8000\n"
"s_and_b32 s53, s53, 0x3ffff\n"
"s_add_u32 s53, s53, 0x400000\n"
"s_mov_b32 m0, 0\n"
"s_nop 0\n"
"buffer_load_dwordx4 v206, s[20:23], s53 offen nt lds\n"
"buffer_load_dwordx4 v207, s[20:23], s53 offen offset:2048 nt lds\n"
"s_mov_b32 m0, 0x380\n"
"s_nop 0\n"
"buffer_load_dwordx4 v206, s[20:23], s53 offen offset:128 nt lds\n"
"buffer_load_dwordx4 v207, s[20:23], s53 offen offset:2176 nt lds\n"
"s_add_u32 s53, s33, 0xa000\n"
"s_and_b32 s53, s53, 0x3ffff\n"
"s_add_u32 s53, s53, 0x400000\n"
"s_mov_b32 m0, 0x2000\n"
"s_nop 0\n"
"buffer_load_dwordx4 v206, s[20:23], s53 offen nt lds\n"
"buffer_load_dwordx4 v207, s[20:23], s53 offen offset:2048 nt lds\n"
"s_mov_b32 m0, 0x2380\n"
"s_nop 0\n"
"buffer_load_dwordx4 v206, s[20:23], s53 offen offset:128 nt lds\n"
"buffer_load_dwordx4 v207, s[20:23], s53 offen offset:2176 nt lds\n"
"s_barrier\n"
"s_waitcnt lgkmcnt(0)\n"
".Lk1_pc92a:\n"
"s_memrealtime s[72:73]\n"
"s_waitcnt lgkmcnt(0)\n"
"s_sub_u32 s73, s72, s65\n"
"s_cmp_lt_u32 s73, 126\n"
"s_cbranch_scc0 .Lk1_pc92b\n"
"s_sleep 1\n"
"s_branch .Lk1_pc92a\n"
".Lk1_pc92b:\n"
"s_mov_b32 s65, s72\n"
"s_add_u32 s53, s33, 0xb000\n"
"s_and_b32 s53, s53, 0x3ffff\n"
"s_add_u32 s53, s53, 0x400000\n"
"s_mov_b32 m0, 0x11000\n"
"s_nop 0\n"
"buffer_load_dwordx4 v208, s[24:27], s53 offen nt lds\n"
"buffer_load_dwordx4 v208, s[24:27], s53 offen offset:2048 nt lds\n"
"s_mov_b32 m0, 0x11380\n"
"s_nop 0\n"
"buffer_load_dwordx4 v208, s[24:27], s53 offen offset:128 nt lds\n"
"buffer_load_dwordx4 v208, s[24:27], s53 offen offset:2176 nt lds\n"
"s_add_u32 s53, s33, 0xd000\n"
"s_and_b32 s53, s53, 0x3ffff\n"
"s_add_u32 s53, s53, 0x400000\n"
"s_mov_b32 m0, 0x13000\n"
"s_nop 0\n"
"buffer_load_dwordx4 v208, s[24:27], s53 offen nt lds\n"
"buffer_load_dwordx4 v208, s[24:27], s53 offen offset:2048 nt lds\n"
"s_mov_b32 m0, 0x13380\n"
"s_nop 0\n"
"buffer_load_dwordx4 v208, s[24:27], s53 offen offset:128 nt lds\n"
"buffer_load_dwordx4 v208, s[24:27], s53 offen offset:2176 nt lds\n"
"s_add_u32 s53, s33, 0xc000\n"
"s_and_b32 s53, s53, 0x3ffff\n"
"s_add_u32 s53, s53, 0x400000\n"
"s_mov_b32 m0, 0x4000\n"
"s_nop 0\n"
"buffer_load_dwordx4 v206, s[20:23], s53 offen nt lds\n"
"buffer_load_dwordx4 v207, s[20:23], s53 offen offset:2048 nt lds\n"
"s_mov_b32 m0, 0x4380\n"
"s_nop 0\n"
"buffer_load_dwordx4 v206, s[20:23], s53 offen offset:128 nt lds\n"
"buffer_load_dwordx4 v207, s[20:23], s53 offen offset:2176 nt lds\n"
"s_add_u32 s53, s33, 0xe000\n"
"s_and_b32 s53, s53, 0x3ffff\n"
"s_add_u32 s53, s53, 0x400000\n"
"s_mov_b32 m0, 0x6000\n"
"s_nop 0\n"
"buffer_load_dwordx4 v206, s[20:23], s53 offen nt lds\n"
"buffer_load_dwordx4 v207, s[20:23], s53 offen offset:2048 nt lds\n"
"s_mov_b32 m0, 0x6380\n"
"s_nop 0\n"
"buffer_load_dwordx4 v206, s[20:23], s53 offen offset:128 nt lds\n"
"buffer_load_dwordx4 v207, s[20:23], s53 offen offset:2176 nt lds\n"
"s_waitcnt vmcnt(16)\n"
"s_barrier\n"
"s_waitcnt lgkmcnt(0)\n"
".Lk1_pc93a:\n"
"s_memrealtime s[72:73]\n"
"s_waitcnt lgkmcnt(0)\n"
"s_sub_u32 s73, s72, s65\n"
"s_cmp_lt_u32 s73, 126\n"
"s_cbranch_scc0 .Lk1_pc93b\n"
"s_sleep 1\n"
"s_branch .Lk1_pc93a\n"
".Lk1_pc93b:\n"
"s_mov_b32 s65, s72\n"
"s_add_u32 s53, s33, 0xf000\n"
"s_and_b32 s53, s53, 0x3ffff\n"
"s_add_u32 s53, s53, 0x400000\n"
"s_mov_b32 m0, 0x15000\n"
"s_nop 0\n"
"buffer_load_dwordx4 v208, s[24:27], s53 offen nt lds\n"
"buffer_load_dwordx4 v208, s[24:27], s53 offen offset:2048 nt lds\n"
"s_mov_b32 m0, 0x15380\n"
"s_nop 0\n"
"buffer_load_dwordx4 v208, s[24:27], s53 offen offset:128 nt lds\n"
"buffer_load_dwordx4 v208, s[24:27], s53 offen offset:2176 nt lds\n"
"s_add_u32 s53, s33, 0x11000\n"
"s_and_b32 s53, s53, 0x3ffff\n"
"s_add_u32 s53, s53, 0x400000\n"
"s_mov_b32 m0, 0x17000\n"
"s_nop 0\n"
"buffer_load_dwordx4 v208, s[24:27], s53 offen nt lds\n"
"buffer_load_dwordx4 v208, s[24:27], s53 offen offset:2048 nt lds\n"
"s_mov_b32 m0, 0x17380\n"
"s_nop 0\n"
"buffer_load_dwordx4 v208, s[24:27], s53 offen offset:128 nt lds\n"
"buffer_load_dwordx4 v208, s[24:27], s53 offen offset:2176 nt lds\n"
"s_add_u32 s53, s33, 0x10000\n"
"s_and_b32 s53, s53, 0x3ffff\n"
"s_add_u32 s53, s53, 0x400000\n"
"s_mov_b32 m0, 0x8000\n"
"s_nop 0\n"
"buffer_load_dwordx4 v206, s[20:23], s53 offen nt lds\n"
"buffer_load_dwordx4 v207, s[20:23], s53 offen offset:2048 nt lds\n"
"s_mov_b32 m0, 0x8380\n"
"s_nop 0\n"
"buffer_load_dwordx4 v206, s[20:23], s53 offen offset:128 nt lds\n"
"buffer_load_dwordx4 v207, s[20:23], s53 offen offset:2176 nt lds\n"
"s_add_u32 s53, s33, 0x12000\n"
"s_and_b32 s53, s53, 0x3ffff\n"
"s_add_u32 s53, s53, 0x400000\n"
"s_mov_b32 m0, 0xa000\n"
"s_nop 0\n"
"buffer_load_dwordx4 v206, s[20:23], s53 offen nt lds\n"
"buffer_load_dwordx4 v207, s[20:23], s53 offen offset:2048 nt lds\n"
"s_mov_b32 m0, 0xa380\n"
"s_nop 0\n"
"buffer_load_dwordx4 v206, s[20:23], s53 offen offset:128 nt lds\n"
"buffer_load_dwordx4 v207, s[20:23], s53 offen offset:2176 nt lds\n"
"s_and_b32 s67, s6, 3\n"
"s_lshr_b32 s61, s6, 3\n"
"s_mul_i32 s68, s61, 43\n"
"s_lshr_b32 s68, s68, 7\n"
"s_mul_i32 s60, s68, 3\n"
"s_sub_u32 s69, s61, s60\n"
"s_lshr_b32 s60, s6, 2\n"
"s_and_b32 s60, s60, 1\n"
"s_mul_i32 s60, s60, 9\n"
"s_add_u32 s68, s68, s60\n"
"s_lshl_b32 s66, s44, 10\n"
"s_mul_i32 s55, s44, 0x2400\n"
"s_mul_i32 s57, s68, 5\n"
"s_and_b32 s57, s57, 15\n"
"s_mov_b32 s58, s57\n"
"v_bfe_u32 v247, v238, 2, 3\n"
"v_bfe_u32 v236, v238, 4, 1\n"
"v_lshrrev_b32 v243, 5, v238\n"
"v_and_b32 v239, 3, v238\n"
"v_lshlrev_b32 v242, 1, v236\n"
"v_xor_b32 v242, v242, v239\n"
"v_lshlrev_b32 v242, 4, v242\n"
"v_lshl_add_u32 v242, v243, 6, v242\n"
"v_mul_u32_u24 v244, 0x480, v247\n"
"v_add_u32 v242, v242, v244\n"
"v_bfe_u32 v247, v238, 2, 2\n"
"v_lshlrev_b32 v240, 8, v243\n"
"v_lshl_add_u32 v240, v247, 6, v240\n"
"v_xor_b32 v247, v236, v243\n"
"v_lshl_add_u32 v240, v247, 5, v240\n"
"v_lshl_add_u32 v240, v239, 3, v240\n"
"v_add_u32 v241, 0x18000, v240\n"
"v_add_u32 v240, 0x8000, v240\n"
"v_and_b32 v247, 15, v238\n"
"v_lshlrev_b32 v245, 4, v247\n"
"v_lshl_add_u32 v245, v243, 8, v245\n"
"v_lshl_add_u32 v245, v236, 17, v245\n"
"v_and_b32 v247, 31, v238\n"
"v_mul_u32_u24 v244, 0x480, v247\n"
"v_lshl_add_u32 v244, v243, 4, v244\n"
"s_lshl_b32 s60, s68, 3\n"
"s_add_u32 s61, s60, 7\n"
"s_mul_i32 s60, s60, 57\n"
"s_lshr_b32 s60, s60, 9\n"
"s_mul_i32 s61, s61, 57\n"
"s_lshr_b32 s61, s61, 9\n"
"s_sub_u32 s61, s61, s60\n"
"s_lshl_b32 s61, s61, 12\n"
"v_bfe_u32 v247, v238, 2, 1\n"
"v_mul_u32_u24 v247, s61, v247\n"
"s_lshl_b32 s60, s60, 12\n"
"s_lshl_b32 s61, s67, 9\n"
"s_add_u32 s60, s60, s61\n"
"s_add_u32 s60, s60, 0x1000\n"
"v_lshl_add_u32 v243, v239, 7, v247\n"
"v_add_u32 v243, s60, v243\n"
"s_lshl_b32 s60, s68, 2\n"
"s_add_u32 s60, s60, s44\n"
"s_lshl_b32 s60, s60, 18\n"
"s_lshl_b32 s61, s67, 14\n"
"s_add_u32 s60, s60, s61\n"
"s_add_u32 s28, s16, s60\n"
"s_addc_u32 s29, s17, 0\n"
"s_and_b32 s29, s29, 0xffff\n"
"s_mov_b32 s28, s28\n"
"s_mov_b32 s30, 0x34000\n"
"s_mov_b32 s31, 0x20000\n"
"s_mul_i32 s60, s67, 0x90000\n"
"s_mul_i32 s61, s69, 0x180\n"
"s_add_u32 s60, s60, s61\n"
"s_add_u32 s36, s18, s60\n"
"s_addc_u32 s37, s19, 0\n"
"s_and_b32 s37, s37, 0xffff\n"
"s_mov_b32 s36, s36\n"
"s_mov_b32 s38, 0x2cfd00\n"
"s_mov_b32 s39, 0x20000\n"
"s_waitcnt vmcnt(16)\n"
"s_barrier\n"
"s_waitcnt lgkmcnt(0)\n"
".Lk1_pc94a:\n"
"s_memrealtime s[72:73]\n"
"s_waitcnt lgkmcnt(0)\n"
"s_sub_u32 s73, s72, s65\n"
"s_cmp_lt_u32 s73, 126\n"
"s_cbranch_scc0 .Lk1_pc94b\n"
"s_sleep 1\n"
"s_branch .Lk1_pc94a\n"
".Lk1_pc94b:\n"
"s_mov_b32 s65, s72\n"
"s_add_u32 s53, s33, 0x13000\n"
"s_and_b32 s53, s53, 0x3ffff\n"
"s_add_u32 s53, s53, 0x400000\n"
"s_mov_b32 m0, 0xd000\n"
"s_nop 0\n"
"buffer_load_dwordx4 v208, s[24:27], s53 offen nt lds\n"
"buffer_load_dwordx4 v208, s[24:27], s53 offen offset:2048 nt lds\n"
"s_mov_b32 m0, 0xd380\n"
"s_nop 0\n"
"buffer_load_dwordx4 v208, s[24:27], s53 offen offset:128 nt lds\n"
"buffer_load_dwordx4 v208, s[24:27], s53 offen offset:2176 nt lds\n"
"s_add_u32 s53, s33, 0x15000\n"
"s_and_b32 s53, s53, 0x3ffff\n"
"s_add_u32 s53, s53, 0x400000\n"
"s_mov_b32 m0, 0xf000\n"
"s_nop 0\n"
"buffer_load_dwordx4 v208, s[24:27], s53 offen nt lds\n"
"buffer_load_dwordx4 v208, s[24:27], s53 offen offset:2048 nt lds\n"
"s_mov_b32 m0, 0xf380\n"
"s_nop 0\n"
"buffer_load_dwordx4 v208, s[24:27], s53 offen offset:128 nt lds\n"
"buffer_load_dwordx4 v208, s[24:27], s53 offen offset:2176 nt lds\n"
"s_add_u32 s53, s33, 0x14000\n"
"s_and_b32 s53, s53, 0x3ffff\n"
"s_add_u32 s53, s53, 0x400000\n"
"s_mov_b32 m0, 0\n"
"s_nop 0\n"
"buffer_load_dwordx4 v206, s[20:23], s53 offen nt lds\n"
"buffer_load_dwordx4 v207, s[20:23], s53 offen offset:2048 nt lds\n"
"s_mov_b32 m0, 0x380\n"
"s_nop 0\n"
"buffer_load_dwordx4 v206, s[20:23], s53 offen offset:128 nt lds\n"
"buffer_load_dwordx4 v207, s[20:23], s53 offen offset:2176 nt lds\n"
"s_add_u32 s53, s33, 0x16000\n"
"s_and_b32 s53, s53, 0x3ffff\n"
"s_add_u32 s53, s53, 0x400000\n"
"s_mov_b32 m0, 0x2000\n"
"s_nop 0\n"
"buffer_load_dwordx4 v206, s[20:23], s53 offen nt lds\n"
"buffer_load_dwordx4 v207, s[20:23], s53 offen offset:2048 nt lds\n"
"s_mov_b32 m0, 0x2380\n"
"s_nop 0\n"
"buffer_load_dwordx4 v206, s[20:23], s53 offen offset:128 nt lds\n"
"buffer_load_dwordx4 v207, s[20:23], s53 offen offset:2176 nt lds\n"
"s_waitcnt vmcnt(16)\n"
"s_barrier\n"
"s_waitcnt lgkmcnt(0)\n"
".Lk1_pc95a:\n"
"s_memrealtime s[72:73]\n"
"s_waitcnt lgkmcnt(0)\n"
"s_sub_u32 s73, s72, s65\n"
"s_cmp_lt_u32 s73, 126\n"
"s_cbranch_scc0 .Lk1_pc95b\n"
"s_sleep 1\n"
"s_branch .Lk1_pc95a\n"
".Lk1_pc95b:\n"
"s_mov_b32 s65, s72\n"
"s_add_u32 s53, s33, 0x17000\n"
"s_and_b32 s53, s53, 0x3ffff\n"
"s_add_u32 s53, s53, 0x400000\n"
"s_mov_b32 m0, 0x11000\n"
"s_nop 0\n"
"buffer_load_dwordx4 v208, s[24:27], s53 offen nt lds\n"
"buffer_load_dwordx4 v208, s[24:27], s53 offen offset:2048 nt lds\n"
"s_mov_b32 m0, 0x11380\n"
"s_nop 0\n"
"buffer_load_dwordx4 v208, s[24:27], s53 offen offset:128 nt lds\n"
"buffer_load_dwordx4 v208, s[24:27], s53 offen offset:2176 nt lds\n"
"s_add_u32 s53, s33, 0x19000\n"
"s_and_b32 s53, s53, 0x3ffff\n"
"s_add_u32 s53, s53, 0x400000\n"
"s_mov_b32 m0, 0x13000\n"
"s_nop 0\n"
"buffer_load_dwordx4 v208, s[24:27], s53 offen nt lds\n"
"buffer_load_dwordx4 v208, s[24:27], s53 offen offset:2048 nt lds\n"
"s_mov_b32 m0, 0x13380\n"
"s_nop 0\n"
"buffer_load_dwordx4 v208, s[24:27], s53 offen offset:128 nt lds\n"
"buffer_load_dwordx4 v208, s[24:27], s53 offen offset:2176 nt lds\n"
"s_add_u32 s53, s33, 0x18000\n"
"s_and_b32 s53, s53, 0x3ffff\n"
"s_add_u32 s53, s53, 0x400000\n"
"s_mov_b32 m0, 0x4000\n"
"s_nop 0\n"
"buffer_load_dwordx4 v206, s[20:23], s53 offen nt lds\n"
"buffer_load_dwordx4 v207, s[20:23], s53 offen offset:2048 nt lds\n"
"s_mov_b32 m0, 0x4380\n"
"s_nop 0\n"
"buffer_load_dwordx4 v206, s[20:23], s53 offen offset:128 nt lds\n"
"buffer_load_dwordx4 v207, s[20:23], s53 offen offset:2176 nt lds\n"
"s_add_u32 s53, s33, 0x1a000\n"
"s_and_b32 s53, s53, 0x3ffff\n"
"s_add_u32 s53, s53, 0x400000\n"
"s_mov_b32 m0, 0x6000\n"
"s_nop 0\n"
"buffer_load_dwordx4 v206, s[20:23], s53 offen nt lds\n"
"buffer_load_dwordx4 v207, s[20:23], s53 offen offset:2048 nt lds\n"
"s_mov_b32 m0, 0x6380\n"
"s_nop 0\n"
"buffer_load_dwordx4 v206, s[20:23], s53 offen offset:128 nt lds\n"
"buffer_load_dwordx4 v207, s[20:23], s53 offen offset:2176 nt lds\n"
"s_waitcnt vmcnt(16)\n"
"s_barrier\n"
"s_waitcnt lgkmcnt(0)\n"
".Lk1_pc96a:\n"
"s_memrealtime s[72:73]\n"
"s_waitcnt lgkmcnt(0)\n"
"s_sub_u32 s73, s72, s65\n"
"s_cmp_lt_u32 s73, 126\n"
"s_cbranch_scc0 .Lk1_pc96b\n"
"s_sleep 1\n"
"s_branch .Lk1_pc96a\n"
".Lk1_pc96b:\n"
"s_mov_b32 s65, s72\n"
"s_add_u32 s53, s33, 0x1b000\n"
"s_and_b32 s53, s53, 0x3ffff\n"
"s_add_u32 s53, s53, 0x400000\n"
"s_mov_b32 m0, 0x15000\n"
"s_nop 0\n"
"buffer_load_dwordx4 v208, s[24:27], s53 offen nt lds\n"
"buffer_load_dwordx4 v208, s[24:27], s53 offen offset:2048 nt lds\n"
"s_mov_b32 m0, 0x15380\n"
"s_nop 0\n"
"buffer_load_dwordx4 v208, s[24:27], s53 offen offset:128 nt lds\n"
"buffer_load_dwordx4 v208, s[24:27], s53 offen offset:2176 nt lds\n"
"s_add_u32 s53, s33, 0x1d000\n"
"s_and_b32 s53, s53, 0x3ffff\n"
"s_add_u32 s53, s53, 0x400000\n"
"s_mov_b32 m0, 0x17000\n"
"s_nop 0\n"
"buffer_load_dwordx4 v208, s[24:27], s53 offen nt lds\n"
"buffer_load_dwordx4 v208, s[24:27], s53 offen offset:2048 nt lds\n"
"s_mov_b32 m0, 0x17380\n"
"s_nop 0\n"
"buffer_load_dwordx4 v208, s[24:27], s53 offen offset:128 nt lds\n"
"buffer_load_dwordx4 v208, s[24:27], s53 offen offset:2176 nt lds\n"
"s_add_u32 s53, s33, 0x1c000\n"
"s_and_b32 s53, s53, 0x3ffff\n"
"s_add_u32 s53, s53, 0x400000\n"
"s_mov_b32 m0, 0x8000\n"
"s_nop 0\n"
"buffer_load_dwordx4 v206, s[20:23], s53 offen nt lds\n"
"buffer_load_dwordx4 v207, s[20:23], s53 offen offset:2048 nt lds\n"
"s_mov_b32 m0, 0x8380\n"
"s_nop 0\n"
"buffer_load_dwordx4 v206, s[20:23], s53 offen offset:128 nt lds\n"
"buffer_load_dwordx4 v207, s[20:23], s53 offen offset:2176 nt lds\n"
"s_add_u32 s53, s33, 0x1e000\n"
"s_and_b32 s53, s53, 0x3ffff\n"
"s_add_u32 s53, s53, 0x400000\n"
"s_mov_b32 m0, 0xa000\n"
"s_nop 0\n"
"buffer_load_dwordx4 v206, s[20:23], s53 offen nt lds\n"
"buffer_load_dwordx4 v207, s[20:23], s53 offen offset:2048 nt lds\n"
"s_mov_b32 m0, 0xa380\n"
"s_nop 0\n"
"buffer_load_dwordx4 v206, s[20:23], s53 offen offset:128 nt lds\n"
"buffer_load_dwordx4 v207, s[20:23], s53 offen offset:2176 nt lds\n"
"s_waitcnt vmcnt(16)\n"
"s_barrier\n"
"s_waitcnt lgkmcnt(0)\n"
".Lk1_pc97a:\n"
"s_memrealtime s[72:73]\n"
"s_waitcnt lgkmcnt(0)\n"
"s_sub_u32 s73, s72, s65\n"
"s_cmp_lt_u32 s73, 126\n"
"s_cbranch_scc0 .Lk1_pc97b\n"
"s_sleep 1\n"
"s_branch .Lk1_pc97a\n"
".Lk1_pc97b:\n"
"s_mov_b32 s65, s72\n"
"s_add_u32 s53, s33, 0x1f000\n"
"s_and_b32 s53, s53, 0x3ffff\n"
"s_add_u32 s53, s53, 0x400000\n"
"s_mov_b32 m0, 0xd000\n"
"s_nop 0\n"
"buffer_load_dwordx4 v208, s[24:27], s53 offen nt lds\n"
"buffer_load_dwordx4 v208, s[24:27], s53 offen offset:2048 nt lds\n"
"s_mov_b32 m0, 0xd380\n"
"s_nop 0\n"
"buffer_load_dwordx4 v208, s[24:27], s53 offen offset:128 nt lds\n"
"buffer_load_dwordx4 v208, s[24:27], s53 offen offset:2176 nt lds\n"
"s_add_u32 s53, s33, 0x21000\n"
"s_and_b32 s53, s53, 0x3ffff\n"
"s_add_u32 s53, s53, 0x400000\n"
"s_mov_b32 m0, 0xf000\n"
"s_nop 0\n"
"buffer_load_dwordx4 v208, s[24:27], s53 offen nt lds\n"
"buffer_load_dwordx4 v208, s[24:27], s53 offen offset:2048 nt lds\n"
"s_mov_b32 m0, 0xf380\n"
"s_nop 0\n"
"buffer_load_dwordx4 v208, s[24:27], s53 offen offset:128 nt lds\n"
"buffer_load_dwordx4 v208, s[24:27], s53 offen offset:2176 nt lds\n"
"s_add_u32 s53, s33, 0x20000\n"
"s_and_b32 s53, s53, 0x3ffff\n"
"s_add_u32 s53, s53, 0x400000\n"
"s_mov_b32 m0, 0\n"
"s_nop 0\n"
"buffer_load_dwordx4 v206, s[20:23], s53 offen nt lds\n"
"buffer_load_dwordx4 v207, s[20:23], s53 offen offset:2048 nt lds\n"
"s_mov_b32 m0, 0x380\n"
"s_nop 0\n"
"buffer_load_dwordx4 v206, s[20:23], s53 offen offset:128 nt lds\n"
"buffer_load_dwordx4 v207, s[20:23], s53 offen offset:2176 nt lds\n"
"s_add_u32 s53, s33, 0x22000\n"
"s_and_b32 s53, s53, 0x3ffff\n"
"s_add_u32 s53, s53, 0x400000\n"
"s_mov_b32 m0, 0x2000\n"
"s_nop 0\n"
"buffer_load_dwordx4 v206, s[20:23], s53 offen nt lds\n"
"buffer_load_dwordx4 v207, s[20:23], s53 offen offset:2048 nt lds\n"
"s_mov_b32 m0, 0x2380\n"
"s_nop 0\n"
"buffer_load_dwordx4 v206, s[20:23], s53 offen offset:128 nt lds\n"
"buffer_load_dwordx4 v207, s[20:23], s53 offen offset:2176 nt lds\n"
"s_waitcnt vmcnt(16)\n"
"s_barrier\n"
"s_waitcnt lgkmcnt(0)\n"
".Lk1_pc98a:\n"
"s_memrealtime s[72:73]\n"
"s_waitcnt lgkmcnt(0)\n"
"s_sub_u32 s73, s72, s65\n"
"s_cmp_lt_u32 s73, 126\n"
"s_cbranch_scc0 .Lk1_pc98b\n"
"s_sleep 1\n"
"s_branch .Lk1_pc98a\n"
".Lk1_pc98b:\n"
"s_mov_b32 s65, s72\n"
"s_add_u32 s53, s33, 0x23000\n"
"s_and_b32 s53, s53, 0x3ffff\n"
"s_add_u32 s53, s53, 0x400000\n"
"s_mov_b32 m0, 0x11000\n"
"s_nop 0\n"
"buffer_load_dwordx4 v208, s[24:27], s53 offen nt lds\n"
"buffer_load_dwordx4 v208, s[24:27], s53 offen offset:2048 nt lds\n"
"s_mov_b32 m0, 0x11380\n"
"s_nop 0\n"
"buffer_load_dwordx4 v208, s[24:27], s53 offen offset:128 nt lds\n"
"buffer_load_dwordx4 v208, s[24:27], s53 offen offset:2176 nt lds\n"
"s_add_u32 s53, s33, 0x25000\n"
"s_and_b32 s53, s53, 0x3ffff\n"
"s_add_u32 s53, s53, 0x400000\n"
"s_mov_b32 m0, 0x13000\n"
"s_nop 0\n"
"buffer_load_dwordx4 v208, s[24:27], s53 offen nt lds\n"
"buffer_load_dwordx4 v208, s[24:27], s53 offen offset:2048 nt lds\n"
"s_mov_b32 m0, 0x13380\n"
"s_nop 0\n"
"buffer_load_dwordx4 v208, s[24:27], s53 offen offset:128 nt lds\n"
"buffer_load_dwordx4 v208, s[24:27], s53 offen offset:2176 nt lds\n"
"s_add_u32 s53, s33, 0x24000\n"
"s_and_b32 s53, s53, 0x3ffff\n"
"s_add_u32 s53, s53, 0x400000\n"
"s_mov_b32 m0, 0x4000\n"
"s_nop 0\n"
"buffer_load_dwordx4 v206, s[20:23], s53 offen nt lds\n"
"buffer_load_dwordx4 v207, s[20:23], s53 offen offset:2048 nt lds\n"
"s_mov_b32 m0, 0x4380\n"
"s_nop 0\n"
"buffer_load_dwordx4 v206, s[20:23], s53 offen offset:128 nt lds\n"
"buffer_load_dwordx4 v207, s[20:23], s53 offen offset:2176 nt lds\n"
"s_add_u32 s53, s33, 0x26000\n"
"s_and_b32 s53, s53, 0x3ffff\n"
"s_add_u32 s53, s53, 0x400000\n"
"s_mov_b32 m0, 0x6000\n"
"s_nop 0\n"
"buffer_load_dwordx4 v206, s[20:23], s53 offen nt lds\n"
"buffer_load_dwordx4 v207, s[20:23], s53 offen offset:2048 nt lds\n"
"s_mov_b32 m0, 0x6380\n"
"s_nop 0\n"
"buffer_load_dwordx4 v206, s[20:23], s53 offen offset:128 nt lds\n"
"buffer_load_dwordx4 v207, s[20:23], s53 offen offset:2176 nt lds\n"
"s_waitcnt vmcnt(16)\n"
"s_barrier\n"
"s_waitcnt lgkmcnt(0)\n"
".Lk1_pc99a:\n"
"s_memrealtime s[72:73]\n"
"s_waitcnt lgkmcnt(0)\n"
"s_sub_u32 s73, s72, s65\n"
"s_cmp_lt_u32 s73, 126\n"
"s_cbranch_scc0 .Lk1_pc99b\n"
"s_sleep 1\n"
"s_branch .Lk1_pc99a\n"
".Lk1_pc99b:\n"
"s_mov_b32 s65, s72\n"
"s_add_u32 s53, s33, 0x27000\n"
"s_and_b32 s53, s53, 0x3ffff\n"
"s_add_u32 s53, s53, 0x400000\n"
"s_mov_b32 m0, 0x15000\n"
"s_nop 0\n"
"buffer_load_dwordx4 v208, s[24:27], s53 offen nt lds\n"
"buffer_load_dwordx4 v208, s[24:27], s53 offen offset:2048 nt lds\n"
"s_mov_b32 m0, 0x15380\n"
"s_nop 0\n"
"buffer_load_dwordx4 v208, s[24:27], s53 offen offset:128 nt lds\n"
"buffer_load_dwordx4 v208, s[24:27], s53 offen offset:2176 nt lds\n"
"s_add_u32 s53, s33, 0x29000\n"
"s_and_b32 s53, s53, 0x3ffff\n"
"s_add_u32 s53, s53, 0x400000\n"
"s_mov_b32 m0, 0x17000\n"
"s_nop 0\n"
"buffer_load_dwordx4 v208, s[24:27], s53 offen nt lds\n"
"buffer_load_dwordx4 v208, s[24:27], s53 offen offset:2048 nt lds\n"
"s_mov_b32 m0, 0x17380\n"
"s_nop 0\n"
"buffer_load_dwordx4 v208, s[24:27], s53 offen offset:128 nt lds\n"
"buffer_load_dwordx4 v208, s[24:27], s53 offen offset:2176 nt lds\n"
"s_add_u32 s53, s33, 0x28000\n"
"s_and_b32 s53, s53, 0x3ffff\n"
"s_add_u32 s53, s53, 0x400000\n"
"s_mov_b32 m0, 0x8000\n"
"s_nop 0\n"
"buffer_load_dwordx4 v206, s[20:23], s53 offen nt lds\n"
"buffer_load_dwordx4 v207, s[20:23], s53 offen offset:2048 nt lds\n"
"s_mov_b32 m0, 0x8380\n"
"s_nop 0\n"
"buffer_load_dwordx4 v206, s[20:23], s53 offen offset:128 nt lds\n"
"buffer_load_dwordx4 v207, s[20:23], s53 offen offset:2176 nt lds\n"
"s_add_u32 s53, s33, 0x2a000\n"
"s_and_b32 s53, s53, 0x3ffff\n"
"s_add_u32 s53, s53, 0x400000\n"
"s_mov_b32 m0, 0xa000\n"
"s_nop 0\n"
"buffer_load_dwordx4 v206, s[20:23], s53 offen nt lds\n"
"buffer_load_dwordx4 v207, s[20:23], s53 offen offset:2048 nt lds\n"
"s_mov_b32 m0, 0xa380\n"
"s_nop 0\n"
"buffer_load_dwordx4 v206, s[20:23], s53 offen offset:128 nt lds\n"
"buffer_load_dwordx4 v207, s[20:23], s53 offen offset:2176 nt lds\n"
"s_mul_i32 s47, s57, 0x9000\n"
"s_mov_b32 m0, 0x19000\n"
"s_nop 0\n"
"buffer_load_dwordx4 v242, s[36:39], s47 offen lds\n"
"s_mov_b32 m0, 0x19f80\n"
"s_nop 0\n"
"buffer_load_dwordx4 v242, s[36:39], s47 offen offset:128 lds\n"
"s_mov_b32 m0, 0x1af00\n"
"s_nop 0\n"
"buffer_load_dwordx4 v242, s[36:39], s47 offen offset:256 lds\n"
"s_add_u32 s47, s47, 0x2400\n"
"s_mov_b32 m0, 0x19400\n"
"s_nop 0\n"
"buffer_load_dwordx4 v242, s[36:39], s47 offen lds\n"
"s_mov_b32 m0, 0x1a380\n"
"s_nop 0\n"
"buffer_load_dwordx4 v242, s[36:39], s47 offen offset:128 lds\n"
"s_mov_b32 m0, 0x1b300\n"
"s_nop 0\n"
"buffer_load_dwordx4 v242, s[36:39], s47 offen offset:256 lds\n"
"s_add_u32 s47, s47, 0x2400\n"
"s_mov_b32 m0, 0x19800\n"
"s_nop 0\n"
"buffer_load_dwordx4 v242, s[36:39], s47 offen lds\n"
"s_mov_b32 m0, 0x1a780\n"
"s_nop 0\n"
"buffer_load_dwordx4 v242, s[36:39], s47 offen offset:128 lds\n"
"s_mov_b32 m0, 0x1b700\n"
"s_nop 0\n"
"buffer_load_dwordx4 v242, s[36:39], s47 offen offset:256 lds\n"
"s_add_u32 s47, s47, 0x2400\n"
"s_mov_b32 m0, 0x19c00\n"
"s_nop 0\n"
"buffer_load_dwordx4 v242, s[36:39], s47 offen lds\n"
"s_mov_b32 m0, 0x1ab80\n"
"s_nop 0\n"
"buffer_load_dwordx4 v242, s[36:39], s47 offen offset:128 lds\n"
"s_mov_b32 m0, 0x1bb00\n"
"s_nop 0\n"
"buffer_load_dwordx4 v242, s[36:39], s47 offen offset:256 lds\n"
"s_waitcnt vmcnt(28)\n"
"s_barrier\n"
"s_waitcnt lgkmcnt(0)\n"
".Lk1_pc100a:\n"
"s_memrealtime s[72:73]\n"
"s_waitcnt lgkmcnt(0)\n"
"s_sub_u32 s73, s72, s65\n"
"s_cmp_lt_u32 s73, 126\n"
"s_cbranch_scc0 .Lk1_pc100b\n"
"s_sleep 1\n"
"s_branch .Lk1_pc100a\n"
".Lk1_pc100b:\n"
"s_mov_b32 s65, s72\n"
"s_add_u32 s53, s33, 0x2b000\n"
"s_and_b32 s53, s53, 0x3ffff\n"
"s_add_u32 s53, s53, 0x400000\n"
"s_mov_b32 m0, 0xd000\n"
"s_nop 0\n"
"buffer_load_dwordx4 v208, s[24:27], s53 offen nt lds\n"
"buffer_load_dwordx4 v208, s[24:27], s53 offen offset:2048 nt lds\n"
"s_mov_b32 m0, 0xd380\n"
"s_nop 0\n"
"buffer_load_dwordx4 v208, s[24:27], s53 offen offset:128 nt lds\n"
"buffer_load_dwordx4 v208, s[24:27], s53 offen offset:2176 nt lds\n"
"s_add_u32 s53, s33, 0x2d000\n"
"s_and_b32 s53, s53, 0x3ffff\n"
"s_add_u32 s53, s53, 0x400000\n"
"s_mov_b32 m0, 0xf000\n"
"s_nop 0\n"
"buffer_load_dwordx4 v208, s[24:27], s53 offen nt lds\n"
"buffer_load_dwordx4 v208, s[24:27], s53 offen offset:2048 nt lds\n"
"s_mov_b32 m0, 0xf380\n"
"s_nop 0\n"
"buffer_load_dwordx4 v208, s[24:27], s53 offen offset:128 nt lds\n"
"buffer_load_dwordx4 v208, s[24:27], s53 offen offset:2176 nt lds\n"
"s_add_u32 s53, s33, 0x2c000\n"
"s_and_b32 s53, s53, 0x3ffff\n"
"s_add_u32 s53, s53, 0x400000\n"
"s_mov_b32 m0, 0\n"
"s_nop 0\n"
"buffer_load_dwordx4 v206, s[20:23], s53 offen nt lds\n"
"buffer_load_dwordx4 v207, s[20:23], s53 offen offset:2048 nt lds\n"
"s_mov_b32 m0, 0x380\n"
"s_nop 0\n"
"buffer_load_dwordx4 v206, s[20:23], s53 offen offset:128 nt lds\n"
"buffer_load_dwordx4 v207, s[20:23], s53 offen offset:2176 nt lds\n"
"s_add_u32 s53, s33, 0x2e000\n"
"s_and_b32 s53, s53, 0x3ffff\n"
"s_add_u32 s53, s53, 0x400000\n"
"s_mov_b32 m0, 0x2000\n"
"s_nop 0\n"
"buffer_load_dwordx4 v206, s[20:23], s53 offen nt lds\n"
"buffer_load_dwordx4 v207, s[20:23], s53 offen offset:2048 nt lds\n"
"s_mov_b32 m0, 0x2380\n"
"s_nop 0\n"
"buffer_load_dwordx4 v206, s[20:23], s53 offen offset:128 nt lds\n"
"buffer_load_dwordx4 v207, s[20:23], s53 offen offset:2176 nt lds\n"
"s_add_u32 s57, s57, 1\n"
"s_and_b32 s57, s57, 15\n"
"s_mul_i32 s47, s57, 0x9000\n"
"s_mov_b32 m0, 0x1c000\n"
"s_nop 0\n"
"buffer_load_dwordx4 v242, s[36:39], s47 offen lds\n"
"s_mov_b32 m0, 0x1cf80\n"
"s_nop 0\n"
"buffer_load_dwordx4 v242, s[36:39], s47 offen offset:128 lds\n"
"s_mov_b32 m0, 0x1df00\n"
"s_nop 0\n"
"buffer_load_dwordx4 v242, s[36:39], s47 offen offset:256 lds\n"
"s_add_u32 s47, s47, 0x2400\n"
"s_mov_b32 m0, 0x1c400\n"
"s_nop 0\n"
"buffer_load_dwordx4 v242, s[36:39], s47 offen lds\n"
"s_mov_b32 m0, 0x1d380\n"
"s_nop 0\n"
"buffer_load_dwordx4 v242, s[36:39], s47 offen offset:128 lds\n"
"s_mov_b32 m0, 0x1e300\n"
"s_nop 0\n"
"buffer_load_dwordx4 v242, s[36:39], s47 offen offset:256 lds\n"
"s_add_u32 s47, s47, 0x2400\n"
"s_mov_b32 m0, 0x1c800\n"
"s_nop 0\n"
"buffer_load_dwordx4 v242, s[36:39], s47 offen lds\n"
"s_mov_b32 m0, 0x1d780\n"
"s_nop 0\n"
"buffer_load_dwordx4 v242, s[36:39], s47 offen offset:128 lds\n"
"s_mov_b32 m0, 0x1e700\n"
"s_nop 0\n"
"buffer_load_dwordx4 v242, s[36:39], s47 offen offset:256 lds\n"
"s_add_u32 s47, s47, 0x2400\n"
"s_mov_b32 m0, 0x1cc00\n"
"s_nop 0\n"
"buffer_load_dwordx4 v242, s[36:39], s47 offen lds\n"
"s_mov_b32 m0, 0x1db80\n"
"s_nop 0\n"
"buffer_load_dwordx4 v242, s[36:39], s47 offen offset:128 lds\n"
"s_mov_b32 m0, 0x1eb00\n"
"s_nop 0\n"
"buffer_load_dwordx4 v242, s[36:39], s47 offen offset:256 lds\n"
"s_waitcnt vmcnt(40)\n"
"s_barrier\n"
"s_waitcnt lgkmcnt(0)\n"
".Lk1_pc101a:\n"
"s_memrealtime s[72:73]\n"
"s_waitcnt lgkmcnt(0)\n"
"s_sub_u32 s73, s72, s65\n"
"s_cmp_lt_u32 s73, 126\n"
"s_cbranch_scc0 .Lk1_pc101b\n"
"s_sleep 1\n"
"s_branch .Lk1_pc101a\n"
".Lk1_pc101b:\n"
"s_mov_b32 s65, s72\n"
"s_add_u32 s53, s33, 0x2f000\n"
"s_and_b32 s53, s53, 0x3ffff\n"
"s_add_u32 s53, s53, 0x400000\n"
"s_mov_b32 m0, 0x11000\n"
"s_nop 0\n"
"buffer_load_dwordx4 v208, s[24:27], s53 offen nt lds\n"
"buffer_load_dwordx4 v208, s[24:27], s53 offen offset:2048 nt lds\n"
"s_mov_b32 m0, 0x11380\n"
"s_nop 0\n"
"buffer_load_dwordx4 v208, s[24:27], s53 offen offset:128 nt lds\n"
"buffer_load_dwordx4 v208, s[24:27], s53 offen offset:2176 nt lds\n"
"s_add_u32 s53, s33, 0x31000\n"
"s_and_b32 s53, s53, 0x3ffff\n"
"s_add_u32 s53, s53, 0x400000\n"
"s_mov_b32 m0, 0x13000\n"
"s_nop 0\n"
"buffer_load_dwordx4 v208, s[24:27], s53 offen nt lds\n"
"buffer_load_dwordx4 v208, s[24:27], s53 offen offset:2048 nt lds\n"
"s_mov_b32 m0, 0x13380\n"
"s_nop 0\n"
"buffer_load_dwordx4 v208, s[24:27], s53 offen offset:128 nt lds\n"
"buffer_load_dwordx4 v208, s[24:27], s53 offen offset:2176 nt lds\n"
"s_add_u32 s53, s33, 0x30000\n"
"s_and_b32 s53, s53, 0x3ffff\n"
"s_add_u32 s53, s53, 0x400000\n"
"s_mov_b32 m0, 0x4000\n"
"s_nop 0\n"
"buffer_load_dwordx4 v206, s[20:23], s53 offen nt lds\n"
"buffer_load_dwordx4 v207, s[20:23], s53 offen offset:2048 nt lds\n"
"s_mov_b32 m0, 0x4380\n"
"s_nop 0\n"
"buffer_load_dwordx4 v206, s[20:23], s53 offen offset:128 nt lds\n"
"buffer_load_dwordx4 v207, s[20:23], s53 offen offset:2176 nt lds\n"
"s_add_u32 s53, s33, 0x32000\n"
"s_and_b32 s53, s53, 0x3ffff\n"
"s_add_u32 s53, s53, 0x400000\n"
"s_mov_b32 m0, 0x6000\n"
"s_nop 0\n"
"buffer_load_dwordx4 v206, s[20:23], s53 offen nt lds\n"
"buffer_load_dwordx4 v207, s[20:23], s53 offen offset:2048 nt lds\n"
"s_mov_b32 m0, 0x6380\n"
"s_nop 0\n"
"buffer_load_dwordx4 v206, s[20:23], s53 offen offset:128 nt lds\n"
"buffer_load_dwordx4 v207, s[20:23], s53 offen offset:2176 nt lds\n"
"s_add_u32 s57, s57, 1\n"
"s_and_b32 s57, s57, 15\n"
"s_mul_i32 s47, s57, 0x9000\n"
"s_mov_b32 m0, 0x1f000\n"
"s_nop 0\n"
"buffer_load_dwordx4 v242, s[36:39], s47 offen lds\n"
"s_mov_b32 m0, 0x1ff80\n"
"s_nop 0\n"
"buffer_load_dwordx4 v242, s[36:39], s47 offen offset:128 lds\n"
"s_mov_b32 m0, 0x20f00\n"
"s_nop 0\n"
"buffer_load_dwordx4 v242, s[36:39], s47 offen offset:256 lds\n"
"s_add_u32 s47, s47, 0x2400\n"
"s_waitcnt vmcnt(55)\n"
"s_mov_b32 m0, 0x1f400\n"
"s_nop 0\n"
"buffer_load_dwordx4 v242, s[36:39], s47 offen lds\n"
"s_mov_b32 m0, 0x20380\n"
"s_nop 0\n"
"buffer_load_dwordx4 v242, s[36:39], s47 offen offset:128 lds\n"
"s_mov_b32 m0, 0x21300\n"
"s_nop 0\n"
"buffer_load_dwordx4 v242, s[36:39], s47 offen offset:256 lds\n"
"s_add_u32 s47, s47, 0x2400\n"
"s_mov_b32 m0, 0x1f800\n"
"s_nop 0\n"
"buffer_load_dwordx4 v242, s[36:39], s47 offen lds\n"
"s_mov_b32 m0, 0x20780\n"
"s_nop 0\n"
"buffer_load_dwordx4 v242, s[36:39], s47 offen offset:128 lds\n"
"s_mov_b32 m0, 0x21700\n"
"s_waitcnt vmcnt(56)\n"
"buffer_load_dwordx4 v242, s[36:39], s47 offen offset:256 lds\n"
"s_add_u32 s47, s47, 0x2400\n"
"s_mov_b32 m0, 0x1fc00\n"
"s_nop 0\n"
"buffer_load_dwordx4 v242, s[36:39], s47 offen lds\n"
"s_mov_b32 m0, 0x20b80\n"
"s_nop 0\n"
"buffer_load_dwordx4 v242, s[36:39], s47 offen offset:128 lds\n"
"s_mov_b32 m0, 0x21b00\n"
"s_nop 0\n"
"buffer_load_dwordx4 v242, s[36:39], s47 offen offset:256 lds\n"
"s_waitcnt vmcnt(40)\n"
"s_barrier\n"
"s_waitcnt lgkmcnt(0)\n"
".Lk1_pc102a:\n"
"s_memrealtime s[72:73]\n"
"s_waitcnt lgkmcnt(0)\n"
"s_sub_u32 s73, s72, s65\n"
"s_cmp_lt_u32 s73, 126\n"
"s_cbranch_scc0 .Lk1_pc102b\n"
"s_sleep 1\n"
"s_branch .Lk1_pc102a\n"
".Lk1_pc102b:\n"
"s_mov_b32 s65, s72\n"
"s_add_u32 s53, s33, 0x33000\n"
"s_and_b32 s53, s53, 0x3ffff\n"
"s_add_u32 s53, s53, 0x400000\n"
"s_mov_b32 m0, 0x15000\n"
"s_nop 0\n"
"buffer_load_dwordx4 v208, s[24:27], s53 offen nt lds\n"
"buffer_load_dwordx4 v208, s[24:27], s53 offen offset:2048 nt lds\n"
"s_mov_b32 m0, 0x15380\n"
"s_nop 0\n"
"buffer_load_dwordx4 v208, s[24:27], s53 offen offset:128 nt lds\n"
"buffer_load_dwordx4 v208, s[24:27], s53 offen offset:2176 nt lds\n"
"s_add_u32 s53, s33, 0x35000\n"
"s_and_b32 s53, s53, 0x3ffff\n"
"s_add_u32 s53, s53, 0x400000\n"
"s_mov_b32 m0, 0x17000\n"
"s_nop 0\n"
"buffer_load_dwordx4 v208, s[24:27], s53 offen nt lds\n"
"buffer_load_dwordx4 v208, s[24:27], s53 offen offset:2048 nt lds\n"
"s_mov_b32 m0, 0x17380\n"
"s_nop 0\n"
"buffer_load_dwordx4 v208, s[24:27], s53 offen offset:128 nt lds\n"
"buffer_load_dwordx4 v208, s[24:27], s53 offen offset:2176 nt lds\n"
"s_add_u32 s53, s33, 0x34000\n"
"s_and_b32 s53, s53, 0x3ffff\n"
"s_add_u32 s53, s53, 0x400000\n"
"s_mov_b32 m0, 0x8000\n"
"s_nop 0\n"
"buffer_load_dwordx4 v206, s[20:23], s53 offen nt lds\n"
"buffer_load_dwordx4 v207, s[20:23], s53 offen offset:2048 nt lds\n"
"s_mov_b32 m0, 0x8380\n"
"s_nop 0\n"
"buffer_load_dwordx4 v206, s[20:23], s53 offen offset:128 nt lds\n"
"buffer_load_dwordx4 v207, s[20:23], s53 offen offset:2176 nt lds\n"
"s_add_u32 s53, s33, 0x36000\n"
"s_and_b32 s53, s53, 0x3ffff\n"
"s_add_u32 s53, s53, 0x400000\n"
"s_mov_b32 m0, 0xa000\n"
"s_nop 0\n"
"buffer_load_dwordx4 v206, s[20:23], s53 offen nt lds\n"
"buffer_load_dwordx4 v207, s[20:23], s53 offen offset:2048 nt lds\n"
"s_mov_b32 m0, 0xa380\n"
"s_nop 0\n"
"buffer_load_dwordx4 v206, s[20:23], s53 offen offset:128 nt lds\n"
"buffer_load_dwordx4 v207, s[20:23], s53 offen offset:2176 nt lds\n"
"s_add_u32 s57, s57, 1\n"
"s_and_b32 s57, s57, 15\n"
"s_mul_i32 s47, s57, 0x9000\n"
"s_mov_b32 m0, 0x22000\n"
"s_nop 0\n"
"buffer_load_dwordx4 v242, s[36:39], s47 offen lds\n"
"s_mov_b32 m0, 0x22f80\n"
"s_nop 0\n"
"buffer_load_dwordx4 v242, s[36:39], s47 offen offset:128 lds\n"
"s_mov_b32 m0, 0x23f00\n"
"s_nop 0\n"
"buffer_load_dwordx4 v242, s[36:39], s47 offen offset:256 lds\n"
"s_add_u32 s47, s47, 0x2400\n"
"s_waitcnt vmcnt(55)\n"
"s_mov_b32 m0, 0x22400\n"
"s_nop 0\n"
"buffer_load_dwordx4 v242, s[36:39], s47 offen lds\n"
"s_mov_b32 m0, 0x23380\n"
"s_nop 0\n"
"buffer_load_dwordx4 v242, s[36:39], s47 offen offset:128 lds\n"
"s_mov_b32 m0, 0x24300\n"
"s_nop 0\n"
"buffer_load_dwordx4 v242, s[36:39], s47 offen offset:256 lds\n"
"s_add_u32 s47, s47, 0x2400\n"
"s_mov_b32 m0, 0x22800\n"
"s_nop 0\n"
"buffer_load_dwordx4 v242, s[36:39], s47 offen lds\n"
"s_mov_b32 m0, 0x23780\n"
"s_nop 0\n"
"buffer_load_dwordx4 v242, s[36:39], s47 offen offset:128 lds\n"
"s_mov_b32 m0, 0x24700\n"
"s_waitcnt vmcnt(56)\n"
"buffer_load_dwordx4 v242, s[36:39], s47 offen offset:256 lds\n"
"s_add_u32 s47, s47, 0x2400\n"
"s_mov_b32 m0, 0x22c00\n"
"s_nop 0\n"
"buffer_load_dwordx4 v242, s[36:39], s47 offen lds\n"
"s_mov_b32 m0, 0x23b80\n"
"s_nop 0\n"
"buffer_load_dwordx4 v242, s[36:39], s47 offen offset:128 lds\n"
"s_mov_b32 m0, 0x24b00\n"
"s_nop 0\n"
"buffer_load_dwordx4 v242, s[36:39], s47 offen offset:256 lds\n"
"s_waitcnt vmcnt(40)\n"
"s_barrier\n"
"s_waitcnt lgkmcnt(0)\n"
".Lk1_pc103a:\n"
"s_memrealtime s[72:73]\n"
"s_waitcnt lgkmcnt(0)\n"
"s_sub_u32 s73, s72, s65\n"
"s_cmp_lt_u32 s73, 126\n"
"s_cbranch_scc0 .Lk1_pc103b\n"
"s_sleep 1\n"
"s_branch .Lk1_pc103a\n"
".Lk1_pc103b:\n"
"s_mov_b32 s65, s72\n"
"s_add_u32 s53, s33, 0x37000\n"
"s_and_b32 s53, s53, 0x3ffff\n"
"s_add_u32 s53, s53, 0x400000\n"
"s_mov_b32 m0, 0xd000\n"
"s_nop 0\n"
"buffer_load_dwordx4 v208, s[24:27], s53 offen nt lds\n"
"buffer_load_dwordx4 v208, s[24:27], s53 offen offset:2048 nt lds\n"
"s_mov_b32 m0, 0xd380\n"
"s_nop 0\n"
"buffer_load_dwordx4 v208, s[24:27], s53 offen offset:128 nt lds\n"
"buffer_load_dwordx4 v208, s[24:27], s53 offen offset:2176 nt lds\n"
"s_add_u32 s53, s33, 0x39000\n"
"s_and_b32 s53, s53, 0x3ffff\n"
"s_add_u32 s53, s53, 0x400000\n"
"s_mov_b32 m0, 0xf000\n"
"s_nop 0\n"
"buffer_load_dwordx4 v208, s[24:27], s53 offen nt lds\n"
"buffer_load_dwordx4 v208, s[24:27], s53 offen offset:2048 nt lds\n"
"s_mov_b32 m0, 0xf380\n"
"s_nop 0\n"
"buffer_load_dwordx4 v208, s[24:27], s53 offen offset:128 nt lds\n"
"buffer_load_dwordx4 v208, s[24:27], s53 offen offset:2176 nt lds\n"
"s_add_u32 s53, s33, 0x38000\n"
"s_and_b32 s53, s53, 0x3ffff\n"
"s_add_u32 s53, s53, 0x400000\n"
"s_mov_b32 m0, 0\n"
"s_nop 0\n"
"buffer_load_dwordx4 v206, s[20:23], s53 offen nt lds\n"
"buffer_load_dwordx4 v207, s[20:23], s53 offen offset:2048 nt lds\n"
"s_mov_b32 m0, 0x380\n"
"s_nop 0\n"
"buffer_load_dwordx4 v206, s[20:23], s53 offen offset:128 nt lds\n"
"buffer_load_dwordx4 v207, s[20:23], s53 offen offset:2176 nt lds\n"
"s_add_u32 s53, s33, 0x3a000\n"
"s_and_b32 s53, s53, 0x3ffff\n"
"s_add_u32 s53, s53, 0x400000\n"
"s_mov_b32 m0, 0x2000\n"
"s_nop 0\n"
"buffer_load_dwordx4 v206, s[20:23], s53 offen nt lds\n"
"buffer_load_dwordx4 v207, s[20:23], s53 offen offset:2048 nt lds\n"
"s_mov_b32 m0, 0x2380\n"
"s_nop 0\n"
"buffer_load_dwordx4 v206, s[20:23], s53 offen offset:128 nt lds\n"
"buffer_load_dwordx4 v207, s[20:23], s53 offen offset:2176 nt lds\n"
"s_add_u32 s57, s57, 1\n"
"s_and_b32 s57, s57, 15\n"
"s_mul_i32 s47, s57, 0x9000\n"
"s_mov_b32 m0, 0x25000\n"
"s_nop 0\n"
"buffer_load_dwordx4 v242, s[36:39], s47 offen lds\n"
"s_mov_b32 m0, 0x25f80\n"
"s_nop 0\n"
"buffer_load_dwordx4 v242, s[36:39], s47 offen offset:128 lds\n"
"s_mov_b32 m0, 0x26f00\n"
"s_nop 0\n"
"buffer_load_dwordx4 v242, s[36:39], s47 offen offset:256 lds\n"
"s_add_u32 s47, s47, 0x2400\n"
"s_waitcnt vmcnt(55)\n"
"s_mov_b32 m0, 0x25400\n"
"s_nop 0\n"
"buffer_load_dwordx4 v242, s[36:39], s47 offen lds\n"
"s_mov_b32 m0, 0x26380\n"
"s_nop 0\n"
"buffer_load_dwordx4 v242, s[36:39], s47 offen offset:128 lds\n"
"s_mov_b32 m0, 0x27300\n"
"s_nop 0\n"
"buffer_load_dwordx4 v242, s[36:39], s47 offen offset:256 lds\n"
"s_add_u32 s47, s47, 0x2400\n"
"s_mov_b32 m0, 0x25800\n"
"s_nop 0\n"
"buffer_load_dwordx4 v242, s[36:39], s47 offen lds\n"
"s_mov_b32 m0, 0x26780\n"
"s_nop 0\n"
"buffer_load_dwordx4 v242, s[36:39], s47 offen offset:128 lds\n"
"s_mov_b32 m0, 0x27700\n"
"s_waitcnt vmcnt(56)\n"
"buffer_load_dwordx4 v242, s[36:39], s47 offen offset:256 lds\n"
"s_add_u32 s47, s47, 0x2400\n"
"s_mov_b32 m0, 0x25c00\n"
"s_nop 0\n"
"buffer_load_dwordx4 v242, s[36:39], s47 offen lds\n"
"s_mov_b32 m0, 0x26b80\n"
"s_nop 0\n"
"buffer_load_dwordx4 v242, s[36:39], s47 offen offset:128 lds\n"
"s_mov_b32 m0, 0x27b00\n"
"s_nop 0\n"
"buffer_load_dwordx4 v242, s[36:39], s47 offen offset:256 lds\n"
"s_waitcnt vmcnt(40)\n"
"s_barrier\n"
"s_waitcnt lgkmcnt(0)\n"
".Lk1_pc104a:\n"
"s_memrealtime s[72:73]\n"
"s_waitcnt lgkmcnt(0)\n"
"s_sub_u32 s73, s72, s65\n"
"s_cmp_lt_u32 s73, 126\n"
"s_cbranch_scc0 .Lk1_pc104b\n"
"s_sleep 1\n"
"s_branch .Lk1_pc104a\n"
".Lk1_pc104b:\n"
"s_mov_b32 s65, s72\n"
"s_add_u32 s53, s33, 0x3b000\n"
"s_and_b32 s53, s53, 0x3ffff\n"
"s_add_u32 s53, s53, 0x400000\n"
"s_mov_b32 m0, 0x11000\n"
"s_nop 0\n"
"buffer_load_dwordx4 v208, s[24:27], s53 offen nt lds\n"
"buffer_load_dwordx4 v208, s[24:27], s53 offen offset:2048 nt lds\n"
"s_mov_b32 m0, 0x11380\n"
"s_nop 0\n"
"buffer_load_dwordx4 v208, s[24:27], s53 offen offset:128 nt lds\n"
"buffer_load_dwordx4 v208, s[24:27], s53 offen offset:2176 nt lds\n"
"s_add_u32 s53, s33, 0x3d000\n"
"s_and_b32 s53, s53, 0x3ffff\n"
"s_add_u32 s53, s53, 0x400000\n"
"s_mov_b32 m0, 0x13000\n"
"s_nop 0\n"
"buffer_load_dwordx4 v208, s[24:27], s53 offen nt lds\n"
"buffer_load_dwordx4 v208, s[24:27], s53 offen offset:2048 nt lds\n"
"s_mov_b32 m0, 0x13380\n"
"s_nop 0\n"
"buffer_load_dwordx4 v208, s[24:27], s53 offen offset:128 nt lds\n"
"buffer_load_dwordx4 v208, s[24:27], s53 offen offset:2176 nt lds\n"
"s_add_u32 s53, s33, 0x3c000\n"
"s_and_b32 s53, s53, 0x3ffff\n"
"s_add_u32 s53, s53, 0x400000\n"
"s_mov_b32 m0, 0x4000\n"
"s_nop 0\n"
"buffer_load_dwordx4 v206, s[20:23], s53 offen nt lds\n"
"buffer_load_dwordx4 v207, s[20:23], s53 offen offset:2048 nt lds\n"
"s_mov_b32 m0, 0x4380\n"
"s_nop 0\n"
"buffer_load_dwordx4 v206, s[20:23], s53 offen offset:128 nt lds\n"
"buffer_load_dwordx4 v207, s[20:23], s53 offen offset:2176 nt lds\n"
"s_add_u32 s53, s33, 0x3e000\n"
"s_and_b32 s53, s53, 0x3ffff\n"
"s_add_u32 s53, s53, 0x400000\n"
"s_mov_b32 m0, 0x6000\n"
"s_nop 0\n"
"buffer_load_dwordx4 v206, s[20:23], s53 offen nt lds\n"
"buffer_load_dwordx4 v207, s[20:23], s53 offen offset:2048 nt lds\n"
"s_mov_b32 m0, 0x6380\n"
"s_nop 0\n"
"buffer_load_dwordx4 v206, s[20:23], s53 offen offset:128 nt lds\n"
"buffer_load_dwordx4 v207, s[20:23], s53 offen offset:2176 nt lds\n"
"s_waitcnt vmcnt(28)\n"
"s_barrier\n"
"s_waitcnt lgkmcnt(0)\n"
".Lk1_pc105a:\n"
"s_memrealtime s[72:73]\n"
"s_waitcnt lgkmcnt(0)\n"
"s_sub_u32 s73, s72, s65\n"
"s_cmp_lt_u32 s73, 126\n"
"s_cbranch_scc0 .Lk1_pc105b\n"
"s_sleep 1\n"
"s_branch .Lk1_pc105a\n"
".Lk1_pc105b:\n"
"s_mov_b32 s65, s72\n"
"s_add_u32 s53, s33, 0x3f000\n"
"s_and_b32 s53, s53, 0x3ffff\n"
"s_add_u32 s53, s53, 0x400000\n"
"s_mov_b32 m0, 0x15000\n"
"s_nop 0\n"
"buffer_load_dwordx4 v208, s[24:27], s53 offen nt lds\n"
"buffer_load_dwordx4 v208, s[24:27], s53 offen offset:2048 nt lds\n"
"s_mov_b32 m0, 0x15380\n"
"s_nop 0\n"
"buffer_load_dwordx4 v208, s[24:27], s53 offen offset:128 nt lds\n"
"buffer_load_dwordx4 v208, s[24:27], s53 offen offset:2176 nt lds\n"
"s_add_u32 s57, s57, 1\n"
"s_and_b32 s57, s57, 15\n"
"s_mul_i32 s47, s57, 0x9000\n"
"s_mov_b32 m0, 0x8000\n"
"s_nop 0\n"
"buffer_load_dwordx4 v242, s[36:39], s47 offen lds\n"
"s_mov_b32 m0, 0x8f80\n"
"s_nop 0\n"
"buffer_load_dwordx4 v242, s[36:39], s47 offen offset:128 lds\n"
"s_mov_b32 m0, 0x9f00\n"
"s_nop 0\n"
"buffer_load_dwordx4 v242, s[36:39], s47 offen offset:256 lds\n"
"s_add_u32 s47, s47, 0x2400\n"
"s_mov_b32 m0, 0x8400\n"
"s_nop 0\n"
"buffer_load_dwordx4 v242, s[36:39], s47 offen lds\n"
"s_mov_b32 m0, 0x9380\n"
"s_nop 0\n"
"buffer_load_dwordx4 v242, s[36:39], s47 offen offset:128 lds\n"
"s_mov_b32 m0, 0xa300\n"
"s_nop 0\n"
"buffer_load_dwordx4 v242, s[36:39], s47 offen offset:256 lds\n"
"s_add_u32 s47, s47, 0x2400\n"
"s_mov_b32 m0, 0x8800\n"
"s_nop 0\n"
"buffer_load_dwordx4 v242, s[36:39], s47 offen lds\n"
"s_mov_b32 m0, 0x9780\n"
"s_nop 0\n"
"buffer_load_dwordx4 v242, s[36:39], s47 offen offset:128 lds\n"
"s_mov_b32 m0, 0xa700\n"
"s_nop 0\n"
"buffer_load_dwordx4 v242, s[36:39], s47 offen offset:256 lds\n"
"s_add_u32 s47, s47, 0x2400\n"
"s_mov_b32 m0, 0x8c00\n"
"s_nop 0\n"
"buffer_load_dwordx4 v242, s[36:39], s47 offen lds\n"
"s_mov_b32 m0, 0x9b80\n"
"s_nop 0\n"
"buffer_load_dwordx4 v242, s[36:39], s47 offen offset:128 lds\n"
"s_mov_b32 m0, 0xab00\n"
"s_nop 0\n"
"buffer_load_dwordx4 v242, s[36:39], s47 offen offset:256 lds\n"
"s_waitcnt vmcnt(16)\n"
"s_barrier\n"
"s_add_u32 s57, s57, 1\n"
"s_and_b32 s57, s57, 15\n"
"s_mul_i32 s47, s57, 0x9000\n"
"s_mov_b32 m0, 0xb000\n"
"s_nop 0\n"
"buffer_load_dwordx4 v242, s[36:39], s47 offen lds\n"
"s_mov_b32 m0, 0xbf80\n"
"s_nop 0\n"
"buffer_load_dwordx4 v242, s[36:39], s47 offen offset:128 lds\n"
"s_mov_b32 m0, 0xcf00\n"
"s_nop 0\n"
"buffer_load_dwordx4 v242, s[36:39], s47 offen offset:256 lds\n"
"s_add_u32 s47, s47, 0x2400\n"
"s_mov_b32 m0, 0xb400\n"
"s_nop 0\n"
"buffer_load_dwordx4 v242, s[36:39], s47 offen lds\n"
"s_mov_b32 m0, 0xc380\n"
"s_nop 0\n"
"buffer_load_dwordx4 v242, s[36:39], s47 offen offset:128 lds\n"
"s_mov_b32 m0, 0xd300\n"
"s_nop 0\n"
"buffer_load_dwordx4 v242, s[36:39], s47 offen offset:256 lds\n"
"s_add_u32 s47, s47, 0x2400\n"
"s_mov_b32 m0, 0xb800\n"
"s_nop 0\n"
"buffer_load_dwordx4 v242, s[36:39], s47 offen lds\n"
"s_mov_b32 m0, 0xc780\n"
"s_nop 0\n"
"buffer_load_dwordx4 v242, s[36:39], s47 offen offset:128 lds\n"
"s_mov_b32 m0, 0xd700\n"
"s_nop 0\n"
"buffer_load_dwordx4 v242, s[36:39], s47 offen offset:256 lds\n"
"s_add_u32 s47, s47, 0x2400\n"
"s_mov_b32 m0, 0xbc00\n"
"s_nop 0\n"
"buffer_load_dwordx4 v242, s[36:39], s47 offen lds\n"
"s_mov_b32 m0, 0xcb80\n"
"s_nop 0\n"
"buffer_load_dwordx4 v242, s[36:39], s47 offen offset:128 lds\n"
"s_mov_b32 m0, 0xdb00\n"
"s_nop 0\n"
"buffer_load_dwordx4 v242, s[36:39], s47 offen offset:256 lds\n"
"s_waitcnt vmcnt(24)\n"
"s_barrier\n"
"s_mov_b64 exec, 1\n"
"ds_add_u32 v224, v225\n"
"s_mov_b64 exec, -1\n"
"s_add_u32 s57, s57, 1\n"
"s_and_b32 s57, s57, 15\n"
"s_mul_i32 s47, s57, 0x9000\n"
"s_mov_b32 m0, 0xe000\n"
"s_nop 0\n"
"buffer_load_dwordx4 v242, s[36:39], s47 offen lds\n"
"s_mov_b32 m0, 0xef80\n"
"s_nop 0\n"
"buffer_load_dwordx4 v242, s[36:39], s47 offen offset:128 lds\n"
"s_mov_b32 m0, 0xff00\n"
"s_nop 0\n"
"buffer_load_dwordx4 v242, s[36:39], s47 offen offset:256 lds\n"
"s_add_u32 s47, s47, 0x2400\n"
"s_mov_b32 m0, 0xe400\n"
"s_nop 0\n"
"buffer_load_dwordx4 v242, s[36:39], s47 offen lds\n"
"s_mov_b32 m0, 0xf380\n"
"s_nop 0\n"
"buffer_load_dwordx4 v242, s[36:39], s47 offen offset:128 lds\n"
"s_mov_b32 m0, 0x10300\n"
"s_nop 0\n"
"buffer_load_dwordx4 v242, s[36:39], s47 offen offset:256 lds\n"
"s_add_u32 s47, s47, 0x2400\n"
"s_mov_b32 m0, 0xe800\n"
"s_nop 0\n"
"buffer_load_dwordx4 v242, s[36:39], s47 offen lds\n"
"s_mov_b32 m0, 0xf780\n"
"s_nop 0\n"
"buffer_load_dwordx4 v242, s[36:39], s47 offen offset:128 lds\n"
"s_mov_b32 m0, 0x10700\n"
"s_nop 0\n"
"buffer_load_dwordx4 v242, s[36:39], s47 offen offset:256 lds\n"
"s_add_u32 s47, s47, 0x2400\n"
"s_mov_b32 m0, 0xec00\n"
"s_nop 0\n"
"buffer_load_dwordx4 v242, s[36:39], s47 offen lds\n"
"s_mov_b32 m0, 0xfb80\n"
"s_nop 0\n"
"buffer_load_dwordx4 v242, s[36:39], s47 offen offset:128 lds\n"
"s_mov_b32 m0, 0x10b00\n"
"s_nop 0\n"
"buffer_load_dwordx4 v242, s[36:39], s47 offen offset:256 lds\n"
"s_add_u32 s57, s57, 1\n"
"s_and_b32 s57, s57, 15\n"
"s_mul_i32 s47, s57, 0x9000\n"
"s_mov_b32 m0, 0x11000\n"
"s_nop 0\n"
"buffer_load_dwordx4 v242, s[36:39], s47 offen lds\n"
"s_mov_b32 m0, 0x11f80\n"
"s_nop 0\n"
"buffer_load_dwordx4 v242, s[36:39], s47 offen offset:128 lds\n"
"s_mov_b32 m0, 0x12f00\n"
"s_nop 0\n"
"buffer_load_dwordx4 v242, s[36:39], s47 offen offset:256 lds\n"
"s_add_u32 s47, s47, 0x2400\n"
"s_mov_b32 m0, 0x11400\n"
"s_nop 0\n"
"buffer_load_dwordx4 v242, s[36:39], s47 offen lds\n"
"s_mov_b32 m0, 0x12380\n"
"s_nop 0\n"
"buffer_load_dwordx4 v242, s[36:39], s47 offen offset:128 lds\n"
"s_mov_b32 m0, 0x13300\n"
"s_nop 0\n"
"buffer_load_dwordx4 v242, s[36:39], s47 offen offset:256 lds\n"
"s_add_u32 s47, s47, 0x2400\n"
"s_mov_b32 m0, 0x11800\n"
"s_nop 0\n"
"buffer_load_dwordx4 v242, s[36:39], s47 offen lds\n"
"s_mov_b32 m0, 0x12780\n"
"s_nop 0\n"
"buffer_load_dwordx4 v242, s[36:39], s47 offen offset:128 lds\n"
"s_mov_b32 m0, 0x13700\n"
"s_nop 0\n"
"buffer_load_dwordx4 v242, s[36:39], s47 offen offset:256 lds\n"
"s_add_u32 s47, s47, 0x2400\n"
"s_mov_b32 m0, 0x11c00\n"
"s_nop 0\n"
"buffer_load_dwordx4 v242, s[36:39], s47 offen lds\n"
"s_mov_b32 m0, 0x12b80\n"
"s_nop 0\n"
"buffer_load_dwordx4 v242, s[36:39], s47 offen offset:128 lds\n"
"s_mov_b32 m0, 0x13b00\n"
"s_nop 0\n"
"buffer_load_dwordx4 v242, s[36:39], s47 offen offset:256 lds\n"
"s_mov_b64 exec, 1\n"
"ds_add_u32 v224, v225 offset:20\n"
"s_mov_b64 exec, -1\n"
"ds_read_b32 v236, v224\n"
"s_waitcnt lgkmcnt(0)\n"
"v_readfirstlane_b32 s60, v236\n"
"s_nop 0\n"
"s_cmp_ge_u32 s60, 5\n"
"s_cbranch_scc0 .Lk1_sp_h2\n"
".Lk1_spr_h2:\n"
"s_waitcnt vmcnt(36)\n"
"s_mov_b64 exec, 1\n"
"ds_add_u32 v224, v225\n"
"s_mov_b64 exec, -1\n"
"ds_read_b32 v236, v224 offset:20\n"
"s_waitcnt lgkmcnt(0)\n"
"v_readfirstlane_b32 s60, v236\n"
"s_nop 0\n"
"s_cmp_ge_u32 s60, 5\n"
"s_cbranch_scc0 .Lk1_sp_h3\n"
".Lk1_spr_h3:\n"
"s_waitcnt vmcnt(24)\n"
"s_mov_b64 exec, 1\n"
"ds_add_u32 v224, v225 offset:20\n"
"s_mov_b64 exec, -1\n"
"ds_read_b32 v236, v224\n"
"s_waitcnt lgkmcnt(0)\n"
"v_readfirstlane_b32 s60, v236\n"
"s_nop 0\n"
"s_cmp_ge_u32 s60, 10\n"
"s_cbranch_scc0 .Lk1_sp_h4\n"
".Lk1_spr_h4:\n"
"s_waitcnt vmcnt(12)\n"
"s_mov_b64 exec, 1\n"
"ds_add_u32 v224, v225\n"
"s_mov_b64 exec, -1\n"
"s_add_u32 s57, s57, 1\n"
"s_and_b32 s57, s57, 15\n"
"s_mul_i32 s47, s57, 0x9000\n"
"s_mov_b32 m0, 0x19000\n"
"s_nop 0\n"
"buffer_load_dwordx4 v242, s[36:39], s47 offen lds\n"
"s_mov_b32 m0, 0x19f80\n"
"s_nop 0\n"
"buffer_load_dwordx4 v242, s[36:39], s47 offen offset:128 lds\n"
"s_mov_b32 m0, 0x1af00\n"
"s_nop 0\n"
"buffer_load_dwordx4 v242, s[36:39], s47 offen offset:256 lds\n"
"s_add_u32 s47, s47, 0x2400\n"
"s_mov_b32 m0, 0x19400\n"
"s_nop 0\n"
"buffer_load_dwordx4 v242, s[36:39], s47 offen lds\n"
"s_mov_b32 m0, 0x1a380\n"
"s_nop 0\n"
"buffer_load_dwordx4 v242, s[36:39], s47 offen offset:128 lds\n"
"s_mov_b32 m0, 0x1b300\n"
"s_nop 0\n"
"buffer_load_dwordx4 v242, s[36:39], s47 offen offset:256 lds\n"
"s_add_u32 s47, s47, 0x2400\n"
"s_mov_b32 m0, 0x19800\n"
"s_nop 0\n"
"buffer_load_dwordx4 v242, s[36:39], s47 offen lds\n"
"s_mov_b32 m0, 0x1a780\n"
"s_nop 0\n"
"buffer_load_dwordx4 v242, s[36:39], s47 offen offset:128 lds\n"
"s_mov_b32 m0, 0x1b700\n"
"s_nop 0\n"
"buffer_load_dwordx4 v242, s[36:39], s47 offen offset:256 lds\n"
"s_add_u32 s47, s47, 0x2400\n"
"s_mov_b32 m0, 0x19c00\n"
"s_nop 0\n"
"buffer_load_dwordx4 v242, s[36:39], s47 offen lds\n"
"s_mov_b32 m0, 0x1ab80\n"
"s_nop 0\n"
"buffer_load_dwordx4 v242, s[36:39], s47 offen offset:128 lds\n"
"s_mov_b32 m0, 0x1bb00\n"
"s_nop 0\n"
"buffer_load_dwordx4 v242, s[36:39], s47 offen offset:256 lds\n"
"s_add_u32 s57, s57, 1\n"
"s_and_b32 s57, s57, 15\n"
"s_mul_i32 s47, s57, 0x9000\n"
"s_mov_b32 m0, 0x1c000\n"
"s_nop 0\n"
"buffer_load_dwordx4 v242, s[36:39], s47 offen lds\n"
"s_mov_b32 m0, 0x1cf80\n"
"s_nop 0\n"
"buffer_load_dwordx4 v242, s[36:39], s47 offen offset:128 lds\n"
"s_mov_b32 m0, 0x1df00\n"
"s_nop 0\n"
"buffer_load_dwordx4 v242, s[36:39], s47 offen offset:256 lds\n"
"s_add_u32 s47, s47, 0x2400\n"
"s_mov_b32 m0, 0x1c400\n"
"s_nop 0\n"
"buffer_load_dwordx4 v242, s[36:39], s47 offen lds\n"
"s_mov_b32 m0, 0x1d380\n"
"s_nop 0\n"
"buffer_load_dwordx4 v242, s[36:39], s47 offen offset:128 lds\n"
"s_mov_b32 m0, 0x1e300\n"
"s_nop 0\n"
"buffer_load_dwordx4 v242, s[36:39], s47 offen offset:256 lds\n"
"s_add_u32 s47, s47, 0x2400\n"
"s_mov_b32 m0, 0x1c800\n"
"s_nop 0\n"
"buffer_load_dwordx4 v242, s[36:39], s47 offen lds\n"
"s_mov_b32 m0, 0x1d780\n"
"s_nop 0\n"
"buffer_load_dwordx4 v242, s[36:39], s47 offen offset:128 lds\n"
"s_mov_b32 m0, 0x1e700\n"
"s_nop 0\n"
"buffer_load_dwordx4 v242, s[36:39], s47 offen offset:256 lds\n"
"s_add_u32 s47, s47, 0x2400\n"
"s_mov_b32 m0, 0x1cc00\n"
"s_nop 0\n"
"buffer_load_dwordx4 v242, s[36:39], s47 offen lds\n"
"s_mov_b32 m0, 0x1db80\n"
"s_nop 0\n"
"buffer_load_dwordx4 v242, s[36:39], s47 offen offset:128 lds\n"
"s_mov_b32 m0, 0x1eb00\n"
"s_nop 0\n"
"buffer_load_dwordx4 v242, s[36:39], s47 offen offset:256 lds\n"
"ds_read_b32 v236, v224 offset:20\n"
"s_waitcnt lgkmcnt(0)\n"
"v_readfirstlane_b32 s60, v236\n"
"s_nop 0\n"
"s_cmp_ge_u32 s60, 10\n"
"s_cbranch_scc0 .Lk1_sp_h5\n"
".Lk1_spr_h5:\n"
"s_waitcnt vmcnt(24)\n"
"s_mov_b64 exec, 1\n"
"ds_add_u32 v224, v225 offset:20\n"
"s_mov_b64 exec, -1\n"
"s_add_u32 s57, s57, 1\n"
"s_and_b32 s57, s57, 15\n"
"s_mul_i32 s47, s57, 0x9000\n"
"s_mov_b32 m0, 0x1f000\n"
"s_nop 0\n"
"buffer_load_dwordx4 v242, s[36:39], s47 offen lds\n"
"s_mov_b32 m0, 0x1ff80\n"
"s_nop 0\n"
"buffer_load_dwordx4 v242, s[36:39], s47 offen offset:128 lds\n"
"s_mov_b32 m0, 0x20f00\n"
"s_nop 0\n"
"buffer_load_dwordx4 v242, s[36:39], s47 offen offset:256 lds\n"
"s_add_u32 s47, s47, 0x2400\n"
"s_mov_b32 m0, 0x1f400\n"
"s_nop 0\n"
"buffer_load_dwordx4 v242, s[36:39], s47 offen lds\n"
"s_mov_b32 m0, 0x20380\n"
"s_nop 0\n"
"buffer_load_dwordx4 v242, s[36:39], s47 offen offset:128 lds\n"
"s_mov_b32 m0, 0x21300\n"
"s_nop 0\n"
"buffer_load_dwordx4 v242, s[36:39], s47 offen offset:256 lds\n"
"s_add_u32 s47, s47, 0x2400\n"
"s_mov_b32 m0, 0x1f800\n"
"s_nop 0\n"
"buffer_load_dwordx4 v242, s[36:39], s47 offen lds\n"
"s_mov_b32 m0, 0x20780\n"
"s_nop 0\n"
"buffer_load_dwordx4 v242, s[36:39], s47 offen offset:128 lds\n"
"s_mov_b32 m0, 0x21700\n"
"s_nop 0\n"
"buffer_load_dwordx4 v242, s[36:39], s47 offen offset:256 lds\n"
"s_add_u32 s47, s47, 0x2400\n"
"s_mov_b32 m0, 0x1fc00\n"
"s_nop 0\n"
"buffer_load_dwordx4 v242, s[36:39], s47 offen lds\n"
"s_mov_b32 m0, 0x20b80\n"
"s_nop 0\n"
"buffer_load_dwordx4 v242, s[36:39], s47 offen offset:128 lds\n"
"s_mov_b32 m0, 0x21b00\n"
"s_nop 0\n"
"buffer_load_dwordx4 v242, s[36:39], s47 offen offset:256 lds\n"
"s_add_u32 s57, s57, 1\n"
"s_and_b32 s57, s57, 15\n"
"s_mul_i32 s47, s57, 0x9000\n"
"s_mov_b32 m0, 0x22000\n"
"s_nop 0\n"
"buffer_load_dwordx4 v242, s[36:39], s47 offen lds\n"
"s_mov_b32 m0, 0x22f80\n"
"s_nop 0\n"
"buffer_load_dwordx4 v242, s[36:39], s47 offen offset:128 lds\n"
"s_mov_b32 m0, 0x23f00\n"
"s_nop 0\n"
"buffer_load_dwordx4 v242, s[36:39], s47 offen offset:256 lds\n"
"s_add_u32 s47, s47, 0x2400\n"
"s_mov_b32 m0, 0x22400\n"
"s_nop 0\n"
"buffer_load_dwordx4 v242, s[36:39], s47 offen lds\n"
"s_mov_b32 m0, 0x23380\n"
"s_nop 0\n"
"buffer_load_dwordx4 v242, s[36:39], s47 offen offset:128 lds\n"
"s_mov_b32 m0, 0x24300\n"
"s_nop 0\n"
"buffer_load_dwordx4 v242, s[36:39], s47 offen offset:256 lds\n"
"s_add_u32 s47, s47, 0x2400\n"
"s_mov_b32 m0, 0x22800\n"
"s_nop 0\n"
"buffer_load_dwordx4 v242, s[36:39], s47 offen lds\n"
"s_mov_b32 m0, 0x23780\n"
"s_nop 0\n"
"buffer_load_dwordx4 v242, s[36:39], s47 offen offset:128 lds\n"
"s_mov_b32 m0, 0x24700\n"
"s_nop 0\n"
"buffer_load_dwordx4 v242, s[36:39], s47 offen offset:256 lds\n"
"s_add_u32 s47, s47, 0x2400\n"
"s_mov_b32 m0, 0x22c00\n"
"s_nop 0\n"
"buffer_load_dwordx4 v242, s[36:39], s47 offen lds\n"
"s_mov_b32 m0, 0x23b80\n"
"s_nop 0\n"
"buffer_load_dwordx4 v242, s[36:39], s47 offen offset:128 lds\n"
"s_mov_b32 m0, 0x24b00\n"
"s_nop 0\n"
"buffer_load_dwordx4 v242, s[36:39], s47 offen offset:256 lds\n"
"ds_read_b32 v236, v224\n"
"s_waitcnt lgkmcnt(0)\n"
"v_readfirstlane_b32 s60, v236\n"
"s_nop 0\n"
"s_cmp_ge_u32 s60, 15\n"
"s_cbranch_scc0 .Lk1_sp_h6\n"
".Lk1_spr_h6:\n"
"s_waitcnt vmcnt(36)\n"
"s_mov_b64 exec, 1\n"
"ds_add_u32 v224, v225\n"
"s_mov_b64 exec, -1\n"
"s_add_u32 s57, s57, 1\n"
"s_and_b32 s57, s57, 15\n"
"s_mul_i32 s47, s57, 0x9000\n"
"s_mov_b32 m0, 0x25000\n"
"s_nop 0\n"
"buffer_load_dwordx4 v242, s[36:39], s47 offen lds\n"
"s_mov_b32 m0, 0x25f80\n"
"s_nop 0\n"
"buffer_load_dwordx4 v242, s[36:39], s47 offen offset:128 lds\n"
"s_mov_b32 m0, 0x26f00\n"
"s_nop 0\n"
"buffer_load_dwordx4 v242, s[36:39], s47 offen offset:256 lds\n"
"s_add_u32 s47, s47, 0x2400\n"
"s_mov_b32 m0, 0x25400\n"
"s_nop 0\n"
"buffer_load_dwordx4 v242, s[36:39], s47 offen lds\n"
"s_mov_b32 m0, 0x26380\n"
"s_nop 0\n"
"buffer_load_dwordx4 v242, s[36:39], s47 offen offset:128 lds\n"
"s_mov_b32 m0, 0x27300\n"
"s_nop 0\n"
"buffer_load_dwordx4 v242, s[36:39], s47 offen offset:256 lds\n"
"s_add_u32 s47, s47, 0x2400\n"
"s_mov_b32 m0, 0x25800\n"
"s_nop 0\n"
"buffer_load_dwordx4 v242, s[36:39], s47 offen lds\n"
"s_mov_b32 m0, 0x26780\n"
"s_nop 0\n"
"buffer_load_dwordx4 v242, s[36:39], s47 offen offset:128 lds\n"
"s_mov_b32 m0, 0x27700\n"
"s_nop 0\n"
"buffer_load_dwordx4 v242, s[36:39], s47 offen offset:256 lds\n"
"s_add_u32 s47, s47, 0x2400\n"
"s_mov_b32 m0, 0x25c00\n"
"s_nop 0\n"
"buffer_load_dwordx4 v242, s[36:39], s47 offen lds\n"
"s_mov_b32 m0, 0x26b80\n"
"s_nop 0\n"
"buffer_load_dwordx4 v242, s[36:39], s47 offen offset:128 lds\n"
"s_mov_b32 m0, 0x27b00\n"
"s_nop 0\n"
"buffer_load_dwordx4 v242, s[36:39], s47 offen offset:256 lds\n"
"ds_read_b32 v236, v224 offset:20\n"
"s_waitcnt lgkmcnt(0)\n"
"v_readfirstlane_b32 s60, v236\n"
"s_nop 0\n"
"s_cmp_ge_u32 s60, 15\n"
"s_cbranch_scc0 .Lk1_sp_h7\n"
".Lk1_spr_h7:\n"
"s_waitcnt vmcnt(36)\n"
"s_mov_b64 exec, 1\n"
"ds_add_u32 v224, v225 offset:20\n"
"s_mov_b64 exec, -1\n"
"s_add_u32 s57, s57, 1\n"
"s_and_b32 s57, s57, 15\n"
"s_mul_i32 s47, s57, 0x9000\n"
"s_mov_b32 m0, 0x8000\n"
"s_nop 0\n"
"buffer_load_dwordx4 v242, s[36:39], s47 offen lds\n"
"s_mov_b32 m0, 0x8f80\n"
"s_nop 0\n"
"buffer_load_dwordx4 v242, s[36:39], s47 offen offset:128 lds\n"
"s_mov_b32 m0, 0x9f00\n"
"s_nop 0\n"
"buffer_load_dwordx4 v242, s[36:39], s47 offen offset:256 lds\n"
"s_add_u32 s47, s47, 0x2400\n"
"s_mov_b32 m0, 0x8400\n"
"s_nop 0\n"
"buffer_load_dwordx4 v242, s[36:39], s47 offen lds\n"
"s_mov_b32 m0, 0x9380\n"
"s_nop 0\n"
"buffer_load_dwordx4 v242, s[36:39], s47 offen offset:128 lds\n"
"s_mov_b32 m0, 0xa300\n"
"s_nop 0\n"
"buffer_load_dwordx4 v242, s[36:39], s47 offen offset:256 lds\n"
"s_add_u32 s47, s47, 0x2400\n"
"s_mov_b32 m0, 0x8800\n"
"s_nop 0\n"
"buffer_load_dwordx4 v242, s[36:39], s47 offen lds\n"
"s_mov_b32 m0, 0x9780\n"
"s_nop 0\n"
"buffer_load_dwordx4 v242, s[36:39], s47 offen offset:128 lds\n"
"s_mov_b32 m0, 0xa700\n"
"s_nop 0\n"
"buffer_load_dwordx4 v242, s[36:39], s47 offen offset:256 lds\n"
"s_add_u32 s47, s47, 0x2400\n"
"s_mov_b32 m0, 0x8c00\n"
"s_nop 0\n"
"buffer_load_dwordx4 v242, s[36:39], s47 offen lds\n"
"s_mov_b32 m0, 0x9b80\n"
"s_nop 0\n"
"buffer_load_dwordx4 v242, s[36:39], s47 offen offset:128 lds\n"
"s_mov_b32 m0, 0xab00\n"
"s_nop 0\n"
"buffer_load_dwordx4 v242, s[36:39], s47 offen offset:256 lds\n"
"ds_read_b32 v236, v224\n"
"s_waitcnt lgkmcnt(0)\n"
"v_readfirstlane_b32 s60, v236\n"
"s_nop 0\n"
"s_cmp_ge_u32 s60, 20\n"
"s_cbranch_scc0 .Lk1_sp_h8\n"
".Lk1_spr_h8:\n"
"s_waitcnt vmcnt(36)\n"
"s_mov_b64 exec, 1\n"
"ds_add_u32 v224, v225\n"
"s_mov_b64 exec, -1\n"
"s_add_u32 s57, s57, 1\n"
"s_and_b32 s57, s57, 15\n"
"s_mul_i32 s47, s57, 0x9000\n"
"s_mov_b32 m0, 0xb000\n"
"s_nop 0\n"
"buffer_load_dwordx4 v242, s[36:39], s47 offen lds\n"
"s_mov_b32 m0, 0xbf80\n"
"s_nop 0\n"
"buffer_load_dwordx4 v242, s[36:39], s47 offen offset:128 lds\n"
"s_mov_b32 m0, 0xcf00\n"
"s_nop 0\n"
"buffer_load_dwordx4 v242, s[36:39], s47 offen offset:256 lds\n"
"s_add_u32 s47, s47, 0x2400\n"
"s_mov_b32 m0, 0xb400\n"
"s_nop 0\n"
"buffer_load_dwordx4 v242, s[36:39], s47 offen lds\n"
"s_mov_b32 m0, 0xc380\n"
"s_nop 0\n"
"buffer_load_dwordx4 v242, s[36:39], s47 offen offset:128 lds\n"
"s_mov_b32 m0, 0xd300\n"
"s_nop 0\n"
"buffer_load_dwordx4 v242, s[36:39], s47 offen offset:256 lds\n"
"s_add_u32 s47, s47, 0x2400\n"
"s_mov_b32 m0, 0xb800\n"
"s_nop 0\n"
"buffer_load_dwordx4 v242, s[36:39], s47 offen lds\n"
"s_mov_b32 m0, 0xc780\n"
"s_nop 0\n"
"buffer_load_dwordx4 v242, s[36:39], s47 offen offset:128 lds\n"
"s_mov_b32 m0, 0xd700\n"
"s_nop 0\n"
"buffer_load_dwordx4 v242, s[36:39], s47 offen offset:256 lds\n"
"s_add_u32 s47, s47, 0x2400\n"
"s_mov_b32 m0, 0xbc00\n"
"s_nop 0\n"
"buffer_load_dwordx4 v242, s[36:39], s47 offen lds\n"
"s_mov_b32 m0, 0xcb80\n"
"s_nop 0\n"
"buffer_load_dwordx4 v242, s[36:39], s47 offen offset:128 lds\n"
"s_mov_b32 m0, 0xdb00\n"
"s_nop 0\n"
"buffer_load_dwordx4 v242, s[36:39], s47 offen offset:256 lds\n"
"ds_read_b32 v236, v224 offset:20\n"
"s_waitcnt lgkmcnt(0)\n"
"v_readfirstlane_b32 s60, v236\n"
"s_nop 0\n"
"s_cmp_ge_u32 s60, 20\n"
"s_cbranch_scc0 .Lk1_sp_h9\n"
".Lk1_spr_h9:\n"
"s_add_u32 s57, s57, 1\n"
"s_and_b32 s57, s57, 15\n"
"s_mul_i32 s47, s57, 0x9000\n"
"s_add_u32 s47, s47, 0x240000\n"
"s_mov_b32 m0, 0xe000\n"
"s_nop 0\n"
"buffer_load_dwordx4 v242, s[36:39], s47 offen lds\n"
"s_mov_b32 m0, 0xef80\n"
"s_nop 0\n"
"buffer_load_dwordx4 v242, s[36:39], s47 offen offset:128 lds\n"
"s_mov_b32 m0, 0xff00\n"
"s_nop 0\n"
"buffer_load_dwordx4 v242, s[36:39], s47 offen offset:256 lds\n"
"s_add_u32 s47, s47, 0x2400\n"
"s_mov_b32 m0, 0xe400\n"
"s_nop 0\n"
"buffer_load_dwordx4 v242, s[36:39], s47 offen lds\n"
"s_mov_b32 m0, 0xf380\n"
"s_nop 0\n"
"buffer_load_dwordx4 v242, s[36:39], s47 offen offset:128 lds\n"
"s_mov_b32 m0, 0x10300\n"
"s_nop 0\n"
"buffer_load_dwordx4 v242, s[36:39], s47 offen offset:256 lds\n"
"s_add_u32 s47, s47, 0x2400\n"
"s_mov_b32 m0, 0xe800\n"
"s_nop 0\n"
"buffer_load_dwordx4 v242, s[36:39], s47 offen lds\n"
"s_mov_b32 m0, 0xf780\n"
"s_nop 0\n"
"buffer_load_dwordx4 v242, s[36:39], s47 offen offset:128 lds\n"
"s_mov_b32 m0, 0x10700\n"
"s_nop 0\n"
"buffer_load_dwordx4 v242, s[36:39], s47 offen offset:256 lds\n"
"s_add_u32 s47, s47, 0x2400\n"
"s_mov_b32 m0, 0xec00\n"
"s_nop 0\n"
"buffer_load_dwordx4 v242, s[36:39], s47 offen lds\n"
"s_mov_b32 m0, 0xfb80\n"
"s_nop 0\n"
"buffer_load_dwordx4 v242, s[36:39], s47 offen offset:128 lds\n"
"s_mov_b32 m0, 0x10b00\n"
"s_nop 0\n"
"buffer_load_dwordx4 v242, s[36:39], s47 offen offset:256 lds\n"
"ds_read_b32 v236, v224\n"
"s_waitcnt lgkmcnt(0)\n"
"v_readfirstlane_b32 s60, v236\n"
"s_nop 0\n"
"s_cmp_ge_u32 s60, 25\n"
"s_cbranch_scc0 .Lk1_sp_h10\n"
".Lk1_spr_h10:\n"
"s_add_u32 s57, s57, 1\n"
"s_and_b32 s57, s57, 15\n"
"s_mul_i32 s47, s57, 0x9000\n"
"s_add_u32 s47, s47, 0x240000\n"
"s_waitcnt vmcnt(55)\n"
"s_mov_b32 m0, 0x11000\n"
"s_nop 0\n"
"buffer_load_dwordx4 v242, s[36:39], s47 offen lds\n"
"s_mov_b32 m0, 0x11f80\n"
"s_nop 0\n"
"buffer_load_dwordx4 v242, s[36:39], s47 offen offset:128 lds\n"
"s_mov_b32 m0, 0x12f00\n"
"s_nop 0\n"
"buffer_load_dwordx4 v242, s[36:39], s47 offen offset:256 lds\n"
"s_add_u32 s47, s47, 0x2400\n"
"s_mov_b32 m0, 0x11400\n"
"s_nop 0\n"
"buffer_load_dwordx4 v242, s[36:39], s47 offen lds\n"
"s_mov_b32 m0, 0x12380\n"
"s_nop 0\n"
"buffer_load_dwordx4 v242, s[36:39], s47 offen offset:128 lds\n"
"s_mov_b32 m0, 0x13300\n"
"s_waitcnt vmcnt(56)\n"
"buffer_load_dwordx4 v242, s[36:39], s47 offen offset:256 lds\n"
"s_add_u32 s47, s47, 0x2400\n"
"s_mov_b32 m0, 0x11800\n"
"s_nop 0\n"
"buffer_load_dwordx4 v242, s[36:39], s47 offen lds\n"
"s_mov_b32 m0, 0x12780\n"
"s_nop 0\n"
"buffer_load_dwordx4 v242, s[36:39], s47 offen offset:128 lds\n"
"s_mov_b32 m0, 0x13700\n"
"s_nop 0\n"
"buffer_load_dwordx4 v242, s[36:39], s47 offen offset:256 lds\n"
"s_add_u32 s47, s47, 0x2400\n"
"s_waitcnt vmcnt(55)\n"
"s_mov_b32 m0, 0x11c00\n"
"s_nop 0\n"
"buffer_load_dwordx4 v242, s[36:39], s47 offen lds\n"
"s_mov_b32 m0, 0x12b80\n"
"s_nop 0\n"
"buffer_load_dwordx4 v242, s[36:39], s47 offen offset:128 lds\n"
"s_mov_b32 m0, 0x13b00\n"
"s_nop 0\n"
"buffer_load_dwordx4 v242, s[36:39], s47 offen offset:256 lds\n"
"s_waitcnt lgkmcnt(0)\n"
"s_waitcnt vmcnt(48)\n"
"s_barrier\n"
"s_add_u32 s57, s57, 1\n"
"s_and_b32 s57, s57, 15\n"
"s_mul_i32 s47, s57, 0x9000\n"
"s_add_u32 s47, s47, 0x240000\n"
"s_mov_b32 m0, 0x19000\n"
"s_nop 0\n"
"buffer_load_dwordx4 v242, s[36:39], s47 offen lds\n"
"s_mov_b32 m0, 0x19f80\n"
"s_nop 0\n"
"buffer_load_dwordx4 v242, s[36:39], s47 offen offset:128 lds\n"
"s_mov_b32 m0, 0x1af00\n"
"s_nop 0\n"
"buffer_load_dwordx4 v242, s[36:39], s47 offen offset:256 lds\n"
"s_add_u32 s47, s47, 0x2400\n"
"s_mov_b32 m0, 0x19400\n"
"s_nop 0\n"
"buffer_load_dwordx4 v242, s[36:39], s47 offen lds\n"
"s_mov_b32 m0, 0x1a380\n"
"s_nop 0\n"
"buffer_load_dwordx4 v242, s[36:39], s47 offen offset:128 lds\n"
"s_mov_b32 m0, 0x1b300\n"
"s_nop 0\n"
"buffer_load_dwordx4 v242, s[36:39], s47 offen offset:256 lds\n"
"s_add_u32 s47, s47, 0x2400\n"
"s_mov_b32 m0, 0x19800\n"
"s_nop 0\n"
"buffer_load_dwordx4 v242, s[36:39], s47 offen lds\n"
"s_mov_b32 m0, 0x1a780\n"
"s_nop 0\n"
"buffer_load_dwordx4 v242, s[36:39], s47 offen offset:128 lds\n"
"s_mov_b32 m0, 0x1b700\n"
"s_nop 0\n"
"buffer_load_dwordx4 v242, s[36:39], s47 offen offset:256 lds\n"
"s_add_u32 s47, s47, 0x2400\n"
"s_mov_b32 m0, 0x19c00\n"
"s_nop 0\n"
"buffer_load_dwordx4 v242, s[36:39], s47 offen lds\n"
"s_mov_b32 m0, 0x1ab80\n"
"s_nop 0\n"
"buffer_load_dwordx4 v242, s[36:39], s47 offen offset:128 lds\n"
"s_mov_b32 m0, 0x1bb00\n"
"s_nop 0\n"
"buffer_load_dwordx4 v242, s[36:39], s47 offen offset:256 lds\n"
"s_add_u32 s57, s57, 1\n"
"s_and_b32 s57, s57, 15\n"
"s_mul_i32 s47, s57, 0x9000\n"
"s_add_u32 s47, s47, 0x240000\n"
"s_waitcnt vmcnt(55)\n"
"s_mov_b32 m0, 0x1c000\n"
"s_nop 0\n"
"buffer_load_dwordx4 v242, s[36:39], s47 offen lds\n"
"s_mov_b32 m0, 0x1cf80\n"
"s_nop 0\n"
"buffer_load_dwordx4 v242, s[36:39], s47 offen offset:128 lds\n"
"s_mov_b32 m0, 0x1df00\n"
"s_nop 0\n"
"buffer_load_dwordx4 v242, s[36:39], s47 offen offset:256 lds\n"
"s_add_u32 s47, s47, 0x2400\n"
"s_mov_b32 m0, 0x1c400\n"
"s_nop 0\n"
"buffer_load_dwordx4 v242, s[36:39], s47 offen lds\n"
"s_mov_b32 m0, 0x1d380\n"
"s_nop 0\n"
"buffer_load_dwordx4 v242, s[36:39], s47 offen offset:128 lds\n"
"s_mov_b32 m0, 0x1e300\n"
"s_waitcnt vmcnt(56)\n"
"buffer_load_dwordx4 v242, s[36:39], s47 offen offset:256 lds\n"
"s_add_u32 s47, s47, 0x2400\n"
"s_mov_b32 m0, 0x1c800\n"
"s_nop 0\n"
"buffer_load_dwordx4 v242, s[36:39], s47 offen lds\n"
"s_mov_b32 m0, 0x1d780\n"
"s_nop 0\n"
"buffer_load_dwordx4 v242, s[36:39], s47 offen offset:128 lds\n"
"s_mov_b32 m0, 0x1e700\n"
"s_nop 0\n"
"buffer_load_dwordx4 v242, s[36:39], s47 offen offset:256 lds\n"
"s_add_u32 s47, s47, 0x2400\n"
"s_waitcnt vmcnt(55)\n"
"s_mov_b32 m0, 0x1cc00\n"
"s_nop 0\n"
"buffer_load_dwordx4 v242, s[36:39], s47 offen lds\n"
"s_mov_b32 m0, 0x1db80\n"
"s_nop 0\n"
"buffer_load_dwordx4 v242, s[36:39], s47 offen offset:128 lds\n"
"s_mov_b32 m0, 0x1eb00\n"
"s_nop 0\n"
"buffer_load_dwordx4 v242, s[36:39], s47 offen offset:256 lds\n"
"s_waitcnt lgkmcnt(0)\n"
"s_waitcnt vmcnt(48)\n"
"s_barrier\n"
"s_add_u32 s57, s57, 1\n"
"s_and_b32 s57, s57, 15\n"
"s_mul_i32 s47, s57, 0x9000\n"
"s_add_u32 s47, s47, 0x240000\n"
"s_mov_b32 m0, 0x1f000\n"
"s_nop 0\n"
"buffer_load_dwordx4 v242, s[36:39], s47 offen lds\n"
"s_mov_b32 m0, 0x1ff80\n"
"s_nop 0\n"
"buffer_load_dwordx4 v242, s[36:39], s47 offen offset:128 lds\n"
"s_mov_b32 m0, 0x20f00\n"
"s_nop 0\n"
"buffer_load_dwordx4 v242, s[36:39], s47 offen offset:256 lds\n"
"s_add_u32 s47, s47, 0x2400\n"
"s_mov_b32 m0, 0x1f400\n"
"s_nop 0\n"
"buffer_load_dwordx4 v242, s[36:39], s47 offen lds\n"
"s_mov_b32 m0, 0x20380\n"
"s_nop 0\n"
"buffer_load_dwordx4 v242, s[36:39], s47 offen offset:128 lds\n"
"s_mov_b32 m0, 0x21300\n"
"s_nop 0\n"
"buffer_load_dwordx4 v242, s[36:39], s47 offen offset:256 lds\n"
"s_add_u32 s47, s47, 0x2400\n"
"s_mov_b32 m0, 0x1f800\n"
"s_nop 0\n"
"buffer_load_dwordx4 v242, s[36:39], s47 offen lds\n"
"s_mov_b32 m0, 0x20780\n"
"s_nop 0\n"
"buffer_load_dwordx4 v242, s[36:39], s47 offen offset:128 lds\n"
"s_mov_b32 m0, 0x21700\n"
"s_nop 0\n"
"buffer_load_dwordx4 v242, s[36:39], s47 offen offset:256 lds\n"
"s_add_u32 s47, s47, 0x2400\n"
"s_mov_b32 m0, 0x1fc00\n"
"s_nop 0\n"
"buffer_load_dwordx4 v242, s[36:39], s47 offen lds\n"
"s_mov_b32 m0, 0x20b80\n"
"s_nop 0\n"
"buffer_load_dwordx4 v242, s[36:39], s47 offen offset:128 lds\n"
"s_mov_b32 m0, 0x21b00\n"
"s_nop 0\n"
"buffer_load_dwordx4 v242, s[36:39], s47 offen offset:256 lds\n"
"s_add_u32 s57, s57, 1\n"
"s_and_b32 s57, s57, 15\n"
"s_mul_i32 s47, s57, 0x9000\n"
"s_add_u32 s47, s47, 0x240000\n"
"s_waitcnt vmcnt(55)\n"
"s_mov_b32 m0, 0x22000\n"
"s_nop 0\n"
"buffer_load_dwordx4 v242, s[36:39], s47 offen lds\n"
"s_mov_b32 m0, 0x22f80\n"
"s_nop 0\n"
"buffer_load_dwordx4 v242, s[36:39], s47 offen offset:128 lds\n"
"s_mov_b32 m0, 0x23f00\n"
"s_nop 0\n"
"buffer_load_dwordx4 v242, s[36:39], s47 offen offset:256 lds\n"
"s_add_u32 s47, s47, 0x2400\n"
"s_mov_b32 m0, 0x22400\n"
"s_nop 0\n"
"buffer_load_dwordx4 v242, s[36:39], s47 offen lds\n"
"s_mov_b32 m0, 0x23380\n"
"s_nop 0\n"
"buffer_load_dwordx4 v242, s[36:39], s47 offen offset:128 lds\n"
"s_mov_b32 m0, 0x24300\n"
"s_waitcnt vmcnt(56)\n"
"buffer_load_dwordx4 v242, s[36:39], s47 offen offset:256 lds\n"
"s_add_u32 s47, s47, 0x2400\n"
"s_mov_b32 m0, 0x22800\n"
"s_nop 0\n"
"buffer_load_dwordx4 v242, s[36:39], s47 offen lds\n"
"s_mov_b32 m0, 0x23780\n"
"s_nop 0\n"
"buffer_load_dwordx4 v242, s[36:39], s47 offen offset:128 lds\n"
"s_mov_b32 m0, 0x24700\n"
"s_nop 0\n"
"buffer_load_dwordx4 v242, s[36:39], s47 offen offset:256 lds\n"
"s_add_u32 s47, s47, 0x2400\n"
"s_waitcnt vmcnt(55)\n"
"s_mov_b32 m0, 0x22c00\n"
"s_nop 0\n"
"buffer_load_dwordx4 v242, s[36:39], s47 offen lds\n"
"s_mov_b32 m0, 0x23b80\n"
"s_nop 0\n"
"buffer_load_dwordx4 v242, s[36:39], s47 offen offset:128 lds\n"
"s_mov_b32 m0, 0x24b00\n"
"s_nop 0\n"
"buffer_load_dwordx4 v242, s[36:39], s47 offen offset:256 lds\n"
"s_waitcnt lgkmcnt(0)\n"
"s_waitcnt vmcnt(48)\n"
"s_barrier\n"
"s_add_u32 s57, s57, 1\n"
"s_and_b32 s57, s57, 15\n"
"s_mul_i32 s47, s57, 0x9000\n"
"s_add_u32 s47, s47, 0x240000\n"
"s_mov_b32 m0, 0x25000\n"
"s_nop 0\n"
"buffer_load_dwordx4 v242, s[36:39], s47 offen lds\n"
"s_mov_b32 m0, 0x25f80\n"
"s_nop 0\n"
"buffer_load_dwordx4 v242, s[36:39], s47 offen offset:128 lds\n"
"s_mov_b32 m0, 0x26f00\n"
"s_nop 0\n"
"buffer_load_dwordx4 v242, s[36:39], s47 offen offset:256 lds\n"
"s_add_u32 s47, s47, 0x2400\n"
"s_mov_b32 m0, 0x25400\n"
"s_nop 0\n"
"buffer_load_dwordx4 v242, s[36:39], s47 offen lds\n"
"s_mov_b32 m0, 0x26380\n"
"s_nop 0\n"
"buffer_load_dwordx4 v242, s[36:39], s47 offen offset:128 lds\n"
"s_mov_b32 m0, 0x27300\n"
"s_nop 0\n"
"buffer_load_dwordx4 v242, s[36:39], s47 offen offset:256 lds\n"
"s_add_u32 s47, s47, 0x2400\n"
"s_mov_b32 m0, 0x25800\n"
"s_nop 0\n"
"buffer_load_dwordx4 v242, s[36:39], s47 offen lds\n"
"s_mov_b32 m0, 0x26780\n"
"s_nop 0\n"
"buffer_load_dwordx4 v242, s[36:39], s47 offen offset:128 lds\n"
"s_mov_b32 m0, 0x27700\n"
"s_nop 0\n"
"buffer_load_dwordx4 v242, s[36:39], s47 offen offset:256 lds\n"
"s_add_u32 s47, s47, 0x2400\n"
"s_mov_b32 m0, 0x25c00\n"
"s_nop 0\n"
"buffer_load_dwordx4 v242, s[36:39], s47 offen lds\n"
"s_mov_b32 m0, 0x26b80\n"
"s_nop 0\n"
"buffer_load_dwordx4 v242, s[36:39], s47 offen offset:128 lds\n"
"s_mov_b32 m0, 0x27b00\n"
"s_nop 0\n"
"buffer_load_dwordx4 v242, s[36:39], s47 offen offset:256 lds\n"
"s_add_u32 s57, s57, 1\n"
"s_and_b32 s57, s57, 15\n"
"s_mul_i32 s47, s57, 0x9000\n"
"s_add_u32 s47, s47, 0x240000\n"
"s_waitcnt vmcnt(55)\n"
"s_mov_b32 m0, 0x8000\n"
"s_nop 0\n"
"buffer_load_dwordx4 v242, s[36:39], s47 offen lds\n"
"s_mov_b32 m0, 0x8f80\n"
"s_nop 0\n"
"buffer_load_dwordx4 v242, s[36:39], s47 offen offset:128 lds\n"
"s_mov_b32 m0, 0x9f00\n"
"s_nop 0\n"
"buffer_load_dwordx4 v242, s[36:39], s47 offen offset:256 lds\n"
"s_add_u32 s47, s47, 0x2400\n"
"s_mov_b32 m0, 0x8400\n"
"s_nop 0\n"
"buffer_load_dwordx4 v242, s[36:39], s47 offen lds\n"
"s_mov_b32 m0, 0x9380\n"
"s_nop 0\n"
"buffer_load_dwordx4 v242, s[36:39], s47 offen offset:128 lds\n"
"s_mov_b32 m0, 0xa300\n"
"s_waitcnt vmcnt(56)\n"
"buffer_load_dwordx4 v242, s[36:39], s47 offen offset:256 lds\n"
"s_add_u32 s47, s47, 0x2400\n"
"s_mov_b32 m0, 0x8800\n"
"s_nop 0\n"
"buffer_load_dwordx4 v242, s[36:39], s47 offen lds\n"
"s_mov_b32 m0, 0x9780\n"
"s_nop 0\n"
"buffer_load_dwordx4 v242, s[36:39], s47 offen offset:128 lds\n"
"s_mov_b32 m0, 0xa700\n"
"s_nop 0\n"
"buffer_load_dwordx4 v242, s[36:39], s47 offen offset:256 lds\n"
"s_add_u32 s47, s47, 0x2400\n"
"s_waitcnt vmcnt(55)\n"
"s_mov_b32 m0, 0x8c00\n"
"s_nop 0\n"
"buffer_load_dwordx4 v242, s[36:39], s47 offen lds\n"
"s_mov_b32 m0, 0x9b80\n"
"s_nop 0\n"
"buffer_load_dwordx4 v242, s[36:39], s47 offen offset:128 lds\n"
"s_mov_b32 m0, 0xab00\n"
"s_nop 0\n"
"buffer_load_dwordx4 v242, s[36:39], s47 offen offset:256 lds\n"
"s_waitcnt lgkmcnt(0)\n"
"s_waitcnt vmcnt(48)\n"
"s_barrier\n"
"s_add_u32 s57, s57, 1\n"
"s_and_b32 s57, s57, 15\n"
"s_mul_i32 s47, s57, 0x9000\n"
"s_add_u32 s47, s47, 0x240000\n"
"s_mov_b32 m0, 0xb000\n"
"s_nop 0\n"
"buffer_load_dwordx4 v242, s[36:39], s47 offen lds\n"
"s_mov_b32 m0, 0xbf80\n"
"s_nop 0\n"
"buffer_load_dwordx4 v242, s[36:39], s47 offen offset:128 lds\n"
"s_mov_b32 m0, 0xcf00\n"
"s_nop 0\n"
"buffer_load_dwordx4 v242, s[36:39], s47 offen offset:256 lds\n"
"s_add_u32 s47, s47, 0x2400\n"
"s_mov_b32 m0, 0xb400\n"
"s_nop 0\n"
"buffer_load_dwordx4 v242, s[36:39], s47 offen lds\n"
"s_mov_b32 m0, 0xc380\n"
"s_nop 0\n"
"buffer_load_dwordx4 v242, s[36:39], s47 offen offset:128 lds\n"
"s_mov_b32 m0, 0xd300\n"
"s_nop 0\n"
"buffer_load_dwordx4 v242, s[36:39], s47 offen offset:256 lds\n"
"s_add_u32 s47, s47, 0x2400\n"
"s_mov_b32 m0, 0xb800\n"
"s_nop 0\n"
"buffer_load_dwordx4 v242, s[36:39], s47 offen lds\n"
"s_mov_b32 m0, 0xc780\n"
"s_nop 0\n"
"buffer_load_dwordx4 v242, s[36:39], s47 offen offset:128 lds\n"
"s_mov_b32 m0, 0xd700\n"
"s_nop 0\n"
"buffer_load_dwordx4 v242, s[36:39], s47 offen offset:256 lds\n"
"s_add_u32 s47, s47, 0x2400\n"
"s_mov_b32 m0, 0xbc00\n"
"s_nop 0\n"
"buffer_load_dwordx4 v242, s[36:39], s47 offen lds\n"
"s_mov_b32 m0, 0xcb80\n"
"s_nop 0\n"
"buffer_load_dwordx4 v242, s[36:39], s47 offen offset:128 lds\n"
"s_mov_b32 m0, 0xdb00\n"
"s_nop 0\n"
"buffer_load_dwordx4 v242, s[36:39], s47 offen offset:256 lds\n"
"s_add_u32 s57, s57, 1\n"
"s_and_b32 s57, s57, 15\n"
"s_mul_i32 s47, s57, 0x9000\n"
"s_add_u32 s47, s47, 0x240000\n"
"s_waitcnt vmcnt(55)\n"
"s_mov_b32 m0, 0xe000\n"
"s_nop 0\n"
"buffer_load_dwordx4 v242, s[36:39], s47 offen lds\n"
"s_mov_b32 m0, 0xef80\n"
"s_nop 0\n"
"buffer_load_dwordx4 v242, s[36:39], s47 offen offset:128 lds\n"
"s_mov_b32 m0, 0xff00\n"
"s_nop 0\n"
"buffer_load_dwordx4 v242, s[36:39], s47 offen offset:256 lds\n"
"s_add_u32 s47, s47, 0x2400\n"
"s_mov_b32 m0, 0xe400\n"
"s_nop 0\n"
"buffer_load_dwordx4 v242, s[36:39], s47 offen lds\n"
"s_mov_b32 m0, 0xf380\n"
"s_nop 0\n"
"buffer_load_dwordx4 v242, s[36:39], s47 offen offset:128 lds\n"
"s_mov_b32 m0, 0x10300\n"
"s_waitcnt vmcnt(56)\n"
"buffer_load_dwordx4 v242, s[36:39], s47 offen offset:256 lds\n"
"s_add_u32 s47, s47, 0x2400\n"
"s_mov_b32 m0, 0xe800\n"
"s_nop 0\n"
"buffer_load_dwordx4 v242, s[36:39], s47 offen lds\n"
"s_mov_b32 m0, 0xf780\n"
"s_nop 0\n"
"buffer_load_dwordx4 v242, s[36:39], s47 offen offset:128 lds\n"
"s_mov_b32 m0, 0x10700\n"
"s_nop 0\n"
"buffer_load_dwordx4 v242, s[36:39], s47 offen offset:256 lds\n"
"s_add_u32 s47, s47, 0x2400\n"
"s_waitcnt vmcnt(55)\n"
"s_mov_b32 m0, 0xec00\n"
"s_nop 0\n"
"buffer_load_dwordx4 v242, s[36:39], s47 offen lds\n"
"s_mov_b32 m0, 0xfb80\n"
"s_nop 0\n"
"buffer_load_dwordx4 v242, s[36:39], s47 offen offset:128 lds\n"
"s_mov_b32 m0, 0x10b00\n"
"s_nop 0\n"
"buffer_load_dwordx4 v242, s[36:39], s47 offen offset:256 lds\n"
"s_waitcnt lgkmcnt(0)\n"
"s_waitcnt vmcnt(48)\n"
"s_barrier\n"
"s_add_u32 s57, s57, 1\n"
"s_and_b32 s57, s57, 15\n"
"s_mul_i32 s47, s57, 0x9000\n"
"s_add_u32 s47, s47, 0x240000\n"
"s_mov_b32 m0, 0x11000\n"
"s_nop 0\n"
"buffer_load_dwordx4 v242, s[36:39], s47 offen lds\n"
"s_mov_b32 m0, 0x11f80\n"
"s_nop 0\n"
"buffer_load_dwordx4 v242, s[36:39], s47 offen offset:128 lds\n"
"s_mov_b32 m0, 0x12f00\n"
"s_nop 0\n"
"buffer_load_dwordx4 v242, s[36:39], s47 offen offset:256 lds\n"
"s_add_u32 s47, s47, 0x2400\n"
"s_mov_b32 m0, 0x11400\n"
"s_nop 0\n"
"buffer_load_dwordx4 v242, s[36:39], s47 offen lds\n"
"s_mov_b32 m0, 0x12380\n"
"s_nop 0\n"
"buffer_load_dwordx4 v242, s[36:39], s47 offen offset:128 lds\n"
"s_mov_b32 m0, 0x13300\n"
"s_nop 0\n"
"buffer_load_dwordx4 v242, s[36:39], s47 offen offset:256 lds\n"
"s_add_u32 s47, s47, 0x2400\n"
"s_mov_b32 m0, 0x11800\n"
"s_nop 0\n"
"buffer_load_dwordx4 v242, s[36:39], s47 offen lds\n"
"s_mov_b32 m0, 0x12780\n"
"s_nop 0\n"
"buffer_load_dwordx4 v242, s[36:39], s47 offen offset:128 lds\n"
"s_mov_b32 m0, 0x13700\n"
"s_nop 0\n"
"buffer_load_dwordx4 v242, s[36:39], s47 offen offset:256 lds\n"
"s_add_u32 s47, s47, 0x2400\n"
"s_mov_b32 m0, 0x11c00\n"
"s_nop 0\n"
"buffer_load_dwordx4 v242, s[36:39], s47 offen lds\n"
"s_mov_b32 m0, 0x12b80\n"
"s_nop 0\n"
"buffer_load_dwordx4 v242, s[36:39], s47 offen offset:128 lds\n"
"s_mov_b32 m0, 0x13b00\n"
"s_nop 0\n"
"buffer_load_dwordx4 v242, s[36:39], s47 offen offset:256 lds\n"
"s_add_u32 s57, s57, 1\n"
"s_and_b32 s57, s57, 15\n"
"s_mul_i32 s47, s57, 0x9000\n"
"s_add_u32 s47, s47, 0x240000\n"
"s_waitcnt vmcnt(55)\n"
"s_mov_b32 m0, 0x19000\n"
"s_nop 0\n"
"buffer_load_dwordx4 v242, s[36:39], s47 offen lds\n"
"s_mov_b32 m0, 0x19f80\n"
"s_nop 0\n"
"buffer_load_dwordx4 v242, s[36:39], s47 offen offset:128 lds\n"
"s_mov_b32 m0, 0x1af00\n"
"s_nop 0\n"
"buffer_load_dwordx4 v242, s[36:39], s47 offen offset:256 lds\n"
"s_add_u32 s47, s47, 0x2400\n"
"s_mov_b32 m0, 0x19400\n"
"s_nop 0\n"
"buffer_load_dwordx4 v242, s[36:39], s47 offen lds\n"
"s_mov_b32 m0, 0x1a380\n"
"s_nop 0\n"
"buffer_load_dwordx4 v242, s[36:39], s47 offen offset:128 lds\n"
"s_mov_b32 m0, 0x1b300\n"
"s_waitcnt vmcnt(56)\n"
"buffer_load_dwordx4 v242, s[36:39], s47 offen offset:256 lds\n"
"s_add_u32 s47, s47, 0x2400\n"
"s_mov_b32 m0, 0x19800\n"
"s_nop 0\n"
"buffer_load_dwordx4 v242, s[36:39], s47 offen lds\n"
"s_mov_b32 m0, 0x1a780\n"
"s_nop 0\n"
"buffer_load_dwordx4 v242, s[36:39], s47 offen offset:128 lds\n"
"s_mov_b32 m0, 0x1b700\n"
"s_nop 0\n"
"buffer_load_dwordx4 v242, s[36:39], s47 offen offset:256 lds\n"
"s_add_u32 s47, s47, 0x2400\n"
"s_waitcnt vmcnt(55)\n"
"s_mov_b32 m0, 0x19c00\n"
"s_nop 0\n"
"buffer_load_dwordx4 v242, s[36:39], s47 offen lds\n"
"s_mov_b32 m0, 0x1ab80\n"
"s_nop 0\n"
"buffer_load_dwordx4 v242, s[36:39], s47 offen offset:128 lds\n"
"s_mov_b32 m0, 0x1bb00\n"
"s_nop 0\n"
"buffer_load_dwordx4 v242, s[36:39], s47 offen offset:256 lds\n"
"s_waitcnt lgkmcnt(0)\n"
"s_waitcnt vmcnt(48)\n"
"s_barrier\n"
"s_add_u32 s57, s57, 1\n"
"s_and_b32 s57, s57, 15\n"
"s_mul_i32 s47, s57, 0x9000\n"
"s_add_u32 s47, s47, 0x240000\n"
"s_mov_b32 m0, 0x1c000\n"
"s_nop 0\n"
"buffer_load_dwordx4 v242, s[36:39], s47 offen lds\n"
"s_mov_b32 m0, 0x1cf80\n"
"s_nop 0\n"
"buffer_load_dwordx4 v242, s[36:39], s47 offen offset:128 lds\n"
"s_mov_b32 m0, 0x1df00\n"
"s_nop 0\n"
"buffer_load_dwordx4 v242, s[36:39], s47 offen offset:256 lds\n"
"s_add_u32 s47, s47, 0x2400\n"
"s_mov_b32 m0, 0x1c400\n"
"s_nop 0\n"
"buffer_load_dwordx4 v242, s[36:39], s47 offen lds\n"
"s_mov_b32 m0, 0x1d380\n"
"s_nop 0\n"
"buffer_load_dwordx4 v242, s[36:39], s47 offen offset:128 lds\n"
"s_mov_b32 m0, 0x1e300\n"
"s_nop 0\n"
"buffer_load_dwordx4 v242, s[36:39], s47 offen offset:256 lds\n"
"s_add_u32 s47, s47, 0x2400\n"
"s_mov_b32 m0, 0x1c800\n"
"s_nop 0\n"
"buffer_load_dwordx4 v242, s[36:39], s47 offen lds\n"
"s_mov_b32 m0, 0x1d780\n"
"s_nop 0\n"
"buffer_load_dwordx4 v242, s[36:39], s47 offen offset:128 lds\n"
"s_mov_b32 m0, 0x1e700\n"
"s_nop 0\n"
"buffer_load_dwordx4 v242, s[36:39], s47 offen offset:256 lds\n"
"s_add_u32 s47, s47, 0x2400\n"
"s_mov_b32 m0, 0x1cc00\n"
"s_nop 0\n"
"buffer_load_dwordx4 v242, s[36:39], s47 offen lds\n"
"s_mov_b32 m0, 0x1db80\n"
"s_nop 0\n"
"buffer_load_dwordx4 v242, s[36:39], s47 offen offset:128 lds\n"
"s_mov_b32 m0, 0x1eb00\n"
"s_nop 0\n"
"buffer_load_dwordx4 v242, s[36:39], s47 offen offset:256 lds\n"
"s_add_u32 s57, s57, 1\n"
"s_and_b32 s57, s57, 15\n"
"s_mul_i32 s47, s57, 0x9000\n"
"s_add_u32 s47, s47, 0x240000\n"
"s_waitcnt vmcnt(55)\n"
"s_mov_b32 m0, 0x1f000\n"
"s_nop 0\n"
"buffer_load_dwordx4 v242, s[36:39], s47 offen lds\n"
"s_mov_b32 m0, 0x1ff80\n"
"s_nop 0\n"
"buffer_load_dwordx4 v242, s[36:39], s47 offen offset:128 lds\n"
"s_mov_b32 m0, 0x20f00\n"
"s_nop 0\n"
"buffer_load_dwordx4 v242, s[36:39], s47 offen offset:256 lds\n"
"s_add_u32 s47, s47, 0x2400\n"
"s_mov_b32 m0, 0x1f400\n"
"s_nop 0\n"
"buffer_load_dwordx4 v242, s[36:39], s47 offen lds\n"
"s_mov_b32 m0, 0x20380\n"
"s_nop 0\n"
"buffer_load_dwordx4 v242, s[36:39], s47 offen offset:128 lds\n"
"s_mov_b32 m0, 0x21300\n"
"s_waitcnt vmcnt(56)\n"
"buffer_load_dwordx4 v242, s[36:39], s47 offen offset:256 lds\n"
"s_add_u32 s47, s47, 0x2400\n"
"s_mov_b32 m0, 0x1f800\n"
"s_nop 0\n"
"buffer_load_dwordx4 v242, s[36:39], s47 offen lds\n"
"s_mov_b32 m0, 0x20780\n"
"s_nop 0\n"
"buffer_load_dwordx4 v242, s[36:39], s47 offen offset:128 lds\n"
"s_mov_b32 m0, 0x21700\n"
"s_nop 0\n"
"buffer_load_dwordx4 v242, s[36:39], s47 offen offset:256 lds\n"
"s_add_u32 s47, s47, 0x2400\n"
"s_waitcnt vmcnt(55)\n"
"s_mov_b32 m0, 0x1fc00\n"
"s_nop 0\n"
"buffer_load_dwordx4 v242, s[36:39], s47 offen lds\n"
"s_mov_b32 m0, 0x20b80\n"
"s_nop 0\n"
"buffer_load_dwordx4 v242, s[36:39], s47 offen offset:128 lds\n"
"s_mov_b32 m0, 0x21b00\n"
"s_nop 0\n"
"buffer_load_dwordx4 v242, s[36:39], s47 offen offset:256 lds\n"
"s_waitcnt lgkmcnt(0)\n"
"s_waitcnt vmcnt(48)\n"
"s_barrier\n"
"s_add_u32 s57, s57, 1\n"
"s_and_b32 s57, s57, 15\n"
"s_mul_i32 s47, s57, 0x9000\n"
"s_add_u32 s47, s47, 0x240000\n"
"s_mov_b32 m0, 0x22000\n"
"s_nop 0\n"
"buffer_load_dwordx4 v242, s[36:39], s47 offen lds\n"
"s_mov_b32 m0, 0x22f80\n"
"s_nop 0\n"
"buffer_load_dwordx4 v242, s[36:39], s47 offen offset:128 lds\n"
"s_mov_b32 m0, 0x23f00\n"
"s_nop 0\n"
"buffer_load_dwordx4 v242, s[36:39], s47 offen offset:256 lds\n"
"s_add_u32 s47, s47, 0x2400\n"
"s_mov_b32 m0, 0x22400\n"
"s_nop 0\n"
"buffer_load_dwordx4 v242, s[36:39], s47 offen lds\n"
"s_mov_b32 m0, 0x23380\n"
"s_nop 0\n"
"buffer_load_dwordx4 v242, s[36:39], s47 offen offset:128 lds\n"
"s_mov_b32 m0, 0x24300\n"
"s_nop 0\n"
"buffer_load_dwordx4 v242, s[36:39], s47 offen offset:256 lds\n"
"s_add_u32 s47, s47, 0x2400\n"
"s_mov_b32 m0, 0x22800\n"
"s_nop 0\n"
"buffer_load_dwordx4 v242, s[36:39], s47 offen lds\n"
"s_mov_b32 m0, 0x23780\n"
"s_nop 0\n"
"buffer_load_dwordx4 v242, s[36:39], s47 offen offset:128 lds\n"
"s_mov_b32 m0, 0x24700\n"
"s_nop 0\n"
"buffer_load_dwordx4 v242, s[36:39], s47 offen offset:256 lds\n"
"s_add_u32 s47, s47, 0x2400\n"
"s_mov_b32 m0, 0x22c00\n"
"s_nop 0\n"
"buffer_load_dwordx4 v242, s[36:39], s47 offen lds\n"
"s_mov_b32 m0, 0x23b80\n"
"s_nop 0\n"
"buffer_load_dwordx4 v242, s[36:39], s47 offen offset:128 lds\n"
"s_mov_b32 m0, 0x24b00\n"
"s_nop 0\n"
"buffer_load_dwordx4 v242, s[36:39], s47 offen offset:256 lds\n"
"s_add_u32 s57, s57, 1\n"
"s_and_b32 s57, s57, 15\n"
"s_mul_i32 s47, s57, 0x9000\n"
"s_add_u32 s47, s47, 0x240000\n"
"s_waitcnt vmcnt(55)\n"
"s_mov_b32 m0, 0x25000\n"
"s_nop 0\n"
"buffer_load_dwordx4 v242, s[36:39], s47 offen lds\n"
"s_mov_b32 m0, 0x25f80\n"
"s_nop 0\n"
"buffer_load_dwordx4 v242, s[36:39], s47 offen offset:128 lds\n"
"s_mov_b32 m0, 0x26f00\n"
"s_nop 0\n"
"buffer_load_dwordx4 v242, s[36:39], s47 offen offset:256 lds\n"
"s_add_u32 s47, s47, 0x2400\n"
"s_mov_b32 m0, 0x25400\n"
"s_nop 0\n"
"buffer_load_dwordx4 v242, s[36:39], s47 offen lds\n"
"s_mov_b32 m0, 0x26380\n"
"s_nop 0\n"
"buffer_load_dwordx4 v242, s[36:39], s47 offen offset:128 lds\n"
"s_mov_b32 m0, 0x27300\n"
"s_waitcnt vmcnt(56)\n"
"buffer_load_dwordx4 v242, s[36:39], s47 offen offset:256 lds\n"
"s_add_u32 s47, s47, 0x2400\n"
"s_mov_b32 m0, 0x25800\n"
"s_nop 0\n"
"buffer_load_dwordx4 v242, s[36:39], s47 offen lds\n"
"s_mov_b32 m0, 0x26780\n"
"s_nop 0\n"
"buffer_load_dwordx4 v242, s[36:39], s47 offen offset:128 lds\n"
"s_mov_b32 m0, 0x27700\n"
"s_nop 0\n"
"buffer_load_dwordx4 v242, s[36:39], s47 offen offset:256 lds\n"
"s_add_u32 s47, s47, 0x2400\n"
"s_waitcnt vmcnt(55)\n"
"s_mov_b32 m0, 0x25c00\n"
"s_nop 0\n"
"buffer_load_dwordx4 v242, s[36:39], s47 offen lds\n"
"s_mov_b32 m0, 0x26b80\n"
"s_nop 0\n"
"buffer_load_dwordx4 v242, s[36:39], s47 offen offset:128 lds\n"
"s_mov_b32 m0, 0x27b00\n"
"s_nop 0\n"
"buffer_load_dwordx4 v242, s[36:39], s47 offen offset:256 lds\n"
"s_waitcnt lgkmcnt(0)\n"
"s_waitcnt vmcnt(48)\n"
"s_barrier\n"
"s_waitcnt lgkmcnt(0)\n"
"s_waitcnt vmcnt(24)\n"
"s_barrier\n"
"s_waitcnt lgkmcnt(0)\n"
"s_waitcnt vmcnt(0)\n"
"s_barrier\n"
"s_branch .Lk1_end\n"
".Lk1_w0r2:\n"
"s_setprio 1\n"
"s_cmp_lt_u32 s6, 0xd8\n"
"s_cbranch_scc1 .Lk1_r2_a1\n"
".Lk1_r2_a0:\n"
"s_barrier\n"
"buffer_store_dwordx4 v[96:99], v237, s[40:43], s80 offen sc1\n"
"buffer_store_dwordx4 v[100:103], v237, s[40:43], s80 offen offset:1024 sc1\n"
"buffer_store_dwordx4 v[104:107], v237, s[40:43], s80 offen offset:2048 sc1\n"
"buffer_store_dwordx4 v[108:111], v237, s[40:43], s80 offen offset:3072 sc1\n"
"buffer_store_dwordx4 v[112:115], v237, s[40:43], s82 offen sc1\n"
"buffer_store_dwordx4 v[116:119], v237, s[40:43], s82 offen offset:1024 sc1\n"
"buffer_store_dwordx4 v[120:123], v237, s[40:43], s82 offen offset:2048 sc1\n"
"buffer_store_dwordx4 v[124:127], v237, s[40:43], s82 offen offset:3072 sc1\n"
"buffer_store_dwordx4 v[128:131], v237, s[40:43], s83 offen sc1\n"
"buffer_store_dwordx4 v[132:135], v237, s[40:43], s83 offen offset:1024 sc1\n"
"buffer_store_dwordx4 v[136:139], v237, s[40:43], s83 offen offset:2048 sc1\n"
"buffer_store_dwordx4 v[140:143], v237, s[40:43], s83 offen offset:3072 sc1\n"
"s_barrier\n"
"s_barrier\n"
"s_barrier\n"
"s_barrier\n"
"s_waitcnt vmcnt(0)\n"
"s_barrier\n"
"s_cmp_eq_u32 s44, 0\n"
"s_cbranch_scc0 .Lk1_nf_a0\n"
"v_mov_b32 v226, s98\n"
"v_mov_b32 v227, s99\n"
"v_mov_b32 v236, 0\n"
"s_mov_b64 exec, 1\n"
"buffer_store_dwordx2 v[226:227], v236, s[84:87], s50 offen sc1\n"
"s_mov_b64 exec, -1\n"
".Lk1_nf_a0:\n"
"s_barrier\n"
"s_barrier\n"
"s_barrier\n"
"s_barrier\n"
"s_barrier\n"
"s_barrier\n"
"s_barrier\n"
"s_barrier\n"
"s_barrier\n"
"s_barrier\n"
"s_cmp_eq_u32 s6, 0xff\n"
"s_cbranch_scc0 .Lk1_end\n"
"s_cmp_eq_u32 s44, 0\n"
"s_cbranch_scc0 .Lk1_end\n"
"v_and_b32 v247, 15, v238\n"
"v_lshrrev_b32 v236, 4, v238\n"
"v_lshlrev_b32 v247, 7, v247\n"
"v_lshl_add_u32 v247, v236, 12, v247\n"
"v_add_u32 v243, 0x1000, v247\n"
"s_mov_b32 s88, 0\n"
"s_mov_b32 s89, 0x4000\n"
"s_mov_b32 s90, 0x8000\n"
"s_mov_b32 s91, 0xc000\n"
"buffer_load_dwordx2 v[228:229], v243, s[84:87], s88 offen sc1\n"
"buffer_load_dwordx2 v[230:231], v243, s[84:87], s89 offen sc1\n"
"buffer_load_dwordx2 v[232:233], v243, s[84:87], s90 offen sc1\n"
"buffer_load_dwordx2 v[234:235], v243, s[84:87], s91 offen sc1\n"
"s_mov_b32 s35, 0\n"
".Lk1_pl_ep:\n"
"s_waitcnt vmcnt(0)\n"
"v_xor_b32 v228, s98, v228\n"
"v_xor_b32 v229, s99, v229\n"
"v_xor_b32 v230, s98, v230\n"
"v_xor_b32 v231, s99, v231\n"
"v_xor_b32 v232, s98, v232\n"
"v_xor_b32 v233, s99, v233\n"
"v_xor_b32 v234, s98, v234\n"
"v_xor_b32 v235, s99, v235\n"
"v_or_b32 v228, v228, v229\n"
"v_or_b32 v228, v228, v230\n"
"v_or_b32 v228, v228, v231\n"
"v_or_b32 v228, v228, v232\n"
"v_or_b32 v228, v228, v233\n"
"v_or_b32 v228, v228, v234\n"
"v_or_b32 v228, v228, v235\n"
"v_cmp_eq_u32 vcc, 0, v228\n"
"s_nop 0\n"
"s_cmp_eq_u64 vcc, -1\n"
"s_cbranch_scc1 .Lk1_pd_ep\n"
"s_add_u32 s35, s35, 1\n"
"s_cmp_lt_u32 s35, 0x40000\n"
"s_cbranch_scc0 .Lk1_pd_ep\n"
"s_sleep 1\n"
"buffer_load_dwordx2 v[228:229], v243, s[84:87], s88 offen sc1\n"
"buffer_load_dwordx2 v[230:231], v243, s[84:87], s89 offen sc1\n"
"buffer_load_dwordx2 v[232:233], v243, s[84:87], s90 offen sc1\n"
"buffer_load_dwordx2 v[234:235], v243, s[84:87], s91 offen sc1\n"
"s_branch .Lk1_pl_ep\n"
".Lk1_pd_ep:\n"
"v_mov_b32 v226, s98\n"
"v_mov_b32 v227, s99\n"
"v_mov_b32 v236, 0\n"
"s_mov_b64 exec, 1\n"
"buffer_store_dwordx2 v[226:227], v236, s[84:87], 0 offen sc1\n"
"s_mov_b64 exec, -1\n"
"s_branch .Lk1_end\n"
".Lk1_r2_a1:\n"
"s_barrier\n"
"buffer_store_dwordx4 v[96:99], v237, s[40:43], s80 offen sc1\n"
"buffer_store_dwordx4 v[100:103], v237, s[40:43], s80 offen offset:1024 sc1\n"
"buffer_store_dwordx4 v[104:107], v237, s[40:43], s80 offen offset:2048 sc1\n"
"buffer_store_dwordx4 v[108:111], v237, s[40:43], s80 offen offset:3072 sc1\n"
"buffer_store_dwordx4 v[112:115], v237, s[40:43], s82 offen sc1\n"
"buffer_store_dwordx4 v[116:119], v237, s[40:43], s82 offen offset:1024 sc1\n"
"buffer_store_dwordx4 v[120:123], v237, s[40:43], s82 offen offset:2048 sc1\n"
"buffer_store_dwordx4 v[124:127], v237, s[40:43], s82 offen offset:3072 sc1\n"
"buffer_store_dwordx4 v[128:131], v237, s[40:43], s83 offen sc1\n"
"buffer_store_dwordx4 v[132:135], v237, s[40:43], s83 offen offset:1024 sc1\n"
"buffer_store_dwordx4 v[136:139], v237, s[40:43], s83 offen offset:2048 sc1\n"
"buffer_store_dwordx4 v[140:143], v237, s[40:43], s83 offen offset:3072 sc1\n"
"s_barrier\n"
"s_barrier\n"
"s_barrier\n"
"s_barrier\n"
"s_waitcnt vmcnt(0)\n"
"s_barrier\n"
"s_cmp_eq_u32 s44, 0\n"
"s_cbranch_scc0 .Lk1_nf_a1\n"
"v_mov_b32 v226, s98\n"
"v_mov_b32 v227, s99\n"
"v_mov_b32 v236, 0\n"
"s_mov_b64 exec, 1\n"
"buffer_store_dwordx2 v[226:227], v236, s[84:87], s50 offen sc1\n"
"s_mov_b64 exec, -1\n"
".Lk1_nf_a1:\n"
"s_barrier\n"
"s_and_b32 s67, s6, 3\n"
"s_lshr_b32 s61, s6, 3\n"
"s_mul_i32 s68, s61, 43\n"
"s_lshr_b32 s68, s68, 7\n"
"s_mul_i32 s60, s68, 3\n"
"s_sub_u32 s69, s61, s60\n"
"s_lshr_b32 s60, s6, 2\n"
"s_and_b32 s60, s60, 1\n"
"s_mul_i32 s60, s60, 9\n"
"s_add_u32 s68, s68, s60\n"
"s_lshl_b32 s66, s44, 10\n"
"s_mul_i32 s55, s44, 0x2400\n"
"s_mul_i32 s57, s68, 5\n"
"s_and_b32 s57, s57, 15\n"
"s_mov_b32 s58, s57\n"
"v_bfe_u32 v247, v238, 2, 3\n"
"v_bfe_u32 v236, v238, 4, 1\n"
"v_lshrrev_b32 v243, 5, v238\n"
"v_and_b32 v239, 3, v238\n"
"v_lshlrev_b32 v242, 1, v236\n"
"v_xor_b32 v242, v242, v239\n"
"v_lshlrev_b32 v242, 4, v242\n"
"v_lshl_add_u32 v242, v243, 6, v242\n"
"v_mul_u32_u24 v244, 0x480, v247\n"
"v_add_u32 v242, v242, v244\n"
"v_bfe_u32 v247, v238, 2, 2\n"
"v_lshlrev_b32 v240, 8, v243\n"
"v_lshl_add_u32 v240, v247, 6, v240\n"
"v_xor_b32 v247, v236, v243\n"
"v_lshl_add_u32 v240, v247, 5, v240\n"
"v_lshl_add_u32 v240, v239, 3, v240\n"
"v_add_u32 v241, 0x18000, v240\n"
"v_add_u32 v240, 0x8000, v240\n"
"v_and_b32 v247, 15, v238\n"
"v_lshlrev_b32 v245, 4, v247\n"
"v_lshl_add_u32 v245, v243, 8, v245\n"
"v_lshl_add_u32 v245, v236, 17, v245\n"
"v_and_b32 v247, 31, v238\n"
"v_mul_u32_u24 v244, 0x480, v247\n"
"v_lshl_add_u32 v244, v243, 4, v244\n"
"s_lshl_b32 s60, s68, 3\n"
"s_add_u32 s61, s60, 7\n"
"s_mul_i32 s60, s60, 57\n"
"s_lshr_b32 s60, s60, 9\n"
"s_mul_i32 s61, s61, 57\n"
"s_lshr_b32 s61, s61, 9\n"
"s_sub_u32 s61, s61, s60\n"
"s_lshl_b32 s61, s61, 12\n"
"v_bfe_u32 v247, v238, 2, 1\n"
"v_mul_u32_u24 v247, s61, v247\n"
"s_lshl_b32 s60, s60, 12\n"
"s_lshl_b32 s61, s67, 9\n"
"s_add_u32 s60, s60, s61\n"
"s_add_u32 s60, s60, 0x1000\n"
"v_lshl_add_u32 v243, v239, 7, v247\n"
"v_add_u32 v243, s60, v243\n"
"s_lshl_b32 s60, s68, 2\n"
"s_add_u32 s60, s60, s44\n"
"s_lshl_b32 s60, s60, 18\n"
"s_lshl_b32 s61, s67, 14\n"
"s_add_u32 s60, s60, s61\n"
"s_add_u32 s28, s16, s60\n"
"s_addc_u32 s29, s17, 0\n"
"s_and_b32 s29, s29, 0xffff\n"
"s_mov_b32 s28, s28\n"
"s_mov_b32 s30, 0x34000\n"
"s_mov_b32 s31, 0x20000\n"
"s_mul_i32 s60, s67, 0x90000\n"
"s_mul_i32 s61, s69, 0x180\n"
"s_add_u32 s60, s60, s61\n"
"s_add_u32 s36, s18, s60\n"
"s_addc_u32 s37, s19, 0\n"
"s_and_b32 s37, s37, 0xffff\n"
"s_mov_b32 s36, s36\n"
"s_mov_b32 s38, 0x2cfd00\n"
"s_mov_b32 s39, 0x20000\n"
"s_barrier\n"
"s_barrier\n"
"s_barrier\n"
"s_barrier\n"
"s_barrier\n"
"s_barrier\n"
"buffer_load_dwordx2 v[228:229], v243, s[84:87], 0 offen sc1\n"
"s_barrier\n"
"s_barrier\n"
"s_mov_b32 s35, 0\n"
"s_waitcnt vmcnt(0)\n"
"s_branch .Lk1_plc_o1a1\n"
".Lk1_pl_o1a1:\n"
"s_waitcnt vmcnt(0)\n"
".Lk1_plc_o1a1:\n"
"v_xor_b32 v228, s98, v228\n"
"v_xor_b32 v229, s99, v229\n"
"v_or_b32 v228, v228, v229\n"
"v_cmp_eq_u32 vcc, 0, v228\n"
"s_nop 0\n"
"s_cmp_eq_u64 vcc, -1\n"
"s_cbranch_scc1 .Lk1_pd_o1a1\n"
"s_add_u32 s35, s35, 1\n"
"s_cmp_lt_u32 s35, 0x40000\n"
"s_cbranch_scc0 .Lk1_pd_o1a1\n"
"s_sleep 1\n"
"buffer_load_dwordx2 v[228:229], v243, s[84:87], 0 offen sc1\n"
"s_branch .Lk1_pl_o1a1\n"
".Lk1_pd_o1a1:\n"
"s_lshl_b32 s81, s58, 10\n"
"buffer_load_dwordx4 v[120:123], v245, s[28:31], s81 offen sc1\n"
"buffer_load_dwordx4 v[124:127], v245, s[28:31], s81 offen offset:512 sc1\n"
"s_add_u32 s58, s58, 1\n"
"s_and_b32 s58, s58, 15\n"
"s_lshl_b32 s81, s58, 10\n"
"buffer_load_dwordx4 v[128:131], v245, s[28:31], s81 offen sc1\n"
"buffer_load_dwordx4 v[132:135], v245, s[28:31], s81 offen offset:512 sc1\n"
"s_add_u32 s58, s58, 1\n"
"s_and_b32 s58, s58, 15\n"
"s_lshl_b32 s81, s58, 10\n"
"buffer_load_dwordx4 v[136:139], v245, s[28:31], s81 offen sc1\n"
"buffer_load_dwordx4 v[140:143], v245, s[28:31], s81 offen offset:512 sc1\n"
"s_add_u32 s58, s58, 1\n"
"s_and_b32 s58, s58, 15\n"
"s_lshl_b32 s81, s58, 10\n"
"buffer_load_dwordx4 v[144:147], v245, s[28:31], s81 offen sc1\n"
"buffer_load_dwordx4 v[148:151], v245, s[28:31], s81 offen offset:512 sc1\n"
"s_add_u32 s58, s58, 1\n"
"s_and_b32 s58, s58, 15\n"
"s_lshl_b32 s81, s58, 10\n"
"buffer_load_dwordx4 v[152:155], v245, s[28:31], s81 offen sc1\n"
"buffer_load_dwordx4 v[156:159], v245, s[28:31], s81 offen offset:512 sc1\n"
"s_add_u32 s58, s58, 1\n"
"s_and_b32 s58, s58, 15\n"
"s_lshl_b32 s81, s58, 10\n"
"buffer_load_dwordx4 v[160:163], v245, s[28:31], s81 offen sc1\n"
"buffer_load_dwordx4 v[164:167], v245, s[28:31], s81 offen offset:512 sc1\n"
"s_add_u32 s58, s58, 1\n"
"s_and_b32 s58, s58, 15\n"
"s_lshl_b32 s81, s58, 10\n"
"buffer_load_dwordx4 v[168:171], v245, s[28:31], s81 offen sc1\n"
"buffer_load_dwordx4 v[172:175], v245, s[28:31], s81 offen offset:512 sc1\n"
"s_add_u32 s58, s58, 1\n"
"s_and_b32 s58, s58, 15\n"
"s_lshl_b32 s81, s58, 10\n"
"buffer_load_dwordx4 v[176:179], v245, s[28:31], s81 offen sc1\n"
"buffer_load_dwordx4 v[180:183], v245, s[28:31], s81 offen offset:512 sc1\n"
"s_barrier\n"
".Lk1_tail:\n"
"s_setprio 2\n"
"ds_read_b64_tr_b16 v[96:97], v241 offset:4096\n"
"ds_read_b64_tr_b16 v[98:99], v241 offset:6144\n"
"ds_read_b64_tr_b16 v[100:101], v241 offset:4608\n"
"ds_read_b64_tr_b16 v[102:103], v241 offset:6656\n"
"ds_read_b64_tr_b16 v[104:105], v241 offset:8192\n"
"ds_read_b64_tr_b16 v[106:107], v241 offset:10240\n"
"s_waitcnt vmcnt(14)\n"
"s_waitcnt lgkmcnt(4)\n"
"v_mfma_f32_32x32x16_bf16 v[0:15], v[96:99], v[120:123], 0\n"
"ds_read_b64_tr_b16 v[108:109], v241 offset:8704\n"
"ds_read_b64_tr_b16 v[110:111], v241 offset:10752\n"
"s_add_u32 s58, s58, 1\n"
"s_and_b32 s58, s58, 15\n"
"s_waitcnt lgkmcnt(4)\n"
"v_mfma_f32_32x32x16_bf16 v[16:31], v[100:103], v[120:123], 0\n"
"ds_read_b64_tr_b16 v[112:113], v241 offset:12288\n"
"ds_read_b64_tr_b16 v[114:115], v241 offset:14336\n"
"s_lshl_b32 s81, s58, 10\n"
"s_waitcnt lgkmcnt(4)\n"
"v_mfma_f32_32x32x16_bf16 v[32:47], v[104:107], v[120:123], 0\n"
"ds_read_b64_tr_b16 v[116:117], v241 offset:12800\n"
"ds_read_b64_tr_b16 v[118:119], v241 offset:14848\n"
"ds_read_b64_tr_b16 v[96:97], v241 offset:5120\n"
"ds_read_b64_tr_b16 v[98:99], v241 offset:7168\n"
"s_waitcnt lgkmcnt(6)\n"
"v_mfma_f32_32x32x16_bf16 v[48:63], v[108:111], v[120:123], 0\n"
"s_waitcnt lgkmcnt(4)\n"
"buffer_load_dwordx4 v[184:187], v245, s[28:31], s81 offen sc1\n"
"v_mfma_f32_32x32x16_bf16 v[64:79], v[112:115], v[120:123], 0\n"
"ds_read_b64_tr_b16 v[100:101], v241 offset:5632\n"
"ds_read_b64_tr_b16 v[102:103], v241 offset:7680\n"
"s_waitcnt lgkmcnt(4)\n"
"v_mfma_f32_32x32x16_bf16 v[80:95], v[116:119], v[120:123], 0\n"
"ds_read_b64_tr_b16 v[104:105], v241 offset:9216\n"
"ds_read_b64_tr_b16 v[106:107], v241 offset:11264\n"
"s_waitcnt lgkmcnt(4)\n"
"v_mfma_f32_32x32x16_bf16 v[0:15], v[96:99], v[124:127], v[0:15]\n"
"ds_read_b64_tr_b16 v[108:109], v241 offset:9728\n"
"ds_read_b64_tr_b16 v[110:111], v241 offset:11776\n"
"ds_read_b64_tr_b16 v[112:113], v241 offset:13312\n"
"ds_read_b64_tr_b16 v[114:115], v241 offset:15360\n"
"s_waitcnt lgkmcnt(6)\n"
"v_mfma_f32_32x32x16_bf16 v[16:31], v[100:103], v[124:127], v[16:31]\n"
"s_waitcnt lgkmcnt(4)\n"
"buffer_load_dwordx4 v[188:191], v245, s[28:31], s81 offen offset:512 sc1\n"
"v_mfma_f32_32x32x16_bf16 v[32:47], v[104:107], v[124:127], v[32:47]\n"
"ds_read_b64_tr_b16 v[116:117], v241 offset:13824\n"
"ds_read_b64_tr_b16 v[118:119], v241 offset:15872\n"
"s_waitcnt lgkmcnt(4)\n"
"v_mfma_f32_32x32x16_bf16 v[48:63], v[108:111], v[124:127], v[48:63]\n"
"s_waitcnt lgkmcnt(2)\n"
"ds_read_b64_tr_b16 v[96:97], v241 offset:16384\n"
"ds_read_b64_tr_b16 v[98:99], v241 offset:18432\n"
"v_mfma_f32_32x32x16_bf16 v[64:79], v[112:115], v[124:127], v[64:79]\n"
"s_waitcnt lgkmcnt(2)\n"
"ds_read_b64_tr_b16 v[100:101], v241 offset:16896\n"
"ds_read_b64_tr_b16 v[102:103], v241 offset:18944\n"
"v_mfma_f32_32x32x16_bf16 v[80:95], v[116:119], v[124:127], v[80:95]\n"
"ds_read_b64_tr_b16 v[104:105], v241 offset:20480\n"
"ds_read_b64_tr_b16 v[106:107], v241 offset:22528\n"
"s_mov_b64 exec, 1\n"
"ds_add_u32 v224, v225\n"
"s_mov_b64 exec, -1\n"
"s_waitcnt vmcnt(14)\n"
"s_waitcnt lgkmcnt(5)\n"
"v_mfma_f32_32x32x16_bf16 v[0:15], v[96:99], v[128:131], v[0:15]\n"
"ds_read_b64_tr_b16 v[108:109], v241 offset:20992\n"
"ds_read_b64_tr_b16 v[110:111], v241 offset:23040\n"
"s_add_u32 s58, s58, 1\n"
"s_and_b32 s58, s58, 15\n"
"s_waitcnt lgkmcnt(5)\n"
"v_mfma_f32_32x32x16_bf16 v[16:31], v[100:103], v[128:131], v[16:31]\n"
"ds_read_b64_tr_b16 v[112:113], v241 offset:24576\n"
"ds_read_b64_tr_b16 v[114:115], v241 offset:26624\n"
"s_lshl_b32 s81, s58, 10\n"
"s_waitcnt lgkmcnt(5)\n"
"v_mfma_f32_32x32x16_bf16 v[32:47], v[104:107], v[128:131], v[32:47]\n"
"ds_read_b64_tr_b16 v[116:117], v241 offset:25088\n"
"ds_read_b64_tr_b16 v[118:119], v241 offset:27136\n"
"ds_read_b64_tr_b16 v[96:97], v241 offset:17408\n"
"ds_read_b64_tr_b16 v[98:99], v241 offset:19456\n"
"s_waitcnt lgkmcnt(6)\n"
"v_mfma_f32_32x32x16_bf16 v[48:63], v[108:111], v[128:131], v[48:63]\n"
"s_waitcnt lgkmcnt(4)\n"
"buffer_load_dwordx4 v[192:195], v245, s[28:31], s81 offen sc1\n"
"v_mfma_f32_32x32x16_bf16 v[64:79], v[112:115], v[128:131], v[64:79]\n"
"ds_read_b64_tr_b16 v[100:101], v241 offset:17920\n"
"ds_read_b64_tr_b16 v[102:103], v241 offset:19968\n"
"s_waitcnt lgkmcnt(4)\n"
"v_mfma_f32_32x32x16_bf16 v[80:95], v[116:119], v[128:131], v[80:95]\n"
"ds_read_b64_tr_b16 v[104:105], v241 offset:21504\n"
"ds_read_b64_tr_b16 v[106:107], v241 offset:23552\n"
"s_waitcnt lgkmcnt(4)\n"
"v_mfma_f32_32x32x16_bf16 v[0:15], v[96:99], v[132:135], v[0:15]\n"
"ds_read_b64_tr_b16 v[108:109], v241 offset:22016\n"
"ds_read_b64_tr_b16 v[110:111], v241 offset:24064\n"
"ds_read_b64_tr_b16 v[112:113], v241 offset:25600\n"
"ds_read_b64_tr_b16 v[114:115], v241 offset:27648\n"
"s_waitcnt lgkmcnt(6)\n"
"v_mfma_f32_32x32x16_bf16 v[16:31], v[100:103], v[132:135], v[16:31]\n"
"s_waitcnt lgkmcnt(4)\n"
"buffer_load_dwordx4 v[196:199], v245, s[28:31], s81 offen offset:512 sc1\n"
"v_mfma_f32_32x32x16_bf16 v[32:47], v[104:107], v[132:135], v[32:47]\n"
"ds_read_b64_tr_b16 v[116:117], v241 offset:26112\n"
"ds_read_b64_tr_b16 v[118:119], v241 offset:28160\n"
"s_waitcnt lgkmcnt(4)\n"
"v_mfma_f32_32x32x16_bf16 v[48:63], v[108:111], v[132:135], v[48:63]\n"
"s_waitcnt lgkmcnt(2)\n"
"ds_read_b64_tr_b16 v[96:97], v241 offset:28672\n"
"ds_read_b64_tr_b16 v[98:99], v241 offset:30720\n"
"v_mfma_f32_32x32x16_bf16 v[64:79], v[112:115], v[132:135], v[64:79]\n"
"s_waitcnt lgkmcnt(2)\n"
"ds_read_b64_tr_b16 v[100:101], v241 offset:29184\n"
"ds_read_b64_tr_b16 v[102:103], v241 offset:31232\n"
"v_mfma_f32_32x32x16_bf16 v[80:95], v[116:119], v[132:135], v[80:95]\n"
"ds_read_b64_tr_b16 v[104:105], v241 offset:32768\n"
"ds_read_b64_tr_b16 v[106:107], v241 offset:34816\n"
"s_mov_b64 exec, 1\n"
"ds_add_u32 v224, v225 offset:20\n"
"s_mov_b64 exec, -1\n"
"s_waitcnt vmcnt(14)\n"
"s_waitcnt lgkmcnt(5)\n"
"v_mfma_f32_32x32x16_bf16 v[0:15], v[96:99], v[136:139], v[0:15]\n"
"ds_read_b64_tr_b16 v[108:109], v241 offset:33280\n"
"ds_read_b64_tr_b16 v[110:111], v241 offset:35328\n"
"s_waitcnt lgkmcnt(5)\n"
"v_mfma_f32_32x32x16_bf16 v[16:31], v[100:103], v[136:139], v[16:31]\n"
"ds_read_b64_tr_b16 v[112:113], v241 offset:36864\n"
"ds_read_b64_tr_b16 v[114:115], v241 offset:38912\n"
"ds_read_b32 v236, v224\n"
"s_waitcnt lgkmcnt(6)\n"
"v_mfma_f32_32x32x16_bf16 v[32:47], v[104:107], v[136:139], v[32:47]\n"
"ds_read_b64_tr_b16 v[116:117], v241 offset:37376\n"
"ds_read_b64_tr_b16 v[118:119], v241 offset:39424\n"
"s_waitcnt lgkmcnt(5)\n"
"v_mfma_f32_32x32x16_bf16 v[48:63], v[108:111], v[136:139], v[48:63]\n"
"ds_read_b64_tr_b16 v[96:97], v241 offset:29696\n"
"ds_read_b64_tr_b16 v[98:99], v241 offset:31744\n"
"s_waitcnt lgkmcnt(5)\n"
"v_mfma_f32_32x32x16_bf16 v[64:79], v[112:115], v[136:139], v[64:79]\n"
"ds_read_b64_tr_b16 v[100:101], v241 offset:30208\n"
"ds_read_b64_tr_b16 v[102:103], v241 offset:32256\n"
"s_waitcnt lgkmcnt(6)\n"
"v_readfirstlane_b32 s60, v236\n"
"s_nop 0\n"
"s_cmp_ge_u32 s60, 5\n"
"s_cbranch_scc0 .Lk1_sp_t2\n"
".Lk1_spr_t2:\n"
"s_waitcnt lgkmcnt(4)\n"
"v_mfma_f32_32x32x16_bf16 v[80:95], v[116:119], v[136:139], v[80:95]\n"
"ds_read_b64_tr_b16 v[104:105], v241 offset:33792\n"
"ds_read_b64_tr_b16 v[106:107], v241 offset:35840\n"
"s_waitcnt lgkmcnt(4)\n"
"v_mfma_f32_32x32x16_bf16 v[0:15], v[96:99], v[140:143], v[0:15]\n"
"ds_read_b64_tr_b16 v[108:109], v241 offset:34304\n"
"ds_read_b64_tr_b16 v[110:111], v241 offset:36352\n"
"s_waitcnt lgkmcnt(4)\n"
"v_mfma_f32_32x32x16_bf16 v[16:31], v[100:103], v[140:143], v[16:31]\n"
"ds_read_b64_tr_b16 v[112:113], v241 offset:37888\n"
"ds_read_b64_tr_b16 v[114:115], v241 offset:39936\n"
"s_waitcnt lgkmcnt(4)\n"
"v_mfma_f32_32x32x16_bf16 v[32:47], v[104:107], v[140:143], v[32:47]\n"
"ds_read_b64_tr_b16 v[116:117], v241 offset:38400\n"
"ds_read_b64_tr_b16 v[118:119], v241 offset:40448\n"
"s_waitcnt lgkmcnt(4)\n"
"v_mfma_f32_32x32x16_bf16 v[48:63], v[108:111], v[140:143], v[48:63]\n"
"s_waitcnt lgkmcnt(2)\n"
"ds_read_b64_tr_b16 v[96:97], v241 offset:40960\n"
"ds_read_b64_tr_b16 v[98:99], v241 offset:43008\n"
"v_mfma_f32_32x32x16_bf16 v[64:79], v[112:115], v[140:143], v[64:79]\n"
"s_waitcnt lgkmcnt(2)\n"
"ds_read_b64_tr_b16 v[100:101], v241 offset:41472\n"
"ds_read_b64_tr_b16 v[102:103], v241 offset:43520\n"
"v_mfma_f32_32x32x16_bf16 v[80:95], v[116:119], v[140:143], v[80:95]\n"
"ds_read_b64_tr_b16 v[104:105], v241 offset:45056\n"
"ds_read_b64_tr_b16 v[106:107], v241 offset:47104\n"
"s_mov_b64 exec, 1\n"
"ds_add_u32 v224, v225\n"
"s_mov_b64 exec, -1\n"
"s_waitcnt vmcnt(12)\n"
"s_waitcnt lgkmcnt(5)\n"
"v_mfma_f32_32x32x16_bf16 v[0:15], v[96:99], v[144:147], v[0:15]\n"
"ds_read_b64_tr_b16 v[108:109], v241 offset:45568\n"
"ds_read_b64_tr_b16 v[110:111], v241 offset:47616\n"
"s_waitcnt lgkmcnt(5)\n"
"v_mfma_f32_32x32x16_bf16 v[16:31], v[100:103], v[144:147], v[16:31]\n"
"ds_read_b64_tr_b16 v[112:113], v241 offset:49152\n"
"ds_read_b64_tr_b16 v[114:115], v241 offset:51200\n"
"ds_read_b32 v236, v224 offset:20\n"
"s_waitcnt lgkmcnt(6)\n"
"v_mfma_f32_32x32x16_bf16 v[32:47], v[104:107], v[144:147], v[32:47]\n"
"ds_read_b64_tr_b16 v[116:117], v241 offset:49664\n"
"ds_read_b64_tr_b16 v[118:119], v241 offset:51712\n"
"s_waitcnt lgkmcnt(5)\n"
"v_mfma_f32_32x32x16_bf16 v[48:63], v[108:111], v[144:147], v[48:63]\n"
"ds_read_b64_tr_b16 v[96:97], v241 offset:41984\n"
"ds_read_b64_tr_b16 v[98:99], v241 offset:44032\n"
"s_waitcnt lgkmcnt(5)\n"
"v_mfma_f32_32x32x16_bf16 v[64:79], v[112:115], v[144:147], v[64:79]\n"
"ds_read_b64_tr_b16 v[100:101], v241 offset:42496\n"
"ds_read_b64_tr_b16 v[102:103], v241 offset:44544\n"
"s_waitcnt lgkmcnt(6)\n"
"v_readfirstlane_b32 s60, v236\n"
"s_nop 0\n"
"s_cmp_ge_u32 s60, 5\n"
"s_cbranch_scc0 .Lk1_sp_t3\n"
".Lk1_spr_t3:\n"
"s_waitcnt lgkmcnt(4)\n"
"v_mfma_f32_32x32x16_bf16 v[80:95], v[116:119], v[144:147], v[80:95]\n"
"ds_read_b64_tr_b16 v[104:105], v241 offset:46080\n"
"ds_read_b64_tr_b16 v[106:107], v241 offset:48128\n"
"s_waitcnt lgkmcnt(4)\n"
"v_mfma_f32_32x32x16_bf16 v[0:15], v[96:99], v[148:151], v[0:15]\n"
"ds_read_b64_tr_b16 v[108:109], v241 offset:46592\n"
"ds_read_b64_tr_b16 v[110:111], v241 offset:48640\n"
"s_waitcnt lgkmcnt(4)\n"
"v_mfma_f32_32x32x16_bf16 v[16:31], v[100:103], v[148:151], v[16:31]\n"
"ds_read_b64_tr_b16 v[112:113], v241 offset:50176\n"
"ds_read_b64_tr_b16 v[114:115], v241 offset:52224\n"
"s_waitcnt lgkmcnt(4)\n"
"v_mfma_f32_32x32x16_bf16 v[32:47], v[104:107], v[148:151], v[32:47]\n"
"ds_read_b64_tr_b16 v[116:117], v241 offset:50688\n"
"ds_read_b64_tr_b16 v[118:119], v241 offset:52736\n"
"s_waitcnt lgkmcnt(4)\n"
"v_mfma_f32_32x32x16_bf16 v[48:63], v[108:111], v[148:151], v[48:63]\n"
"s_waitcnt lgkmcnt(2)\n"
"ds_read_b64_tr_b16 v[96:97], v241 offset:53248\n"
"ds_read_b64_tr_b16 v[98:99], v241 offset:55296\n"
"v_mfma_f32_32x32x16_bf16 v[64:79], v[112:115], v[148:151], v[64:79]\n"
"s_waitcnt lgkmcnt(2)\n"
"ds_read_b64_tr_b16 v[100:101], v241 offset:53760\n"
"ds_read_b64_tr_b16 v[102:103], v241 offset:55808\n"
"v_mfma_f32_32x32x16_bf16 v[80:95], v[116:119], v[148:151], v[80:95]\n"
"ds_read_b64_tr_b16 v[104:105], v241 offset:57344\n"
"ds_read_b64_tr_b16 v[106:107], v241 offset:59392\n"
"s_mov_b64 exec, 1\n"
"ds_add_u32 v224, v225 offset:20\n"
"s_mov_b64 exec, -1\n"
"s_waitcnt vmcnt(10)\n"
"s_waitcnt lgkmcnt(5)\n"
"v_mfma_f32_32x32x16_bf16 v[0:15], v[96:99], v[152:155], v[0:15]\n"
"ds_read_b64_tr_b16 v[108:109], v241 offset:57856\n"
"ds_read_b64_tr_b16 v[110:111], v241 offset:59904\n"
"s_add_u32 s58, s58, 1\n"
"s_and_b32 s58, s58, 15\n"
"s_waitcnt lgkmcnt(5)\n"
"v_mfma_f32_32x32x16_bf16 v[16:31], v[100:103], v[152:155], v[16:31]\n"
"ds_read_b64_tr_b16 v[112:113], v241 offset:61440\n"
"ds_read_b64_tr_b16 v[114:115], v241 offset:63488\n"
"ds_read_b32 v236, v224\n"
"s_lshl_b32 s81, s58, 10\n"
"s_waitcnt lgkmcnt(6)\n"
"v_mfma_f32_32x32x16_bf16 v[32:47], v[104:107], v[152:155], v[32:47]\n"
"ds_read_b64_tr_b16 v[116:117], v241 offset:61952\n"
"ds_read_b64_tr_b16 v[118:119], v241 offset:64000\n"
"ds_read_b64_tr_b16 v[96:97], v241 offset:54272\n"
"ds_read_b64_tr_b16 v[98:99], v241 offset:56320\n"
"s_waitcnt lgkmcnt(7)\n"
"v_mfma_f32_32x32x16_bf16 v[48:63], v[108:111], v[152:155], v[48:63]\n"
"s_waitcnt lgkmcnt(5)\n"
"buffer_load_dwordx4 v[200:203], v245, s[28:31], s81 offen sc1\n"
"v_mfma_f32_32x32x16_bf16 v[64:79], v[112:115], v[152:155], v[64:79]\n"
"ds_read_b64_tr_b16 v[100:101], v241 offset:54784\n"
"ds_read_b64_tr_b16 v[102:103], v241 offset:56832\n"
"s_waitcnt lgkmcnt(6)\n"
"v_readfirstlane_b32 s60, v236\n"
"s_nop 0\n"
"s_cmp_ge_u32 s60, 10\n"
"s_cbranch_scc0 .Lk1_sp_t4\n"
".Lk1_spr_t4:\n"
"s_waitcnt lgkmcnt(4)\n"
"v_mfma_f32_32x32x16_bf16 v[80:95], v[116:119], v[152:155], v[80:95]\n"
"ds_read_b64_tr_b16 v[104:105], v241 offset:58368\n"
"ds_read_b64_tr_b16 v[106:107], v241 offset:60416\n"
"s_waitcnt lgkmcnt(4)\n"
"v_mfma_f32_32x32x16_bf16 v[0:15], v[96:99], v[156:159], v[0:15]\n"
"ds_read_b64_tr_b16 v[108:109], v241 offset:58880\n"
"ds_read_b64_tr_b16 v[110:111], v241 offset:60928\n"
"ds_read_b64_tr_b16 v[112:113], v241 offset:62464\n"
"ds_read_b64_tr_b16 v[114:115], v241 offset:64512\n"
"s_waitcnt lgkmcnt(6)\n"
"v_mfma_f32_32x32x16_bf16 v[16:31], v[100:103], v[156:159], v[16:31]\n"
"s_waitcnt lgkmcnt(4)\n"
"buffer_load_dwordx4 v[204:207], v245, s[28:31], s81 offen offset:512 sc1\n"
"v_mfma_f32_32x32x16_bf16 v[32:47], v[104:107], v[156:159], v[32:47]\n"
"ds_read_b64_tr_b16 v[116:117], v241 offset:62976\n"
"ds_read_b64_tr_b16 v[118:119], v241 offset:65024\n"
"s_waitcnt lgkmcnt(4)\n"
"v_mfma_f32_32x32x16_bf16 v[48:63], v[108:111], v[156:159], v[48:63]\n"
"s_waitcnt lgkmcnt(2)\n"
"ds_read_b64_tr_b16 v[96:97], v240\n"
"ds_read_b64_tr_b16 v[98:99], v240 offset:2048\n"
"v_mfma_f32_32x32x16_bf16 v[64:79], v[112:115], v[156:159], v[64:79]\n"
"s_waitcnt lgkmcnt(2)\n"
"ds_read_b64_tr_b16 v[100:101], v240 offset:512\n"
"ds_read_b64_tr_b16 v[102:103], v240 offset:2560\n"
"v_mfma_f32_32x32x16_bf16 v[80:95], v[116:119], v[156:159], v[80:95]\n"
"ds_read_b64_tr_b16 v[104:105], v240 offset:4096\n"
"ds_read_b64_tr_b16 v[106:107], v240 offset:6144\n"
"s_mov_b64 exec, 1\n"
"ds_add_u32 v224, v225\n"
"s_mov_b64 exec, -1\n"
"s_waitcnt vmcnt(10)\n"
"s_waitcnt lgkmcnt(5)\n"
"v_mfma_f32_32x32x16_bf16 v[0:15], v[96:99], v[160:163], v[0:15]\n"
"ds_read_b64_tr_b16 v[108:109], v240 offset:4608\n"
"ds_read_b64_tr_b16 v[110:111], v240 offset:6656\n"
"s_add_u32 s58, s58, 1\n"
"s_and_b32 s58, s58, 15\n"
"s_waitcnt lgkmcnt(5)\n"
"v_mfma_f32_32x32x16_bf16 v[16:31], v[100:103], v[160:163], v[16:31]\n"
"ds_read_b64_tr_b16 v[112:113], v240 offset:8192\n"
"ds_read_b64_tr_b16 v[114:115], v240 offset:10240\n"
"ds_read_b32 v236, v224 offset:20\n"
"s_lshl_b32 s81, s58, 10\n"
"s_waitcnt lgkmcnt(6)\n"
"v_mfma_f32_32x32x16_bf16 v[32:47], v[104:107], v[160:163], v[32:47]\n"
"ds_read_b64_tr_b16 v[116:117], v240 offset:8704\n"
"ds_read_b64_tr_b16 v[118:119], v240 offset:10752\n"
"ds_read_b64_tr_b16 v[96:97], v240 offset:1024\n"
"ds_read_b64_tr_b16 v[98:99], v240 offset:3072\n"
"s_waitcnt lgkmcnt(7)\n"
"v_mfma_f32_32x32x16_bf16 v[48:63], v[108:111], v[160:163], v[48:63]\n"
"s_waitcnt lgkmcnt(5)\n"
"buffer_load_dwordx4 v[208:211], v245, s[28:31], s81 offen sc1\n"
"v_mfma_f32_32x32x16_bf16 v[64:79], v[112:115], v[160:163], v[64:79]\n"
"ds_read_b64_tr_b16 v[100:101], v240 offset:1536\n"
"ds_read_b64_tr_b16 v[102:103], v240 offset:3584\n"
"s_waitcnt lgkmcnt(6)\n"
"v_readfirstlane_b32 s60, v236\n"
"s_nop 0\n"
"s_cmp_ge_u32 s60, 10\n"
"s_cbranch_scc0 .Lk1_sp_t5\n"
".Lk1_spr_t5:\n"
"s_waitcnt lgkmcnt(4)\n"
"v_mfma_f32_32x32x16_bf16 v[80:95], v[116:119], v[160:163], v[80:95]\n"
"ds_read_b64_tr_b16 v[104:105], v240 offset:5120\n"
"ds_read_b64_tr_b16 v[106:107], v240 offset:7168\n"
"s_waitcnt lgkmcnt(4)\n"
"v_mfma_f32_32x32x16_bf16 v[0:15], v[96:99], v[164:167], v[0:15]\n"
"ds_read_b64_tr_b16 v[108:109], v240 offset:5632\n"
"ds_read_b64_tr_b16 v[110:111], v240 offset:7680\n"
"ds_read_b64_tr_b16 v[112:113], v240 offset:9216\n"
"ds_read_b64_tr_b16 v[114:115], v240 offset:11264\n"
"s_waitcnt lgkmcnt(6)\n"
"v_mfma_f32_32x32x16_bf16 v[16:31], v[100:103], v[164:167], v[16:31]\n"
"s_waitcnt lgkmcnt(4)\n"
"buffer_load_dwordx4 v[212:215], v245, s[28:31], s81 offen offset:512 sc1\n"
"v_mfma_f32_32x32x16_bf16 v[32:47], v[104:107], v[164:167], v[32:47]\n"
"ds_read_b64_tr_b16 v[116:117], v240 offset:9728\n"
"ds_read_b64_tr_b16 v[118:119], v240 offset:11776\n"
"s_waitcnt lgkmcnt(4)\n"
"v_mfma_f32_32x32x16_bf16 v[48:63], v[108:111], v[164:167], v[48:63]\n"
"s_waitcnt lgkmcnt(2)\n"
"ds_read_b64_tr_b16 v[96:97], v240 offset:12288\n"
"ds_read_b64_tr_b16 v[98:99], v240 offset:14336\n"
"v_mfma_f32_32x32x16_bf16 v[64:79], v[112:115], v[164:167], v[64:79]\n"
"s_waitcnt lgkmcnt(2)\n"
"ds_read_b64_tr_b16 v[100:101], v240 offset:12800\n"
"ds_read_b64_tr_b16 v[102:103], v240 offset:14848\n"
"v_mfma_f32_32x32x16_bf16 v[80:95], v[116:119], v[164:167], v[80:95]\n"
"ds_read_b64_tr_b16 v[104:105], v240 offset:16384\n"
"ds_read_b64_tr_b16 v[106:107], v240 offset:18432\n"
"s_mov_b64 exec, 1\n"
"ds_add_u32 v224, v225 offset:20\n"
"s_mov_b64 exec, -1\n"
"s_waitcnt vmcnt(10)\n"
"s_waitcnt lgkmcnt(5)\n"
"v_mfma_f32_32x32x16_bf16 v[0:15], v[96:99], v[168:171], v[0:15]\n"
"ds_read_b64_tr_b16 v[108:109], v240 offset:16896\n"
"ds_read_b64_tr_b16 v[110:111], v240 offset:18944\n"
"s_add_u32 s58, s58, 1\n"
"s_and_b32 s58, s58, 15\n"
"s_waitcnt lgkmcnt(5)\n"
"v_mfma_f32_32x32x16_bf16 v[16:31], v[100:103], v[168:171], v[16:31]\n"
"ds_read_b64_tr_b16 v[112:113], v240 offset:20480\n"
"ds_read_b64_tr_b16 v[114:115], v240 offset:22528\n"
"ds_read_b32 v236, v224\n"
"s_lshl_b32 s81, s58, 10\n"
"s_waitcnt lgkmcnt(6)\n"
"v_mfma_f32_32x32x16_bf16 v[32:47], v[104:107], v[168:171], v[32:47]\n"
"ds_read_b64_tr_b16 v[116:117], v240 offset:20992\n"
"ds_read_b64_tr_b16 v[118:119], v240 offset:23040\n"
"ds_read_b64_tr_b16 v[96:97], v240 offset:13312\n"
"ds_read_b64_tr_b16 v[98:99], v240 offset:15360\n"
"s_waitcnt lgkmcnt(7)\n"
"v_mfma_f32_32x32x16_bf16 v[48:63], v[108:111], v[168:171], v[48:63]\n"
"s_waitcnt lgkmcnt(5)\n"
"buffer_load_dwordx4 v[216:219], v245, s[28:31], s81 offen sc1\n"
"v_mfma_f32_32x32x16_bf16 v[64:79], v[112:115], v[168:171], v[64:79]\n"
"ds_read_b64_tr_b16 v[100:101], v240 offset:13824\n"
"ds_read_b64_tr_b16 v[102:103], v240 offset:15872\n"
"s_waitcnt lgkmcnt(6)\n"
"v_readfirstlane_b32 s60, v236\n"
"s_nop 0\n"
"s_cmp_ge_u32 s60, 15\n"
"s_cbranch_scc0 .Lk1_sp_t6\n"
".Lk1_spr_t6:\n"
"s_waitcnt lgkmcnt(4)\n"
"v_mfma_f32_32x32x16_bf16 v[80:95], v[116:119], v[168:171], v[80:95]\n"
"ds_read_b64_tr_b16 v[104:105], v240 offset:17408\n"
"ds_read_b64_tr_b16 v[106:107], v240 offset:19456\n"
"s_waitcnt lgkmcnt(4)\n"
"v_mfma_f32_32x32x16_bf16 v[0:15], v[96:99], v[172:175], v[0:15]\n"
"ds_read_b64_tr_b16 v[108:109], v240 offset:17920\n"
"ds_read_b64_tr_b16 v[110:111], v240 offset:19968\n"
"ds_read_b64_tr_b16 v[112:113], v240 offset:21504\n"
"ds_read_b64_tr_b16 v[114:115], v240 offset:23552\n"
"s_waitcnt lgkmcnt(6)\n"
"v_mfma_f32_32x32x16_bf16 v[16:31], v[100:103], v[172:175], v[16:31]\n"
"s_waitcnt lgkmcnt(4)\n"
"buffer_load_dwordx4 v[220:223], v245, s[28:31], s81 offen offset:512 sc1\n"
"v_mfma_f32_32x32x16_bf16 v[32:47], v[104:107], v[172:175], v[32:47]\n"
"ds_read_b64_tr_b16 v[116:117], v240 offset:22016\n"
"ds_read_b64_tr_b16 v[118:119], v240 offset:24064\n"
"s_waitcnt lgkmcnt(4)\n"
"v_mfma_f32_32x32x16_bf16 v[48:63], v[108:111], v[172:175], v[48:63]\n"
"s_waitcnt lgkmcnt(2)\n"
"ds_read_b64_tr_b16 v[96:97], v240 offset:24576\n"
"ds_read_b64_tr_b16 v[98:99], v240 offset:26624\n"
"v_mfma_f32_32x32x16_bf16 v[64:79], v[112:115], v[172:175], v[64:79]\n"
"s_waitcnt lgkmcnt(2)\n"
"ds_read_b64_tr_b16 v[100:101], v240 offset:25088\n"
"ds_read_b64_tr_b16 v[102:103], v240 offset:27136\n"
"v_mfma_f32_32x32x16_bf16 v[80:95], v[116:119], v[172:175], v[80:95]\n"
"ds_read_b64_tr_b16 v[104:105], v240 offset:28672\n"
"ds_read_b64_tr_b16 v[106:107], v240 offset:30720\n"
"s_mov_b64 exec, 1\n"
"ds_add_u32 v224, v225\n"
"s_mov_b64 exec, -1\n"
"s_waitcnt vmcnt(10)\n"
"s_waitcnt lgkmcnt(5)\n"
"v_mfma_f32_32x32x16_bf16 v[0:15], v[96:99], v[176:179], v[0:15]\n"
"ds_read_b64_tr_b16 v[108:109], v240 offset:29184\n"
"ds_read_b64_tr_b16 v[110:111], v240 offset:31232\n"
"s_add_u32 s58, s58, 1\n"
"s_and_b32 s58, s58, 15\n"
"s_waitcnt lgkmcnt(5)\n"
"v_mfma_f32_32x32x16_bf16 v[16:31], v[100:103], v[176:179], v[16:31]\n"
"ds_read_b64_tr_b16 v[112:113], v240 offset:32768\n"
"ds_read_b64_tr_b16 v[114:115], v240 offset:34816\n"
"ds_read_b32 v236, v224 offset:20\n"
"s_lshl_b32 s81, s58, 10\n"
"s_waitcnt lgkmcnt(6)\n"
"v_mfma_f32_32x32x16_bf16 v[32:47], v[104:107], v[176:179], v[32:47]\n"
"ds_read_b64_tr_b16 v[116:117], v240 offset:33280\n"
"ds_read_b64_tr_b16 v[118:119], v240 offset:35328\n"
"ds_read_b64_tr_b16 v[96:97], v240 offset:25600\n"
"ds_read_b64_tr_b16 v[98:99], v240 offset:27648\n"
"s_waitcnt lgkmcnt(7)\n"
"v_mfma_f32_32x32x16_bf16 v[48:63], v[108:111], v[176:179], v[48:63]\n"
"s_waitcnt lgkmcnt(5)\n"
"buffer_load_dwordx4 v[120:123], v245, s[28:31], s81 offen sc1\n"
"v_mfma_f32_32x32x16_bf16 v[64:79], v[112:115], v[176:179], v[64:79]\n"
"ds_read_b64_tr_b16 v[100:101], v240 offset:26112\n"
"ds_read_b64_tr_b16 v[102:103], v240 offset:28160\n"
"s_waitcnt lgkmcnt(6)\n"
"v_readfirstlane_b32 s60, v236\n"
"s_nop 0\n"
"s_cmp_ge_u32 s60, 15\n"
"s_cbranch_scc0 .Lk1_sp_t7\n"
".Lk1_spr_t7:\n"
"s_waitcnt lgkmcnt(4)\n"
"v_mfma_f32_32x32x16_bf16 v[80:95], v[116:119], v[176:179], v[80:95]\n"
"ds_read_b64_tr_b16 v[104:105], v240 offset:29696\n"
"ds_read_b64_tr_b16 v[106:107], v240 offset:31744\n"
"s_waitcnt lgkmcnt(4)\n"
"v_mfma_f32_32x32x16_bf16 v[0:15], v[96:99], v[180:183], v[0:15]\n"
"ds_read_b64_tr_b16 v[108:109], v240 offset:30208\n"
"ds_read_b64_tr_b16 v[110:111], v240 offset:32256\n"
"ds_read_b64_tr_b16 v[112:113], v240 offset:33792\n"
"ds_read_b64_tr_b16 v[114:115], v240 offset:35840\n"
"s_waitcnt lgkmcnt(6)\n"
"v_mfma_f32_32x32x16_bf16 v[16:31], v[100:103], v[180:183], v[16:31]\n"
"s_waitcnt lgkmcnt(4)\n"
"buffer_load_dwordx4 v[124:127], v245, s[28:31], s81 offen offset:512 sc1\n"
"v_mfma_f32_32x32x16_bf16 v[32:47], v[104:107], v[180:183], v[32:47]\n"
"ds_read_b64_tr_b16 v[116:117], v240 offset:34304\n"
"ds_read_b64_tr_b16 v[118:119], v240 offset:36352\n"
"s_waitcnt lgkmcnt(4)\n"
"v_mfma_f32_32x32x16_bf16 v[48:63], v[108:111], v[180:183], v[48:63]\n"
"s_waitcnt lgkmcnt(2)\n"
"ds_read_b64_tr_b16 v[96:97], v240 offset:36864\n"
"ds_read_b64_tr_b16 v[98:99], v240 offset:38912\n"
"v_mfma_f32_32x32x16_bf16 v[64:79], v[112:115], v[180:183], v[64:79]\n"
"s_waitcnt lgkmcnt(2)\n"
"ds_read_b64_tr_b16 v[100:101], v240 offset:37376\n"
"ds_read_b64_tr_b16 v[102:103], v240 offset:39424\n"
"v_mfma_f32_32x32x16_bf16 v[80:95], v[116:119], v[180:183], v[80:95]\n"
"ds_read_b64_tr_b16 v[104:105], v240 offset:40960\n"
"ds_read_b64_tr_b16 v[106:107], v240 offset:43008\n"
"s_mov_b64 exec, 1\n"
"ds_add_u32 v224, v225 offset:20\n"
"s_mov_b64 exec, -1\n"
"s_waitcnt vmcnt(10)\n"
"s_waitcnt lgkmcnt(5)\n"
"v_mfma_f32_32x32x16_bf16 v[0:15], v[96:99], v[184:187], v[0:15]\n"
"ds_read_b64_tr_b16 v[108:109], v240 offset:41472\n"
"ds_read_b64_tr_b16 v[110:111], v240 offset:43520\n"
"s_add_u32 s58, s58, 1\n"
"s_and_b32 s58, s58, 15\n"
"s_waitcnt lgkmcnt(5)\n"
"v_mfma_f32_32x32x16_bf16 v[16:31], v[100:103], v[184:187], v[16:31]\n"
"ds_read_b64_tr_b16 v[112:113], v240 offset:45056\n"
"ds_read_b64_tr_b16 v[114:115], v240 offset:47104\n"
"ds_read_b32 v236, v224\n"
"s_lshl_b32 s81, s58, 10\n"
"s_waitcnt lgkmcnt(6)\n"
"v_mfma_f32_32x32x16_bf16 v[32:47], v[104:107], v[184:187], v[32:47]\n"
"ds_read_b64_tr_b16 v[116:117], v240 offset:45568\n"
"ds_read_b64_tr_b16 v[118:119], v240 offset:47616\n"
"ds_read_b64_tr_b16 v[96:97], v240 offset:37888\n"
"ds_read_b64_tr_b16 v[98:99], v240 offset:39936\n"
"s_waitcnt lgkmcnt(7)\n"
"v_mfma_f32_32x32x16_bf16 v[48:63], v[108:111], v[184:187], v[48:63]\n"
"s_waitcnt lgkmcnt(5)\n"
"buffer_load_dwordx4 v[128:131], v245, s[28:31], s81 offen sc1\n"
"v_mfma_f32_32x32x16_bf16 v[64:79], v[112:115], v[184:187], v[64:79]\n"
"ds_read_b64_tr_b16 v[100:101], v240 offset:38400\n"
"ds_read_b64_tr_b16 v[102:103], v240 offset:40448\n"
"s_waitcnt lgkmcnt(6)\n"
"v_readfirstlane_b32 s60, v236\n"
"s_nop 0\n"
"s_cmp_ge_u32 s60, 20\n"
"s_cbranch_scc0 .Lk1_sp_t8\n"
".Lk1_spr_t8:\n"
"s_waitcnt lgkmcnt(4)\n"
"v_mfma_f32_32x32x16_bf16 v[80:95], v[116:119], v[184:187], v[80:95]\n"
"ds_read_b64_tr_b16 v[104:105], v240 offset:41984\n"
"ds_read_b64_tr_b16 v[106:107], v240 offset:44032\n"
"s_waitcnt lgkmcnt(4)\n"
"v_mfma_f32_32x32x16_bf16 v[0:15], v[96:99], v[188:191], v[0:15]\n"
"ds_read_b64_tr_b16 v[108:109], v240 offset:42496\n"
"ds_read_b64_tr_b16 v[110:111], v240 offset:44544\n"
"ds_read_b64_tr_b16 v[112:113], v240 offset:46080\n"
"ds_read_b64_tr_b16 v[114:115], v240 offset:48128\n"
"s_waitcnt lgkmcnt(6)\n"
"v_mfma_f32_32x32x16_bf16 v[16:31], v[100:103], v[188:191], v[16:31]\n"
"s_waitcnt lgkmcnt(4)\n"
"buffer_load_dwordx4 v[132:135], v245, s[28:31], s81 offen offset:512 sc1\n"
"v_mfma_f32_32x32x16_bf16 v[32:47], v[104:107], v[188:191], v[32:47]\n"
"ds_read_b64_tr_b16 v[116:117], v240 offset:46592\n"
"ds_read_b64_tr_b16 v[118:119], v240 offset:48640\n"
"s_waitcnt lgkmcnt(4)\n"
"v_mfma_f32_32x32x16_bf16 v[48:63], v[108:111], v[188:191], v[48:63]\n"
"s_waitcnt lgkmcnt(2)\n"
"ds_read_b64_tr_b16 v[96:97], v241 offset:4096\n"
"ds_read_b64_tr_b16 v[98:99], v241 offset:6144\n"
"v_mfma_f32_32x32x16_bf16 v[64:79], v[112:115], v[188:191], v[64:79]\n"
"s_waitcnt lgkmcnt(2)\n"
"ds_read_b64_tr_b16 v[100:101], v241 offset:4608\n"
"ds_read_b64_tr_b16 v[102:103], v241 offset:6656\n"
"v_mfma_f32_32x32x16_bf16 v[80:95], v[116:119], v[188:191], v[80:95]\n"
"ds_read_b64_tr_b16 v[104:105], v241 offset:8192\n"
"ds_read_b64_tr_b16 v[106:107], v241 offset:10240\n"
"s_mov_b64 exec, 1\n"
"ds_add_u32 v224, v225\n"
"s_mov_b64 exec, -1\n"
"s_waitcnt vmcnt(10)\n"
"s_waitcnt lgkmcnt(5)\n"
"v_mfma_f32_32x32x16_bf16 v[0:15], v[96:99], v[192:195], v[0:15]\n"
"ds_read_b64_tr_b16 v[108:109], v241 offset:8704\n"
"ds_read_b64_tr_b16 v[110:111], v241 offset:10752\n"
"s_add_u32 s58, s58, 1\n"
"s_and_b32 s58, s58, 15\n"
"s_waitcnt lgkmcnt(5)\n"
"v_mfma_f32_32x32x16_bf16 v[16:31], v[100:103], v[192:195], v[16:31]\n"
"ds_read_b64_tr_b16 v[112:113], v241 offset:12288\n"
"ds_read_b64_tr_b16 v[114:115], v241 offset:14336\n"
"ds_read_b32 v236, v224 offset:20\n"
"s_lshl_b32 s81, s58, 10\n"
"s_waitcnt lgkmcnt(6)\n"
"v_mfma_f32_32x32x16_bf16 v[32:47], v[104:107], v[192:195], v[32:47]\n"
"ds_read_b64_tr_b16 v[116:117], v241 offset:12800\n"
"ds_read_b64_tr_b16 v[118:119], v241 offset:14848\n"
"ds_read_b64_tr_b16 v[96:97], v241 offset:5120\n"
"ds_read_b64_tr_b16 v[98:99], v241 offset:7168\n"
"s_waitcnt lgkmcnt(7)\n"
"v_mfma_f32_32x32x16_bf16 v[48:63], v[108:111], v[192:195], v[48:63]\n"
"s_waitcnt lgkmcnt(5)\n"
"buffer_load_dwordx4 v[136:139], v245, s[28:31], s81 offen sc1\n"
"v_mfma_f32_32x32x16_bf16 v[64:79], v[112:115], v[192:195], v[64:79]\n"
"ds_read_b64_tr_b16 v[100:101], v241 offset:5632\n"
"ds_read_b64_tr_b16 v[102:103], v241 offset:7680\n"
"s_waitcnt lgkmcnt(6)\n"
"v_readfirstlane_b32 s60, v236\n"
"s_nop 0\n"
"s_cmp_ge_u32 s60, 20\n"
"s_cbranch_scc0 .Lk1_sp_t9\n"
".Lk1_spr_t9:\n"
"s_waitcnt lgkmcnt(4)\n"
"v_mfma_f32_32x32x16_bf16 v[80:95], v[116:119], v[192:195], v[80:95]\n"
"ds_read_b64_tr_b16 v[104:105], v241 offset:9216\n"
"ds_read_b64_tr_b16 v[106:107], v241 offset:11264\n"
"s_waitcnt lgkmcnt(4)\n"
"v_mfma_f32_32x32x16_bf16 v[0:15], v[96:99], v[196:199], v[0:15]\n"
"ds_read_b64_tr_b16 v[108:109], v241 offset:9728\n"
"ds_read_b64_tr_b16 v[110:111], v241 offset:11776\n"
"ds_read_b64_tr_b16 v[112:113], v241 offset:13312\n"
"ds_read_b64_tr_b16 v[114:115], v241 offset:15360\n"
"s_waitcnt lgkmcnt(6)\n"
"v_mfma_f32_32x32x16_bf16 v[16:31], v[100:103], v[196:199], v[16:31]\n"
"s_waitcnt lgkmcnt(4)\n"
"buffer_load_dwordx4 v[140:143], v245, s[28:31], s81 offen offset:512 sc1\n"
"v_mfma_f32_32x32x16_bf16 v[32:47], v[104:107], v[196:199], v[32:47]\n"
"ds_read_b64_tr_b16 v[116:117], v241 offset:13824\n"
"ds_read_b64_tr_b16 v[118:119], v241 offset:15872\n"
"s_waitcnt lgkmcnt(4)\n"
"v_mfma_f32_32x32x16_bf16 v[48:63], v[108:111], v[196:199], v[48:63]\n"
"s_waitcnt lgkmcnt(2)\n"
"ds_read_b64_tr_b16 v[96:97], v241 offset:16384\n"
"ds_read_b64_tr_b16 v[98:99], v241 offset:18432\n"
"v_mfma_f32_32x32x16_bf16 v[64:79], v[112:115], v[196:199], v[64:79]\n"
"s_waitcnt lgkmcnt(2)\n"
"ds_read_b64_tr_b16 v[100:101], v241 offset:16896\n"
"ds_read_b64_tr_b16 v[102:103], v241 offset:18944\n"
"v_mfma_f32_32x32x16_bf16 v[80:95], v[116:119], v[196:199], v[80:95]\n"
"ds_read_b64_tr_b16 v[104:105], v241 offset:20480\n"
"ds_read_b64_tr_b16 v[106:107], v241 offset:22528\n"
"s_waitcnt vmcnt(10)\n"
"s_waitcnt lgkmcnt(4)\n"
"v_mfma_f32_32x32x16_bf16 v[0:15], v[96:99], v[200:203], v[0:15]\n"
"ds_read_b64_tr_b16 v[108:109], v241 offset:20992\n"
"ds_read_b64_tr_b16 v[110:111], v241 offset:23040\n"
"s_waitcnt lgkmcnt(4)\n"
"v_mfma_f32_32x32x16_bf16 v[16:31], v[100:103], v[200:203], v[16:31]\n"
"ds_read_b64_tr_b16 v[112:113], v241 offset:24576\n"
"ds_read_b64_tr_b16 v[114:115], v241 offset:26624\n"
"ds_read_b32 v236, v224\n"
"s_waitcnt lgkmcnt(5)\n"
"v_mfma_f32_32x32x16_bf16 v[32:47], v[104:107], v[200:203], v[32:47]\n"
"ds_read_b64_tr_b16 v[116:117], v241 offset:25088\n"
"ds_read_b64_tr_b16 v[118:119], v241 offset:27136\n"
"s_waitcnt lgkmcnt(5)\n"
"v_mfma_f32_32x32x16_bf16 v[48:63], v[108:111], v[200:203], v[48:63]\n"
"ds_read_b64_tr_b16 v[96:97], v241 offset:17408\n"
"ds_read_b64_tr_b16 v[98:99], v241 offset:19456\n"
"s_waitcnt lgkmcnt(5)\n"
"v_mfma_f32_32x32x16_bf16 v[64:79], v[112:115], v[200:203], v[64:79]\n"
"ds_read_b64_tr_b16 v[100:101], v241 offset:17920\n"
"ds_read_b64_tr_b16 v[102:103], v241 offset:19968\n"
"s_waitcnt lgkmcnt(6)\n"
"v_readfirstlane_b32 s60, v236\n"
"s_nop 0\n"
"s_cmp_ge_u32 s60, 25\n"
"s_cbranch_scc0 .Lk1_sp_t10\n"
".Lk1_spr_t10:\n"
"s_waitcnt lgkmcnt(4)\n"
"v_mfma_f32_32x32x16_bf16 v[80:95], v[116:119], v[200:203], v[80:95]\n"
"ds_read_b64_tr_b16 v[104:105], v241 offset:21504\n"
"ds_read_b64_tr_b16 v[106:107], v241 offset:23552\n"
"s_waitcnt lgkmcnt(4)\n"
"v_mfma_f32_32x32x16_bf16 v[0:15], v[96:99], v[204:207], v[0:15]\n"
"ds_read_b64_tr_b16 v[108:109], v241 offset:22016\n"
"ds_read_b64_tr_b16 v[110:111], v241 offset:24064\n"
"s_waitcnt lgkmcnt(4)\n"
"v_mfma_f32_32x32x16_bf16 v[16:31], v[100:103], v[204:207], v[16:31]\n"
"ds_read_b64_tr_b16 v[112:113], v241 offset:25600\n"
"ds_read_b64_tr_b16 v[114:115], v241 offset:27648\n"
"s_waitcnt lgkmcnt(4)\n"
"v_mfma_f32_32x32x16_bf16 v[32:47], v[104:107], v[204:207], v[32:47]\n"
"ds_read_b64_tr_b16 v[116:117], v241 offset:26112\n"
"ds_read_b64_tr_b16 v[118:119], v241 offset:28160\n"
"s_waitcnt lgkmcnt(4)\n"
"v_mfma_f32_32x32x16_bf16 v[48:63], v[108:111], v[204:207], v[48:63]\n"
"s_waitcnt lgkmcnt(2)\n"
"ds_read_b64_tr_b16 v[96:97], v241 offset:28672\n"
"ds_read_b64_tr_b16 v[98:99], v241 offset:30720\n"
"v_mfma_f32_32x32x16_bf16 v[64:79], v[112:115], v[204:207], v[64:79]\n"
"s_waitcnt lgkmcnt(2)\n"
"ds_read_b64_tr_b16 v[100:101], v241 offset:29184\n"
"ds_read_b64_tr_b16 v[102:103], v241 offset:31232\n"
"v_mfma_f32_32x32x16_bf16 v[80:95], v[116:119], v[204:207], v[80:95]\n"
"ds_read_b64_tr_b16 v[104:105], v241 offset:32768\n"
"ds_read_b64_tr_b16 v[106:107], v241 offset:34816\n"
"s_barrier\n"
"s_waitcnt vmcnt(8)\n"
"s_waitcnt lgkmcnt(4)\n"
"v_mfma_f32_32x32x16_bf16 v[0:15], v[96:99], v[208:211], v[0:15]\n"
"ds_read_b64_tr_b16 v[108:109], v241 offset:33280\n"
"ds_read_b64_tr_b16 v[110:111], v241 offset:35328\n"
"s_waitcnt lgkmcnt(4)\n"
"v_mfma_f32_32x32x16_bf16 v[16:31], v[100:103], v[208:211], v[16:31]\n"
"ds_read_b64_tr_b16 v[112:113], v241 offset:36864\n"
"ds_read_b64_tr_b16 v[114:115], v241 offset:38912\n"
"s_waitcnt lgkmcnt(4)\n"
"v_mfma_f32_32x32x16_bf16 v[32:47], v[104:107], v[208:211], v[32:47]\n"
"ds_read_b64_tr_b16 v[116:117], v241 offset:37376\n"
"ds_read_b64_tr_b16 v[118:119], v241 offset:39424\n"
"s_waitcnt lgkmcnt(4)\n"
"v_mfma_f32_32x32x16_bf16 v[48:63], v[108:111], v[208:211], v[48:63]\n"
"ds_read_b64_tr_b16 v[96:97], v241 offset:29696\n"
"ds_read_b64_tr_b16 v[98:99], v241 offset:31744\n"
"s_waitcnt lgkmcnt(4)\n"
"v_mfma_f32_32x32x16_bf16 v[64:79], v[112:115], v[208:211], v[64:79]\n"
"ds_read_b64_tr_b16 v[100:101], v241 offset:30208\n"
"ds_read_b64_tr_b16 v[102:103], v241 offset:32256\n"
"s_waitcnt lgkmcnt(4)\n"
"v_mfma_f32_32x32x16_bf16 v[80:95], v[116:119], v[208:211], v[80:95]\n"
"ds_read_b64_tr_b16 v[104:105], v241 offset:33792\n"
"ds_read_b64_tr_b16 v[106:107], v241 offset:35840\n"
"s_waitcnt lgkmcnt(4)\n"
"v_mfma_f32_32x32x16_bf16 v[0:15], v[96:99], v[212:215], v[0:15]\n"
"ds_read_b64_tr_b16 v[108:109], v241 offset:34304\n"
"ds_read_b64_tr_b16 v[110:111], v241 offset:36352\n"
"s_waitcnt lgkmcnt(4)\n"
"v_mfma_f32_32x32x16_bf16 v[16:31], v[100:103], v[212:215], v[16:31]\n"
"ds_read_b64_tr_b16 v[112:113], v241 offset:37888\n"
"ds_read_b64_tr_b16 v[114:115], v241 offset:39936\n"
"s_waitcnt lgkmcnt(4)\n"
"v_mfma_f32_32x32x16_bf16 v[32:47], v[104:107], v[212:215], v[32:47]\n"
"ds_read_b64_tr_b16 v[116:117], v241 offset:38400\n"
"ds_read_b64_tr_b16 v[118:119], v241 offset:40448\n"
"s_waitcnt lgkmcnt(4)\n"
"v_mfma_f32_32x32x16_bf16 v[48:63], v[108:111], v[212:215], v[48:63]\n"
"s_waitcnt lgkmcnt(2)\n"
"ds_read_b64_tr_b16 v[96:97], v241 offset:40960\n"
"ds_read_b64_tr_b16 v[98:99], v241 offset:43008\n"
"v_mfma_f32_32x32x16_bf16 v[64:79], v[112:115], v[212:215], v[64:79]\n"
"s_waitcnt lgkmcnt(2)\n"
"ds_read_b64_tr_b16 v[100:101], v241 offset:41472\n"
"ds_read_b64_tr_b16 v[102:103], v241 offset:43520\n"
"v_mfma_f32_32x32x16_bf16 v[80:95], v[116:119], v[212:215], v[80:95]\n"
"ds_read_b64_tr_b16 v[104:105], v241 offset:45056\n"
"ds_read_b64_tr_b16 v[106:107], v241 offset:47104\n"
"s_waitcnt vmcnt(6)\n"
"s_waitcnt lgkmcnt(4)\n"
"v_mfma_f32_32x32x16_bf16 v[0:15], v[96:99], v[216:219], v[0:15]\n"
"ds_read_b64_tr_b16 v[108:109], v241 offset:45568\n"
"ds_read_b64_tr_b16 v[110:111], v241 offset:47616\n"
"s_waitcnt lgkmcnt(4)\n"
"v_mfma_f32_32x32x16_bf16 v[16:31], v[100:103], v[216:219], v[16:31]\n"
"ds_read_b64_tr_b16 v[112:113], v241 offset:49152\n"
"ds_read_b64_tr_b16 v[114:115], v241 offset:51200\n"
"s_waitcnt lgkmcnt(4)\n"
"v_mfma_f32_32x32x16_bf16 v[32:47], v[104:107], v[216:219], v[32:47]\n"
"ds_read_b64_tr_b16 v[116:117], v241 offset:49664\n"
"ds_read_b64_tr_b16 v[118:119], v241 offset:51712\n"
"s_waitcnt lgkmcnt(4)\n"
"v_mfma_f32_32x32x16_bf16 v[48:63], v[108:111], v[216:219], v[48:63]\n"
"ds_read_b64_tr_b16 v[96:97], v241 offset:41984\n"
"ds_read_b64_tr_b16 v[98:99], v241 offset:44032\n"
"s_waitcnt lgkmcnt(4)\n"
"v_mfma_f32_32x32x16_bf16 v[64:79], v[112:115], v[216:219], v[64:79]\n"
"ds_read_b64_tr_b16 v[100:101], v241 offset:42496\n"
"ds_read_b64_tr_b16 v[102:103], v241 offset:44544\n"
"s_waitcnt lgkmcnt(4)\n"
"v_mfma_f32_32x32x16_bf16 v[80:95], v[116:119], v[216:219], v[80:95]\n"
"ds_read_b64_tr_b16 v[104:105], v241 offset:46080\n"
"ds_read_b64_tr_b16 v[106:107], v241 offset:48128\n"
"s_waitcnt lgkmcnt(4)\n"
"v_mfma_f32_32x32x16_bf16 v[0:15], v[96:99], v[220:223], v[0:15]\n"
"ds_read_b64_tr_b16 v[108:109], v241 offset:46592\n"
"ds_read_b64_tr_b16 v[110:111], v241 offset:48640\n"
"s_waitcnt lgkmcnt(4)\n"
"v_mfma_f32_32x32x16_bf16 v[16:31], v[100:103], v[220:223], v[16:31]\n"
"ds_read_b64_tr_b16 v[112:113], v241 offset:50176\n"
"ds_read_b64_tr_b16 v[114:115], v241 offset:52224\n"
"s_waitcnt lgkmcnt(4)\n"
"v_mfma_f32_32x32x16_bf16 v[32:47], v[104:107], v[220:223], v[32:47]\n"
"ds_read_b64_tr_b16 v[116:117], v241 offset:50688\n"
"ds_read_b64_tr_b16 v[118:119], v241 offset:52736\n"
"s_waitcnt lgkmcnt(4)\n"
"v_mfma_f32_32x32x16_bf16 v[48:63], v[108:111], v[220:223], v[48:63]\n"
"s_waitcnt lgkmcnt(2)\n"
"ds_read_b64_tr_b16 v[96:97], v241 offset:53248\n"
"ds_read_b64_tr_b16 v[98:99], v241 offset:55296\n"
"v_mfma_f32_32x32x16_bf16 v[64:79], v[112:115], v[220:223], v[64:79]\n"
"s_waitcnt lgkmcnt(2)\n"
"ds_read_b64_tr_b16 v[100:101], v241 offset:53760\n"
"ds_read_b64_tr_b16 v[102:103], v241 offset:55808\n"
"v_mfma_f32_32x32x16_bf16 v[80:95], v[116:119], v[220:223], v[80:95]\n"
"ds_read_b64_tr_b16 v[104:105], v241 offset:57344\n"
"ds_read_b64_tr_b16 v[106:107], v241 offset:59392\n"
"v_add_u32 v243, 0x800, v243\n"
"buffer_load_dwordx2 v[228:229], v243, s[84:87], 0 offen sc1\n"
"s_barrier\n"
"s_waitcnt vmcnt(5)\n"
"s_waitcnt lgkmcnt(4)\n"
"v_mfma_f32_32x32x16_bf16 v[0:15], v[96:99], v[120:123], v[0:15]\n"
"ds_read_b64_tr_b16 v[108:109], v241 offset:57856\n"
"ds_read_b64_tr_b16 v[110:111], v241 offset:59904\n"
"s_waitcnt lgkmcnt(4)\n"
"v_mfma_f32_32x32x16_bf16 v[16:31], v[100:103], v[120:123], v[16:31]\n"
"ds_read_b64_tr_b16 v[112:113], v241 offset:61440\n"
"ds_read_b64_tr_b16 v[114:115], v241 offset:63488\n"
"s_waitcnt lgkmcnt(4)\n"
"v_mfma_f32_32x32x16_bf16 v[32:47], v[104:107], v[120:123], v[32:47]\n"
"ds_read_b64_tr_b16 v[116:117], v241 offset:61952\n"
"ds_read_b64_tr_b16 v[118:119], v241 offset:64000\n"
"s_waitcnt lgkmcnt(4)\n"
"v_mfma_f32_32x32x16_bf16 v[48:63], v[108:111], v[120:123], v[48:63]\n"
"ds_read_b64_tr_b16 v[96:97], v241 offset:54272\n"
"ds_read_b64_tr_b16 v[98:99], v241 offset:56320\n"
"s_waitcnt lgkmcnt(4)\n"
"v_mfma_f32_32x32x16_bf16 v[64:79], v[112:115], v[120:123], v[64:79]\n"
"ds_read_b64_tr_b16 v[100:101], v241 offset:54784\n"
"ds_read_b64_tr_b16 v[102:103], v241 offset:56832\n"
"s_waitcnt lgkmcnt(4)\n"
"v_mfma_f32_32x32x16_bf16 v[80:95], v[116:119], v[120:123], v[80:95]\n"
"ds_read_b64_tr_b16 v[104:105], v241 offset:58368\n"
"ds_read_b64_tr_b16 v[106:107], v241 offset:60416\n"
"s_waitcnt lgkmcnt(4)\n"
"v_mfma_f32_32x32x16_bf16 v[0:15], v[96:99], v[124:127], v[0:15]\n"
"ds_read_b64_tr_b16 v[108:109], v241 offset:58880\n"
"ds_read_b64_tr_b16 v[110:111], v241 offset:60928\n"
"s_waitcnt lgkmcnt(4)\n"
"v_mfma_f32_32x32x16_bf16 v[16:31], v[100:103], v[124:127], v[16:31]\n"
"ds_read_b64_tr_b16 v[112:113], v241 offset:62464\n"
"ds_read_b64_tr_b16 v[114:115], v241 offset:64512\n"
"s_waitcnt lgkmcnt(4)\n"
"v_mfma_f32_32x32x16_bf16 v[32:47], v[104:107], v[124:127], v[32:47]\n"
"ds_read_b64_tr_b16 v[116:117], v241 offset:62976\n"
"ds_read_b64_tr_b16 v[118:119], v241 offset:65024\n"
"s_waitcnt lgkmcnt(4)\n"
"v_mfma_f32_32x32x16_bf16 v[48:63], v[108:111], v[124:127], v[48:63]\n"
"s_waitcnt lgkmcnt(2)\n"
"ds_read_b64_tr_b16 v[96:97], v240\n"
"ds_read_b64_tr_b16 v[98:99], v240 offset:2048\n"
"v_mfma_f32_32x32x16_bf16 v[64:79], v[112:115], v[124:127], v[64:79]\n"
"s_waitcnt lgkmcnt(2)\n"
"ds_read_b64_tr_b16 v[100:101], v240 offset:512\n"
"ds_read_b64_tr_b16 v[102:103], v240 offset:2560\n"
"v_mfma_f32_32x32x16_bf16 v[80:95], v[116:119], v[124:127], v[80:95]\n"
"ds_read_b64_tr_b16 v[104:105], v240 offset:4096\n"
"ds_read_b64_tr_b16 v[106:107], v240 offset:6144\n"
"s_waitcnt vmcnt(3)\n"
"s_waitcnt lgkmcnt(4)\n"
"v_mfma_f32_32x32x16_bf16 v[0:15], v[96:99], v[128:131], v[0:15]\n"
"ds_read_b64_tr_b16 v[108:109], v240 offset:4608\n"
"ds_read_b64_tr_b16 v[110:111], v240 offset:6656\n"
"s_waitcnt lgkmcnt(4)\n"
"v_mfma_f32_32x32x16_bf16 v[16:31], v[100:103], v[128:131], v[16:31]\n"
"ds_read_b64_tr_b16 v[112:113], v240 offset:8192\n"
"ds_read_b64_tr_b16 v[114:115], v240 offset:10240\n"
"s_waitcnt lgkmcnt(4)\n"
"v_mfma_f32_32x32x16_bf16 v[32:47], v[104:107], v[128:131], v[32:47]\n"
"ds_read_b64_tr_b16 v[116:117], v240 offset:8704\n"
"ds_read_b64_tr_b16 v[118:119], v240 offset:10752\n"
"s_waitcnt lgkmcnt(4)\n"
"v_mfma_f32_32x32x16_bf16 v[48:63], v[108:111], v[128:131], v[48:63]\n"
"ds_read_b64_tr_b16 v[96:97], v240 offset:1024\n"
"ds_read_b64_tr_b16 v[98:99], v240 offset:3072\n"
"s_waitcnt lgkmcnt(4)\n"
"v_mfma_f32_32x32x16_bf16 v[64:79], v[112:115], v[128:131], v[64:79]\n"
"ds_read_b64_tr_b16 v[100:101], v240 offset:1536\n"
"ds_read_b64_tr_b16 v[102:103], v240 offset:3584\n"
"s_waitcnt lgkmcnt(4)\n"
"v_mfma_f32_32x32x16_bf16 v[80:95], v[116:119], v[128:131], v[80:95]\n"
"ds_read_b64_tr_b16 v[104:105], v240 offset:5120\n"
"ds_read_b64_tr_b16 v[106:107], v240 offset:7168\n"
"s_waitcnt lgkmcnt(4)\n"
"v_mfma_f32_32x32x16_bf16 v[0:15], v[96:99], v[132:135], v[0:15]\n"
"ds_read_b64_tr_b16 v[108:109], v240 offset:5632\n"
"ds_read_b64_tr_b16 v[110:111], v240 offset:7680\n"
"s_waitcnt lgkmcnt(4)\n"
"v_mfma_f32_32x32x16_bf16 v[16:31], v[100:103], v[132:135], v[16:31]\n"
"ds_read_b64_tr_b16 v[112:113], v240 offset:9216\n"
"ds_read_b64_tr_b16 v[114:115], v240 offset:11264\n"
"s_waitcnt lgkmcnt(4)\n"
"v_mfma_f32_32x32x16_bf16 v[32:47], v[104:107], v[132:135], v[32:47]\n"
"ds_read_b64_tr_b16 v[116:117], v240 offset:9728\n"
"ds_read_b64_tr_b16 v[118:119], v240 offset:11776\n"
"s_waitcnt lgkmcnt(4)\n"
"v_mfma_f32_32x32x16_bf16 v[48:63], v[108:111], v[132:135], v[48:63]\n"
"s_waitcnt lgkmcnt(2)\n"
"ds_read_b64_tr_b16 v[96:97], v240 offset:12288\n"
"ds_read_b64_tr_b16 v[98:99], v240 offset:14336\n"
"v_mfma_f32_32x32x16_bf16 v[64:79], v[112:115], v[132:135], v[64:79]\n"
"s_waitcnt lgkmcnt(2)\n"
"ds_read_b64_tr_b16 v[100:101], v240 offset:12800\n"
"ds_read_b64_tr_b16 v[102:103], v240 offset:14848\n"
"v_mfma_f32_32x32x16_bf16 v[80:95], v[116:119], v[132:135], v[80:95]\n"
"ds_read_b64_tr_b16 v[104:105], v240 offset:16384\n"
"ds_read_b64_tr_b16 v[106:107], v240 offset:18432\n"
"s_mov_b32 s35, 0\n"
"s_waitcnt vmcnt(0)\n"
"s_branch .Lk1_plc_o2\n"
".Lk1_pl_o2:\n"
"s_waitcnt vmcnt(0)\n"
".Lk1_plc_o2:\n"
"v_xor_b32 v228, s98, v228\n"
"v_xor_b32 v229, s99, v229\n"
"v_or_b32 v228, v228, v229\n"
"v_cmp_eq_u32 vcc, 0, v228\n"
"s_nop 0\n"
"s_cmp_eq_u64 vcc, -1\n"
"s_cbranch_scc1 .Lk1_pd_o2\n"
"s_add_u32 s35, s35, 1\n"
"s_cmp_lt_u32 s35, 0x40000\n"
"s_cbranch_scc0 .Lk1_pd_o2\n"
"s_sleep 1\n"
"buffer_load_dwordx2 v[228:229], v243, s[84:87], 0 offen sc1\n"
"s_branch .Lk1_pl_o2\n"
".Lk1_pd_o2:\n"
"s_add_u32 s58, s58, 1\n"
"s_and_b32 s58, s58, 15\n"
"s_lshl_b32 s81, s58, 10\n"
"s_add_u32 s81, s81, 0x10000\n"
"buffer_load_dwordx4 v[144:147], v245, s[28:31], s81 offen sc1\n"
"buffer_load_dwordx4 v[148:151], v245, s[28:31], s81 offen offset:512 sc1\n"
"s_add_u32 s58, s58, 1\n"
"s_and_b32 s58, s58, 15\n"
"s_lshl_b32 s81, s58, 10\n"
"s_add_u32 s81, s81, 0x10000\n"
"buffer_load_dwordx4 v[152:155], v245, s[28:31], s81 offen sc1\n"
"buffer_load_dwordx4 v[156:159], v245, s[28:31], s81 offen offset:512 sc1\n"
"s_add_u32 s58, s58, 1\n"
"s_and_b32 s58, s58, 15\n"
"s_lshl_b32 s81, s58, 10\n"
"s_add_u32 s81, s81, 0x10000\n"
"buffer_load_dwordx4 v[160:163], v245, s[28:31], s81 offen sc1\n"
"buffer_load_dwordx4 v[164:167], v245, s[28:31], s81 offen offset:512 sc1\n"
"s_add_u32 s58, s58, 1\n"
"s_and_b32 s58, s58, 15\n"
"s_lshl_b32 s81, s58, 10\n"
"s_add_u32 s81, s81, 0x10000\n"
"buffer_load_dwordx4 v[168:171], v245, s[28:31], s81 offen sc1\n"
"buffer_load_dwordx4 v[172:175], v245, s[28:31], s81 offen offset:512 sc1\n"
"s_add_u32 s58, s58, 1\n"
"s_and_b32 s58, s58, 15\n"
"s_lshl_b32 s81, s58, 10\n"
"s_add_u32 s81, s81, 0x10000\n"
"buffer_load_dwordx4 v[176:179], v245, s[28:31], s81 offen sc1\n"
"buffer_load_dwordx4 v[180:183], v245, s[28:31], s81 offen offset:512 sc1\n"
"s_barrier\n"
"s_waitcnt lgkmcnt(4)\n"
"v_mfma_f32_32x32x16_bf16 v[0:15], v[96:99], v[136:139], v[0:15]\n"
"ds_read_b64_tr_b16 v[108:109], v240 offset:16896\n"
"ds_read_b64_tr_b16 v[110:111], v240 offset:18944\n"
"s_add_u32 s58, s58, 1\n"
"s_and_b32 s58, s58, 15\n"
"s_waitcnt lgkmcnt(4)\n"
"v_mfma_f32_32x32x16_bf16 v[16:31], v[100:103], v[136:139], v[16:31]\n"
"ds_read_b64_tr_b16 v[112:113], v240 offset:20480\n"
"ds_read_b64_tr_b16 v[114:115], v240 offset:22528\n"
"s_lshl_b32 s81, s58, 10\n"
"s_add_u32 s81, s81, 0x10000\n"
"s_waitcnt lgkmcnt(4)\n"
"v_mfma_f32_32x32x16_bf16 v[32:47], v[104:107], v[136:139], v[32:47]\n"
"ds_read_b64_tr_b16 v[116:117], v240 offset:20992\n"
"ds_read_b64_tr_b16 v[118:119], v240 offset:23040\n"
"ds_read_b64_tr_b16 v[96:97], v240 offset:13312\n"
"ds_read_b64_tr_b16 v[98:99], v240 offset:15360\n"
"s_waitcnt lgkmcnt(6)\n"
"v_mfma_f32_32x32x16_bf16 v[48:63], v[108:111], v[136:139], v[48:63]\n"
"s_waitcnt lgkmcnt(4)\n"
"buffer_load_dwordx4 v[184:187], v245, s[28:31], s81 offen sc1\n"
"v_mfma_f32_32x32x16_bf16 v[64:79], v[112:115], v[136:139], v[64:79]\n"
"ds_read_b64_tr_b16 v[100:101], v240 offset:13824\n"
"ds_read_b64_tr_b16 v[102:103], v240 offset:15872\n"
"s_waitcnt lgkmcnt(4)\n"
"v_mfma_f32_32x32x16_bf16 v[80:95], v[116:119], v[136:139], v[80:95]\n"
"ds_read_b64_tr_b16 v[104:105], v240 offset:17408\n"
"ds_read_b64_tr_b16 v[106:107], v240 offset:19456\n"
"s_waitcnt lgkmcnt(4)\n"
"v_mfma_f32_32x32x16_bf16 v[0:15], v[96:99], v[140:143], v[0:15]\n"
"ds_read_b64_tr_b16 v[108:109], v240 offset:17920\n"
"ds_read_b64_tr_b16 v[110:111], v240 offset:19968\n"
"ds_read_b64_tr_b16 v[112:113], v240 offset:21504\n"
"ds_read_b64_tr_b16 v[114:115], v240 offset:23552\n"
"s_waitcnt lgkmcnt(6)\n"
"v_mfma_f32_32x32x16_bf16 v[16:31], v[100:103], v[140:143], v[16:31]\n"
"s_waitcnt lgkmcnt(4)\n"
"buffer_load_dwordx4 v[188:191], v245, s[28:31], s81 offen offset:512 sc1\n"
"v_mfma_f32_32x32x16_bf16 v[32:47], v[104:107], v[140:143], v[32:47]\n"
"ds_read_b64_tr_b16 v[116:117], v240 offset:22016\n"
"ds_read_b64_tr_b16 v[118:119], v240 offset:24064\n"
"s_waitcnt lgkmcnt(4)\n"
"v_mfma_f32_32x32x16_bf16 v[48:63], v[108:111], v[140:143], v[48:63]\n"
"s_waitcnt lgkmcnt(2)\n"
"ds_read_b64_tr_b16 v[96:97], v240 offset:24576\n"
"ds_read_b64_tr_b16 v[98:99], v240 offset:26624\n"
"v_mfma_f32_32x32x16_bf16 v[64:79], v[112:115], v[140:143], v[64:79]\n"
"s_waitcnt lgkmcnt(2)\n"
"ds_read_b64_tr_b16 v[100:101], v240 offset:25088\n"
"ds_read_b64_tr_b16 v[102:103], v240 offset:27136\n"
"v_mfma_f32_32x32x16_bf16 v[80:95], v[116:119], v[140:143], v[80:95]\n"
"ds_read_b64_tr_b16 v[104:105], v240 offset:28672\n"
"ds_read_b64_tr_b16 v[106:107], v240 offset:30720\n"
"s_waitcnt vmcnt(10)\n"
"s_waitcnt lgkmcnt(4)\n"
"v_mfma_f32_32x32x16_bf16 v[0:15], v[96:99], v[144:147], v[0:15]\n"
"ds_read_b64_tr_b16 v[108:109], v240 offset:29184\n"
"ds_read_b64_tr_b16 v[110:111], v240 offset:31232\n"
"s_add_u32 s58, s58, 1\n"
"s_and_b32 s58, s58, 15\n"
"s_waitcnt lgkmcnt(4)\n"
"v_mfma_f32_32x32x16_bf16 v[16:31], v[100:103], v[144:147], v[16:31]\n"
"ds_read_b64_tr_b16 v[112:113], v240 offset:32768\n"
"ds_read_b64_tr_b16 v[114:115], v240 offset:34816\n"
"s_lshl_b32 s81, s58, 10\n"
"s_add_u32 s81, s81, 0x10000\n"
"s_waitcnt lgkmcnt(4)\n"
"v_mfma_f32_32x32x16_bf16 v[32:47], v[104:107], v[144:147], v[32:47]\n"
"ds_read_b64_tr_b16 v[116:117], v240 offset:33280\n"
"ds_read_b64_tr_b16 v[118:119], v240 offset:35328\n"
"ds_read_b64_tr_b16 v[96:97], v240 offset:25600\n"
"ds_read_b64_tr_b16 v[98:99], v240 offset:27648\n"
"s_waitcnt lgkmcnt(6)\n"
"v_mfma_f32_32x32x16_bf16 v[48:63], v[108:111], v[144:147], v[48:63]\n"
"s_waitcnt lgkmcnt(4)\n"
"buffer_load_dwordx4 v[192:195], v245, s[28:31], s81 offen sc1\n"
"v_mfma_f32_32x32x16_bf16 v[64:79], v[112:115], v[144:147], v[64:79]\n"
"ds_read_b64_tr_b16 v[100:101], v240 offset:26112\n"
"ds_read_b64_tr_b16 v[102:103], v240 offset:28160\n"
"s_waitcnt lgkmcnt(4)\n"
"v_mfma_f32_32x32x16_bf16 v[80:95], v[116:119], v[144:147], v[80:95]\n"
"ds_read_b64_tr_b16 v[104:105], v240 offset:29696\n"
"ds_read_b64_tr_b16 v[106:107], v240 offset:31744\n"
"s_waitcnt lgkmcnt(4)\n"
"v_mfma_f32_32x32x16_bf16 v[0:15], v[96:99], v[148:151], v[0:15]\n"
"ds_read_b64_tr_b16 v[108:109], v240 offset:30208\n"
"ds_read_b64_tr_b16 v[110:111], v240 offset:32256\n"
"ds_read_b64_tr_b16 v[112:113], v240 offset:33792\n"
"ds_read_b64_tr_b16 v[114:115], v240 offset:35840\n"
"s_waitcnt lgkmcnt(6)\n"
"v_mfma_f32_32x32x16_bf16 v[16:31], v[100:103], v[148:151], v[16:31]\n"
"s_waitcnt lgkmcnt(4)\n"
"buffer_load_dwordx4 v[196:199], v245, s[28:31], s81 offen offset:512 sc1\n"
"v_mfma_f32_32x32x16_bf16 v[32:47], v[104:107], v[148:151], v[32:47]\n"
"ds_read_b64_tr_b16 v[116:117], v240 offset:34304\n"
"ds_read_b64_tr_b16 v[118:119], v240 offset:36352\n"
"s_waitcnt lgkmcnt(4)\n"
"v_mfma_f32_32x32x16_bf16 v[48:63], v[108:111], v[148:151], v[48:63]\n"
"s_waitcnt lgkmcnt(2)\n"
"ds_read_b64_tr_b16 v[96:97], v240 offset:36864\n"
"ds_read_b64_tr_b16 v[98:99], v240 offset:38912\n"
"v_mfma_f32_32x32x16_bf16 v[64:79], v[112:115], v[148:151], v[64:79]\n"
"s_waitcnt lgkmcnt(2)\n"
"ds_read_b64_tr_b16 v[100:101], v240 offset:37376\n"
"ds_read_b64_tr_b16 v[102:103], v240 offset:39424\n"
"v_mfma_f32_32x32x16_bf16 v[80:95], v[116:119], v[148:151], v[80:95]\n"
"ds_read_b64_tr_b16 v[104:105], v240 offset:40960\n"
"ds_read_b64_tr_b16 v[106:107], v240 offset:43008\n"
"s_barrier\n"
"s_waitcnt vmcnt(10)\n"
"s_waitcnt lgkmcnt(4)\n"
"v_mfma_f32_32x32x16_bf16 v[0:15], v[96:99], v[152:155], v[0:15]\n"
"ds_read_b64_tr_b16 v[108:109], v240 offset:41472\n"
"ds_read_b64_tr_b16 v[110:111], v240 offset:43520\n"
"s_add_u32 s58, s58, 1\n"
"s_and_b32 s58, s58, 15\n"
"s_waitcnt lgkmcnt(4)\n"
"v_mfma_f32_32x32x16_bf16 v[16:31], v[100:103], v[152:155], v[16:31]\n"
"ds_read_b64_tr_b16 v[112:113], v240 offset:45056\n"
"ds_read_b64_tr_b16 v[114:115], v240 offset:47104\n"
"s_lshl_b32 s81, s58, 10\n"
"s_add_u32 s81, s81, 0x10000\n"
"s_waitcnt lgkmcnt(4)\n"
"v_mfma_f32_32x32x16_bf16 v[32:47], v[104:107], v[152:155], v[32:47]\n"
"ds_read_b64_tr_b16 v[116:117], v240 offset:45568\n"
"ds_read_b64_tr_b16 v[118:119], v240 offset:47616\n"
"ds_read_b64_tr_b16 v[96:97], v240 offset:37888\n"
"ds_read_b64_tr_b16 v[98:99], v240 offset:39936\n"
"s_waitcnt lgkmcnt(6)\n"
"v_mfma_f32_32x32x16_bf16 v[48:63], v[108:111], v[152:155], v[48:63]\n"
"s_waitcnt lgkmcnt(4)\n"
"buffer_load_dwordx4 v[200:203], v245, s[28:31], s81 offen sc1\n"
"v_mfma_f32_32x32x16_bf16 v[64:79], v[112:115], v[152:155], v[64:79]\n"
"ds_read_b64_tr_b16 v[100:101], v240 offset:38400\n"
"ds_read_b64_tr_b16 v[102:103], v240 offset:40448\n"
"s_waitcnt lgkmcnt(4)\n"
"v_mfma_f32_32x32x16_bf16 v[80:95], v[116:119], v[152:155], v[80:95]\n"
"ds_read_b64_tr_b16 v[104:105], v240 offset:41984\n"
"ds_read_b64_tr_b16 v[106:107], v240 offset:44032\n"
"s_waitcnt lgkmcnt(4)\n"
"v_mfma_f32_32x32x16_bf16 v[0:15], v[96:99], v[156:159], v[0:15]\n"
"ds_read_b64_tr_b16 v[108:109], v240 offset:42496\n"
"ds_read_b64_tr_b16 v[110:111], v240 offset:44544\n"
"ds_read_b64_tr_b16 v[112:113], v240 offset:46080\n"
"ds_read_b64_tr_b16 v[114:115], v240 offset:48128\n"
"s_waitcnt lgkmcnt(6)\n"
"v_mfma_f32_32x32x16_bf16 v[16:31], v[100:103], v[156:159], v[16:31]\n"
"s_waitcnt lgkmcnt(4)\n"
"buffer_load_dwordx4 v[204:207], v245, s[28:31], s81 offen offset:512 sc1\n"
"v_mfma_f32_32x32x16_bf16 v[32:47], v[104:107], v[156:159], v[32:47]\n"
"ds_read_b64_tr_b16 v[116:117], v240 offset:46592\n"
"ds_read_b64_tr_b16 v[118:119], v240 offset:48640\n"
"s_waitcnt lgkmcnt(4)\n"
"v_mfma_f32_32x32x16_bf16 v[48:63], v[108:111], v[156:159], v[48:63]\n"
"s_waitcnt lgkmcnt(2)\n"
"ds_read_b64_tr_b16 v[96:97], v241 offset:4096\n"
"ds_read_b64_tr_b16 v[98:99], v241 offset:6144\n"
"v_mfma_f32_32x32x16_bf16 v[64:79], v[112:115], v[156:159], v[64:79]\n"
"s_waitcnt lgkmcnt(2)\n"
"ds_read_b64_tr_b16 v[100:101], v241 offset:4608\n"
"ds_read_b64_tr_b16 v[102:103], v241 offset:6656\n"
"v_mfma_f32_32x32x16_bf16 v[80:95], v[116:119], v[156:159], v[80:95]\n"
"ds_read_b64_tr_b16 v[104:105], v241 offset:8192\n"
"ds_read_b64_tr_b16 v[106:107], v241 offset:10240\n"
"s_waitcnt vmcnt(10)\n"
"s_waitcnt lgkmcnt(4)\n"
"v_mfma_f32_32x32x16_bf16 v[0:15], v[96:99], v[160:163], v[0:15]\n"
"ds_read_b64_tr_b16 v[108:109], v241 offset:8704\n"
"ds_read_b64_tr_b16 v[110:111], v241 offset:10752\n"
"s_add_u32 s58, s58, 1\n"
"s_and_b32 s58, s58, 15\n"
"s_waitcnt lgkmcnt(4)\n"
"v_mfma_f32_32x32x16_bf16 v[16:31], v[100:103], v[160:163], v[16:31]\n"
"ds_read_b64_tr_b16 v[112:113], v241 offset:12288\n"
"ds_read_b64_tr_b16 v[114:115], v241 offset:14336\n"
"s_lshl_b32 s81, s58, 10\n"
"s_add_u32 s81, s81, 0x10000\n"
"s_waitcnt lgkmcnt(4)\n"
"v_mfma_f32_32x32x16_bf16 v[32:47], v[104:107], v[160:163], v[32:47]\n"
"ds_read_b64_tr_b16 v[116:117], v241 offset:12800\n"
"ds_read_b64_tr_b16 v[118:119], v241 offset:14848\n"
"ds_read_b64_tr_b16 v[96:97], v241 offset:5120\n"
"ds_read_b64_tr_b16 v[98:99], v241 offset:7168\n"
"s_waitcnt lgkmcnt(6)\n"
"v_mfma_f32_32x32x16_bf16 v[48:63], v[108:111], v[160:163], v[48:63]\n"
"s_waitcnt lgkmcnt(4)\n"
"buffer_load_dwordx4 v[208:211], v245, s[28:31], s81 offen sc1\n"
"v_mfma_f32_32x32x16_bf16 v[64:79], v[112:115], v[160:163], v[64:79]\n"
"ds_read_b64_tr_b16 v[100:101], v241 offset:5632\n"
"ds_read_b64_tr_b16 v[102:103], v241 offset:7680\n"
"s_waitcnt lgkmcnt(4)\n"
"v_mfma_f32_32x32x16_bf16 v[80:95], v[116:119], v[160:163], v[80:95]\n"
"ds_read_b64_tr_b16 v[104:105], v241 offset:9216\n"
"ds_read_b64_tr_b16 v[106:107], v241 offset:11264\n"
"s_waitcnt lgkmcnt(4)\n"
"v_mfma_f32_32x32x16_bf16 v[0:15], v[96:99], v[164:167], v[0:15]\n"
"ds_read_b64_tr_b16 v[108:109], v241 offset:9728\n"
"ds_read_b64_tr_b16 v[110:111], v241 offset:11776\n"
"ds_read_b64_tr_b16 v[112:113], v241 offset:13312\n"
"ds_read_b64_tr_b16 v[114:115], v241 offset:15360\n"
"s_waitcnt lgkmcnt(6)\n"
"v_mfma_f32_32x32x16_bf16 v[16:31], v[100:103], v[164:167], v[16:31]\n"
"s_waitcnt lgkmcnt(4)\n"
"buffer_load_dwordx4 v[212:215], v245, s[28:31], s81 offen offset:512 sc1\n"
"v_mfma_f32_32x32x16_bf16 v[32:47], v[104:107], v[164:167], v[32:47]\n"
"ds_read_b64_tr_b16 v[116:117], v241 offset:13824\n"
"ds_read_b64_tr_b16 v[118:119], v241 offset:15872\n"
"s_waitcnt lgkmcnt(4)\n"
"v_mfma_f32_32x32x16_bf16 v[48:63], v[108:111], v[164:167], v[48:63]\n"
"s_waitcnt lgkmcnt(2)\n"
"ds_read_b64_tr_b16 v[96:97], v241 offset:16384\n"
"ds_read_b64_tr_b16 v[98:99], v241 offset:18432\n"
"v_mfma_f32_32x32x16_bf16 v[64:79], v[112:115], v[164:167], v[64:79]\n"
"s_waitcnt lgkmcnt(2)\n"
"ds_read_b64_tr_b16 v[100:101], v241 offset:16896\n"
"ds_read_b64_tr_b16 v[102:103], v241 offset:18944\n"
"v_mfma_f32_32x32x16_bf16 v[80:95], v[116:119], v[164:167], v[80:95]\n"
"ds_read_b64_tr_b16 v[104:105], v241 offset:20480\n"
"ds_read_b64_tr_b16 v[106:107], v241 offset:22528\n"
"s_barrier\n"
"s_waitcnt vmcnt(10)\n"
"s_waitcnt lgkmcnt(4)\n"
"v_mfma_f32_32x32x16_bf16 v[0:15], v[96:99], v[168:171], v[0:15]\n"
"ds_read_b64_tr_b16 v[108:109], v241 offset:20992\n"
"ds_read_b64_tr_b16 v[110:111], v241 offset:23040\n"
"s_add_u32 s58, s58, 1\n"
"s_and_b32 s58, s58, 15\n"
"s_waitcnt lgkmcnt(4)\n"
"v_mfma_f32_32x32x16_bf16 v[16:31], v[100:103], v[168:171], v[16:31]\n"
"ds_read_b64_tr_b16 v[112:113], v241 offset:24576\n"
"ds_read_b64_tr_b16 v[114:115], v241 offset:26624\n"
"s_lshl_b32 s81, s58, 10\n"
"s_add_u32 s81, s81, 0x10000\n"
"s_waitcnt lgkmcnt(4)\n"
"v_mfma_f32_32x32x16_bf16 v[32:47], v[104:107], v[168:171], v[32:47]\n"
"ds_read_b64_tr_b16 v[116:117], v241 offset:25088\n"
"ds_read_b64_tr_b16 v[118:119], v241 offset:27136\n"
"ds_read_b64_tr_b16 v[96:97], v241 offset:17408\n"
"ds_read_b64_tr_b16 v[98:99], v241 offset:19456\n"
"s_waitcnt lgkmcnt(6)\n"
"v_mfma_f32_32x32x16_bf16 v[48:63], v[108:111], v[168:171], v[48:63]\n"
"s_waitcnt lgkmcnt(4)\n"
"buffer_load_dwordx4 v[216:219], v245, s[28:31], s81 offen sc1\n"
"v_mfma_f32_32x32x16_bf16 v[64:79], v[112:115], v[168:171], v[64:79]\n"
"ds_read_b64_tr_b16 v[100:101], v241 offset:17920\n"
"ds_read_b64_tr_b16 v[102:103], v241 offset:19968\n"
"s_waitcnt lgkmcnt(4)\n"
"v_mfma_f32_32x32x16_bf16 v[80:95], v[116:119], v[168:171], v[80:95]\n"
"ds_read_b64_tr_b16 v[104:105], v241 offset:21504\n"
"ds_read_b64_tr_b16 v[106:107], v241 offset:23552\n"
"s_waitcnt lgkmcnt(4)\n"
"v_mfma_f32_32x32x16_bf16 v[0:15], v[96:99], v[172:175], v[0:15]\n"
"ds_read_b64_tr_b16 v[108:109], v241 offset:22016\n"
"ds_read_b64_tr_b16 v[110:111], v241 offset:24064\n"
"ds_read_b64_tr_b16 v[112:113], v241 offset:25600\n"
"ds_read_b64_tr_b16 v[114:115], v241 offset:27648\n"
"s_waitcnt lgkmcnt(6)\n"
"v_mfma_f32_32x32x16_bf16 v[16:31], v[100:103], v[172:175], v[16:31]\n"
"s_waitcnt lgkmcnt(4)\n"
"buffer_load_dwordx4 v[220:223], v245, s[28:31], s81 offen offset:512 sc1\n"
"v_mfma_f32_32x32x16_bf16 v[32:47], v[104:107], v[172:175], v[32:47]\n"
"ds_read_b64_tr_b16 v[116:117], v241 offset:26112\n"
"ds_read_b64_tr_b16 v[118:119], v241 offset:28160\n"
"s_waitcnt lgkmcnt(4)\n"
"v_mfma_f32_32x32x16_bf16 v[48:63], v[108:111], v[172:175], v[48:63]\n"
"s_waitcnt lgkmcnt(2)\n"
"ds_read_b64_tr_b16 v[96:97], v241 offset:28672\n"
"ds_read_b64_tr_b16 v[98:99], v241 offset:30720\n"
"v_mfma_f32_32x32x16_bf16 v[64:79], v[112:115], v[172:175], v[64:79]\n"
"s_waitcnt lgkmcnt(2)\n"
"ds_read_b64_tr_b16 v[100:101], v241 offset:29184\n"
"ds_read_b64_tr_b16 v[102:103], v241 offset:31232\n"
"v_mfma_f32_32x32x16_bf16 v[80:95], v[116:119], v[172:175], v[80:95]\n"
"ds_read_b64_tr_b16 v[104:105], v241 offset:32768\n"
"ds_read_b64_tr_b16 v[106:107], v241 offset:34816\n"
"s_waitcnt vmcnt(10)\n"
"s_waitcnt lgkmcnt(4)\n"
"v_mfma_f32_32x32x16_bf16 v[0:15], v[96:99], v[176:179], v[0:15]\n"
"ds_read_b64_tr_b16 v[108:109], v241 offset:33280\n"
"ds_read_b64_tr_b16 v[110:111], v241 offset:35328\n"
"s_add_u32 s58, s58, 1\n"
"s_and_b32 s58, s58, 15\n"
"s_waitcnt lgkmcnt(4)\n"
"v_mfma_f32_32x32x16_bf16 v[16:31], v[100:103], v[176:179], v[16:31]\n"
"ds_read_b64_tr_b16 v[112:113], v241 offset:36864\n"
"ds_read_b64_tr_b16 v[114:115], v241 offset:38912\n"
"s_lshl_b32 s81, s58, 10\n"
"s_add_u32 s81, s81, 0x10000\n"
"s_waitcnt lgkmcnt(4)\n"
"v_mfma_f32_32x32x16_bf16 v[32:47], v[104:107], v[176:179], v[32:47]\n"
"ds_read_b64_tr_b16 v[116:117], v241 offset:37376\n"
"ds_read_b64_tr_b16 v[118:119], v241 offset:39424\n"
"ds_read_b64_tr_b16 v[96:97], v241 offset:29696\n"
"ds_read_b64_tr_b16 v[98:99], v241 offset:31744\n"
"s_waitcnt lgkmcnt(6)\n"
"v_mfma_f32_32x32x16_bf16 v[48:63], v[108:111], v[176:179], v[48:63]\n"
"s_waitcnt lgkmcnt(4)\n"
"buffer_load_dwordx4 v[120:123], v245, s[28:31], s81 offen sc1\n"
"v_mfma_f32_32x32x16_bf16 v[64:79], v[112:115], v[176:179], v[64:79]\n"
"ds_read_b64_tr_b16 v[100:101], v241 offset:30208\n"
"ds_read_b64_tr_b16 v[102:103], v241 offset:32256\n"
"s_waitcnt lgkmcnt(4)\n"
"v_mfma_f32_32x32x16_bf16 v[80:95], v[116:119], v[176:179], v[80:95]\n"
"ds_read_b64_tr_b16 v[104:105], v241 offset:33792\n"
"ds_read_b64_tr_b16 v[106:107], v241 offset:35840\n"
"s_waitcnt lgkmcnt(4)\n"
"v_mfma_f32_32x32x16_bf16 v[0:15], v[96:99], v[180:183], v[0:15]\n"
"ds_read_b64_tr_b16 v[108:109], v241 offset:34304\n"
"ds_read_b64_tr_b16 v[110:111], v241 offset:36352\n"
"ds_read_b64_tr_b16 v[112:113], v241 offset:37888\n"
"ds_read_b64_tr_b16 v[114:115], v241 offset:39936\n"
"s_waitcnt lgkmcnt(6)\n"
"v_mfma_f32_32x32x16_bf16 v[16:31], v[100:103], v[180:183], v[16:31]\n"
"s_waitcnt lgkmcnt(4)\n"
"buffer_load_dwordx4 v[124:127], v245, s[28:31], s81 offen offset:512 sc1\n"
"v_mfma_f32_32x32x16_bf16 v[32:47], v[104:107], v[180:183], v[32:47]\n"
"ds_read_b64_tr_b16 v[116:117], v241 offset:38400\n"
"ds_read_b64_tr_b16 v[118:119], v241 offset:40448\n"
"s_waitcnt lgkmcnt(4)\n"
"v_mfma_f32_32x32x16_bf16 v[48:63], v[108:111], v[180:183], v[48:63]\n"
"s_waitcnt lgkmcnt(2)\n"
"ds_read_b64_tr_b16 v[96:97], v241 offset:40960\n"
"ds_read_b64_tr_b16 v[98:99], v241 offset:43008\n"
"v_mfma_f32_32x32x16_bf16 v[64:79], v[112:115], v[180:183], v[64:79]\n"
"s_waitcnt lgkmcnt(2)\n"
"ds_read_b64_tr_b16 v[100:101], v241 offset:41472\n"
"ds_read_b64_tr_b16 v[102:103], v241 offset:43520\n"
"v_mfma_f32_32x32x16_bf16 v[80:95], v[116:119], v[180:183], v[80:95]\n"
"ds_read_b64_tr_b16 v[104:105], v241 offset:45056\n"
"ds_read_b64_tr_b16 v[106:107], v241 offset:47104\n"
"s_barrier\n"
"s_waitcnt vmcnt(10)\n"
"s_waitcnt lgkmcnt(4)\n"
"v_mfma_f32_32x32x16_bf16 v[0:15], v[96:99], v[184:187], v[0:15]\n"
"ds_read_b64_tr_b16 v[108:109], v241 offset:45568\n"
"ds_read_b64_tr_b16 v[110:111], v241 offset:47616\n"
"s_add_u32 s58, s58, 1\n"
"s_and_b32 s58, s58, 15\n"
"s_waitcnt lgkmcnt(4)\n"
"v_mfma_f32_32x32x16_bf16 v[16:31], v[100:103], v[184:187], v[16:31]\n"
"ds_read_b64_tr_b16 v[112:113], v241 offset:49152\n"
"ds_read_b64_tr_b16 v[114:115], v241 offset:51200\n"
"s_lshl_b32 s81, s58, 10\n"
"s_add_u32 s81, s81, 0x10000\n"
"s_waitcnt lgkmcnt(4)\n"
"v_mfma_f32_32x32x16_bf16 v[32:47], v[104:107], v[184:187], v[32:47]\n"
"ds_read_b64_tr_b16 v[116:117], v241 offset:49664\n"
"ds_read_b64_tr_b16 v[118:119], v241 offset:51712\n"
"ds_read_b64_tr_b16 v[96:97], v241 offset:41984\n"
"ds_read_b64_tr_b16 v[98:99], v241 offset:44032\n"
"s_waitcnt lgkmcnt(6)\n"
"v_mfma_f32_32x32x16_bf16 v[48:63], v[108:111], v[184:187], v[48:63]\n"
"s_waitcnt lgkmcnt(4)\n"
"buffer_load_dwordx4 v[128:131], v245, s[28:31], s81 offen sc1\n"
"v_mfma_f32_32x32x16_bf16 v[64:79], v[112:115], v[184:187], v[64:79]\n"
"ds_read_b64_tr_b16 v[100:101], v241 offset:42496\n"
"ds_read_b64_tr_b16 v[102:103], v241 offset:44544\n"
"s_waitcnt lgkmcnt(4)\n"
"v_mfma_f32_32x32x16_bf16 v[80:95], v[116:119], v[184:187], v[80:95]\n"
"ds_read_b64_tr_b16 v[104:105], v241 offset:46080\n"
"ds_read_b64_tr_b16 v[106:107], v241 offset:48128\n"
"s_waitcnt lgkmcnt(4)\n"
"v_mfma_f32_32x32x16_bf16 v[0:15], v[96:99], v[188:191], v[0:15]\n"
"ds_read_b64_tr_b16 v[108:109], v241 offset:46592\n"
"ds_read_b64_tr_b16 v[110:111], v241 offset:48640\n"
"ds_read_b64_tr_b16 v[112:113], v241 offset:50176\n"
"ds_read_b64_tr_b16 v[114:115], v241 offset:52224\n"
"s_waitcnt lgkmcnt(6)\n"
"v_mfma_f32_32x32x16_bf16 v[16:31], v[100:103], v[188:191], v[16:31]\n"
"s_waitcnt lgkmcnt(4)\n"
"buffer_load_dwordx4 v[132:135], v245, s[28:31], s81 offen offset:512 sc1\n"
"v_mfma_f32_32x32x16_bf16 v[32:47], v[104:107], v[188:191], v[32:47]\n"
"ds_read_b64_tr_b16 v[116:117], v241 offset:50688\n"
"ds_read_b64_tr_b16 v[118:119], v241 offset:52736\n"
"s_waitcnt lgkmcnt(4)\n"
"v_mfma_f32_32x32x16_bf16 v[48:63], v[108:111], v[188:191], v[48:63]\n"
"s_waitcnt lgkmcnt(2)\n"
"ds_read_b64_tr_b16 v[96:97], v241 offset:53248\n"
"ds_read_b64_tr_b16 v[98:99], v241 offset:55296\n"
"v_mfma_f32_32x32x16_bf16 v[64:79], v[112:115], v[188:191], v[64:79]\n"
"s_waitcnt lgkmcnt(2)\n"
"ds_read_b64_tr_b16 v[100:101], v241 offset:53760\n"
"ds_read_b64_tr_b16 v[102:103], v241 offset:55808\n"
"v_mfma_f32_32x32x16_bf16 v[80:95], v[116:119], v[188:191], v[80:95]\n"
"ds_read_b64_tr_b16 v[104:105], v241 offset:57344\n"
"ds_read_b64_tr_b16 v[106:107], v241 offset:59392\n"
"s_waitcnt vmcnt(10)\n"
"s_waitcnt lgkmcnt(4)\n"
"v_mfma_f32_32x32x16_bf16 v[0:15], v[96:99], v[192:195], v[0:15]\n"
"ds_read_b64_tr_b16 v[108:109], v241 offset:57856\n"
"ds_read_b64_tr_b16 v[110:111], v241 offset:59904\n"
"s_add_u32 s58, s58, 1\n"
"s_and_b32 s58, s58, 15\n"
"s_waitcnt lgkmcnt(4)\n"
"v_mfma_f32_32x32x16_bf16 v[16:31], v[100:103], v[192:195], v[16:31]\n"
"ds_read_b64_tr_b16 v[112:113], v241 offset:61440\n"
"ds_read_b64_tr_b16 v[114:115], v241 offset:63488\n"
"s_lshl_b32 s81, s58, 10\n"
"s_add_u32 s81, s81, 0x10000\n"
"s_waitcnt lgkmcnt(4)\n"
"v_mfma_f32_32x32x16_bf16 v[32:47], v[104:107], v[192:195], v[32:47]\n"
"ds_read_b64_tr_b16 v[116:117], v241 offset:61952\n"
"ds_read_b64_tr_b16 v[118:119], v241 offset:64000\n"
"ds_read_b64_tr_b16 v[96:97], v241 offset:54272\n"
"ds_read_b64_tr_b16 v[98:99], v241 offset:56320\n"
"s_waitcnt lgkmcnt(6)\n"
"v_mfma_f32_32x32x16_bf16 v[48:63], v[108:111], v[192:195], v[48:63]\n"
"s_waitcnt lgkmcnt(4)\n"
"buffer_load_dwordx4 v[136:139], v245, s[28:31], s81 offen sc1\n"
"v_mfma_f32_32x32x16_bf16 v[64:79], v[112:115], v[192:195], v[64:79]\n"
"ds_read_b64_tr_b16 v[100:101], v241 offset:54784\n"
"ds_read_b64_tr_b16 v[102:103], v241 offset:56832\n"
"s_waitcnt lgkmcnt(4)\n"
"v_mfma_f32_32x32x16_bf16 v[80:95], v[116:119], v[192:195], v[80:95]\n"
"ds_read_b64_tr_b16 v[104:105], v241 offset:58368\n"
"ds_read_b64_tr_b16 v[106:107], v241 offset:60416\n"
"s_waitcnt lgkmcnt(4)\n"
"v_mfma_f32_32x32x16_bf16 v[0:15], v[96:99], v[196:199], v[0:15]\n"
"ds_read_b64_tr_b16 v[108:109], v241 offset:58880\n"
"ds_read_b64_tr_b16 v[110:111], v241 offset:60928\n"
"ds_read_b64_tr_b16 v[112:113], v241 offset:62464\n"
"ds_read_b64_tr_b16 v[114:115], v241 offset:64512\n"
"s_waitcnt lgkmcnt(6)\n"
"v_mfma_f32_32x32x16_bf16 v[16:31], v[100:103], v[196:199], v[16:31]\n"
"s_waitcnt lgkmcnt(4)\n"
"buffer_load_dwordx4 v[140:143], v245, s[28:31], s81 offen offset:512 sc1\n"
"v_mfma_f32_32x32x16_bf16 v[32:47], v[104:107], v[196:199], v[32:47]\n"
"ds_read_b64_tr_b16 v[116:117], v241 offset:62976\n"
"ds_read_b64_tr_b16 v[118:119], v241 offset:65024\n"
"s_waitcnt lgkmcnt(4)\n"
"v_mfma_f32_32x32x16_bf16 v[48:63], v[108:111], v[196:199], v[48:63]\n"
"s_waitcnt lgkmcnt(2)\n"
"ds_read_b64_tr_b16 v[96:97], v240\n"
"ds_read_b64_tr_b16 v[98:99], v240 offset:2048\n"
"v_mfma_f32_32x32x16_bf16 v[64:79], v[112:115], v[196:199], v[64:79]\n"
"s_waitcnt lgkmcnt(2)\n"
"ds_read_b64_tr_b16 v[100:101], v240 offset:512\n"
"ds_read_b64_tr_b16 v[102:103], v240 offset:2560\n"
"v_mfma_f32_32x32x16_bf16 v[80:95], v[116:119], v[196:199], v[80:95]\n"
"ds_read_b64_tr_b16 v[104:105], v240 offset:4096\n"
"ds_read_b64_tr_b16 v[106:107], v240 offset:6144\n"
"s_barrier\n"
"s_waitcnt vmcnt(10)\n"
"s_waitcnt lgkmcnt(4)\n"
"v_mfma_f32_32x32x16_bf16 v[0:15], v[96:99], v[200:203], v[0:15]\n"
"ds_read_b64_tr_b16 v[108:109], v240 offset:4608\n"
"ds_read_b64_tr_b16 v[110:111], v240 offset:6656\n"
"s_add_u32 s58, s58, 1\n"
"s_and_b32 s58, s58, 15\n"
"s_waitcnt lgkmcnt(4)\n"
"v_mfma_f32_32x32x16_bf16 v[16:31], v[100:103], v[200:203], v[16:31]\n"
"ds_read_b64_tr_b16 v[112:113], v240 offset:8192\n"
"ds_read_b64_tr_b16 v[114:115], v240 offset:10240\n"
"s_lshl_b32 s81, s58, 10\n"
"s_add_u32 s81, s81, 0x10000\n"
"s_waitcnt lgkmcnt(4)\n"
"v_mfma_f32_32x32x16_bf16 v[32:47], v[104:107], v[200:203], v[32:47]\n"
"ds_read_b64_tr_b16 v[116:117], v240 offset:8704\n"
"ds_read_b64_tr_b16 v[118:119], v240 offset:10752\n"
"ds_read_b64_tr_b16 v[96:97], v240 offset:1024\n"
"ds_read_b64_tr_b16 v[98:99], v240 offset:3072\n"
"s_waitcnt lgkmcnt(6)\n"
"v_mfma_f32_32x32x16_bf16 v[48:63], v[108:111], v[200:203], v[48:63]\n"
"s_waitcnt lgkmcnt(4)\n"
"buffer_load_dwordx4 v[144:147], v245, s[28:31], s81 offen sc1\n"
"v_mfma_f32_32x32x16_bf16 v[64:79], v[112:115], v[200:203], v[64:79]\n"
"ds_read_b64_tr_b16 v[100:101], v240 offset:1536\n"
"ds_read_b64_tr_b16 v[102:103], v240 offset:3584\n"
"s_waitcnt lgkmcnt(4)\n"
"v_mfma_f32_32x32x16_bf16 v[80:95], v[116:119], v[200:203], v[80:95]\n"
"ds_read_b64_tr_b16 v[104:105], v240 offset:5120\n"
"ds_read_b64_tr_b16 v[106:107], v240 offset:7168\n"
"s_waitcnt lgkmcnt(4)\n"
"v_mfma_f32_32x32x16_bf16 v[0:15], v[96:99], v[204:207], v[0:15]\n"
"ds_read_b64_tr_b16 v[108:109], v240 offset:5632\n"
"ds_read_b64_tr_b16 v[110:111], v240 offset:7680\n"
"ds_read_b64_tr_b16 v[112:113], v240 offset:9216\n"
"ds_read_b64_tr_b16 v[114:115], v240 offset:11264\n"
"s_waitcnt lgkmcnt(6)\n"
"v_mfma_f32_32x32x16_bf16 v[16:31], v[100:103], v[204:207], v[16:31]\n"
"s_waitcnt lgkmcnt(4)\n"
"buffer_load_dwordx4 v[148:151], v245, s[28:31], s81 offen offset:512 sc1\n"
"v_mfma_f32_32x32x16_bf16 v[32:47], v[104:107], v[204:207], v[32:47]\n"
"ds_read_b64_tr_b16 v[116:117], v240 offset:9728\n"
"ds_read_b64_tr_b16 v[118:119], v240 offset:11776\n"
"s_waitcnt lgkmcnt(4)\n"
"v_mfma_f32_32x32x16_bf16 v[48:63], v[108:111], v[204:207], v[48:63]\n"
"s_waitcnt lgkmcnt(2)\n"
"ds_read_b64_tr_b16 v[96:97], v240 offset:12288\n"
"ds_read_b64_tr_b16 v[98:99], v240 offset:14336\n"
"v_mfma_f32_32x32x16_bf16 v[64:79], v[112:115], v[204:207], v[64:79]\n"
"s_waitcnt lgkmcnt(2)\n"
"ds_read_b64_tr_b16 v[100:101], v240 offset:12800\n"
"ds_read_b64_tr_b16 v[102:103], v240 offset:14848\n"
"v_mfma_f32_32x32x16_bf16 v[80:95], v[116:119], v[204:207], v[80:95]\n"
"ds_read_b64_tr_b16 v[104:105], v240 offset:16384\n"
"ds_read_b64_tr_b16 v[106:107], v240 offset:18432\n"
"s_waitcnt vmcnt(10)\n"
"s_waitcnt lgkmcnt(4)\n"
"v_mfma_f32_32x32x16_bf16 v[0:15], v[96:99], v[208:211], v[0:15]\n"
"ds_read_b64_tr_b16 v[108:109], v240 offset:16896\n"
"ds_read_b64_tr_b16 v[110:111], v240 offset:18944\n"
"s_add_u32 s58, s58, 1\n"
"s_and_b32 s58, s58, 15\n"
"s_waitcnt lgkmcnt(4)\n"
"v_mfma_f32_32x32x16_bf16 v[16:31], v[100:103], v[208:211], v[16:31]\n"
"ds_read_b64_tr_b16 v[112:113], v240 offset:20480\n"
"ds_read_b64_tr_b16 v[114:115], v240 offset:22528\n"
"s_lshl_b32 s81, s58, 10\n"
"s_add_u32 s81, s81, 0x10000\n"
"s_waitcnt lgkmcnt(4)\n"
"v_mfma_f32_32x32x16_bf16 v[32:47], v[104:107], v[208:211], v[32:47]\n"
"ds_read_b64_tr_b16 v[116:117], v240 offset:20992\n"
"ds_read_b64_tr_b16 v[118:119], v240 offset:23040\n"
"ds_read_b64_tr_b16 v[96:97], v240 offset:13312\n"
"ds_read_b64_tr_b16 v[98:99], v240 offset:15360\n"
"s_waitcnt lgkmcnt(6)\n"
"v_mfma_f32_32x32x16_bf16 v[48:63], v[108:111], v[208:211], v[48:63]\n"
"s_waitcnt lgkmcnt(4)\n"
"buffer_load_dwordx4 v[152:155], v245, s[28:31], s81 offen sc1\n"
"v_mfma_f32_32x32x16_bf16 v[64:79], v[112:115], v[208:211], v[64:79]\n"
"ds_read_b64_tr_b16 v[100:101], v240 offset:13824\n"
"ds_read_b64_tr_b16 v[102:103], v240 offset:15872\n"
"s_waitcnt lgkmcnt(4)\n"
"v_mfma_f32_32x32x16_bf16 v[80:95], v[116:119], v[208:211], v[80:95]\n"
"ds_read_b64_tr_b16 v[104:105], v240 offset:17408\n"
"ds_read_b64_tr_b16 v[106:107], v240 offset:19456\n"
"s_waitcnt lgkmcnt(4)\n"
"v_mfma_f32_32x32x16_bf16 v[0:15], v[96:99], v[212:215], v[0:15]\n"
"ds_read_b64_tr_b16 v[108:109], v240 offset:17920\n"
"ds_read_b64_tr_b16 v[110:111], v240 offset:19968\n"
"ds_read_b64_tr_b16 v[112:113], v240 offset:21504\n"
"ds_read_b64_tr_b16 v[114:115], v240 offset:23552\n"
"s_waitcnt lgkmcnt(6)\n"
"v_mfma_f32_32x32x16_bf16 v[16:31], v[100:103], v[212:215], v[16:31]\n"
"s_waitcnt lgkmcnt(4)\n"
"buffer_load_dwordx4 v[156:159], v245, s[28:31], s81 offen offset:512 sc1\n"
"v_mfma_f32_32x32x16_bf16 v[32:47], v[104:107], v[212:215], v[32:47]\n"
"ds_read_b64_tr_b16 v[116:117], v240 offset:22016\n"
"ds_read_b64_tr_b16 v[118:119], v240 offset:24064\n"
"s_waitcnt lgkmcnt(4)\n"
"v_mfma_f32_32x32x16_bf16 v[48:63], v[108:111], v[212:215], v[48:63]\n"
"s_waitcnt lgkmcnt(2)\n"
"ds_read_b64_tr_b16 v[96:97], v240 offset:24576\n"
"ds_read_b64_tr_b16 v[98:99], v240 offset:26624\n"
"v_mfma_f32_32x32x16_bf16 v[64:79], v[112:115], v[212:215], v[64:79]\n"
"s_waitcnt lgkmcnt(2)\n"
"ds_read_b64_tr_b16 v[100:101], v240 offset:25088\n"
"ds_read_b64_tr_b16 v[102:103], v240 offset:27136\n"
"v_mfma_f32_32x32x16_bf16 v[80:95], v[116:119], v[212:215], v[80:95]\n"
"ds_read_b64_tr_b16 v[104:105], v240 offset:28672\n"
"ds_read_b64_tr_b16 v[106:107], v240 offset:30720\n"
"s_barrier\n"
"s_waitcnt vmcnt(10)\n"
"s_waitcnt lgkmcnt(4)\n"
"v_mfma_f32_32x32x16_bf16 v[0:15], v[96:99], v[216:219], v[0:15]\n"
"ds_read_b64_tr_b16 v[108:109], v240 offset:29184\n"
"ds_read_b64_tr_b16 v[110:111], v240 offset:31232\n"
"s_add_u32 s58, s58, 1\n"
"s_and_b32 s58, s58, 15\n"
"s_waitcnt lgkmcnt(4)\n"
"v_mfma_f32_32x32x16_bf16 v[16:31], v[100:103], v[216:219], v[16:31]\n"
"ds_read_b64_tr_b16 v[112:113], v240 offset:32768\n"
"ds_read_b64_tr_b16 v[114:115], v240 offset:34816\n"
"s_lshl_b32 s81, s58, 10\n"
"s_add_u32 s81, s81, 0x10000\n"
"s_waitcnt lgkmcnt(4)\n"
"v_mfma_f32_32x32x16_bf16 v[32:47], v[104:107], v[216:219], v[32:47]\n"
"ds_read_b64_tr_b16 v[116:117], v240 offset:33280\n"
"ds_read_b64_tr_b16 v[118:119], v240 offset:35328\n"
"ds_read_b64_tr_b16 v[96:97], v240 offset:25600\n"
"ds_read_b64_tr_b16 v[98:99], v240 offset:27648\n"
"s_waitcnt lgkmcnt(6)\n"
"v_mfma_f32_32x32x16_bf16 v[48:63], v[108:111], v[216:219], v[48:63]\n"
"s_waitcnt lgkmcnt(4)\n"
"buffer_load_dwordx4 v[160:163], v245, s[28:31], s81 offen sc1\n"
"v_mfma_f32_32x32x16_bf16 v[64:79], v[112:115], v[216:219], v[64:79]\n"
"ds_read_b64_tr_b16 v[100:101], v240 offset:26112\n"
"ds_read_b64_tr_b16 v[102:103], v240 offset:28160\n"
"s_waitcnt lgkmcnt(4)\n"
"v_mfma_f32_32x32x16_bf16 v[80:95], v[116:119], v[216:219], v[80:95]\n"
"ds_read_b64_tr_b16 v[104:105], v240 offset:29696\n"
"ds_read_b64_tr_b16 v[106:107], v240 offset:31744\n"
"s_waitcnt lgkmcnt(4)\n"
"v_mfma_f32_32x32x16_bf16 v[0:15], v[96:99], v[220:223], v[0:15]\n"
"ds_read_b64_tr_b16 v[108:109], v240 offset:30208\n"
"ds_read_b64_tr_b16 v[110:111], v240 offset:32256\n"
"ds_read_b64_tr_b16 v[112:113], v240 offset:33792\n"
"ds_read_b64_tr_b16 v[114:115], v240 offset:35840\n"
"s_waitcnt lgkmcnt(6)\n"
"v_mfma_f32_32x32x16_bf16 v[16:31], v[100:103], v[220:223], v[16:31]\n"
"s_waitcnt lgkmcnt(4)\n"
"buffer_load_dwordx4 v[164:167], v245, s[28:31], s81 offen offset:512 sc1\n"
"v_mfma_f32_32x32x16_bf16 v[32:47], v[104:107], v[220:223], v[32:47]\n"
"ds_read_b64_tr_b16 v[116:117], v240 offset:34304\n"
"ds_read_b64_tr_b16 v[118:119], v240 offset:36352\n"
"s_waitcnt lgkmcnt(4)\n"
"v_mfma_f32_32x32x16_bf16 v[48:63], v[108:111], v[220:223], v[48:63]\n"
"s_waitcnt lgkmcnt(2)\n"
"ds_read_b64_tr_b16 v[96:97], v240 offset:36864\n"
"ds_read_b64_tr_b16 v[98:99], v240 offset:38912\n"
"v_mfma_f32_32x32x16_bf16 v[64:79], v[112:115], v[220:223], v[64:79]\n"
"s_waitcnt lgkmcnt(2)\n"
"ds_read_b64_tr_b16 v[100:101], v240 offset:37376\n"
"ds_read_b64_tr_b16 v[102:103], v240 offset:39424\n"
"v_mfma_f32_32x32x16_bf16 v[80:95], v[116:119], v[220:223], v[80:95]\n"
"ds_read_b64_tr_b16 v[104:105], v240 offset:40960\n"
"ds_read_b64_tr_b16 v[106:107], v240 offset:43008\n"
"s_waitcnt vmcnt(10)\n"
"s_waitcnt lgkmcnt(4)\n"
"v_mfma_f32_32x32x16_bf16 v[0:15], v[96:99], v[120:123], v[0:15]\n"
"ds_read_b64_tr_b16 v[108:109], v240 offset:41472\n"
"ds_read_b64_tr_b16 v[110:111], v240 offset:43520\n"
"s_waitcnt lgkmcnt(4)\n"
"v_mfma_f32_32x32x16_bf16 v[16:31], v[100:103], v[120:123], v[16:31]\n"
"ds_read_b64_tr_b16 v[112:113], v240 offset:45056\n"
"ds_read_b64_tr_b16 v[114:115], v240 offset:47104\n"
"s_waitcnt lgkmcnt(4)\n"
"v_mfma_f32_32x32x16_bf16 v[32:47], v[104:107], v[120:123], v[32:47]\n"
"ds_read_b64_tr_b16 v[116:117], v240 offset:45568\n"
"ds_read_b64_tr_b16 v[118:119], v240 offset:47616\n"
"s_waitcnt lgkmcnt(4)\n"
"v_mfma_f32_32x32x16_bf16 v[48:63], v[108:111], v[120:123], v[48:63]\n"
"ds_read_b64_tr_b16 v[96:97], v240 offset:37888\n"
"ds_read_b64_tr_b16 v[98:99], v240 offset:39936\n"
"s_waitcnt lgkmcnt(4)\n"
"v_mfma_f32_32x32x16_bf16 v[64:79], v[112:115], v[120:123], v[64:79]\n"
"ds_read_b64_tr_b16 v[100:101], v240 offset:38400\n"
"ds_read_b64_tr_b16 v[102:103], v240 offset:40448\n"
"s_waitcnt lgkmcnt(4)\n"
"v_mfma_f32_32x32x16_bf16 v[80:95], v[116:119], v[120:123], v[80:95]\n"
"ds_read_b64_tr_b16 v[104:105], v240 offset:41984\n"
"ds_read_b64_tr_b16 v[106:107], v240 offset:44032\n"
"s_waitcnt lgkmcnt(4)\n"
"v_mfma_f32_32x32x16_bf16 v[0:15], v[96:99], v[124:127], v[0:15]\n"
"ds_read_b64_tr_b16 v[108:109], v240 offset:42496\n"
"ds_read_b64_tr_b16 v[110:111], v240 offset:44544\n"
"s_waitcnt lgkmcnt(4)\n"
"v_mfma_f32_32x32x16_bf16 v[16:31], v[100:103], v[124:127], v[16:31]\n"
"ds_read_b64_tr_b16 v[112:113], v240 offset:46080\n"
"ds_read_b64_tr_b16 v[114:115], v240 offset:48128\n"
"s_waitcnt lgkmcnt(4)\n"
"v_mfma_f32_32x32x16_bf16 v[32:47], v[104:107], v[124:127], v[32:47]\n"
"ds_read_b64_tr_b16 v[116:117], v240 offset:46592\n"
"ds_read_b64_tr_b16 v[118:119], v240 offset:48640\n"
"s_waitcnt lgkmcnt(4)\n"
"v_mfma_f32_32x32x16_bf16 v[48:63], v[108:111], v[124:127], v[48:63]\n"
"s_waitcnt lgkmcnt(2)\n"
"ds_read_b64_tr_b16 v[96:97], v241 offset:4096\n"
"ds_read_b64_tr_b16 v[98:99], v241 offset:6144\n"
"v_mfma_f32_32x32x16_bf16 v[64:79], v[112:115], v[124:127], v[64:79]\n"
"s_waitcnt lgkmcnt(2)\n"
"ds_read_b64_tr_b16 v[100:101], v241 offset:4608\n"
"ds_read_b64_tr_b16 v[102:103], v241 offset:6656\n"
"v_mfma_f32_32x32x16_bf16 v[80:95], v[116:119], v[124:127], v[80:95]\n"
"ds_read_b64_tr_b16 v[104:105], v241 offset:8192\n"
"ds_read_b64_tr_b16 v[106:107], v241 offset:10240\n"
"s_barrier\n"
"s_waitcnt vmcnt(8)\n"
"s_waitcnt lgkmcnt(4)\n"
"v_mfma_f32_32x32x16_bf16 v[0:15], v[96:99], v[128:131], v[0:15]\n"
"ds_read_b64_tr_b16 v[108:109], v241 offset:8704\n"
"ds_read_b64_tr_b16 v[110:111], v241 offset:10752\n"
"s_waitcnt lgkmcnt(4)\n"
"v_mfma_f32_32x32x16_bf16 v[16:31], v[100:103], v[128:131], v[16:31]\n"
"ds_read_b64_tr_b16 v[112:113], v241 offset:12288\n"
"ds_read_b64_tr_b16 v[114:115], v241 offset:14336\n"
"s_waitcnt lgkmcnt(4)\n"
"v_mfma_f32_32x32x16_bf16 v[32:47], v[104:107], v[128:131], v[32:47]\n"
"ds_read_b64_tr_b16 v[116:117], v241 offset:12800\n"
"ds_read_b64_tr_b16 v[118:119], v241 offset:14848\n"
"s_waitcnt lgkmcnt(4)\n"
"v_mfma_f32_32x32x16_bf16 v[48:63], v[108:111], v[128:131], v[48:63]\n"
"ds_read_b64_tr_b16 v[96:97], v241 offset:5120\n"
"ds_read_b64_tr_b16 v[98:99], v241 offset:7168\n"
"s_waitcnt lgkmcnt(4)\n"
"v_mfma_f32_32x32x16_bf16 v[64:79], v[112:115], v[128:131], v[64:79]\n"
"ds_read_b64_tr_b16 v[100:101], v241 offset:5632\n"
"ds_read_b64_tr_b16 v[102:103], v241 offset:7680\n"
"s_waitcnt lgkmcnt(4)\n"
"v_mfma_f32_32x32x16_bf16 v[80:95], v[116:119], v[128:131], v[80:95]\n"
"ds_read_b64_tr_b16 v[104:105], v241 offset:9216\n"
"ds_read_b64_tr_b16 v[106:107], v241 offset:11264\n"
"s_waitcnt lgkmcnt(4)\n"
"v_mfma_f32_32x32x16_bf16 v[0:15], v[96:99], v[132:135], v[0:15]\n"
"ds_read_b64_tr_b16 v[108:109], v241 offset:9728\n"
"ds_read_b64_tr_b16 v[110:111], v241 offset:11776\n"
"s_waitcnt lgkmcnt(4)\n"
"v_mfma_f32_32x32x16_bf16 v[16:31], v[100:103], v[132:135], v[16:31]\n"
"ds_read_b64_tr_b16 v[112:113], v241 offset:13312\n"
"ds_read_b64_tr_b16 v[114:115], v241 offset:15360\n"
"s_waitcnt lgkmcnt(4)\n"
"v_mfma_f32_32x32x16_bf16 v[32:47], v[104:107], v[132:135], v[32:47]\n"
"ds_read_b64_tr_b16 v[116:117], v241 offset:13824\n"
"ds_read_b64_tr_b16 v[118:119], v241 offset:15872\n"
"s_waitcnt lgkmcnt(4)\n"
"v_mfma_f32_32x32x16_bf16 v[48:63], v[108:111], v[132:135], v[48:63]\n"
"s_waitcnt lgkmcnt(2)\n"
"ds_read_b64_tr_b16 v[96:97], v241 offset:16384\n"
"ds_read_b64_tr_b16 v[98:99], v241 offset:18432\n"
"v_mfma_f32_32x32x16_bf16 v[64:79], v[112:115], v[132:135], v[64:79]\n"
"s_waitcnt lgkmcnt(2)\n"
"ds_read_b64_tr_b16 v[100:101], v241 offset:17408\n"
"ds_read_b64_tr_b16 v[102:103], v241 offset:19456\n"
"v_mfma_f32_32x32x16_bf16 v[80:95], v[116:119], v[132:135], v[80:95]\n"
"ds_read_b64_tr_b16 v[104:105], v241 offset:28672\n"
"ds_read_b64_tr_b16 v[106:107], v241 offset:30720\n"
"s_barrier\n"
"s_mul_i32 s61, s68, 3\n"
"s_add_u32 s61, s61, s69\n"
"s_mul_i32 s60, s67, 54\n"
"s_add_u32 s60, s60, s61\n"
"s_lshl_b32 s60, s60, 2\n"
"s_add_u32 s60, s60, s44\n"
"s_mul_i32 s60, s60, 0x3000\n"
"s_add_u32 s60, s60, 0x1400000\n"
"s_add_u32 s88, s16, s60\n"
"s_addc_u32 s89, s17, 0\n"
"s_and_b32 s89, s89, 0xffff\n"
"s_mov_b32 s88, s88\n"
"s_mov_b32 s90, 0x3000\n"
"s_mov_b32 s91, 0x20000\n"
"s_mov_b32 s57, 0x1000\n"
"s_mov_b32 s58, 0x2000\n"
"s_waitcnt vmcnt(0)\n"
"s_waitcnt lgkmcnt(4)\n"
"v_mfma_f32_32x32x16_bf16 v[0:15], v[96:99], v[136:139], v[0:15]\n"
"ds_read_b64_tr_b16 v[108:109], v241 offset:29696\n"
"ds_read_b64_tr_b16 v[110:111], v241 offset:31744\n"
"s_waitcnt lgkmcnt(4)\n"
"v_mfma_f32_32x32x16_bf16 v[0:15], v[100:103], v[140:143], v[0:15]\n"
"ds_read_b64_tr_b16 v[112:113], v241 offset:40960\n"
"ds_read_b64_tr_b16 v[114:115], v241 offset:43008\n"
"s_waitcnt lgkmcnt(4)\n"
"v_mfma_f32_32x32x16_bf16 v[0:15], v[104:107], v[144:147], v[0:15]\n"
"ds_read_b64_tr_b16 v[116:117], v241 offset:41984\n"
"ds_read_b64_tr_b16 v[118:119], v241 offset:44032\n"
"s_waitcnt lgkmcnt(4)\n"
"v_mfma_f32_32x32x16_bf16 v[0:15], v[108:111], v[148:151], v[0:15]\n"
"ds_read_b64_tr_b16 v[96:97], v241 offset:53248\n"
"ds_read_b64_tr_b16 v[98:99], v241 offset:55296\n"
"s_waitcnt lgkmcnt(4)\n"
"v_mfma_f32_32x32x16_bf16 v[0:15], v[112:115], v[152:155], v[0:15]\n"
"ds_read_b64_tr_b16 v[100:101], v241 offset:54272\n"
"ds_read_b64_tr_b16 v[102:103], v241 offset:56320\n"
"s_waitcnt lgkmcnt(4)\n"
"v_mfma_f32_32x32x16_bf16 v[0:15], v[116:119], v[156:159], v[0:15]\n"
"ds_read_b64_tr_b16 v[104:105], v241 offset:16896\n"
"ds_read_b64_tr_b16 v[106:107], v241 offset:18944\n"
"s_waitcnt lgkmcnt(4)\n"
"v_mfma_f32_32x32x16_bf16 v[0:15], v[96:99], v[160:163], v[0:15]\n"
"ds_read_b64_tr_b16 v[108:109], v241 offset:17920\n"
"ds_read_b64_tr_b16 v[110:111], v241 offset:19968\n"
"s_waitcnt lgkmcnt(4)\n"
"v_mfma_f32_32x32x16_bf16 v[0:15], v[100:103], v[164:167], v[0:15]\n"
"ds_read_b64_tr_b16 v[112:113], v241 offset:29184\n"
"ds_read_b64_tr_b16 v[114:115], v241 offset:31232\n"
"s_waitcnt lgkmcnt(4)\n"
"v_mfma_f32_32x32x16_bf16 v[16:31], v[104:107], v[136:139], v[16:31]\n"
"ds_read_b64_tr_b16 v[116:117], v241 offset:30208\n"
"ds_read_b64_tr_b16 v[118:119], v241 offset:32256\n"
"s_waitcnt lgkmcnt(4)\n"
"v_mfma_f32_32x32x16_bf16 v[16:31], v[108:111], v[140:143], v[16:31]\n"
"ds_read_b64_tr_b16 v[96:97], v241 offset:41472\n"
"ds_read_b64_tr_b16 v[98:99], v241 offset:43520\n"
"s_waitcnt lgkmcnt(4)\n"
"v_mfma_f32_32x32x16_bf16 v[16:31], v[112:115], v[144:147], v[16:31]\n"
"ds_read_b64_tr_b16 v[100:101], v241 offset:42496\n"
"ds_read_b64_tr_b16 v[102:103], v241 offset:44544\n"
"v_cvt_pk_bf16_f32 v192, v0, v1\n"
"v_cvt_pk_bf16_f32 v193, v2, v3\n"
"v_cvt_pk_bf16_f32 v194, v4, v5\n"
"v_cvt_pk_bf16_f32 v195, v6, v7\n"
"s_waitcnt lgkmcnt(4)\n"
"v_mfma_f32_32x32x16_bf16 v[16:31], v[116:119], v[148:151], v[16:31]\n"
"ds_read_b64_tr_b16 v[104:105], v241 offset:53760\n"
"ds_read_b64_tr_b16 v[106:107], v241 offset:55808\n"
"v_cvt_pk_bf16_f32 v196, v8, v9\n"
"v_cvt_pk_bf16_f32 v197, v10, v11\n"
"v_cvt_pk_bf16_f32 v198, v12, v13\n"
"v_cvt_pk_bf16_f32 v199, v14, v15\n"
"s_waitcnt lgkmcnt(4)\n"
"v_mfma_f32_32x32x16_bf16 v[16:31], v[96:99], v[152:155], v[16:31]\n"
"ds_read_b64_tr_b16 v[108:109], v241 offset:54784\n"
"ds_read_b64_tr_b16 v[110:111], v241 offset:56832\n"
"v_permlane32_swap_b32 v192, v194\n"
"v_permlane32_swap_b32 v193, v195\n"
"v_permlane32_swap_b32 v196, v198\n"
"v_permlane32_swap_b32 v197, v199\n"
"s_waitcnt lgkmcnt(4)\n"
"v_mfma_f32_32x32x16_bf16 v[16:31], v[100:103], v[156:159], v[16:31]\n"
"ds_read_b64_tr_b16 v[112:113], v241 offset:20480\n"
"ds_read_b64_tr_b16 v[114:115], v241 offset:22528\n"
"buffer_store_dwordx4 v[192:195], v237, s[88:91], 0 offen sc1\n"
"buffer_store_dwordx4 v[196:199], v237, s[88:91], 0 offen offset:1024 sc1\n"
"s_waitcnt lgkmcnt(4)\n"
"v_mfma_f32_32x32x16_bf16 v[16:31], v[104:107], v[160:163], v[16:31]\n"
"ds_read_b64_tr_b16 v[116:117], v241 offset:21504\n"
"ds_read_b64_tr_b16 v[118:119], v241 offset:23552\n"
"s_waitcnt lgkmcnt(4)\n"
"v_mfma_f32_32x32x16_bf16 v[16:31], v[108:111], v[164:167], v[16:31]\n"
"ds_read_b64_tr_b16 v[96:97], v241 offset:32768\n"
"ds_read_b64_tr_b16 v[98:99], v241 offset:34816\n"
"s_waitcnt lgkmcnt(4)\n"
"v_mfma_f32_32x32x16_bf16 v[32:47], v[112:115], v[136:139], v[32:47]\n"
"ds_read_b64_tr_b16 v[100:101], v241 offset:33792\n"
"ds_read_b64_tr_b16 v[102:103], v241 offset:35840\n"
"s_waitcnt lgkmcnt(4)\n"
"v_mfma_f32_32x32x16_bf16 v[32:47], v[116:119], v[140:143], v[32:47]\n"
"ds_read_b64_tr_b16 v[104:105], v241 offset:45056\n"
"ds_read_b64_tr_b16 v[106:107], v241 offset:47104\n"
"s_waitcnt lgkmcnt(4)\n"
"v_mfma_f32_32x32x16_bf16 v[32:47], v[96:99], v[144:147], v[32:47]\n"
"ds_read_b64_tr_b16 v[108:109], v241 offset:46080\n"
"ds_read_b64_tr_b16 v[110:111], v241 offset:48128\n"
"v_cvt_pk_bf16_f32 v200, v16, v17\n"
"v_cvt_pk_bf16_f32 v201, v18, v19\n"
"v_cvt_pk_bf16_f32 v202, v20, v21\n"
"v_cvt_pk_bf16_f32 v203, v22, v23\n"
"s_waitcnt lgkmcnt(4)\n"
"v_mfma_f32_32x32x16_bf16 v[32:47], v[100:103], v[148:151], v[32:47]\n"
"ds_read_b64_tr_b16 v[112:113], v241 offset:57344\n"
"ds_read_b64_tr_b16 v[114:115], v241 offset:59392\n"
"v_cvt_pk_bf16_f32 v204, v24, v25\n"
"v_cvt_pk_bf16_f32 v205, v26, v27\n"
"v_cvt_pk_bf16_f32 v206, v28, v29\n"
"v_cvt_pk_bf16_f32 v207, v30, v31\n"
"s_waitcnt lgkmcnt(4)\n"
"v_mfma_f32_32x32x16_bf16 v[32:47], v[104:107], v[152:155], v[32:47]\n"
"ds_read_b64_tr_b16 v[116:117], v241 offset:58368\n"
"ds_read_b64_tr_b16 v[118:119], v241 offset:60416\n"
"v_permlane32_swap_b32 v200, v202\n"
"v_permlane32_swap_b32 v201, v203\n"
"v_permlane32_swap_b32 v204, v206\n"
"v_permlane32_swap_b32 v205, v207\n"
"s_waitcnt lgkmcnt(4)\n"
"v_mfma_f32_32x32x16_bf16 v[32:47], v[108:111], v[156:159], v[32:47]\n"
"ds_read_b64_tr_b16 v[96:97], v241 offset:20992\n"
"ds_read_b64_tr_b16 v[98:99], v241 offset:23040\n"
"buffer_store_dwordx4 v[200:203], v237, s[88:91], 0 offen offset:2048 sc1\n"
"buffer_store_dwordx4 v[204:207], v237, s[88:91], 0 offen offset:3072 sc1\n"
"s_waitcnt lgkmcnt(4)\n"
"v_mfma_f32_32x32x16_bf16 v[32:47], v[112:115], v[160:163], v[32:47]\n"
"ds_read_b64_tr_b16 v[100:101], v241 offset:22016\n"
"ds_read_b64_tr_b16 v[102:103], v241 offset:24064\n"
"s_waitcnt lgkmcnt(4)\n"
"v_mfma_f32_32x32x16_bf16 v[32:47], v[116:119], v[164:167], v[32:47]\n"
"ds_read_b64_tr_b16 v[104:105], v241 offset:33280\n"
"ds_read_b64_tr_b16 v[106:107], v241 offset:35328\n"
"s_waitcnt lgkmcnt(4)\n"
"v_mfma_f32_32x32x16_bf16 v[48:63], v[96:99], v[136:139], v[48:63]\n"
"ds_read_b64_tr_b16 v[108:109], v241 offset:34304\n"
"ds_read_b64_tr_b16 v[110:111], v241 offset:36352\n"
"s_waitcnt lgkmcnt(4)\n"
"v_mfma_f32_32x32x16_bf16 v[48:63], v[100:103], v[140:143], v[48:63]\n"
"ds_read_b64_tr_b16 v[112:113], v241 offset:45568\n"
"ds_read_b64_tr_b16 v[114:115], v241 offset:47616\n"
"s_waitcnt lgkmcnt(4)\n"
"v_mfma_f32_32x32x16_bf16 v[48:63], v[104:107], v[144:147], v[48:63]\n"
"ds_read_b64_tr_b16 v[116:117], v241 offset:46592\n"
"ds_read_b64_tr_b16 v[118:119], v241 offset:48640\n"
"v_cvt_pk_bf16_f32 v208, v32, v33\n"
"v_cvt_pk_bf16_f32 v209, v34, v35\n"
"v_cvt_pk_bf16_f32 v210, v36, v37\n"
"v_cvt_pk_bf16_f32 v211, v38, v39\n"
"s_waitcnt lgkmcnt(4)\n"
"v_mfma_f32_32x32x16_bf16 v[48:63], v[108:111], v[148:151], v[48:63]\n"
"ds_read_b64_tr_b16 v[96:97], v241 offset:57856\n"
"ds_read_b64_tr_b16 v[98:99], v241 offset:59904\n"
"v_cvt_pk_bf16_f32 v212, v40, v41\n"
"v_cvt_pk_bf16_f32 v213, v42, v43\n"
"v_cvt_pk_bf16_f32 v214, v44, v45\n"
"v_cvt_pk_bf16_f32 v215, v46, v47\n"
"s_waitcnt lgkmcnt(4)\n"
"v_mfma_f32_32x32x16_bf16 v[48:63], v[112:115], v[152:155], v[48:63]\n"
"ds_read_b64_tr_b16 v[100:101], v241 offset:58880\n"
"ds_read_b64_tr_b16 v[102:103], v241 offset:60928\n"
"v_permlane32_swap_b32 v208, v210\n"
"v_permlane32_swap_b32 v209, v211\n"
"v_permlane32_swap_b32 v212, v214\n"
"v_permlane32_swap_b32 v213, v215\n"
"s_waitcnt lgkmcnt(4)\n"
"v_mfma_f32_32x32x16_bf16 v[48:63], v[116:119], v[156:159], v[48:63]\n"
"ds_read_b64_tr_b16 v[104:105], v241 offset:24576\n"
"ds_read_b64_tr_b16 v[106:107], v241 offset:26624\n"
"buffer_store_dwordx4 v[208:211], v237, s[88:91], s57 offen sc1\n"
"buffer_store_dwordx4 v[212:215], v237, s[88:91], s57 offen offset:1024 sc1\n"
"s_waitcnt lgkmcnt(4)\n"
"v_mfma_f32_32x32x16_bf16 v[48:63], v[96:99], v[160:163], v[48:63]\n"
"ds_read_b64_tr_b16 v[108:109], v241 offset:25600\n"
"ds_read_b64_tr_b16 v[110:111], v241 offset:27648\n"
"s_waitcnt lgkmcnt(4)\n"
"v_mfma_f32_32x32x16_bf16 v[48:63], v[100:103], v[164:167], v[48:63]\n"
"ds_read_b64_tr_b16 v[112:113], v241 offset:36864\n"
"ds_read_b64_tr_b16 v[114:115], v241 offset:38912\n"
"s_waitcnt lgkmcnt(4)\n"
"v_mfma_f32_32x32x16_bf16 v[64:79], v[104:107], v[136:139], v[64:79]\n"
"ds_read_b64_tr_b16 v[116:117], v241 offset:37888\n"
"ds_read_b64_tr_b16 v[118:119], v241 offset:39936\n"
"s_waitcnt lgkmcnt(4)\n"
"v_mfma_f32_32x32x16_bf16 v[64:79], v[108:111], v[140:143], v[64:79]\n"
"ds_read_b64_tr_b16 v[96:97], v241 offset:49152\n"
"ds_read_b64_tr_b16 v[98:99], v241 offset:51200\n"
"s_waitcnt lgkmcnt(4)\n"
"v_mfma_f32_32x32x16_bf16 v[64:79], v[112:115], v[144:147], v[64:79]\n"
"ds_read_b64_tr_b16 v[100:101], v241 offset:50176\n"
"ds_read_b64_tr_b16 v[102:103], v241 offset:52224\n"
"v_cvt_pk_bf16_f32 v216, v48, v49\n"
"v_cvt_pk_bf16_f32 v217, v50, v51\n"
"v_cvt_pk_bf16_f32 v218, v52, v53\n"
"v_cvt_pk_bf16_f32 v219, v54, v55\n"
"s_waitcnt lgkmcnt(4)\n"
"v_mfma_f32_32x32x16_bf16 v[64:79], v[116:119], v[148:151], v[64:79]\n"
"ds_read_b64_tr_b16 v[104:105], v241 offset:61440\n"
"ds_read_b64_tr_b16 v[106:107], v241 offset:63488\n"
"v_cvt_pk_bf16_f32 v220, v56, v57\n"
"v_cvt_pk_bf16_f32 v221, v58, v59\n"
"v_cvt_pk_bf16_f32 v222, v60, v61\n"
"v_cvt_pk_bf16_f32 v223, v62, v63\n"
"s_waitcnt lgkmcnt(4)\n"
"v_mfma_f32_32x32x16_bf16 v[64:79], v[96:99], v[152:155], v[64:79]\n"
"ds_read_b64_tr_b16 v[108:109], v241 offset:62464\n"
"ds_read_b64_tr_b16 v[110:111], v241 offset:64512\n"
"v_permlane32_swap_b32 v216, v218\n"
"v_permlane32_swap_b32 v217, v219\n"
"v_permlane32_swap_b32 v220, v222\n"
"v_permlane32_swap_b32 v221, v223\n"
"s_waitcnt lgkmcnt(4)\n"
"v_mfma_f32_32x32x16_bf16 v[64:79], v[100:103], v[156:159], v[64:79]\n"
"ds_read_b64_tr_b16 v[112:113], v241 offset:25088\n"
"ds_read_b64_tr_b16 v[114:115], v241 offset:27136\n"
"buffer_store_dwordx4 v[216:219], v237, s[88:91], s57 offen offset:2048 sc1\n"
"buffer_store_dwordx4 v[220:223], v237, s[88:91], s57 offen offset:3072 sc1\n"
"s_waitcnt lgkmcnt(4)\n"
"v_mfma_f32_32x32x16_bf16 v[64:79], v[104:107], v[160:163], v[64:79]\n"
"ds_read_b64_tr_b16 v[116:117], v241 offset:26112\n"
"ds_read_b64_tr_b16 v[118:119], v241 offset:28160\n"
"s_waitcnt lgkmcnt(4)\n"
"v_mfma_f32_32x32x16_bf16 v[64:79], v[108:111], v[164:167], v[64:79]\n"
"ds_read_b64_tr_b16 v[96:97], v241 offset:37376\n"
"ds_read_b64_tr_b16 v[98:99], v241 offset:39424\n"
"s_waitcnt lgkmcnt(4)\n"
"v_mfma_f32_32x32x16_bf16 v[80:95], v[112:115], v[136:139], v[80:95]\n"
"ds_read_b64_tr_b16 v[100:101], v241 offset:38400\n"
"ds_read_b64_tr_b16 v[102:103], v241 offset:40448\n"
"s_waitcnt lgkmcnt(4)\n"
"v_mfma_f32_32x32x16_bf16 v[80:95], v[116:119], v[140:143], v[80:95]\n"
"ds_read_b64_tr_b16 v[104:105], v241 offset:49664\n"
"ds_read_b64_tr_b16 v[106:107], v241 offset:51712\n"
"s_waitcnt lgkmcnt(4)\n"
"v_mfma_f32_32x32x16_bf16 v[80:95], v[96:99], v[144:147], v[80:95]\n"
"ds_read_b64_tr_b16 v[108:109], v241 offset:50688\n"
"ds_read_b64_tr_b16 v[110:111], v241 offset:52736\n"
"v_cvt_pk_bf16_f32 v192, v64, v65\n"
"v_cvt_pk_bf16_f32 v193, v66, v67\n"
"v_cvt_pk_bf16_f32 v194, v68, v69\n"
"v_cvt_pk_bf16_f32 v195, v70, v71\n"
"s_waitcnt lgkmcnt(4)\n"
"v_mfma_f32_32x32x16_bf16 v[80:95], v[100:103], v[148:151], v[80:95]\n"
"ds_read_b64_tr_b16 v[112:113], v241 offset:61952\n"
"ds_read_b64_tr_b16 v[114:115], v241 offset:64000\n"
"v_cvt_pk_bf16_f32 v196, v72, v73\n"
"v_cvt_pk_bf16_f32 v197, v74, v75\n"
"v_cvt_pk_bf16_f32 v198, v76, v77\n"
"v_cvt_pk_bf16_f32 v199, v78, v79\n"
"s_waitcnt lgkmcnt(4)\n"
"v_mfma_f32_32x32x16_bf16 v[80:95], v[104:107], v[152:155], v[80:95]\n"
"ds_read_b64_tr_b16 v[116:117], v241 offset:62976\n"
"ds_read_b64_tr_b16 v[118:119], v241 offset:65024\n"
"v_permlane32_swap_b32 v192, v194\n"
"v_permlane32_swap_b32 v193, v195\n"
"v_permlane32_swap_b32 v196, v198\n"
"v_permlane32_swap_b32 v197, v199\n"
"s_waitcnt lgkmcnt(4)\n"
"v_mfma_f32_32x32x16_bf16 v[80:95], v[108:111], v[156:159], v[80:95]\n"
"s_waitcnt lgkmcnt(2)\n"
"buffer_store_dwordx4 v[192:195], v237, s[88:91], s58 offen sc1\n"
"buffer_store_dwordx4 v[196:199], v237, s[88:91], s58 offen offset:1024 sc1\n"
"v_mfma_f32_32x32x16_bf16 v[80:95], v[112:115], v[160:163], v[80:95]\n"
"s_waitcnt lgkmcnt(0)\n"
"v_mfma_f32_32x32x16_bf16 v[80:95], v[116:119], v[164:167], v[80:95]\n"
"s_nop 11\n"
"v_cvt_pk_bf16_f32 v200, v80, v81\n"
"v_cvt_pk_bf16_f32 v201, v82, v83\n"
"v_cvt_pk_bf16_f32 v202, v84, v85\n"
"v_cvt_pk_bf16_f32 v203, v86, v87\n"
"v_cvt_pk_bf16_f32 v204, v88, v89\n"
"v_cvt_pk_bf16_f32 v205, v90, v91\n"
"v_cvt_pk_bf16_f32 v206, v92, v93\n"
"v_cvt_pk_bf16_f32 v207, v94, v95\n"
"v_permlane32_swap_b32 v200, v202\n"
"v_permlane32_swap_b32 v201, v203\n"
"v_permlane32_swap_b32 v204, v206\n"
"v_permlane32_swap_b32 v205, v207\n"
"buffer_store_dwordx4 v[200:203], v237, s[88:91], s58 offen offset:2048 sc1\n"
"buffer_store_dwordx4 v[204:207], v237, s[88:91], s58 offen offset:3072 sc1\n"
"s_lshl_b32 s50, s6, 7\n"
"s_add_u32 s50, s50, 0x11000\n"
"s_and_b32 s60, s6, 0xfc\n"
"s_lshl_b32 s60, s60, 7\n"
"s_add_u32 s60, s60, 0x11000\n"
"v_and_b32 v247, 3, v238\n"
"v_max_u32 v247, 1, v247\n"
"v_add_u32 v247, s67, v247\n"
"v_and_b32 v247, 3, v247\n"
"v_lshlrev_b32 v247, 7, v247\n"
"v_add_u32 v243, s60, v247\n"
"s_lshl_b32 s60, s61, 2\n"
"s_add_u32 s60, s60, s67\n"
"s_mul_i32 s60, s60, 0x3000\n"
"s_mul_i32 s81, s44, 0xc00\n"
"s_add_u32 s60, s60, s81\n"
"s_add_u32 s60, s60, 0x1400000\n"
"s_add_u32 s28, s16, s60\n"
"s_addc_u32 s29, s17, 0\n"
"s_and_b32 s29, s29, 0xffff\n"
"s_mov_b32 s28, s28\n"
"s_mov_b32 s30, 0x798c00\n"
"s_mov_b32 s31, 0x20000\n"
"s_lshl_b32 s60, s68, 2\n"
"s_add_u32 s60, s60, s67\n"
"s_mul_i32 s60, s60, 0x9000\n"
"s_mul_i32 s81, s69, 0x180\n"
"s_add_u32 s60, s60, s81\n"
"s_mul_i32 s81, s44, 0x60\n"
"s_add_u32 s60, s60, s81\n"
"s_add_u32 s92, s96, s60\n"
"s_addc_u32 s93, s97, 0\n"
"s_and_b32 s93, s93, 0xffff\n"
"s_mov_b32 s92, s92\n"
"s_mov_b32 s94, 0x8be0\n"
"s_mov_b32 s95, 0x20000\n"
"buffer_load_dwordx2 v[228:229], v243, s[84:87], 0 offen sc1\n"
"s_waitcnt vmcnt(0)\n"
"s_mov_b64 exec, 1\n"
"ds_add_rtn_u32 v236, v224, v225 offset:12\n"
"s_mov_b64 exec, -1\n"
"s_waitcnt lgkmcnt(0)\n"
"v_readfirstlane_b32 s60, v236\n"
"s_nop 0\n"
"s_cmp_eq_u32 s60, 3\n"
"s_cbranch_scc0 .Lk1_pb_pp\n"
"v_mov_b32 v226, s98\n"
"v_mov_b32 v227, s99\n"
"v_mov_b32 v236, 0\n"
"s_mov_b64 exec, 1\n"
"buffer_store_dwordx2 v[226:227], v236, s[84:87], s50 offen sc1\n"
"s_mov_b64 exec, -1\n"
".Lk1_pb_pp:\n"
"buffer_load_dwordx2 v[230:231], v243, s[84:87], 0 offen sc1\n"
"buffer_load_dwordx2 v[232:233], v243, s[84:87], 0 offen sc1\n"
"s_mov_b32 s35, 0\n"
".Lk1_fp:\n"
"s_waitcnt vmcnt(2)\n"
"v_xor_b32 v228, s98, v228\n"
"v_xor_b32 v229, s99, v229\n"
"v_or_b32 v228, v228, v229\n"
"v_cmp_eq_u32 vcc, 0, v228\n"
"s_nop 0\n"
"s_cmp_eq_u64 vcc, -1\n"
"s_cbranch_scc1 .Lk1_fpd\n"
"s_sleep 1\n"
"buffer_load_dwordx2 v[228:229], v243, s[84:87], 0 offen sc1\n"
"s_waitcnt vmcnt(2)\n"
"v_xor_b32 v230, s98, v230\n"
"v_xor_b32 v231, s99, v231\n"
"v_or_b32 v230, v230, v231\n"
"v_cmp_eq_u32 vcc, 0, v230\n"
"s_nop 0\n"
"s_cmp_eq_u64 vcc, -1\n"
"s_cbranch_scc1 .Lk1_fpd\n"
"s_sleep 1\n"
"buffer_load_dwordx2 v[230:231], v243, s[84:87], 0 offen sc1\n"
"s_waitcnt vmcnt(2)\n"
"v_xor_b32 v232, s98, v232\n"
"v_xor_b32 v233, s99, v233\n"
"v_or_b32 v232, v232, v233\n"
"v_cmp_eq_u32 vcc, 0, v232\n"
"s_nop 0\n"
"s_cmp_eq_u64 vcc, -1\n"
"s_cbranch_scc1 .Lk1_fpd\n"
"s_sleep 1\n"
"buffer_load_dwordx2 v[232:233], v243, s[84:87], 0 offen sc1\n"
"s_add_u32 s35, s35, 1\n"
"s_cmp_lt_u32 s35, 0x40000\n"
"s_cbranch_scc1 .Lk1_fp\n"
".Lk1_fpd:\n"
"ds_read_b32 v236, v224 offset:12\n"
"s_waitcnt lgkmcnt(0)\n"
"v_readfirstlane_b32 s60, v236\n"
"s_nop 0\n"
"s_cmp_ge_u32 s60, 4\n"
"s_cbranch_scc0 .Lk1_sp_own\n"
".Lk1_spr_own:\n"
"s_mov_b32 s88, 0\n"
"buffer_load_dwordx4 v[0:3], v237, s[28:31], s88 offen sc1\n"
"s_mov_b32 s89, 0x288000\n"
"buffer_load_dwordx4 v[4:7], v237, s[28:31], s89 offen sc1\n"
"s_mov_b32 s90, 0x510000\n"
"buffer_load_dwordx4 v[8:11], v237, s[28:31], s90 offen sc1\n"
"s_mov_b32 s91, 0x798000\n"
"buffer_load_dwordx4 v[12:15], v237, s[28:31], s91 offen sc1\n"
"buffer_load_dwordx4 v[16:19], v237, s[28:31], s88 offen offset:1024 sc1\n"
"buffer_load_dwordx4 v[20:23], v237, s[28:31], s89 offen offset:1024 sc1\n"
"buffer_load_dwordx4 v[24:27], v237, s[28:31], s90 offen offset:1024 sc1\n"
"buffer_load_dwordx4 v[28:31], v237, s[28:31], s91 offen offset:1024 sc1\n"
"buffer_load_dwordx4 v[32:35], v237, s[28:31], s88 offen offset:2048 sc1\n"
"buffer_load_dwordx4 v[36:39], v237, s[28:31], s89 offen offset:2048 sc1\n"
"buffer_load_dwordx4 v[40:43], v237, s[28:31], s90 offen offset:2048 sc1\n"
"buffer_load_dwordx4 v[44:47], v237, s[28:31], s91 offen offset:2048 sc1\n"
"s_waitcnt vmcnt(8)\n"
"v_lshlrev_b32 v64, 16, v0\n"
"v_and_b32 v65, 0xffff0000, v0\n"
"v_lshlrev_b32 v66, 16, v1\n"
"v_and_b32 v67, 0xffff0000, v1\n"
"v_lshlrev_b32 v68, 16, v2\n"
"v_and_b32 v69, 0xffff0000, v2\n"
"v_lshlrev_b32 v70, 16, v3\n"
"v_and_b32 v71, 0xffff0000, v3\n"
"v_lshlrev_b32 v72, 16, v4\n"
"v_and_b32 v73, 0xffff0000, v4\n"
"v_lshlrev_b32 v74, 16, v5\n"
"v_and_b32 v75, 0xffff0000, v5\n"
"v_lshlrev_b32 v76, 16, v6\n"
"v_and_b32 v77, 0xffff0000, v6\n"
"v_lshlrev_b32 v78, 16, v7\n"
"v_and_b32 v79, 0xffff0000, v7\n"
"v_pk_add_f32 v[64:65], v[64:65], v[72:73]\n"
"v_pk_add_f32 v[66:67], v[66:67], v[74:75]\n"
"v_pk_add_f32 v[68:69], v[68:69], v[76:77]\n"
"v_pk_add_f32 v[70:71], v[70:71], v[78:79]\n"
"v_lshlrev_b32 v72, 16, v8\n"
"v_and_b32 v73, 0xffff0000, v8\n"
"v_lshlrev_b32 v74, 16, v9\n"
"v_and_b32 v75, 0xffff0000, v9\n"
"v_lshlrev_b32 v76, 16, v10\n"
"v_and_b32 v77, 0xffff0000, v10\n"
"v_lshlrev_b32 v78, 16, v11\n"
"v_and_b32 v79, 0xffff0000, v11\n"
"v_pk_add_f32 v[64:65], v[64:65], v[72:73]\n"
"v_pk_add_f32 v[66:67], v[66:67], v[74:75]\n"
"v_pk_add_f32 v[68:69], v[68:69], v[76:77]\n"
"v_pk_add_f32 v[70:71], v[70:71], v[78:79]\n"
"v_lshlrev_b32 v72, 16, v12\n"
"v_and_b32 v73, 0xffff0000, v12\n"
"v_lshlrev_b32 v74, 16, v13\n"
"v_and_b32 v75, 0xffff0000, v13\n"
"v_lshlrev_b32 v76, 16, v14\n"
"v_and_b32 v77, 0xffff0000, v14\n"
"v_lshlrev_b32 v78, 16, v15\n"
"v_and_b32 v79, 0xffff0000, v15\n"
"v_pk_add_f32 v[64:65], v[64:65], v[72:73]\n"
"v_pk_add_f32 v[66:67], v[66:67], v[74:75]\n"
"v_pk_add_f32 v[68:69], v[68:69], v[76:77]\n"
"v_pk_add_f32 v[70:71], v[70:71], v[78:79]\n"
"v_cvt_pk_bf16_f32 v52, v64, v65\n"
"v_cvt_pk_bf16_f32 v53, v66, v67\n"
"v_cvt_pk_bf16_f32 v54, v68, v69\n"
"v_cvt_pk_bf16_f32 v55, v70, v71\n"
"buffer_store_dwordx4 v[52:55], v244, s[92:95], 0 offen\n"
"s_waitcnt vmcnt(5)\n"
"v_lshlrev_b32 v64, 16, v16\n"
"v_and_b32 v65, 0xffff0000, v16\n"
"v_lshlrev_b32 v66, 16, v17\n"
"v_and_b32 v67, 0xffff0000, v17\n"
"v_lshlrev_b32 v68, 16, v18\n"
"v_and_b32 v69, 0xffff0000, v18\n"
"v_lshlrev_b32 v70, 16, v19\n"
"v_and_b32 v71, 0xffff0000, v19\n"
"v_lshlrev_b32 v72, 16, v20\n"
"v_and_b32 v73, 0xffff0000, v20\n"
"v_lshlrev_b32 v74, 16, v21\n"
"v_and_b32 v75, 0xffff0000, v21\n"
"v_lshlrev_b32 v76, 16, v22\n"
"v_and_b32 v77, 0xffff0000, v22\n"
"v_lshlrev_b32 v78, 16, v23\n"
"v_and_b32 v79, 0xffff0000, v23\n"
"v_pk_add_f32 v[64:65], v[64:65], v[72:73]\n"
"v_pk_add_f32 v[66:67], v[66:67], v[74:75]\n"
"v_pk_add_f32 v[68:69], v[68:69], v[76:77]\n"
"v_pk_add_f32 v[70:71], v[70:71], v[78:79]\n"
"v_lshlrev_b32 v72, 16, v24\n"
"v_and_b32 v73, 0xffff0000, v24\n"
"v_lshlrev_b32 v74, 16, v25\n"
"v_and_b32 v75, 0xffff0000, v25\n"
"v_lshlrev_b32 v76, 16, v26\n"
"v_and_b32 v77, 0xffff0000, v26\n"
"v_lshlrev_b32 v78, 16, v27\n"
"v_and_b32 v79, 0xffff0000, v27\n"
"v_pk_add_f32 v[64:65], v[64:65], v[72:73]\n"
"v_pk_add_f32 v[66:67], v[66:67], v[74:75]\n"
"v_pk_add_f32 v[68:69], v[68:69], v[76:77]\n"
"v_pk_add_f32 v[70:71], v[70:71], v[78:79]\n"
"v_lshlrev_b32 v72, 16, v28\n"
"v_and_b32 v73, 0xffff0000, v28\n"
"v_lshlrev_b32 v74, 16, v29\n"
"v_and_b32 v75, 0xffff0000, v29\n"
"v_lshlrev_b32 v76, 16, v30\n"
"v_and_b32 v77, 0xffff0000, v30\n"
"v_lshlrev_b32 v78, 16, v31\n"
"v_and_b32 v79, 0xffff0000, v31\n"
"v_pk_add_f32 v[64:65], v[64:65], v[72:73]\n"
"v_pk_add_f32 v[66:67], v[66:67], v[74:75]\n"
"v_pk_add_f32 v[68:69], v[68:69], v[76:77]\n"
"v_pk_add_f32 v[70:71], v[70:71], v[78:79]\n"
"v_cvt_pk_bf16_f32 v56, v64, v65\n"
"v_cvt_pk_bf16_f32 v57, v66, v67\n"
"v_cvt_pk_bf16_f32 v58, v68, v69\n"
"v_cvt_pk_bf16_f32 v59, v70, v71\n"
"buffer_store_dwordx4 v[56:59], v244, s[92:95], 0 offen offset:32\n"
"s_waitcnt vmcnt(2)\n"
"v_lshlrev_b32 v64, 16, v32\n"
"v_and_b32 v65, 0xffff0000, v32\n"
"v_lshlrev_b32 v66, 16, v33\n"
"v_and_b32 v67, 0xffff0000, v33\n"
"v_lshlrev_b32 v68, 16, v34\n"
"v_and_b32 v69, 0xffff0000, v34\n"
"v_lshlrev_b32 v70, 16, v35\n"
"v_and_b32 v71, 0xffff0000, v35\n"
"v_lshlrev_b32 v72, 16, v36\n"
"v_and_b32 v73, 0xffff0000, v36\n"
"v_lshlrev_b32 v74, 16, v37\n"
"v_and_b32 v75, 0xffff0000, v37\n"
"v_lshlrev_b32 v76, 16, v38\n"
"v_and_b32 v77, 0xffff0000, v38\n"
"v_lshlrev_b32 v78, 16, v39\n"
"v_and_b32 v79, 0xffff0000, v39\n"
"v_pk_add_f32 v[64:65], v[64:65], v[72:73]\n"
"v_pk_add_f32 v[66:67], v[66:67], v[74:75]\n"
"v_pk_add_f32 v[68:69], v[68:69], v[76:77]\n"
"v_pk_add_f32 v[70:71], v[70:71], v[78:79]\n"
"v_lshlrev_b32 v72, 16, v40\n"
"v_and_b32 v73, 0xffff0000, v40\n"
"v_lshlrev_b32 v74, 16, v41\n"
"v_and_b32 v75, 0xffff0000, v41\n"
"v_lshlrev_b32 v76, 16, v42\n"
"v_and_b32 v77, 0xffff0000, v42\n"
"v_lshlrev_b32 v78, 16, v43\n"
"v_and_b32 v79, 0xffff0000, v43\n"
"v_pk_add_f32 v[64:65], v[64:65], v[72:73]\n"
"v_pk_add_f32 v[66:67], v[66:67], v[74:75]\n"
"v_pk_add_f32 v[68:69], v[68:69], v[76:77]\n"
"v_pk_add_f32 v[70:71], v[70:71], v[78:79]\n"
"v_lshlrev_b32 v72, 16, v44\n"
"v_and_b32 v73, 0xffff0000, v44\n"
"v_lshlrev_b32 v74, 16, v45\n"
"v_and_b32 v75, 0xffff0000, v45\n"
"v_lshlrev_b32 v76, 16, v46\n"
"v_and_b32 v77, 0xffff0000, v46\n"
"v_lshlrev_b32 v78, 16, v47\n"
"v_and_b32 v79, 0xffff0000, v47\n"
"v_pk_add_f32 v[64:65], v[64:65], v[72:73]\n"
"v_pk_add_f32 v[66:67], v[66:67], v[74:75]\n"
"v_pk_add_f32 v[68:69], v[68:69], v[76:77]\n"
"v_pk_add_f32 v[70:71], v[70:71], v[78:79]\n"
"v_cvt_pk_bf16_f32 v60, v64, v65\n"
"v_cvt_pk_bf16_f32 v61, v66, v67\n"
"v_cvt_pk_bf16_f32 v62, v68, v69\n"
"v_cvt_pk_bf16_f32 v63, v70, v71\n"
"buffer_store_dwordx4 v[60:63], v244, s[92:95], 0 offen offset:64\n"
"s_branch .Lk1_end\n"
".Lk1_sp_h2:\n"
"s_mov_b32 s35, 0\n"
".Lk1_spl_h2:\n"
"s_sleep 1\n"
"ds_read_b32 v236, v224\n"
"s_waitcnt lgkmcnt(0)\n"
"v_readfirstlane_b32 s60, v236\n"
"s_nop 0\n"
"s_cmp_ge_u32 s60, 5\n"
"s_cbranch_scc1 .Lk1_spr_h2\n"
"s_add_u32 s35, s35, 1\n"
"s_cmp_lt_u32 s35, 0x40000\n"
"s_cbranch_scc1 .Lk1_spl_h2\n"
"s_branch .Lk1_spr_h2\n"
".Lk1_sp_h3:\n"
"s_mov_b32 s35, 0\n"
".Lk1_spl_h3:\n"
"s_sleep 1\n"
"ds_read_b32 v236, v224 offset:20\n"
"s_waitcnt lgkmcnt(0)\n"
"v_readfirstlane_b32 s60, v236\n"
"s_nop 0\n"
"s_cmp_ge_u32 s60, 5\n"
"s_cbranch_scc1 .Lk1_spr_h3\n"
"s_add_u32 s35, s35, 1\n"
"s_cmp_lt_u32 s35, 0x40000\n"
"s_cbranch_scc1 .Lk1_spl_h3\n"
"s_branch .Lk1_spr_h3\n"
".Lk1_sp_h4:\n"
"s_mov_b32 s35, 0\n"
".Lk1_spl_h4:\n"
"s_sleep 1\n"
"ds_read_b32 v236, v224\n"
"s_waitcnt lgkmcnt(0)\n"
"v_readfirstlane_b32 s60, v236\n"
"s_nop 0\n"
"s_cmp_ge_u32 s60, 10\n"
"s_cbranch_scc1 .Lk1_spr_h4\n"
"s_add_u32 s35, s35, 1\n"
"s_cmp_lt_u32 s35, 0x40000\n"
"s_cbranch_scc1 .Lk1_spl_h4\n"
"s_branch .Lk1_spr_h4\n"
".Lk1_sp_h5:\n"
"s_mov_b32 s35, 0\n"
".Lk1_spl_h5:\n"
"s_sleep 1\n"
"ds_read_b32 v236, v224 offset:20\n"
"s_waitcnt lgkmcnt(0)\n"
"v_readfirstlane_b32 s60, v236\n"
"s_nop 0\n"
"s_cmp_ge_u32 s60, 10\n"
"s_cbranch_scc1 .Lk1_spr_h5\n"
"s_add_u32 s35, s35, 1\n"
"s_cmp_lt_u32 s35, 0x40000\n"
"s_cbranch_scc1 .Lk1_spl_h5\n"
"s_branch .Lk1_spr_h5\n"
".Lk1_sp_h6:\n"
"s_mov_b32 s35, 0\n"
".Lk1_spl_h6:\n"
"s_sleep 1\n"
"ds_read_b32 v236, v224\n"
"s_waitcnt lgkmcnt(0)\n"
"v_readfirstlane_b32 s60, v236\n"
"s_nop 0\n"
"s_cmp_ge_u32 s60, 15\n"
"s_cbranch_scc1 .Lk1_spr_h6\n"
"s_add_u32 s35, s35, 1\n"
"s_cmp_lt_u32 s35, 0x40000\n"
"s_cbranch_scc1 .Lk1_spl_h6\n"
"s_branch .Lk1_spr_h6\n"
".Lk1_sp_h7:\n"
"s_mov_b32 s35, 0\n"
".Lk1_spl_h7:\n"
"s_sleep 1\n"
"ds_read_b32 v236, v224 offset:20\n"
"s_waitcnt lgkmcnt(0)\n"
"v_readfirstlane_b32 s60, v236\n"
"s_nop 0\n"
"s_cmp_ge_u32 s60, 15\n"
"s_cbranch_scc1 .Lk1_spr_h7\n"
"s_add_u32 s35, s35, 1\n"
"s_cmp_lt_u32 s35, 0x40000\n"
"s_cbranch_scc1 .Lk1_spl_h7\n"
"s_branch .Lk1_spr_h7\n"
".Lk1_sp_h8:\n"
"s_mov_b32 s35, 0\n"
".Lk1_spl_h8:\n"
"s_sleep 1\n"
"ds_read_b32 v236, v224\n"
"s_waitcnt lgkmcnt(0)\n"
"v_readfirstlane_b32 s60, v236\n"
"s_nop 0\n"
"s_cmp_ge_u32 s60, 20\n"
"s_cbranch_scc1 .Lk1_spr_h8\n"
"s_add_u32 s35, s35, 1\n"
"s_cmp_lt_u32 s35, 0x40000\n"
"s_cbranch_scc1 .Lk1_spl_h8\n"
"s_branch .Lk1_spr_h8\n"
".Lk1_sp_h9:\n"
"s_mov_b32 s35, 0\n"
".Lk1_spl_h9:\n"
"s_sleep 1\n"
"ds_read_b32 v236, v224 offset:20\n"
"s_waitcnt lgkmcnt(0)\n"
"v_readfirstlane_b32 s60, v236\n"
"s_nop 0\n"
"s_cmp_ge_u32 s60, 20\n"
"s_cbranch_scc1 .Lk1_spr_h9\n"
"s_add_u32 s35, s35, 1\n"
"s_cmp_lt_u32 s35, 0x40000\n"
"s_cbranch_scc1 .Lk1_spl_h9\n"
"s_branch .Lk1_spr_h9\n"
".Lk1_sp_h10:\n"
"s_mov_b32 s35, 0\n"
".Lk1_spl_h10:\n"
"s_sleep 1\n"
"ds_read_b32 v236, v224\n"
"s_waitcnt lgkmcnt(0)\n"
"v_readfirstlane_b32 s60, v236\n"
"s_nop 0\n"
"s_cmp_ge_u32 s60, 25\n"
"s_cbranch_scc1 .Lk1_spr_h10\n"
"s_add_u32 s35, s35, 1\n"
"s_cmp_lt_u32 s35, 0x40000\n"
"s_cbranch_scc1 .Lk1_spl_h10\n"
"s_branch .Lk1_spr_h10\n"
".Lk1_sp_t2:\n"
"s_mov_b32 s35, 0\n"
".Lk1_spl_t2:\n"
"s_sleep 1\n"
"ds_read_b32 v236, v224\n"
"s_waitcnt lgkmcnt(0)\n"
"v_readfirstlane_b32 s60, v236\n"
"s_nop 0\n"
"s_cmp_ge_u32 s60, 5\n"
"s_cbranch_scc1 .Lk1_spr_t2\n"
"s_add_u32 s35, s35, 1\n"
"s_cmp_lt_u32 s35, 0x40000\n"
"s_cbranch_scc1 .Lk1_spl_t2\n"
"s_branch .Lk1_spr_t2\n"
".Lk1_sp_t3:\n"
"s_mov_b32 s35, 0\n"
".Lk1_spl_t3:\n"
"s_sleep 1\n"
"ds_read_b32 v236, v224 offset:20\n"
"s_waitcnt lgkmcnt(0)\n"
"v_readfirstlane_b32 s60, v236\n"
"s_nop 0\n"
"s_cmp_ge_u32 s60, 5\n"
"s_cbranch_scc1 .Lk1_spr_t3\n"
"s_add_u32 s35, s35, 1\n"
"s_cmp_lt_u32 s35, 0x40000\n"
"s_cbranch_scc1 .Lk1_spl_t3\n"
"s_branch .Lk1_spr_t3\n"
".Lk1_sp_t4:\n"
"s_mov_b32 s35, 0\n"
".Lk1_spl_t4:\n"
"s_sleep 1\n"
"ds_read_b32 v236, v224\n"
"s_waitcnt lgkmcnt(0)\n"
"v_readfirstlane_b32 s60, v236\n"
"s_nop 0\n"
"s_cmp_ge_u32 s60, 10\n"
"s_cbranch_scc1 .Lk1_spr_t4\n"
"s_add_u32 s35, s35, 1\n"
"s_cmp_lt_u32 s35, 0x40000\n"
"s_cbranch_scc1 .Lk1_spl_t4\n"
"s_branch .Lk1_spr_t4\n"
".Lk1_sp_t5:\n"
"s_mov_b32 s35, 0\n"
".Lk1_spl_t5:\n"
"s_sleep 1\n"
"ds_read_b32 v236, v224 offset:20\n"
"s_waitcnt lgkmcnt(0)\n"
"v_readfirstlane_b32 s60, v236\n"
"s_nop 0\n"
"s_cmp_ge_u32 s60, 10\n"
"s_cbranch_scc1 .Lk1_spr_t5\n"
"s_add_u32 s35, s35, 1\n"
"s_cmp_lt_u32 s35, 0x40000\n"
"s_cbranch_scc1 .Lk1_spl_t5\n"
"s_branch .Lk1_spr_t5\n"
".Lk1_sp_t6:\n"
"s_mov_b32 s35, 0\n"
".Lk1_spl_t6:\n"
"s_sleep 1\n"
"ds_read_b32 v236, v224\n"
"s_waitcnt lgkmcnt(0)\n"
"v_readfirstlane_b32 s60, v236\n"
"s_nop 0\n"
"s_cmp_ge_u32 s60, 15\n"
"s_cbranch_scc1 .Lk1_spr_t6\n"
"s_add_u32 s35, s35, 1\n"
"s_cmp_lt_u32 s35, 0x40000\n"
"s_cbranch_scc1 .Lk1_spl_t6\n"
"s_branch .Lk1_spr_t6\n"
".Lk1_sp_t7:\n"
"s_mov_b32 s35, 0\n"
".Lk1_spl_t7:\n"
"s_sleep 1\n"
"ds_read_b32 v236, v224 offset:20\n"
"s_waitcnt lgkmcnt(0)\n"
"v_readfirstlane_b32 s60, v236\n"
"s_nop 0\n"
"s_cmp_ge_u32 s60, 15\n"
"s_cbranch_scc1 .Lk1_spr_t7\n"
"s_add_u32 s35, s35, 1\n"
"s_cmp_lt_u32 s35, 0x40000\n"
"s_cbranch_scc1 .Lk1_spl_t7\n"
"s_branch .Lk1_spr_t7\n"
".Lk1_sp_t8:\n"
"s_mov_b32 s35, 0\n"
".Lk1_spl_t8:\n"
"s_sleep 1\n"
"ds_read_b32 v236, v224\n"
"s_waitcnt lgkmcnt(0)\n"
"v_readfirstlane_b32 s60, v236\n"
"s_nop 0\n"
"s_cmp_ge_u32 s60, 20\n"
"s_cbranch_scc1 .Lk1_spr_t8\n"
"s_add_u32 s35, s35, 1\n"
"s_cmp_lt_u32 s35, 0x40000\n"
"s_cbranch_scc1 .Lk1_spl_t8\n"
"s_branch .Lk1_spr_t8\n"
".Lk1_sp_t9:\n"
"s_mov_b32 s35, 0\n"
".Lk1_spl_t9:\n"
"s_sleep 1\n"
"ds_read_b32 v236, v224 offset:20\n"
"s_waitcnt lgkmcnt(0)\n"
"v_readfirstlane_b32 s60, v236\n"
"s_nop 0\n"
"s_cmp_ge_u32 s60, 20\n"
"s_cbranch_scc1 .Lk1_spr_t9\n"
"s_add_u32 s35, s35, 1\n"
"s_cmp_lt_u32 s35, 0x40000\n"
"s_cbranch_scc1 .Lk1_spl_t9\n"
"s_branch .Lk1_spr_t9\n"
".Lk1_sp_t10:\n"
"s_mov_b32 s35, 0\n"
".Lk1_spl_t10:\n"
"s_sleep 1\n"
"ds_read_b32 v236, v224\n"
"s_waitcnt lgkmcnt(0)\n"
"v_readfirstlane_b32 s60, v236\n"
"s_nop 0\n"
"s_cmp_ge_u32 s60, 25\n"
"s_cbranch_scc1 .Lk1_spr_t10\n"
"s_add_u32 s35, s35, 1\n"
"s_cmp_lt_u32 s35, 0x40000\n"
"s_cbranch_scc1 .Lk1_spl_t10\n"
"s_branch .Lk1_spr_t10\n"
".Lk1_sp_own:\n"
"s_mov_b32 s35, 0\n"
".Lk1_spl_own:\n"
"s_sleep 1\n"
"ds_read_b32 v236, v224 offset:12\n"
"s_waitcnt lgkmcnt(0)\n"
"v_readfirstlane_b32 s60, v236\n"
"s_nop 0\n"
"s_cmp_ge_u32 s60, 4\n"
"s_cbranch_scc1 .Lk1_spr_own\n"
"s_add_u32 s35, s35, 1\n"
"s_cmp_lt_u32 s35, 0x40000\n"
"s_cbranch_scc1 .Lk1_spl_own\n"
"s_branch .Lk1_spr_own\n"
".Lk1_slow0:\n"
"s_waitcnt lgkmcnt(0)\n"
"s_nop 7\n"
"ds_read_b128 v[204:207], v248\n"
"ds_read_b128 v[208:211], v248 offset:512\n"
"s_waitcnt lgkmcnt(0)\n"
"v_mfma_f32_16x16x32_bf16 v[156:159], v[204:207], v[96:99], 0\n"
"v_mfma_f32_16x16x32_bf16 v[164:167], v[204:207], v[112:115], 0\n"
"v_mfma_f32_16x16x32_bf16 v[172:175], v[204:207], v[128:131], 0\n"
"v_mfma_f32_16x16x32_bf16 v[156:159], v[208:211], v[100:103], v[156:159]\n"
"v_mfma_f32_16x16x32_bf16 v[164:167], v[208:211], v[116:119], v[164:167]\n"
"v_mfma_f32_16x16x32_bf16 v[172:175], v[208:211], v[132:135], v[172:175]\n"
"ds_read_b128 v[204:207], v248 offset:1024\n"
"ds_read_b128 v[208:211], v248 offset:1536\n"
"s_waitcnt lgkmcnt(0)\n"
"v_mfma_f32_16x16x32_bf16 v[156:159], v[204:207], v[104:107], v[156:159]\n"
"v_mfma_f32_16x16x32_bf16 v[164:167], v[204:207], v[120:123], v[164:167]\n"
"v_mfma_f32_16x16x32_bf16 v[172:175], v[204:207], v[136:139], v[172:175]\n"
"v_mfma_f32_16x16x32_bf16 v[156:159], v[208:211], v[108:111], v[156:159]\n"
"v_mfma_f32_16x16x32_bf16 v[164:167], v[208:211], v[124:127], v[164:167]\n"
"v_mfma_f32_16x16x32_bf16 v[172:175], v[208:211], v[140:143], v[172:175]\n"
"ds_read_b128 v[204:207], v248 offset:4096\n"
"ds_read_b128 v[208:211], v248 offset:4608\n"
"s_waitcnt lgkmcnt(0)\n"
"v_mfma_f32_16x16x32_bf16 v[160:163], v[204:207], v[96:99], 0\n"
"v_mfma_f32_16x16x32_bf16 v[168:171], v[204:207], v[112:115], 0\n"
"v_mfma_f32_16x16x32_bf16 v[176:179], v[204:207], v[128:131], 0\n"
"v_mfma_f32_16x16x32_bf16 v[160:163], v[208:211], v[100:103], v[160:163]\n"
"v_mfma_f32_16x16x32_bf16 v[168:171], v[208:211], v[116:119], v[168:171]\n"
"v_mfma_f32_16x16x32_bf16 v[176:179], v[208:211], v[132:135], v[176:179]\n"
"ds_read_b128 v[204:207], v248 offset:5120\n"
"ds_read_b128 v[208:211], v248 offset:5632\n"
"s_waitcnt lgkmcnt(0)\n"
"v_mfma_f32_16x16x32_bf16 v[160:163], v[204:207], v[104:107], v[160:163]\n"
"v_mfma_f32_16x16x32_bf16 v[168:171], v[204:207], v[120:123], v[168:171]\n"
"v_mfma_f32_16x16x32_bf16 v[176:179], v[204:207], v[136:139], v[176:179]\n"
"v_mfma_f32_16x16x32_bf16 v[160:163], v[208:211], v[108:111], v[160:163]\n"
"v_mfma_f32_16x16x32_bf16 v[168:171], v[208:211], v[124:127], v[168:171]\n"
"v_mfma_f32_16x16x32_bf16 v[176:179], v[208:211], v[140:143], v[176:179]\n"
"s_nop 7\n"
"v_max3_f32 v206, v156, v157, v158\n"
"v_max3_f32 v206, v206, v159, v160\n"
"v_max3_f32 v206, v206, v161, v162\n"
"v_max_f32 v206, v206, v163\n"
"v_mov_b32 v204, v206\n"
"v_mov_b32 v205, v206\n"
"s_nop 1\n"
"v_permlane32_swap_b32 v204, v205\n"
"v_max_f32 v206, v204, v205\n"
"v_mov_b32 v204, v206\n"
"v_mov_b32 v205, v206\n"
"s_nop 1\n"
"v_permlane16_swap_b32 v204, v205\n"
"v_max_f32 v206, v204, v205\n"
"v_sub_f32 v206, 0, v206\n"
"v_min_f32 v206, v206, v144\n"
"v_sub_f32 v204, v206, v144\n"
"v_exp_f32 v204, v204\n"
"v_mov_b32 v144, v206\n"
"v_mov_b32 v145, v206\n"
"v_mov_b32 v146, v206\n"
"v_mov_b32 v147, v206\n"
"v_add_f32 v156, v156, v206\n"
"v_add_f32 v157, v157, v206\n"
"v_add_f32 v158, v158, v206\n"
"v_add_f32 v159, v159, v206\n"
"v_add_f32 v160, v160, v206\n"
"v_add_f32 v161, v161, v206\n"
"v_add_f32 v162, v162, v206\n"
"v_add_f32 v163, v163, v206\n"
"v_mul_f32 v244, v244, v204\n"
"v_mul_f32 v0, v0, v204\n"
"v_mul_f32 v1, v1, v204\n"
"v_mul_f32 v2, v2, v204\n"
"v_mul_f32 v3, v3, v204\n"
"v_mul_f32 v4, v4, v204\n"
"v_mul_f32 v5, v5, v204\n"
"v_mul_f32 v6, v6, v204\n"
"v_mul_f32 v7, v7, v204\n"
"v_mul_f32 v8, v8, v204\n"
"v_mul_f32 v9, v9, v204\n"
"v_mul_f32 v10, v10, v204\n"
"v_mul_f32 v11, v11, v204\n"
"v_mul_f32 v12, v12, v204\n"
"v_mul_f32 v13, v13, v204\n"
"v_mul_f32 v14, v14, v204\n"
"v_mul_f32 v15, v15, v204\n"
"v_mul_f32 v16, v16, v204\n"
"v_mul_f32 v17, v17, v204\n"
"v_mul_f32 v18, v18, v204\n"
"v_mul_f32 v19, v19, v204\n"
"v_mul_f32 v20, v20, v204\n"
"v_mul_f32 v21, v21, v204\n"
"v_mul_f32 v22, v22, v204\n"
"v_mul_f32 v23, v23, v204\n"
"v_mul_f32 v24, v24, v204\n"
"v_mul_f32 v25, v25, v204\n"
"v_mul_f32 v26, v26, v204\n"
"v_mul_f32 v27, v27, v204\n"
"v_mul_f32 v28, v28, v204\n"
"v_mul_f32 v29, v29, v204\n"
"v_mul_f32 v30, v30, v204\n"
"v_mul_f32 v31, v31, v204\n"
"v_max3_f32 v206, v164, v165, v166\n"
"v_max3_f32 v206, v206, v167, v168\n"
"v_max3_f32 v206, v206, v169, v170\n"
"v_max_f32 v206, v206, v171\n"
"v_mov_b32 v204, v206\n"
"v_mov_b32 v205, v206\n"
"s_nop 1\n"
"v_permlane32_swap_b32 v204, v205\n"
"v_max_f32 v206, v204, v205\n"
"v_mov_b32 v204, v206\n"
"v_mov_b32 v205, v206\n"
"s_nop 1\n"
"v_permlane16_swap_b32 v204, v205\n"
"v_max_f32 v206, v204, v205\n"
"v_sub_f32 v206, 0, v206\n"
"v_min_f32 v206, v206, v148\n"
"v_sub_f32 v204, v206, v148\n"
"v_exp_f32 v204, v204\n"
"v_mov_b32 v148, v206\n"
"v_mov_b32 v149, v206\n"
"v_mov_b32 v150, v206\n"
"v_mov_b32 v151, v206\n"
"v_add_f32 v164, v164, v206\n"
"v_add_f32 v165, v165, v206\n"
"v_add_f32 v166, v166, v206\n"
"v_add_f32 v167, v167, v206\n"
"v_add_f32 v168, v168, v206\n"
"v_add_f32 v169, v169, v206\n"
"v_add_f32 v170, v170, v206\n"
"v_add_f32 v171, v171, v206\n"
"v_mul_f32 v245, v245, v204\n"
"v_mul_f32 v32, v32, v204\n"
"v_mul_f32 v33, v33, v204\n"
"v_mul_f32 v34, v34, v204\n"
"v_mul_f32 v35, v35, v204\n"
"v_mul_f32 v36, v36, v204\n"
"v_mul_f32 v37, v37, v204\n"
"v_mul_f32 v38, v38, v204\n"
"v_mul_f32 v39, v39, v204\n"
"v_mul_f32 v40, v40, v204\n"
"v_mul_f32 v41, v41, v204\n"
"v_mul_f32 v42, v42, v204\n"
"v_mul_f32 v43, v43, v204\n"
"v_mul_f32 v44, v44, v204\n"
"v_mul_f32 v45, v45, v204\n"
"v_mul_f32 v46, v46, v204\n"
"v_mul_f32 v47, v47, v204\n"
"v_mul_f32 v48, v48, v204\n"
"v_mul_f32 v49, v49, v204\n"
"v_mul_f32 v50, v50, v204\n"
"v_mul_f32 v51, v51, v204\n"
"v_mul_f32 v52, v52, v204\n"
"v_mul_f32 v53, v53, v204\n"
"v_mul_f32 v54, v54, v204\n"
"v_mul_f32 v55, v55, v204\n"
"v_mul_f32 v56, v56, v204\n"
"v_mul_f32 v57, v57, v204\n"
"v_mul_f32 v58, v58, v204\n"
"v_mul_f32 v59, v59, v204\n"
"v_mul_f32 v60, v60, v204\n"
"v_mul_f32 v61, v61, v204\n"
"v_mul_f32 v62, v62, v204\n"
"v_mul_f32 v63, v63, v204\n"
"v_max3_f32 v206, v172, v173, v174\n"
"v_max3_f32 v206, v206, v175, v176\n"
"v_max3_f32 v206, v206, v177, v178\n"
"v_max_f32 v206, v206, v179\n"
"v_mov_b32 v204, v206\n"
"v_mov_b32 v205, v206\n"
"s_nop 1\n"
"v_permlane32_swap_b32 v204, v205\n"
"v_max_f32 v206, v204, v205\n"
"v_mov_b32 v204, v206\n"
"v_mov_b32 v205, v206\n"
"s_nop 1\n"
"v_permlane16_swap_b32 v204, v205\n"
"v_max_f32 v206, v204, v205\n"
"v_sub_f32 v206, 0, v206\n"
"v_min_f32 v206, v206, v152\n"
"v_sub_f32 v204, v206, v152\n"
"v_exp_f32 v204, v204\n"
"v_mov_b32 v152, v206\n"
"v_mov_b32 v153, v206\n"
"v_mov_b32 v154, v206\n"
"v_mov_b32 v155, v206\n"
"v_add_f32 v172, v172, v206\n"
"v_add_f32 v173, v173, v206\n"
"v_add_f32 v174, v174, v206\n"
"v_add_f32 v175, v175, v206\n"
"v_add_f32 v176, v176, v206\n"
"v_add_f32 v177, v177, v206\n"
"v_add_f32 v178, v178, v206\n"
"v_add_f32 v179, v179, v206\n"
"v_mul_f32 v246, v246, v204\n"
"v_mul_f32 v64, v64, v204\n"
"v_mul_f32 v65, v65, v204\n"
"v_mul_f32 v66, v66, v204\n"
"v_mul_f32 v67, v67, v204\n"
"v_mul_f32 v68, v68, v204\n"
"v_mul_f32 v69, v69, v204\n"
"v_mul_f32 v70, v70, v204\n"
"v_mul_f32 v71, v71, v204\n"
"v_mul_f32 v72, v72, v204\n"
"v_mul_f32 v73, v73, v204\n"
"v_mul_f32 v74, v74, v204\n"
"v_mul_f32 v75, v75, v204\n"
"v_mul_f32 v76, v76, v204\n"
"v_mul_f32 v77, v77, v204\n"
"v_mul_f32 v78, v78, v204\n"
"v_mul_f32 v79, v79, v204\n"
"v_mul_f32 v80, v80, v204\n"
"v_mul_f32 v81, v81, v204\n"
"v_mul_f32 v82, v82, v204\n"
"v_mul_f32 v83, v83, v204\n"
"v_mul_f32 v84, v84, v204\n"
"v_mul_f32 v85, v85, v204\n"
"v_mul_f32 v86, v86, v204\n"
"v_mul_f32 v87, v87, v204\n"
"v_mul_f32 v88, v88, v204\n"
"v_mul_f32 v89, v89, v204\n"
"v_mul_f32 v90, v90, v204\n"
"v_mul_f32 v91, v91, v204\n"
"v_mul_f32 v92, v92, v204\n"
"v_mul_f32 v93, v93, v204\n"
"v_mul_f32 v94, v94, v204\n"
"v_mul_f32 v95, v95, v204\n"
"v_exp_f32 v156, v156\n"
"v_exp_f32 v157, v157\n"
"v_exp_f32 v158, v158\n"
"v_exp_f32 v159, v159\n"
"v_exp_f32 v164, v164\n"
"v_exp_f32 v165, v165\n"
"v_exp_f32 v166, v166\n"
"v_exp_f32 v167, v167\n"
"v_cvt_pk_bf16_f32 v156, v156, v157\n"
"v_cvt_pk_bf16_f32 v157, v158, v159\n"
"v_exp_f32 v172, v172\n"
"v_exp_f32 v173, v173\n"
"v_exp_f32 v174, v174\n"
"v_exp_f32 v175, v175\n"
"v_cvt_pk_bf16_f32 v164, v164, v165\n"
"v_cvt_pk_bf16_f32 v165, v166, v167\n"
"v_cvt_pk_bf16_f32 v172, v172, v173\n"
"v_cvt_pk_bf16_f32 v173, v174, v175\n"
"v_exp_f32 v160, v160\n"
"v_exp_f32 v161, v161\n"
"v_exp_f32 v162, v162\n"
"v_exp_f32 v163, v163\n"
"v_exp_f32 v168, v168\n"
"v_exp_f32 v169, v169\n"
"v_exp_f32 v170, v170\n"
"v_exp_f32 v171, v171\n"
"v_cvt_pk_bf16_f32 v158, v160, v161\n"
"v_cvt_pk_bf16_f32 v159, v162, v163\n"
"v_exp_f32 v176, v176\n"
"v_exp_f32 v177, v177\n"
"v_exp_f32 v178, v178\n"
"v_exp_f32 v179, v179\n"
"v_cvt_pk_bf16_f32 v166, v168, v169\n"
"v_cvt_pk_bf16_f32 v167, v170, v171\n"
"v_cvt_pk_bf16_f32 v174, v176, v177\n"
"v_cvt_pk_bf16_f32 v175, v178, v179\n"
"v_mfma_f32_16x16x32_bf16 v[228:231], v[240:243], v[156:159], 0\n"
"v_mfma_f32_16x16x32_bf16 v[232:235], v[240:243], v[164:167], 0\n"
"v_mfma_f32_16x16x32_bf16 v[236:239], v[240:243], v[172:175], 0\n"
"ds_read_b128 v[204:207], v248 offset:8192\n"
"ds_read_b128 v[208:211], v248 offset:8704\n"
"s_waitcnt lgkmcnt(0)\n"
"v_mfma_f32_16x16x32_bf16 v[180:183], v[204:207], v[96:99], v[144:147]\n"
"v_mfma_f32_16x16x32_bf16 v[188:191], v[204:207], v[112:115], v[148:151]\n"
"v_mfma_f32_16x16x32_bf16 v[196:199], v[204:207], v[128:131], v[152:155]\n"
"v_mfma_f32_16x16x32_bf16 v[180:183], v[208:211], v[100:103], v[180:183]\n"
"v_mfma_f32_16x16x32_bf16 v[188:191], v[208:211], v[116:119], v[188:191]\n"
"v_mfma_f32_16x16x32_bf16 v[196:199], v[208:211], v[132:135], v[196:199]\n"
"ds_read_b128 v[204:207], v248 offset:9216\n"
"ds_read_b128 v[208:211], v248 offset:9728\n"
"s_waitcnt lgkmcnt(0)\n"
"v_mfma_f32_16x16x32_bf16 v[180:183], v[204:207], v[104:107], v[180:183]\n"
"v_mfma_f32_16x16x32_bf16 v[188:191], v[204:207], v[120:123], v[188:191]\n"
"v_mfma_f32_16x16x32_bf16 v[196:199], v[204:207], v[136:139], v[196:199]\n"
"v_mfma_f32_16x16x32_bf16 v[180:183], v[208:211], v[108:111], v[180:183]\n"
"v_mfma_f32_16x16x32_bf16 v[188:191], v[208:211], v[124:127], v[188:191]\n"
"v_mfma_f32_16x16x32_bf16 v[196:199], v[208:211], v[140:143], v[196:199]\n"
"ds_read_b128 v[204:207], v248 offset:12288\n"
"ds_read_b128 v[208:211], v248 offset:12800\n"
"s_waitcnt lgkmcnt(0)\n"
"v_mfma_f32_16x16x32_bf16 v[184:187], v[204:207], v[96:99], v[144:147]\n"
"v_mfma_f32_16x16x32_bf16 v[192:195], v[204:207], v[112:115], v[148:151]\n"
"v_mfma_f32_16x16x32_bf16 v[200:203], v[204:207], v[128:131], v[152:155]\n"
"v_mfma_f32_16x16x32_bf16 v[184:187], v[208:211], v[100:103], v[184:187]\n"
"v_mfma_f32_16x16x32_bf16 v[192:195], v[208:211], v[116:119], v[192:195]\n"
"v_mfma_f32_16x16x32_bf16 v[200:203], v[208:211], v[132:135], v[200:203]\n"
"ds_read_b128 v[204:207], v248 offset:13312\n"
"ds_read_b128 v[208:211], v248 offset:13824\n"
"s_waitcnt lgkmcnt(0)\n"
"v_mfma_f32_16x16x32_bf16 v[184:187], v[204:207], v[104:107], v[184:187]\n"
"v_mfma_f32_16x16x32_bf16 v[192:195], v[204:207], v[120:123], v[192:195]\n"
"v_mfma_f32_16x16x32_bf16 v[200:203], v[204:207], v[136:139], v[200:203]\n"
"v_mfma_f32_16x16x32_bf16 v[184:187], v[208:211], v[108:111], v[184:187]\n"
"v_mfma_f32_16x16x32_bf16 v[192:195], v[208:211], v[124:127], v[192:195]\n"
"v_mfma_f32_16x16x32_bf16 v[200:203], v[208:211], v[140:143], v[200:203]\n"
"s_nop 7\n"
"v_exp_f32 v180, v180\n"
"v_exp_f32 v181, v181\n"
"v_exp_f32 v182, v182\n"
"v_exp_f32 v183, v183\n"
"v_exp_f32 v188, v188\n"
"v_exp_f32 v189, v189\n"
"v_exp_f32 v190, v190\n"
"v_exp_f32 v191, v191\n"
"v_cvt_pk_bf16_f32 v180, v180, v181\n"
"v_cvt_pk_bf16_f32 v181, v182, v183\n"
"v_exp_f32 v196, v196\n"
"v_exp_f32 v197, v197\n"
"v_exp_f32 v198, v198\n"
"v_exp_f32 v199, v199\n"
"v_cvt_pk_bf16_f32 v188, v188, v189\n"
"v_cvt_pk_bf16_f32 v189, v190, v191\n"
"v_cvt_pk_bf16_f32 v196, v196, v197\n"
"v_cvt_pk_bf16_f32 v197, v198, v199\n"
"s_nop 7\n"
"s_branch .Lk1_ret0\n"
".Lk1_slow1:\n"
"s_waitcnt lgkmcnt(0)\n"
"s_nop 7\n"
"v_mfma_f32_16x16x32_bf16 v[28:31], v[224:227], v[156:159], v[28:31]\n"
"v_mfma_f32_16x16x32_bf16 v[60:63], v[224:227], v[164:167], v[60:63]\n"
"v_mfma_f32_16x16x32_bf16 v[92:95], v[224:227], v[172:175], v[92:95]\n"
"s_nop 3\n"
"v_mov_b32 v156, 0\n"
"v_mov_b32 v157, 0\n"
"v_mov_b32 v158, 0\n"
"v_mov_b32 v159, 0\n"
"v_mov_b32 v164, 0\n"
"v_mov_b32 v165, 0\n"
"v_mov_b32 v166, 0\n"
"v_mov_b32 v167, 0\n"
"v_mov_b32 v172, 0\n"
"v_mov_b32 v173, 0\n"
"v_mov_b32 v174, 0\n"
"v_mov_b32 v175, 0\n"
"ds_read_b128 v[204:207], v248 offset:8192\n"
"ds_read_b128 v[208:211], v248 offset:8704\n"
"s_waitcnt lgkmcnt(0)\n"
"v_mfma_f32_16x16x32_bf16 v[180:183], v[204:207], v[96:99], 0\n"
"v_mfma_f32_16x16x32_bf16 v[188:191], v[204:207], v[112:115], 0\n"
"v_mfma_f32_16x16x32_bf16 v[196:199], v[204:207], v[128:131], 0\n"
"v_mfma_f32_16x16x32_bf16 v[180:183], v[208:211], v[100:103], v[180:183]\n"
"v_mfma_f32_16x16x32_bf16 v[188:191], v[208:211], v[116:119], v[188:191]\n"
"v_mfma_f32_16x16x32_bf16 v[196:199], v[208:211], v[132:135], v[196:199]\n"
"ds_read_b128 v[204:207], v248 offset:9216\n"
"ds_read_b128 v[208:211], v248 offset:9728\n"
"s_waitcnt lgkmcnt(0)\n"
"v_mfma_f32_16x16x32_bf16 v[180:183], v[204:207], v[104:107], v[180:183]\n"
"v_mfma_f32_16x16x32_bf16 v[188:191], v[204:207], v[120:123], v[188:191]\n"
"v_mfma_f32_16x16x32_bf16 v[196:199], v[204:207], v[136:139], v[196:199]\n"
"v_mfma_f32_16x16x32_bf16 v[180:183], v[208:211], v[108:111], v[180:183]\n"
"v_mfma_f32_16x16x32_bf16 v[188:191], v[208:211], v[124:127], v[188:191]\n"
"v_mfma_f32_16x16x32_bf16 v[196:199], v[208:211], v[140:143], v[196:199]\n"
"ds_read_b128 v[204:207], v248 offset:12288\n"
"ds_read_b128 v[208:211], v248 offset:12800\n"
"s_waitcnt lgkmcnt(0)\n"
"v_mfma_f32_16x16x32_bf16 v[184:187], v[204:207], v[96:99], 0\n"
"v_mfma_f32_16x16x32_bf16 v[192:195], v[204:207], v[112:115], 0\n"
"v_mfma_f32_16x16x32_bf16 v[200:203], v[204:207], v[128:131], 0\n"
"v_mfma_f32_16x16x32_bf16 v[184:187], v[208:211], v[100:103], v[184:187]\n"
"v_mfma_f32_16x16x32_bf16 v[192:195], v[208:211], v[116:119], v[192:195]\n"
"v_mfma_f32_16x16x32_bf16 v[200:203], v[208:211], v[132:135], v[200:203]\n"
"ds_read_b128 v[204:207], v248 offset:13312\n"
"ds_read_b128 v[208:211], v248 offset:13824\n"
"s_waitcnt lgkmcnt(0)\n"
"v_mfma_f32_16x16x32_bf16 v[184:187], v[204:207], v[104:107], v[184:187]\n"
"v_mfma_f32_16x16x32_bf16 v[192:195], v[204:207], v[120:123], v[192:195]\n"
"v_mfma_f32_16x16x32_bf16 v[200:203], v[204:207], v[136:139], v[200:203]\n"
"v_mfma_f32_16x16x32_bf16 v[184:187], v[208:211], v[108:111], v[184:187]\n"
"v_mfma_f32_16x16x32_bf16 v[192:195], v[208:211], v[124:127], v[192:195]\n"
"v_mfma_f32_16x16x32_bf16 v[200:203], v[208:211], v[140:143], v[200:203]\n"
"s_nop 7\n"
"v_max3_f32 v206, v180, v181, v182\n"
"v_max3_f32 v206, v206, v183, v184\n"
"v_max3_f32 v206, v206, v185, v186\n"
"v_max_f32 v206, v206, v187\n"
"v_mov_b32 v204, v206\n"
"v_mov_b32 v205, v206\n"
"s_nop 1\n"
"v_permlane32_swap_b32 v204, v205\n"
"v_max_f32 v206, v204, v205\n"
"v_mov_b32 v204, v206\n"
"v_mov_b32 v205, v206\n"
"s_nop 1\n"
"v_permlane16_swap_b32 v204, v205\n"
"v_max_f32 v206, v204, v205\n"
"v_sub_f32 v206, 0, v206\n"
"v_min_f32 v206, v206, v144\n"
"v_sub_f32 v204, v206, v144\n"
"v_exp_f32 v204, v204\n"
"v_mov_b32 v144, v206\n"
"v_mov_b32 v145, v206\n"
"v_mov_b32 v146, v206\n"
"v_mov_b32 v147, v206\n"
"v_add_f32 v180, v180, v206\n"
"v_add_f32 v181, v181, v206\n"
"v_add_f32 v182, v182, v206\n"
"v_add_f32 v183, v183, v206\n"
"v_add_f32 v184, v184, v206\n"
"v_add_f32 v185, v185, v206\n"
"v_add_f32 v186, v186, v206\n"
"v_add_f32 v187, v187, v206\n"
"v_mul_f32 v244, v244, v204\n"
"v_mul_f32 v0, v0, v204\n"
"v_mul_f32 v1, v1, v204\n"
"v_mul_f32 v2, v2, v204\n"
"v_mul_f32 v3, v3, v204\n"
"v_mul_f32 v4, v4, v204\n"
"v_mul_f32 v5, v5, v204\n"
"v_mul_f32 v6, v6, v204\n"
"v_mul_f32 v7, v7, v204\n"
"v_mul_f32 v8, v8, v204\n"
"v_mul_f32 v9, v9, v204\n"
"v_mul_f32 v10, v10, v204\n"
"v_mul_f32 v11, v11, v204\n"
"v_mul_f32 v12, v12, v204\n"
"v_mul_f32 v13, v13, v204\n"
"v_mul_f32 v14, v14, v204\n"
"v_mul_f32 v15, v15, v204\n"
"v_mul_f32 v16, v16, v204\n"
"v_mul_f32 v17, v17, v204\n"
"v_mul_f32 v18, v18, v204\n"
"v_mul_f32 v19, v19, v204\n"
"v_mul_f32 v20, v20, v204\n"
"v_mul_f32 v21, v21, v204\n"
"v_mul_f32 v22, v22, v204\n"
"v_mul_f32 v23, v23, v204\n"
"v_mul_f32 v24, v24, v204\n"
"v_mul_f32 v25, v25, v204\n"
"v_mul_f32 v26, v26, v204\n"
"v_mul_f32 v27, v27, v204\n"
"v_mul_f32 v28, v28, v204\n"
"v_mul_f32 v29, v29, v204\n"
"v_mul_f32 v30, v30, v204\n"
"v_mul_f32 v31, v31, v204\n"
"v_max3_f32 v206, v188, v189, v190\n"
"v_max3_f32 v206, v206, v191, v192\n"
"v_max3_f32 v206, v206, v193, v194\n"
"v_max_f32 v206, v206, v195\n"
"v_mov_b32 v204, v206\n"
"v_mov_b32 v205, v206\n"
"s_nop 1\n"
"v_permlane32_swap_b32 v204, v205\n"
"v_max_f32 v206, v204, v205\n"
"v_mov_b32 v204, v206\n"
"v_mov_b32 v205, v206\n"
"s_nop 1\n"
"v_permlane16_swap_b32 v204, v205\n"
"v_max_f32 v206, v204, v205\n"
"v_sub_f32 v206, 0, v206\n"
"v_min_f32 v206, v206, v148\n"
"v_sub_f32 v204, v206, v148\n"
"v_exp_f32 v204, v204\n"
"v_mov_b32 v148, v206\n"
"v_mov_b32 v149, v206\n"
"v_mov_b32 v150, v206\n"
"v_mov_b32 v151, v206\n"
"v_add_f32 v188, v188, v206\n"
"v_add_f32 v189, v189, v206\n"
"v_add_f32 v190, v190, v206\n"
"v_add_f32 v191, v191, v206\n"
"v_add_f32 v192, v192, v206\n"
"v_add_f32 v193, v193, v206\n"
"v_add_f32 v194, v194, v206\n"
"v_add_f32 v195, v195, v206\n"
"v_mul_f32 v245, v245, v204\n"
"v_mul_f32 v32, v32, v204\n"
"v_mul_f32 v33, v33, v204\n"
"v_mul_f32 v34, v34, v204\n"
"v_mul_f32 v35, v35, v204\n"
"v_mul_f32 v36, v36, v204\n"
"v_mul_f32 v37, v37, v204\n"
"v_mul_f32 v38, v38, v204\n"
"v_mul_f32 v39, v39, v204\n"
"v_mul_f32 v40, v40, v204\n"
"v_mul_f32 v41, v41, v204\n"
"v_mul_f32 v42, v42, v204\n"
"v_mul_f32 v43, v43, v204\n"
"v_mul_f32 v44, v44, v204\n"
"v_mul_f32 v45, v45, v204\n"
"v_mul_f32 v46, v46, v204\n"
"v_mul_f32 v47, v47, v204\n"
"v_mul_f32 v48, v48, v204\n"
"v_mul_f32 v49, v49, v204\n"
"v_mul_f32 v50, v50, v204\n"
"v_mul_f32 v51, v51, v204\n"
"v_mul_f32 v52, v52, v204\n"
"v_mul_f32 v53, v53, v204\n"
"v_mul_f32 v54, v54, v204\n"
"v_mul_f32 v55, v55, v204\n"
"v_mul_f32 v56, v56, v204\n"
"v_mul_f32 v57, v57, v204\n"
"v_mul_f32 v58, v58, v204\n"
"v_mul_f32 v59, v59, v204\n"
"v_mul_f32 v60, v60, v204\n"
"v_mul_f32 v61, v61, v204\n"
"v_mul_f32 v62, v62, v204\n"
"v_mul_f32 v63, v63, v204\n"
"v_max3_f32 v206, v196, v197, v198\n"
"v_max3_f32 v206, v206, v199, v200\n"
"v_max3_f32 v206, v206, v201, v202\n"
"v_max_f32 v206, v206, v203\n"
"v_mov_b32 v204, v206\n"
"v_mov_b32 v205, v206\n"
"s_nop 1\n"
"v_permlane32_swap_b32 v204, v205\n"
"v_max_f32 v206, v204, v205\n"
"v_mov_b32 v204, v206\n"
"v_mov_b32 v205, v206\n"
"s_nop 1\n"
"v_permlane16_swap_b32 v204, v205\n"
"v_max_f32 v206, v204, v205\n"
"v_sub_f32 v206, 0, v206\n"
"v_min_f32 v206, v206, v152\n"
"v_sub_f32 v204, v206, v152\n"
"v_exp_f32 v204, v204\n"
"v_mov_b32 v152, v206\n"
"v_mov_b32 v153, v206\n"
"v_mov_b32 v154, v206\n"
"v_mov_b32 v155, v206\n"
"v_add_f32 v196, v196, v206\n"
"v_add_f32 v197, v197, v206\n"
"v_add_f32 v198, v198, v206\n"
"v_add_f32 v199, v199, v206\n"
"v_add_f32 v200, v200, v206\n"
"v_add_f32 v201, v201, v206\n"
"v_add_f32 v202, v202, v206\n"
"v_add_f32 v203, v203, v206\n"
"v_mul_f32 v246, v246, v204\n"
"v_mul_f32 v64, v64, v204\n"
"v_mul_f32 v65, v65, v204\n"
"v_mul_f32 v66, v66, v204\n"
"v_mul_f32 v67, v67, v204\n"
"v_mul_f32 v68, v68, v204\n"
"v_mul_f32 v69, v69, v204\n"
"v_mul_f32 v70, v70, v204\n"
"v_mul_f32 v71, v71, v204\n"
"v_mul_f32 v72, v72, v204\n"
"v_mul_f32 v73, v73, v204\n"
"v_mul_f32 v74, v74, v204\n"
"v_mul_f32 v75, v75, v204\n"
"v_mul_f32 v76, v76, v204\n"
"v_mul_f32 v77, v77, v204\n"
"v_mul_f32 v78, v78, v204\n"
"v_mul_f32 v79, v79, v204\n"
"v_mul_f32 v80, v80, v204\n"
"v_mul_f32 v81, v81, v204\n"
"v_mul_f32 v82, v82, v204\n"
"v_mul_f32 v83, v83, v204\n"
"v_mul_f32 v84, v84, v204\n"
"v_mul_f32 v85, v85, v204\n"
"v_mul_f32 v86, v86, v204\n"
"v_mul_f32 v87, v87, v204\n"
"v_mul_f32 v88, v88, v204\n"
"v_mul_f32 v89, v89, v204\n"
"v_mul_f32 v90, v90, v204\n"
"v_mul_f32 v91, v91, v204\n"
"v_mul_f32 v92, v92, v204\n"
"v_mul_f32 v93, v93, v204\n"
"v_mul_f32 v94, v94, v204\n"
"v_mul_f32 v95, v95, v204\n"
"v_exp_f32 v180, v180\n"
"v_exp_f32 v181, v181\n"
"v_exp_f32 v182, v182\n"
"v_exp_f32 v183, v183\n"
"v_exp_f32 v188, v188\n"
"v_exp_f32 v189, v189\n"
"v_exp_f32 v190, v190\n"
"v_exp_f32 v191, v191\n"
"v_cvt_pk_bf16_f32 v180, v180, v181\n"
"v_cvt_pk_bf16_f32 v181, v182, v183\n"
"v_exp_f32 v196, v196\n"
"v_exp_f32 v197, v197\n"
"v_exp_f32 v198, v198\n"
"v_exp_f32 v199, v199\n"
"v_cvt_pk_bf16_f32 v188, v188, v189\n"
"v_cvt_pk_bf16_f32 v189, v190, v191\n"
"v_cvt_pk_bf16_f32 v196, v196, v197\n"
"v_cvt_pk_bf16_f32 v197, v198, v199\n"
"v_exp_f32 v184, v184\n"
"v_exp_f32 v185, v185\n"
"v_exp_f32 v186, v186\n"
"v_exp_f32 v187, v187\n"
"v_exp_f32 v192, v192\n"
"v_exp_f32 v193, v193\n"
"v_exp_f32 v194, v194\n"
"v_exp_f32 v195, v195\n"
"v_cvt_pk_bf16_f32 v182, v184, v185\n"
"v_cvt_pk_bf16_f32 v183, v186, v187\n"
"v_exp_f32 v200, v200\n"
"v_exp_f32 v201, v201\n"
"v_exp_f32 v202, v202\n"
"v_exp_f32 v203, v203\n"
"v_cvt_pk_bf16_f32 v190, v192, v193\n"
"v_cvt_pk_bf16_f32 v191, v194, v195\n"
"v_cvt_pk_bf16_f32 v198, v200, v201\n"
"v_cvt_pk_bf16_f32 v199, v202, v203\n"
"v_mfma_f32_16x16x32_bf16 v[228:231], v[240:243], v[180:183], 0\n"
"v_mfma_f32_16x16x32_bf16 v[232:235], v[240:243], v[188:191], 0\n"
"v_mfma_f32_16x16x32_bf16 v[236:239], v[240:243], v[196:199], 0\n"
"s_nop 7\n"
"s_branch .Lk1_ret1\n"
".Lk1_end:\n"
".Lk1_fin:\n"
  :: "s"(ka),"s"(blockIdx.x),"s"(w) : "v0","v1","v2","v3","v4","v5","v6","v7","v8","v9","v10","v11","v12","v13","v14","v15","v16","v17","v18","v19","v20","v21","v22","v23","v24","v25","v26","v27","v28","v29","v30","v31","v32","v33","v34","v35","v36","v37","v38","v39","v40","v41","v42","v43","v44","v45","v46","v47","v48","v49","v50","v51","v52","v53","v54","v55","v56","v57","v58","v59","v60","v61","v62","v63","v64","v65","v66","v67","v68","v69","v70","v71","v72","v73","v74","v75","v76","v77","v78","v79","v80","v81","v82","v83","v84","v85","v86","v87","v88","v89","v90","v91","v92","v93","v94","v95","v96","v97","v98","v99","v100","v101","v102","v103","v104","v105","v106","v107","v108","v109","v110","v111","v112","v113","v114","v115","v116","v117","v118","v119","v120","v121","v122","v123","v124","v125","v126","v127","v128","v129","v130","v131","v132","v133","v134","v135","v136","v137","v138","v139","v140","v141","v142","v143","v144","v145","v146","v147","v148","v149","v150","v151","v152","v153","v154","v155","v156","v157","v158","v159","v160","v161","v162","v163","v164","v165","v166","v167","v168","v169","v170","v171","v172","v173","v174","v175","v176","v177","v178","v179","v180","v181","v182","v183","v184","v185","v186","v187","v188","v189","v190","v191","v192","v193","v194","v195","v196","v197","v198","v199","v200","v201","v202","v203","v204","v205","v206","v207","v208","v209","v210","v211","v212","v213","v214","v215","v216","v217","v218","v219","v220","v221","v222","v223","v224","v225","v226","v227","v228","v229","v230","v231","v232","v233","v234","v235","v236","v237","v238","v239","v240","v241","v242","v243","v244","v245","v246","v247","v248","v249","v250","v251","v252","v253","v254","v255","s4","s5","s6","s7","s8","s9","s10","s11","s12","s13","s14","s15","s16","s17","s18","s19","s20","s21","s22","s23","s24","s25","s26","s27","s28","s29","s30","s31","s33","s34","s35","s36","s37","s38","s39","s40","s41","s42","s43","s44","s45","s46","s47","s48","s49","s50","s51","s52","s53","s54","s55","s56","s57","s58","s59","s60","s61","s62","s63","s64","s65","s66","s67","s68","s69","s70","s71","s72","s73","s74","s75","s76","s77","s78","s79","s80","s81","s82","s83","s84","s85","s86","s87","s88","s89","s90","s91","s92","s93","s94","s95","s96","s97","s98","s99","vcc","memory");
}
extern "C" void kernel_launch(void* const* d_in, const int* in_sizes, int n_in, void* d_out, int out_size, void* d_ws, size_t ws_size, hipStream_t stream) {
  (void)hipFuncSetAttribute((const void*)mha_k1,hipFuncAttributeMaxDynamicSharedMemorySize,163840);
  mha_k1<<<256,512,163840,stream>>>(d_in[0],d_in[1],d_in[2],d_in[3],d_ws,d_in[4],d_out);
}
```
